```python
import torch

def _softmax(x, axis=-1):
    return torch.softmax(x, dim=axis)
def _gelu(x):
    return torch.nn.functional.gelu(x, approximate="tanh")
def _silu(x):
    return torch.nn.functional.silu(x)
def _sigmoid(x):
    return torch.sigmoid(x)
def _layernorm(x, axis=-1, eps=1e-5):
    m = x.mean(dim=axis, keepdim=True)
    v = x.var(dim=axis, keepdim=True, unbiased=False)
    return (x - m) / torch.sqrt(v + eps)
def _scatter_add(tgt, idx, src, axis=0):
    return tgt.clone().index_add_(axis, idx, src)
def _segment_sum(src, seg, n):
    out = torch.zeros((n,) + src.shape[1:], dtype=src.dtype)
    return out.index_add_(0, seg, src)
def _pad(x, pw):
    flat = tuple(v for pair in reversed(pw) for v in pair)
    return torch.nn.functional.pad(x, flat)

kernel_name = 'sequential_preLN__ssm_conv_s4d__moe_arch_mod__prompt'

def setup_inputs():

    _w = (lambda a: (lambda t: t.to(torch.bfloat16) if t.is_floating_point() else t)(torch.as_tensor(a)))
    def _A_setup():
        Bsz, L, D, N = 1, 2048, 576, 128
        return {
            "u": _w((lambda *s: torch.randn(*s, dtype=torch.bfloat16) * 0.2)(Bsz, L, D)),
            "log_A_real": _w((lambda *s: torch.randn(*s, dtype=torch.bfloat16) * 6.4)(D, N) * 0.5),
            "A_imag": _w((lambda *s: torch.randn(*s, dtype=torch.bfloat16) * 0.2)(D, N) * 3.14),
            "C_real": _w((lambda *s: torch.randn(*s, dtype=torch.bfloat16) * 0.2)(D, N) / N ** 0.5),
            "C_imag": _w((lambda *s: torch.randn(*s, dtype=torch.bfloat16) * 0.2)(D, N) / N ** 0.5),
            "log_dt": _w((lambda *s: torch.randn(*s, dtype=torch.bfloat16) * 0.2)(D) * 0.5 - 4.0),
            "D_skip": _w((lambda *s: torch.randn(*s, dtype=torch.bfloat16) * 0.2)(D)),
        }
    _A_in = _A_setup()

    def _B_setup():
        B, S, D, F, C = 1, 2048, 576, 4608, 8
        return {
            "x": _w((lambda *s: torch.randn(*s, dtype=torch.bfloat16) * 0.2)(B, S, D)),
            "w_r": _w((lambda *s: torch.randn(*s, dtype=torch.bfloat16) * 0.2)(D)),
            "w_gate": _w((lambda *s: torch.randn(*s, dtype=torch.bfloat16) * 0.2)(D, F)),
            "w_up": _w((lambda *s: torch.randn(*s, dtype=torch.bfloat16) * 0.2)(D, F)),
            "w_down": _w((lambda *s: torch.randn(*s, dtype=torch.bfloat16) * 0.2)(F, D)),
        }
    _B_in = _B_setup()

    return {
        "A_u": _A_in["u"],
        "A_log_A_real": _A_in["log_A_real"],
        "A_A_imag": _A_in["A_imag"],
        "A_C_real": _A_in["C_real"],
        "A_C_imag": _A_in["C_imag"],
        "A_log_dt": _A_in["log_dt"],
        "A_D_skip": _A_in["D_skip"],
        "B_x": _B_in["x"],
        "B_w_r": _B_in["w_r"],
        "B_w_gate": _B_in["w_gate"],
        "B_w_up": _B_in["w_up"],
        "B_w_down": _B_in["w_down"],
    }

def reference(A_u, A_log_A_real, A_A_imag, A_C_real, A_C_imag, A_log_dt, A_D_skip, B_x, B_w_r, B_w_gate, B_w_up, B_w_down):
    def _A_ref(u, log_A_real, A_imag, C_real, C_imag, log_dt, D_skip):
        L = u.shape[1]
        dt = torch.exp(log_dt)
        A = -torch.exp(log_A_real) + 1j * A_imag
        C = (C_real + 1j * C_imag) * dt[:, None]
        pos = (lambda x, dt: x.to(dt))(torch.arange(L), log_dt.dtype)
        P = torch.exp((A * dt[:, None])[:, :, None] * pos[None, None, :])
        K = 2.0 * torch.einsum("dn,dnl->dl", C, P).real
        L2 = 2 * L
        u_t = (lambda x, *d: x.permute(*d))(u, 0, 2, 1)
        Kf = (lambda x, *a, **k: torch.fft.rfft(x.float(), *a, **k))(K, L2)
        Uf = (lambda x, *a, **k: torch.fft.rfft(x.float(), *a, **k))(u_t, L2)
        y = (lambda x, *a, **k: torch.fft.irfft(x, *a, **k).to(torch.bfloat16))(Uf * Kf[None, :, :], L2)[:, :, :L]
        y = (lambda x, *d: x.permute(*d))(y, 0, 2, 1)
        return y + u * D_skip[None, None, :]

    def _B_ref(x, w_r, w_gate, w_up, w_down):
        B, S, D = x.shape
        C = 8
        r = x @ w_r
        idx = torch.argsort(-r, axis=-1)[:, :C]
        idx_e = (lambda x, n, axis: torch.repeat_interleave(x, n, dim=axis))(idx.reshape(B, C, 1), D, axis=2)
        x_sel = (lambda x, i, axis: torch.take_along_dim(x, i, dim=axis))(x, idx_e, axis=1)
        g = _silu(x_sel @ w_gate)
        u = x_sel @ w_up
        y = (g * u) @ w_down
        r_sel = _sigmoid((lambda x, i, axis: torch.take_along_dim(x, i, dim=axis))(r, idx, axis=1))
        y = y * r_sel.reshape(B, C, 1)
        b_off = (torch.arange(B) * S).reshape(B, 1)
        flat_idx = (idx + b_off).reshape(B * C)
        zero = (x * 0.0).reshape(B * S, D)
        upd = _scatter_add(zero, flat_idx, y.reshape(B * C, D), axis=0)
        return x + upd.reshape(B, S, D)

    x = A_u
    h = x + _A_ref(_layernorm(x, axis=-1), A_log_A_real, A_A_imag, A_C_real, A_C_imag, A_log_dt, A_D_skip)
    h2 = h + _B_ref(_layernorm(h, axis=-1), B_w_r, B_w_gate, B_w_up, B_w_down)
    return h2
```

```cpp
#include <hip/hip_runtime.h>
#include <stdint.h>

namespace {

constexpr int SEQ = 2048, DM = 576, NM = 128, FF = 4608;
constexpr int NT = 576;
constexpr int NCONV = 144, NFFN = 96;
constexpr unsigned SENT = 0xFFFFFFFFu;
constexpr int SC1 = 16;
constexpr int SPIN_CAP = 1 << 16;

typedef unsigned short u16;
typedef unsigned v4u __attribute__((ext_vector_type(4)));
typedef unsigned v2u __attribute__((ext_vector_type(2)));
typedef float f32x2 __attribute__((ext_vector_type(2)));
typedef float f32x4 __attribute__((ext_vector_type(4)));
typedef float f32x16 __attribute__((ext_vector_type(16)));
typedef _Float16 f16x8 __attribute__((ext_vector_type(8)));
typedef __bf16 bf16x2 __attribute__((ext_vector_type(2)));
typedef __bf16 bf16x8 __attribute__((ext_vector_type(8)));
typedef __amdgpu_buffer_rsrc_t rsrc_t;

constexpr int WS_EPOCH = 0;
constexpr int WS_INIT = 4096;
constexpr int WS_STAT = WS_INIT + 256 * 64;
constexpr int WS_RREC = WS_STAT + SEQ * 8;
constexpr int WS_DONE = WS_RREC + 128 * 64;
constexpr int WS_HT = 65536;
constexpr int WS_WROW = WS_HT + SEQ * DM * 2;
constexpr int WS_PART = WS_WROW + SEQ * DM * 2;
constexpr int WS_END = WS_PART + NFFN * 8 * DM * 4;
static_assert(WS_DONE + 128 * 64 <= WS_HT, "ws map");

constexpr int TROW = 528;
constexpr int WSCR = 32 * TROW;
constexpr int PAIR = 2 * WSCR;
constexpr int L_ACC = 0;
constexpr int L_KD = 4096;
constexpr int L_KIMG = 6144;
constexpr int KCOPY = 4160;
constexpr int L_UIMG = WSCR + 6144;
constexpr int L_Y = WSCR + 11264;
constexpr int L_XCOL = 4 * PAIR;
constexpr int L_MISC = L_XCOL + 16384;
constexpr int L_WR = L_MISC + 256;
constexpr int LDS_BYTES = L_WR + 1152;
constexpr int HS_RS = 1160;
constexpr int L_HS = 0, L_WT = PAIR, L_OT = 2 * PAIR;
static_assert(L_KIMG + 2 * KCOPY <= WSCR && L_Y + 8 * 640 <= PAIR && 16 * HS_RS <= WSCR + 11264, "lds map");
static_assert(LDS_BYTES <= 160 * 1024, "lds size");

#define MEMBAR() asm volatile("" ::: "memory")
#define BAR() asm volatile("s_waitcnt lgkmcnt(0)\n\ts_barrier" ::: "memory")
#define DRAIN() asm volatile("s_waitcnt vmcnt(0)" ::: "memory")

#define STAMP(i) do {} while (0)
#define STAMP_INIT() do {} while (0)
#define STAMP_FLUSH() do {} while (0)

__device__ __forceinline__ float blo(unsigned v) { return __uint_as_float(v << 16); }
__device__ __forceinline__ float bhi(unsigned v) { return __uint_as_float(v & 0xffff0000u); }
__device__ __forceinline__ unsigned pkbf(float a, float b) {
  const f32x2 f = {a, b};
  return __builtin_bit_cast(unsigned, __builtin_convertvector(f, bf16x2));
}
__device__ __forceinline__ float rbf(float f) { return blo(pkbf(f, f)); }
__device__ __forceinline__ unsigned pkh(float a, float b) {
  return __builtin_bit_cast(unsigned, __builtin_amdgcn_cvt_pkrtz(a, b));
}
__device__ __forceinline__ unsigned nosent(unsigned v) { return min(v, 0xFFFFFFFEu); }
__device__ __forceinline__ v4u nosent4(v4u v) { return v4u{nosent(v.x), nosent(v.y), nosent(v.z), nosent(v.w)}; }
__device__ __forceinline__ bool valid4(v4u v) { return max(max(v.x, v.y), max(v.z, v.w)) != SENT; }

template <int CTRL, int RM> __device__ __forceinline__ float dppf(float v) {
  return __int_as_float(__builtin_amdgcn_update_dpp(0, __float_as_int(v), CTRL, RM, 0xf, false));
}
template <int CTRL, int RM> __device__ __forceinline__ unsigned dppu(unsigned v) {
  return (unsigned)__builtin_amdgcn_update_dpp(0, (int)v, CTRL, RM, 0xf, false);
}
__device__ __forceinline__ float wave_sum(float v) {
  v += dppf<0x111, 0xf>(v);
  v += dppf<0x112, 0xf>(v);
  v += dppf<0x114, 0xf>(v);
  v += dppf<0x118, 0xf>(v);
  v += dppf<0x142, 0xa>(v);
  v += dppf<0x143, 0xc>(v);
  return __int_as_float(__builtin_amdgcn_readlane(__float_as_int(v), 63));
}
__device__ __forceinline__ unsigned half_max_u(unsigned v) {
  v = max(v, dppu<0x111, 0xf>(v));
  v = max(v, dppu<0x112, 0xf>(v));
  v = max(v, dppu<0x114, 0xf>(v));
  v = max(v, dppu<0x118, 0xf>(v));
  v = max(v, dppu<0x142, 0xa>(v));
  return v;
}
__device__ __forceinline__ unsigned wave_max_u(unsigned v) {
  v = half_max_u(v);
  return max((unsigned)__builtin_amdgcn_readlane((int)v, 31), (unsigned)__builtin_amdgcn_readlane((int)v, 63));
}

__device__ __forceinline__ v4u ld4(rsrc_t rs, int off) { return __builtin_amdgcn_raw_buffer_load_b128(rs, off, 0, SC1); }
__device__ __forceinline__ v2u ld2(rsrc_t rs, int off) { return __builtin_amdgcn_raw_buffer_load_b64(rs, off, 0, SC1); }
__device__ __forceinline__ unsigned ld1(rsrc_t rs, int off) { return __builtin_amdgcn_raw_buffer_load_b32(rs, off, 0, SC1); }
__device__ __forceinline__ void st4(v4u v, rsrc_t rs, int off) { __builtin_amdgcn_raw_buffer_store_b128(v, rs, off, 0, SC1); }
__device__ __forceinline__ void st2(v2u v, rsrc_t rs, int off) { __builtin_amdgcn_raw_buffer_store_b64(v, rs, off, 0, SC1); }
__device__ __forceinline__ void st1(unsigned v, rsrc_t rs, int off) { __builtin_amdgcn_raw_buffer_store_b32(v, rs, off, 0, SC1); }

__device__ __forceinline__ void ln_stats(const unsigned (&xw)[5], int lane, float& mbf, float& sbf) {
  float s = 0.f;
#pragma unroll
  for (int k = 0; k < 5; k++) s += blo(xw[k]) + bhi(xw[k]);
  const float mean = wave_sum(s) * (1.0f / DM);
  float q = 0.f;
#pragma unroll
  for (int k = 0; k < 4; k++) {
    const float d0 = blo(xw[k]) - mean, d1 = bhi(xw[k]) - mean;
    q = fmaf(d0, d0, fmaf(d1, d1, q));
  }
  {
    const float d0 = blo(xw[4]) - mean, d1 = bhi(xw[4]) - mean;
    q += lane < 32 ? fmaf(d0, d0, d1 * d1) : 0.f;
  }
  q = wave_sum(q);
  mbf = rbf(mean);
  sbf = rbf(__builtin_amdgcn_sqrtf(rbf(rbf(q * (1.0f / DM)) + 1e-5f)));
}
__device__ __forceinline__ unsigned ln_apply(unsigned xp, float m, float rinv) {
  const unsigned d = pkbf(blo(xp) - m, bhi(xp) - m);
  return pkbf(blo(d) * rinv, bhi(d) * rinv);
}

__device__ __forceinline__ void sweep_init(rsrc_t rs, unsigned tag, int lane) {
  for (int sp = 0; sp < SPIN_CAP; sp++) {
    MEMBAR();
    bool ok = true;
#pragma unroll
    for (int k = 0; k < 4; k++) {
      const int i = min(lane + 64 * k, NCONV + NFFN - 1);
      ok &= ld1(rs, WS_INIT + i * 64) == tag;
    }
    if (__all(ok)) break;
    __builtin_amdgcn_s_sleep(4);
  }
}

struct cf { float re, im; };
__device__ __forceinline__ cf cmul(cf a, cf b) { return cf{fmaf(a.re, b.re, -a.im * b.im), fmaf(a.re, b.im, a.im * b.re)}; }
__device__ __forceinline__ cf cexp_p2(float p, float ch, float ph, float pl) {
  const float mag = __builtin_amdgcn_exp2f(p * ch);
  const float fr = fmaf(p, pl, __builtin_amdgcn_fractf(p * ph));
  return cf{mag * __builtin_amdgcn_cosf(fr), mag * __builtin_amdgcn_sinf(fr)};
}
__device__ __forceinline__ void put_row(char* scr, int row, cf t) {
  const auto h = __builtin_amdgcn_cvt_pkrtz(t.re, t.im);
  const unsigned lo = pkh(t.re - (float)h[0], t.im - (float)h[1]);
  *(unsigned*)(scr + row * TROW) = __builtin_bit_cast(unsigned, h);
  *(unsigned*)(scr + row * TROW + 256) = lo;
}

__global__ __launch_bounds__(NT, 3) void k_main(const u16* __restrict__ x, const u16* __restrict__ lar,
                                                const u16* __restrict__ aim, const u16* __restrict__ crl,
                                                const u16* __restrict__ cim, const u16* __restrict__ ldt,
                                                const u16* __restrict__ dskip, const u16* __restrict__ wr,
                                                const u16* __restrict__ wg, const u16* __restrict__ wu,
                                                const u16* __restrict__ wd, u16* __restrict__ out, char* ws) {
  extern __shared__ __attribute__((aligned(16))) char smem[];
  const int tid = threadIdx.x, lane = tid & 63;
  const int wave = __builtin_amdgcn_readfirstlane(tid >> 6);
  const int xcd = blockIdx.x & 7, loc = blockIdx.x >> 3;
  const rsrc_t rs = __builtin_amdgcn_make_buffer_rsrc((void*)ws, 0, WS_END, 0x00020000);
  const rsrc_t ro = __builtin_amdgcn_make_buffer_rsrc((void*)out, 0, SEQ * DM * 2, 0x00020000);
  const v4u sent4 = {SENT, SENT, SENT, SENT};
  STAMP_INIT();
  STAMP(0);
  const unsigned tag = ld1(rs, WS_EPOCH) + 1u;

  if (loc < 18) {
    const int cg = xcd * 18 + loc;
    const int rb = xcd * 16 + loc;
    const int pch = wave >> 1;
    char* const pair = smem + pch * PAIR;
    char* const scr = smem + wave * WSCR;
    unsigned pr[2][4];
    unsigned pdt = 0;
    const v2u dk = *(const v2u*)(dskip + cg * 4);
    if (wave < 8) {
      const int d = cg * 4 + pch;
      pdt = ldt[d];
#pragma unroll
      for (int ps = 0; ps < 2; ps++) {
        const int i = d * NM + (wave & 1) * 64 + ps * 32 + (lane & 31);
        pr[ps][0] = lar[i]; pr[ps][1] = aim[i]; pr[ps][2] = crl[i]; pr[ps][3] = cim[i];
      }
      st4(sent4, rs, WS_HT + cg * 16384 + tid * 16);
      st4(sent4, rs, WS_HT + cg * 16384 + 8192 + tid * 16);
      if (loc < 16) {
        st4(sent4, rs, WS_WROW + rb * 18432 + tid * 16);
        st4(sent4, rs, WS_WROW + rb * 18432 + 8192 + tid * 16);
        if (tid < 128) st4(sent4, rs, WS_WROW + rb * 18432 + 16384 + tid * 16);
        else if (tid < 130) st4(sent4, rs, WS_RREC + rb * 64 + (tid - 128) * 16);
        else if (tid == 130) st4(sent4, rs, WS_DONE + rb * 64);
      }
    } else if (lane < 8) {
      *(unsigned*)(smem + L_MISC + lane * 4) = 0u;
    }
    BAR();
    v4u sg[2];
    if (wave == 8) {
      v2u xc[32];
      unsigned wv[5];
#pragma unroll
      for (int k = 0; k < 5; k++) wv[k] = ((const unsigned*)wr)[min(lane + 64 * k, 287)];
#pragma unroll
      for (int i = 0; i < 32; i++) xc[i] = *(const v2u*)(x + (size_t)(i * 64 + lane) * DM + cg * 4);
      __builtin_amdgcn_sched_barrier(0);
#pragma unroll
      for (int k = 0; k < 5; k++) *(unsigned*)(smem + L_WR + min(lane + 64 * k, 287) * 4) = wv[k];
#pragma unroll
      for (int i = 0; i < 32; i++) *(v2u*)(smem + L_XCOL + (i * 64 + lane) * 8) = xc[i];
      STAMP(1);
    } else {
      const bool hf = lane >= 32;
      const float dt = rbf(expf(blo(pdt)));
      unsigned cm = 0;
#pragma unroll
      for (int ps = 0; ps < 2; ps++)
        cm = max(cm, max(__float_as_uint(fabsf(blo(pr[ps][2]) * dt)), __float_as_uint(fabsf(blo(pr[ps][3]) * dt))));
      const unsigned ce = min(max(wave_max_u(cm) >> 23, 40u), 254u);
      const float csc = __uint_as_float((267u - ce) << 23), cinv = __uint_as_float((ce - 26u) << 23);
      STAMP(1);
      f32x16 acc;
#pragma unroll
      for (int i = 0; i < 16; i++) acc[i] = 0.f;
#pragma unroll 1
      for (int ps = 0; ps < 2; ps++) {
        const unsigned p0 = ps ? pr[1][0] : pr[0][0], p1 = ps ? pr[1][1] : pr[0][1];
        const unsigned p2 = ps ? pr[1][2] : pr[0][2], p3 = ps ? pr[1][3] : pr[0][3];
        const float are = -rbf(expf(blo(p0))) * dt;
        const float aie = (hf ? -blo(p1) : blo(p1)) * dt;
        const float ch = are * 1.44269504f;
        const float ph = aie * 0.159154936f;
        const float pl = fmaf(aie, 0.159154936f, -ph) + aie * 7.13048051e-9f;
        const cf b0 = cexp_p2(hf ? 1.f : 8.f, ch, ph, pl), b1 = cexp_p2(hf ? 2.f : 16.f, ch, ph, pl);
        const cf b2 = cexp_p2(hf ? 4.f : 32.f, ch, ph, pl), b3 = cexp_p2(hf ? 256.f : 64.f, ch, ph, pl);
        const cf b4 = cexp_p2(hf ? 512.f : 128.f, ch, ph, pl), b5 = cexp_p2(hf ? 1024.f : 8.f, ch, ph, pl);
        const cf b6 = cf{hf ? b3.re : b1.re, hf ? b3.im : b1.im};
        char* const sl = scr + lane * 4;
        cf T[32];
        T[0] = cf{hf ? blo(p2) * dt * csc : 16384.f, hf ? -(blo(p3) * dt * csc) : 0.f};
        put_row(sl, 0, T[0]);
#pragma unroll
        for (int i = 1; i < 32; i++) {
          if (i == 9 || i == 17 || i == 25) T[i] = cmul(T[i - 1], b5);
          else if (i == 26 || i == 27) T[i] = cmul(T[i - 2], b6);
          else if (i & 1) T[i] = cmul(T[i - 1], b0);
          else if (i & 2) T[i] = cmul(T[i - 2], b1);
          else if (i & 4) T[i] = cmul(T[i - 4], b2);
          else if (i & 8) T[i] = cmul(T[i - 8], b3);
          else T[i] = cmul(T[i - 16], b4);
          put_row(sl, i, T[i]);
        }
        MEMBAR();
        const char* fr = scr + (lane & 31) * TROW + (lane >> 5) * 16;
#pragma unroll
        for (int ks = 0; ks < 4; ks++) {
          const f16x8 bh = *(const f16x8*)(fr + ks * 32), ah = *(const f16x8*)(fr + 128 + ks * 32);
          const f16x8 bl = *(const f16x8*)(fr + 256 + ks * 32), al = *(const f16x8*)(fr + 384 + ks * 32);
          acc = __builtin_amdgcn_mfma_f32_32x32x16_f16(ah, bh, acc, 0, 0, 0);
          acc = __builtin_amdgcn_mfma_f32_32x32x16_f16(ah, bl, acc, 0, 0, 0);
          acc = __builtin_amdgcn_mfma_f32_32x32x16_f16(al, bh, acc, 0, 0, 0);
        }
        MEMBAR();
      }
      STAMP(2);
      sg[0] = ld4(rs, WS_STAT + tid * 16);
      sg[1] = ld4(rs, WS_STAT + 8192 + tid * 16);
#pragma unroll
      for (int i = 0; i < 16; i++) acc[i] *= cinv;
#pragma unroll
      for (int q = 0; q < 4; q++)
        *(f32x4*)(scr + L_ACC + q * 1024 + lane * 16) = f32x4{acc[4 * q], acc[4 * q + 1], acc[4 * q + 2], acc[4 * q + 3]};
      asm volatile("s_waitcnt lgkmcnt(0)" ::: "memory");
      volatile unsigned* const pf = (volatile unsigned*)(smem + L_MISC);
      if (lane == 0) pf[wave] = 1u;
      for (int sp = 0; sp < SPIN_CAP * 16 && pf[wave ^ 1] == 0u; sp++) {}
      MEMBAR();
      {
        const char* oth = smem + (wave ^ 1) * WSCR + L_ACC + lane * 16;
#pragma unroll
        for (int q = 0; q < 4; q++) {
          const f32x4 o = *(const f32x4*)(oth + q * 1024);
#pragma unroll
          for (int i = 0; i < 4; i++) acc[4 * q + i] += o[i];
        }
      }
      unsigned km = 0;
#pragma unroll
      for (int i = 0; i < 16; i++) km = max(km, __float_as_uint(fabsf(acc[i])));
      const unsigned ke = min(max(wave_max_u(km) >> 23, 40u), 254u);
      const float ksc = __uint_as_float((268u - ke) << 23);
      if (lane == 0) *(float*)(smem + L_MISC + 32 + pch * 4) = __uint_as_float((ke - 14u) << 23);
      {
        _Float16* kd = (_Float16*)(scr + L_KD);
        const int j = lane & 31, h = lane >> 5;
#pragma unroll
        for (int i = 0; i < 4; i++) kd[8 * j + 4 * h + i] = (_Float16)(acc[i] * ksc);
        kd[256 + 4 * j + 2 * h] = (_Float16)(acc[4] * ksc);
        kd[257 + 4 * j + 2 * h] = (_Float16)(acc[6] * ksc);
        kd[384 + 2 * j + h] = (_Float16)(acc[8] * ksc);
        kd[448 + 2 * j + h] = (_Float16)(acc[12] * ksc);
        if (h == 0) {
          kd[512 + j] = (_Float16)(acc[14] * ksc);
          kd[544 + j] = (_Float16)(acc[5] * ksc);
          kd[576 + j] = (_Float16)(acc[9] * ksc);
          kd[608 + j] = (_Float16)(acc[13] * ksc);
          kd[640 + j] = (_Float16)(j == 0 ? acc[15] * ksc : 0.f);
        }
        MEMBAR();
        const int pt = (wave & 1) * 64 + lane;
        unsigned v[17];
#pragma unroll
        for (int k = 0; k < 17; k++) {
          const int l = 2047 - 16 * pt - k;
          const int bi = (int)(pkbf((float)l, 0.f) & 0xffffu) - (0x4300 - 128);
          const int idx = l < 0 ? 641 : (l < 128 ? l : bi);
          v[k] = ((const u16*)kd)[idx];
        }
        v4u c0[2], c1[2];
#pragma unroll
        for (int k = 0; k < 8; k++) {
          c0[k >> 2][k & 3] = v[2 * k] | (v[2 * k + 1] << 16);
          c1[k >> 2][k & 3] = v[2 * k + 1] | (v[2 * k + 2] << 16);
        }
        char* ki = pair + L_KIMG + pt * 32;
        *(v4u*)ki = c0[0];
        *(v4u*)(ki + 16) = c0[1];
        *(v4u*)(ki + KCOPY) = c1[0];
        *(v4u*)(ki + KCOPY + 16) = c1[1];
        const v4u z4 = {0u, 0u, 0u, 0u};
        if (pt < 4) *(v4u*)(pair + L_KIMG + (pt >> 1) * KCOPY + 4096 + (pt & 1) * 16) = z4;
        else if (pt < 34) *(v4u*)(pair + L_UIMG + (pt - 4) * 16) = z4;
      }
      STAMP(3);
      for (int sp = 0; sp < SPIN_CAP; sp++) {
        if (__all(sg[0].y == tag && sg[0].w == tag && sg[1].y == tag && sg[1].w == tag)) break;
        MEMBAR();
        sg[0] = ld4(rs, WS_STAT + tid * 16);
        sg[1] = ld4(rs, WS_STAT + 8192 + tid * 16);
      }
      DRAIN();
      STAMP(4);
    }
    BAR();
    v4u xk[2], ud[2];
    if (wave == 8) {
      if (lane == 0) st1(tag, rs, WS_INIT + cg * 64);
    } else {
#pragma unroll
      for (int k = 0; k < 2; k++) {
        xk[k] = *(const v4u*)(smem + L_XCOL + k * 8192 + tid * 16);
        const float m0 = blo(sg[k].x), r0 = __builtin_amdgcn_rcpf(bhi(sg[k].x));
        const float m1 = blo(sg[k].z), r1 = __builtin_amdgcn_rcpf(bhi(sg[k].z));
        const unsigned ua = ln_apply(xk[k].x, m0, r0), ub = ln_apply(xk[k].y, m0, r0);
        const unsigned uc = ln_apply(xk[k].z, m1, r1), ue = ln_apply(xk[k].w, m1, r1);
        char* up = smem + L_UIMG + (240 + 1024 * k + 2 * tid) * 2;
        *(unsigned*)(up) = pkh(blo(ua), blo(uc));
        *(unsigned*)(up + PAIR) = pkh(bhi(ua), bhi(uc));
        *(unsigned*)(up + 2 * PAIR) = pkh(blo(ub), blo(ue));
        *(unsigned*)(up + 3 * PAIR) = pkh(bhi(ub), bhi(ue));
        ud[k].x = pkbf(blo(ua) * blo(dk.x), bhi(ua) * bhi(dk.x));
        ud[k].y = pkbf(blo(ub) * blo(dk.y), bhi(ub) * bhi(dk.y));
        ud[k].z = pkbf(blo(uc) * blo(dk.x), bhi(uc) * bhi(dk.x));
        ud[k].w = pkbf(blo(ue) * blo(dk.y), bhi(ue) * bhi(dk.y));
      }
    }
    BAR();
    STAMP(5);
    if (wave < 4) {
      const int rc = lane & 15, g = lane >> 4;
      const char* cp = smem + wave * PAIR;
      const char* ka = cp + L_KIMG + ((15 - rc) & 1) * KCOPY + (896 + 4 * g + ((15 - rc) >> 1)) * 4;
      const char* ub = cp + L_UIMG + (2 * rc + g) * 16;
      f32x4 acc[8];
#pragma unroll
      for (int m = 0; m < 8; m++) acc[m] = f32x4{0.f, 0.f, 0.f, 0.f};
#pragma unroll 1
      for (int ph = 0; ph < 8; ph++) {
        v4u A[8];
        f16x8 B[8];
#pragma unroll
        for (int e = 0; e < 8; e++) {
          const unsigned* p = (const unsigned*)(ka - 512 * e + 64 * ph);
          A[e] = v4u{p[0], p[1], p[2], p[3]};
          B[e] = *(const f16x8*)(ub + 512 * e + 64 * ph);
        }
#pragma unroll
        for (int m = 0; m < 8; m++)
#pragma unroll
          for (int q = 0; q <= m; q++)
            acc[m] = __builtin_amdgcn_mfma_f32_16x16x32_f16(__builtin_bit_cast(f16x8, A[m - q]), B[q], acc[m], 0, 0, 0);
      }
      const float kinv = *(const float*)(smem + L_MISC + 32 + wave * 4);
#pragma unroll
      for (int m = 0; m < 8; m++)
        *(v2u*)(smem + wave * PAIR + L_Y + m * 640 + g * 160 + rc * 8) =
            v2u{pkbf(acc[m][0] * kinv, acc[m][1] * kinv), pkbf(acc[m][2] * kinv, acc[m][3] * kinv)};
    } else if (wave == 8 && loc < 16) {
      sweep_init(rs, tag, lane);
    }
    STAMP(6);
    BAR();
    if (wave < 8) {
#pragma unroll
      for (int k = 0; k < 2; k++) {
        const char* yp = smem + L_Y + (4 * k + (tid >> 7)) * 640 + ((tid >> 1) & 3) * 160 + ((tid >> 3) & 15) * 8 + (tid & 1) * 4;
        const unsigned y0 = *(const unsigned*)yp, y1 = *(const unsigned*)(yp + PAIR);
        const unsigned y2 = *(const unsigned*)(yp + 2 * PAIR), y3 = *(const unsigned*)(yp + 3 * PAIR);
        const unsigned za = pkbf(blo(y0) + blo(ud[k].x), blo(y1) + bhi(ud[k].x));
        const unsigned zb = pkbf(blo(y2) + blo(ud[k].y), blo(y3) + bhi(ud[k].y));
        const unsigned zc = pkbf(bhi(y0) + blo(ud[k].z), bhi(y1) + bhi(ud[k].z));
        const unsigned zd = pkbf(bhi(y2) + blo(ud[k].w), bhi(y3) + bhi(ud[k].w));
        v4u h;
        h.x = pkbf(blo(xk[k].x) + blo(za), bhi(xk[k].x) + bhi(za));
        h.y = pkbf(blo(xk[k].y) + blo(zb), bhi(xk[k].y) + bhi(zb));
        h.z = pkbf(blo(xk[k].z) + blo(zc), bhi(xk[k].z) + bhi(zc));
        h.w = pkbf(blo(xk[k].w) + blo(zd), bhi(xk[k].w) + bhi(zd));
        st4(nosent4(h), rs, WS_HT + cg * 16384 + k * 8192 + tid * 16);
      }
    }
    STAMP(7);
    if (loc >= 16) { STAMP_FLUSH(); return; }
    __builtin_amdgcn_s_sleep(16);
    {
      v4u hv[2];
      const int o0 = WS_HT + (tid >> 3) * 16384 + rb * 128 + (tid & 7) * 16;
      for (int sp = 0; sp < SPIN_CAP; sp++) {
        MEMBAR();
        hv[0] = ld4(rs, o0);
        hv[1] = ld4(rs, o0 + 72 * 16384);
        if (__all(valid4(hv[0]) && valid4(hv[1]))) break;
        __builtin_amdgcn_s_sleep(2);
      }
#pragma unroll
      for (int k = 0; k < 2; k++) {
        char* hp = smem + L_HS + (tid & 7) * 2 * HS_RS + ((tid >> 3) + 72 * k) * 8;
        *(v2u*)hp = v2u{hv[k].x, hv[k].y};
        *(v2u*)(hp + HS_RS) = v2u{hv[k].z, hv[k].w};
      }
    }
    STAMP(8);
    BAR();
    if (wave < 8) {
      unsigned hw[2][5], wv[5];
#pragma unroll
      for (int k = 0; k < 5; k++) {
        const bool ok = k < 4 || lane < 32;
        const int idx = ok ? lane + 64 * k : 0;
        hw[0][k] = *(const unsigned*)(smem + L_HS + (2 * wave) * HS_RS + idx * 4);
        hw[1][k] = *(const unsigned*)(smem + L_HS + (2 * wave + 1) * HS_RS + idx * 4);
        wv[k] = *(const unsigned*)(smem + L_WR + idx * 4);
        if (!ok) { hw[0][k] = 0u; hw[1][k] = 0u; wv[k] = 0u; }
      }
      float m[2], s[2], dot[2];
      ln_stats(hw[0], lane, m[0], s[0]);
      ln_stats(hw[1], lane, m[1], s[1]);
#pragma unroll
      for (int i = 0; i < 2; i++) {
        const float rinv = __builtin_amdgcn_rcpf(s[i]);
        float dd = 0.f;
#pragma unroll
        for (int k = 0; k < 5; k++) {
          const unsigned n = ln_apply(hw[i][k], m[i], rinv);
          dd = fmaf(blo(n), blo(wv[k]), fmaf(bhi(n), bhi(wv[k]), dd));
          if (k < 4 || lane < 32) {
            *(unsigned*)(smem + L_WT + (2 * wave + i) * 1152 + (lane + 64 * k) * 4) = nosent(n);
            *(unsigned*)(smem + L_OT + (2 * wave + i) * 1152 + (lane + 64 * k) * 4) =
                pkbf(blo(hw[i][k]) + blo(n), bhi(hw[i][k]) + bhi(n));
          }
        }
        dot[i] = dd;
      }
      dot[0] = wave_sum(dot[0]);
      dot[1] = wave_sum(dot[1]);
      if (lane == 0) *(unsigned*)(smem + L_MISC + 64 + wave * 4) = nosent(pkbf(dot[0], dot[1]));
    }
    BAR();
    if (tid < 2) st4(*(const v4u*)(smem + L_MISC + 64 + tid * 16), rs, WS_RREC + rb * 64 + tid * 16);
#pragma unroll
    for (int k = 0; k < 2; k++) st4(*(const v4u*)(smem + L_WT + k * 9216 + tid * 16), rs, WS_WROW + rb * 18432 + k * 9216 + tid * 16);
#pragma unroll
    for (int k = 0; k < 2; k++) st4(*(const v4u*)(smem + L_OT + k * 9216 + tid * 16), ro, rb * 18432 + k * 9216 + tid * 16);
    STAMP(9);
    DRAIN();
    BAR();
    if (tid == 0) st1(1u, rs, WS_DONE + rb * 64);
    STAMP(10);
    STAMP_FLUSH();
    return;
  }

  const bool ffn = loc < 30;
  const int fj = xcd * 12 + (loc - 18);
  {
    const int slot = ffn ? fj : 96 + 2 * (xcd * 2 + (loc - 30));
    unsigned xw[4][5];
    if (wave < 8) {
#pragma unroll
      for (int i = 0; i < 4; i++) {
        if (i < 2 || !ffn) {
          const unsigned* xr = (const unsigned*)(x + (size_t)(slot * 16 + (i >> 1) * 16 + 2 * wave + (i & 1)) * DM);
#pragma unroll
          for (int k = 0; k < 5; k++) xw[i][k] = (k < 4 || lane < 32) ? xr[lane + 64 * k] : 0u;
        }
      }
    }
    if (ffn) {
      st4(sent4, rs, WS_PART + fj * 18432 + tid * 16);
      st4(sent4, rs, WS_PART + fj * 18432 + 9216 + tid * 16);
    }
    if (wave < 8) {
#pragma unroll
      for (int i = 0; i < 2; i++) {
        if (i == 0 || !ffn) {
          float m0, s0, m1, s1;
          ln_stats(xw[2 * i], lane, m0, s0);
          ln_stats(xw[2 * i + 1], lane, m1, s1);
          const v4u g = {(__float_as_uint(m0) >> 16) | __float_as_uint(s0), tag, (__float_as_uint(m1) >> 16) | __float_as_uint(s1), tag};
          if (lane == 0) st4(g, rs, WS_STAT + (slot * 16 + i * 16 + 2 * wave) * 8);
        }
      }
    }
    STAMP(1);
    if (!ffn) { STAMP_FLUSH(); return; }
  }
  DRAIN();
  if (tid < 2) *(unsigned*)(smem + tid * 4) = 0u;
  BAR();
  if (tid == 0) st1(tag, rs, WS_INIT + (NCONV + fj) * 64);
  const int cl = lane & 15, g = lane >> 4;
  v4u FG[2][4], FU[2][4], FD[2][4];
  {
    v2u G[2][8], U[2][8], Dn[2][8];
    const int cc = 48 * fj + 4 * min(cl, 11);
#pragma unroll
    for (int ks = 0; ks < 2; ks++)
#pragma unroll
      for (int j = 0; j < 8; j++) {
        const int k = 64 * wave + 32 * ks + 8 * g + j;
        G[ks][j] = *(const v2u*)(wg + (size_t)k * FF + cc);
        U[ks][j] = *(const v2u*)(wu + (size_t)k * FF + cc);
      }
#pragma unroll
    for (int ks = 0; ks < 2; ks++)
#pragma unroll
      for (int j = 0; j < 8; j++) {
        const int f = 48 * fj + 32 * ks + 8 * (ks ? (g & 1) : g) + j;
        Dn[ks][j] = *(const v2u*)(wd + (size_t)f * DM + 64 * wave + 4 * cl);
      }
    __builtin_amdgcn_sched_barrier(0);
#pragma unroll
    for (int ks = 0; ks < 2; ks++)
#pragma unroll
      for (int c = 0; c < 4; c++)
#pragma unroll
        for (int i = 0; i < 4; i++) {
          const unsigned sel = (c & 1) ? 0x07060302u : 0x05040100u;
          FG[ks][c][i] = __builtin_amdgcn_perm(G[ks][2 * i + 1][c >> 1], G[ks][2 * i][c >> 1], sel);
          FU[ks][c][i] = __builtin_amdgcn_perm(U[ks][2 * i + 1][c >> 1], U[ks][2 * i][c >> 1], sel);
          FD[ks][c][i] = __builtin_amdgcn_perm(Dn[ks][2 * i + 1][c >> 1], Dn[ks][2 * i][c >> 1], sel);
        }
  }
  STAMP(2);
  constexpr int F_GM = 16, F_SEL = 64, F_GU = 512, F_CAND = 2048, F_RED = 16384;
  if (tid < 256) *(unsigned*)(smem + F_GU + tid * 4) = 0u;
  if (wave == 8) sweep_init(rs, tag, lane);
  BAR();
  STAMP(3);
  unsigned key[8];
  if (wave < 4) {
    v4u rv;
    for (int sp = 0; sp < SPIN_CAP; sp++) {
      MEMBAR();
      rv = ld4(rs, WS_RREC + (tid >> 1) * 64 + (tid & 1) * 16);
      if (__all(valid4(rv))) break;
      __builtin_amdgcn_s_sleep(2);
    }
    STAMP(4);
    unsigned mx = 0;
#pragma unroll
    for (int i = 0; i < 8; i++) {
      const unsigned b = (i & 1) ? (rv[i >> 1] & 0xffff0000u) : (rv[i >> 1] << 16);
      const unsigned o = (b & 0x80000000u) ? (~b & 0xffff0000u) : (b | 0x80000000u);
      key[i] = o | (unsigned)(SEQ - 1 - (tid * 8 + i));
      mx = max(mx, key[i]);
    }
    mx = half_max_u(mx);
    if ((lane & 31) == 31) *(unsigned*)(smem + F_GM + (tid >> 5) * 4) = mx;
  }
  BAR();
  if (wave < 4) {
    const v4u ga = *(const v4u*)(smem + F_GM), gb = *(const v4u*)(smem + F_GM + 16);
    const unsigned t0 = min(min(min(ga.x, ga.y), min(ga.z, ga.w)), min(min(gb.x, gb.y), min(gb.z, gb.w)));
#pragma unroll
    for (int i = 0; i < 8; i++)
      if (key[i] >= t0) {
        const unsigned p = atomicAdd((unsigned*)smem, 1u);
        *(unsigned*)(smem + F_CAND + p * 4) = key[i];
      }
  }
  BAR();
  unsigned mykey;
  {
    const int n = *(const int*)smem;
    const unsigned* cand = (const unsigned*)(smem + F_CAND);
    unsigned* sel = (unsigned*)(smem + F_SEL + wave * 32);
    for (int c = lane; c < n; c += 64) {
      const unsigned kc = cand[c];
      int rank = 0;
      for (int i = 0; i < n; i++) rank += cand[i] > kc;
      if (rank < 8) sel[rank] = kc;
    }
    asm volatile("s_waitcnt lgkmcnt(0)" ::: "memory");
    mykey = sel[lane & 7];
  }
  STAMP(5);
  const int myrow = SEQ - 1 - (int)(mykey & 0xffffu);
  v4u xa[2];
  {
    const int o0 = WS_WROW + myrow * 1152 + (64 * wave + 8 * g) * 2;
    for (int sp = 0; sp < SPIN_CAP; sp++) {
      MEMBAR();
      xa[0] = ld4(rs, o0);
      xa[1] = ld4(rs, o0 + 64);
      if (__all(valid4(xa[0]) && valid4(xa[1]))) break;
      __builtin_amdgcn_s_sleep(1);
    }
  }
  STAMP(6);
  {
#pragma unroll
    for (int c = 0; c < 4; c++) {
      f32x4 ag = {0.f, 0.f, 0.f, 0.f}, au = {0.f, 0.f, 0.f, 0.f};
#pragma unroll
      for (int ks = 0; ks < 2; ks++) {
        ag = __builtin_amdgcn_mfma_f32_16x16x32_bf16(__builtin_bit_cast(bf16x8, xa[ks]), __builtin_bit_cast(bf16x8, FG[ks][c]), ag, 0, 0, 0);
        au = __builtin_amdgcn_mfma_f32_16x16x32_bf16(__builtin_bit_cast(bf16x8, xa[ks]), __builtin_bit_cast(bf16x8, FU[ks][c]), au, 0, 0, 0);
      }
      *(f32x4*)(smem + F_RED + wave * 4096 + c * 1024 + lane * 16) = g < 2 ? ag : au;
    }
  }
  BAR();
  const int tk = fj / 12, cb = fj - tk * 12;
  const unsigned fkey = (unsigned)__builtin_amdgcn_readlane((int)mykey, tk);
  const int frow = SEQ - 1 - (int)(fkey & 0xffffu);
  v2u fh = {0u, 0u}, fw = {0u, 0u};
  unsigned fdone = 0;
  if (wave == 8) {
    const int q = min(lane, 11);
    fh = ld2(rs, WS_HT + (cb * 12 + q) * 16384 + frow * 8);
    fw = ld2(rs, WS_WROW + frow * 1152 + (cb * 48 + q * 4) * 2);
    fdone = ld1(rs, WS_DONE + (frow >> 4) * 64);
  } else {
    const int c = tid >> 7, wi = tid & 127;
    float gs = 0.f, us = 0.f;
#pragma unroll
    for (int w = 0; w < 9; w++) {
      gs += *(const float*)(smem + F_RED + w * 4096 + c * 1024 + wi * 4);
      us += *(const float*)(smem + F_RED + w * 4096 + c * 1024 + 512 + wi * 4);
    }
    const float gp = rbf(gs);
    const float gq = rbf(gp / (1.0f + __expf(-gp)));
    const int tok = (wi >> 6) * 4 + (wi & 3), col = ((wi >> 2) & 15);
    if (col < 12) *(u16*)(smem + F_GU + tok * 128 + (col * 4 + c) * 2) = (u16)(pkbf(gq * rbf(us), 0.f) & 0xffffu);
  }
  BAR();
  {
    const bf16x8 a0 = *(const bf16x8*)(smem + F_GU + (lane & 7) * 128 + g * 16);
    const bf16x8 a1 = *(const bf16x8*)(smem + F_GU + (lane & 7) * 128 + 64 + g * 16);
    f32x4 o[4];
#pragma unroll
    for (int c = 0; c < 4; c++) {
      o[c] = f32x4{0.f, 0.f, 0.f, 0.f};
      o[c] = __builtin_amdgcn_mfma_f32_16x16x32_bf16(a0, __builtin_bit_cast(bf16x8, FD[0][c]), o[c], 0, 0, 0);
      o[c] = __builtin_amdgcn_mfma_f32_16x16x32_bf16(a1, __builtin_bit_cast(bf16x8, FD[1][c]), o[c], 0, 0, 0);
    }
#pragma unroll
    for (int i = 0; i < 2; i++) {
      v4u v;
#pragma unroll
      for (int c = 0; c < 4; c++) v[c] = nosent(__float_as_uint(g < 2 ? o[c][i] : o[c][2 + i]));
      const int tok = 4 * (g & 1) + 2 * (g >> 1) + i;
      st4(v, rs, WS_PART + fj * 18432 + (tok * DM + 64 * wave + 4 * cl) * 4);
    }
  }
  STAMP(7);
  __builtin_amdgcn_s_sleep(16);
  {
    const int p = tid / 12, q = tid - p * 12;
    const int o0 = WS_PART + p * 18432 + (tk * DM + cb * 48 + q * 4) * 4;
    v4u a, b;
    for (int sp = 0; sp < SPIN_CAP; sp++) {
      MEMBAR();
      a = ld4(rs, o0);
      b = ld4(rs, o0 + 48 * 18432);
      if (__all(valid4(a) && valid4(b))) break;
      __builtin_amdgcn_s_sleep(2);
    }
    *(f32x4*)(smem + F_CAND + tid * 16) = __builtin_bit_cast(f32x4, a) + __builtin_bit_cast(f32x4, b);
  }
  STAMP(8);
  BAR();
  if (wave == 8) {
    const int q = min(cl, 11);
    f32x4 s = {0.f, 0.f, 0.f, 0.f};
#pragma unroll
    for (int i = 0; i < 12; i++) s += *(const f32x4*)(smem + F_CAND + ((g * 12 + i) * 12 + q) * 16);
#pragma unroll
    for (int i = 0; i < 4; i++) {
      s[i] += __shfl_xor(s[i], 16);
      s[i] += __shfl_xor(s[i], 32);
    }
    for (int sp = 0; sp < SPIN_CAP && __builtin_amdgcn_readfirstlane(fdone) == SENT; sp++) {
      MEMBAR();
      fdone = ld1(rs, WS_DONE + (frow >> 4) * 64);
    }
    const unsigned kb = fkey & 0xffff0000u;
    const float rv = __uint_as_float((kb & 0x80000000u) ? (kb & 0x7fffffffu) : (~kb & 0xffff0000u));
    const float sgm = rbf(1.0f / (1.0f + __expf(-rv)));
    const unsigned ya = pkbf(s[0], s[1]), yb = pkbf(s[2], s[3]);
    const unsigned ta = pkbf(blo(ya) * sgm, bhi(ya) * sgm), tb = pkbf(blo(yb) * sgm, bhi(yb) * sgm);
    const unsigned wa = pkbf(blo(fw.x) + blo(ta), bhi(fw.x) + bhi(ta)), wb = pkbf(blo(fw.y) + blo(tb), bhi(fw.y) + bhi(tb));
    const v2u o = {pkbf(blo(fh.x) + blo(wa), bhi(fh.x) + bhi(wa)), pkbf(blo(fh.y) + blo(wb), bhi(fh.y) + bhi(wb))};
    if (lane < 12) st2(o, ro, frow * 1152 + (cb * 48 + lane * 4) * 2);
    if (fj == 0 && lane == 0) st1(tag, rs, WS_EPOCH);
  }
  STAMP(9);
  STAMP_FLUSH();
}

}

extern "C" void kernel_launch(void* const* d_in, const int* in_sizes, int n_in, void* d_out, int out_size,
                              void* d_ws, size_t ws_size, hipStream_t stream) {
  (void)hipFuncSetAttribute((const void*)k_main, hipFuncAttributeMaxDynamicSharedMemorySize, LDS_BYTES);
  k_main<<<256, NT, LDS_BYTES, stream>>>((const u16*)d_in[0], (const u16*)d_in[1], (const u16*)d_in[2],
                                         (const u16*)d_in[3], (const u16*)d_in[4], (const u16*)d_in[5],
                                         (const u16*)d_in[6], (const u16*)d_in[8], (const u16*)d_in[9],
                                         (const u16*)d_in[10], (const u16*)d_in[11], (u16*)d_out, (char*)d_ws);
}
```

```cpp
#include <hip/hip_runtime.h>
#include <stdint.h>

namespace {

constexpr int SEQ = 2048, DM = 576, NM = 128, FF = 4608;
constexpr int NT = 576;
constexpr int NCONV = 144, NFFN = 72;
constexpr unsigned SENT = 0xFFFFFFFFu;
constexpr int SC1 = 16;
constexpr int SPIN_CAP = 1 << 16;
#define SLEEP_H 8
#define SLEEP_X 4
#define XC_FIRST 16
#define HELP_PRIO 2
#define W_EARLY 8
#define CODE_BYTES 18432
#define SLEEP_P 8

typedef unsigned short u16;
typedef unsigned v4u __attribute__((ext_vector_type(4)));
typedef unsigned v2u __attribute__((ext_vector_type(2)));
typedef float f32x2 __attribute__((ext_vector_type(2)));
typedef float f32x4 __attribute__((ext_vector_type(4)));
typedef float f32x16 __attribute__((ext_vector_type(16)));
typedef _Float16 f16x2 __attribute__((ext_vector_type(2)));
typedef _Float16 f16x8 __attribute__((ext_vector_type(8)));
typedef __bf16 bf16x2 __attribute__((ext_vector_type(2)));
typedef __bf16 bf16x8 __attribute__((ext_vector_type(8)));
typedef __amdgpu_buffer_rsrc_t rsrc_t;

constexpr int WS_EPOCH = 0;
constexpr int WS_INIT = 4096;
constexpr int WS_STAT = WS_INIT + 256 * 64;
constexpr int WS_RREC = WS_STAT + SEQ * 8;
constexpr int WS_XSEL = WS_RREC + SEQ * 4;
constexpr int XSLOT = 2560;
constexpr int WS_HT = 65536;
constexpr int WS_PART = WS_HT + SEQ * DM * 2;
constexpr int WS_END = WS_PART + NFFN * 8 * DM * 4;
constexpr int OOB = 0x7ffffff0;
static_assert(WS_XSEL + 8 * XSLOT <= WS_HT, "ws map");

constexpr int WSCR = 16384;
constexpr int PAIR = 2 * WSCR;
constexpr int L_KIMG = 4096;
constexpr int KCOPY = 4160;
constexpr int L_UIMG = WSCR + 4096;
constexpr int L_Y = WSCR + 8704;
constexpr int L_XCOL = 4 * PAIR;
constexpr int L_STAT = L_XCOL + 16384;
constexpr int L_MISC = L_STAT + 8192;
constexpr int L_WR = L_MISC + 256;
constexpr int L_RED = L_WR + 1152;
constexpr int LDS_BYTES = L_RED + 2 * 2304;
static_assert(L_KIMG + 2 * KCOPY <= WSCR && L_Y + 8 * 640 <= PAIR, "lds map");
static_assert(LDS_BYTES <= 160 * 1024, "lds size");

#define MEMBAR() asm volatile("" ::: "memory")
#define BAR() do { __builtin_amdgcn_sched_barrier(0); asm volatile("s_waitcnt lgkmcnt(0)\n\ts_barrier" ::: "memory"); __builtin_amdgcn_sched_barrier(0); } while (0)
#define LAS __attribute__((address_space(3)))
#define DRAIN() asm volatile("s_waitcnt vmcnt(0)" ::: "memory")
#define LGKM0() asm volatile("s_waitcnt lgkmcnt(0)" ::: "memory")
#define PIN(x) asm volatile("" : "+v"(x))
#define SCHED() __builtin_amdgcn_sched_barrier(0)

#define STAMP(i) do {} while (0)
#define STAMP_INIT() do {} while (0)
#define STAMP_FLUSH() do {} while (0)

__device__ __forceinline__ float blo(unsigned v) { return __uint_as_float(v << 16); }
__device__ __forceinline__ float bhi(unsigned v) { return __uint_as_float(v & 0xffff0000u); }
__device__ __forceinline__ unsigned pkbf(float a, float b) {
  const f32x2 f = {a, b};
  return __builtin_bit_cast(unsigned, __builtin_convertvector(f, bf16x2));
}
__device__ __forceinline__ float rbf(float f) { return blo(pkbf(f, f)); }
__device__ __forceinline__ unsigned pkh(float a, float b) {
  const f32x2 f = {a, b};
  return __builtin_bit_cast(unsigned, __builtin_convertvector(f, f16x2));
}
__device__ __forceinline__ unsigned nosent(unsigned v) { return min(v, 0xFFFFFFFEu); }
__device__ __forceinline__ bool valid4(v4u v) { return max(max(v.x, v.y), max(v.z, v.w)) != SENT; }

template <int CTRL, int RM> __device__ __forceinline__ float dppf(float v) {
  return __int_as_float(__builtin_amdgcn_update_dpp(0, __float_as_int(v), CTRL, RM, 0xf, false));
}
template <int CTRL, int RM> __device__ __forceinline__ unsigned dppu(unsigned v) {
  return (unsigned)__builtin_amdgcn_update_dpp(0, (int)v, CTRL, RM, 0xf, false);
}
__device__ __forceinline__ float wave_sum(float v) {
  v += dppf<0x111, 0xf>(v);
  v += dppf<0x112, 0xf>(v);
  v += dppf<0x114, 0xf>(v);
  v += dppf<0x118, 0xf>(v);
  v += dppf<0x142, 0xa>(v);
  v += dppf<0x143, 0xc>(v);
  return __int_as_float(__builtin_amdgcn_readlane(__float_as_int(v), 63));
}
__device__ __forceinline__ unsigned wave_max_u(unsigned v) {
  v = max(v, dppu<0x111, 0xf>(v));
  v = max(v, dppu<0x112, 0xf>(v));
  v = max(v, dppu<0x114, 0xf>(v));
  v = max(v, dppu<0x118, 0xf>(v));
  const unsigned a = (unsigned)__builtin_amdgcn_readlane((int)v, 15), b = (unsigned)__builtin_amdgcn_readlane((int)v, 31);
  const unsigned c = (unsigned)__builtin_amdgcn_readlane((int)v, 47), d = (unsigned)__builtin_amdgcn_readlane((int)v, 63);
  return max(max(a, b), max(c, d));
}

__device__ __forceinline__ unsigned row_max_u(unsigned v) {
  v = max(v, dppu<0x111, 0xf>(v));
  v = max(v, dppu<0x112, 0xf>(v));
  v = max(v, dppu<0x114, 0xf>(v));
  v = max(v, dppu<0x118, 0xf>(v));
  return (unsigned)__builtin_amdgcn_readlane((int)v, 15);
}
__device__ __forceinline__ float rdlane(float v, int l) { return __int_as_float(__builtin_amdgcn_readlane(__float_as_int(v), l)); }

__device__ __forceinline__ v4u ld4(rsrc_t rs, int off) { return __builtin_amdgcn_raw_buffer_load_b128(rs, off, 0, SC1); }
__device__ __forceinline__ v2u ld2(rsrc_t rs, int off) { return __builtin_amdgcn_raw_buffer_load_b64(rs, off, 0, SC1); }
__device__ __forceinline__ unsigned ld1(rsrc_t rs, int off) { return __builtin_amdgcn_raw_buffer_load_b32(rs, off, 0, SC1); }
__device__ __forceinline__ v2u ldp2(rsrc_t rs, int off, int soff) { return __builtin_amdgcn_raw_buffer_load_b64(rs, off, soff, 0); }
__device__ __forceinline__ void st4(v4u v, rsrc_t rs, int off) { __builtin_amdgcn_raw_buffer_store_b128(v, rs, off, 0, SC1); }
__device__ __forceinline__ void st2(v2u v, rsrc_t rs, int off) { __builtin_amdgcn_raw_buffer_store_b64(v, rs, off, 0, SC1); }
__device__ __forceinline__ void st1(unsigned v, rsrc_t rs, int off) { __builtin_amdgcn_raw_buffer_store_b32(v, rs, off, 0, SC1); }

__device__ __forceinline__ f32x4 mfma_bf(v4u a, v4u b, f32x4 c) {
  return __builtin_amdgcn_mfma_f32_16x16x32_bf16(__builtin_bit_cast(bf16x8, a), __builtin_bit_cast(bf16x8, b), c, 0, 0, 0);
}
__device__ __forceinline__ f32x4 mfma_h(v4u a, v4u b, f32x4 c) {
  return __builtin_amdgcn_mfma_f32_16x16x32_f16(__builtin_bit_cast(f16x8, a), __builtin_bit_cast(f16x8, b), c, 0, 0, 0);
}

__device__ __forceinline__ float diag(f32x4 q, int lane) {
  const float qa = (lane & 1) ? q[1] : q[0], qb = (lane & 1) ? q[3] : q[2];
  return (lane & 2) ? qb : qa;
}
__device__ __forceinline__ f32x4 sum9(const char* red) {
  f32x4 t = *(const f32x4*)red;
#pragma unroll
  for (int w = 1; w < 9; w++) t += *(const f32x4*)(red + w * 256);
  return t;
}
__device__ __forceinline__ void ln_round(float sum, float sq, float& m, float& s) {
  const float mean = sum * (1.0f / DM);
  const float var = fmaxf(fmaf(-mean, mean, sq * (1.0f / DM)), 0.f);
  const unsigned mv = pkbf(mean, var);
  m = blo(mv);
  s = rbf(__builtin_amdgcn_sqrtf(rbf(bhi(mv) + 1e-5f)));
}
__device__ __forceinline__ unsigned ln_apply(unsigned xp, float m, float rinv) {
  const unsigned d = pkbf(blo(xp) - m, bhi(xp) - m);
  return pkbf(blo(d) * rinv, bhi(d) * rinv);
}
__device__ __forceinline__ unsigned addbf(unsigned a, unsigned b) { return pkbf(blo(a) + blo(b), bhi(a) + bhi(b)); }

__device__ __forceinline__ void sweep_init(rsrc_t rs, unsigned tag, int lane, int first, int n) {
#pragma unroll 1
  for (int sp = 0; sp < SPIN_CAP; sp++) {
    MEMBAR();
    bool ok = true;
#pragma unroll
    for (int k = 0; k < 3; k++) ok &= ld1(rs, WS_INIT + (first + min(lane + 64 * k, n - 1)) * 64) == tag;
    if (__all(ok)) break;
    __builtin_amdgcn_s_sleep(4);
  }
}

struct cz { f32x2 v, s; };
__device__ __forceinline__ cz mkz(f32x2 v) {
  cz z;
  z.v = v;
  asm("v_pk_mov_b32 %0, %1, %1 op_sel:[1,0]" : "=v"(z.s) : "v"(v));
  return z;
}
#define CMUL_TXT(r, a, b, bs) "v_pk_mul_f32 " r ", " a ", " bs " op_sel:[1,0] neg_lo:[0,1]\n\tv_pk_fma_f32 " r ", " a ", " b ", " r " op_sel_hi:[0,1,1]\n\t"
#define CMULC_TXT(r, a, b, bs) "v_pk_mul_f32 " r ", " a ", " bs " op_sel:[1,0]\n\tv_pk_fma_f32 " r ", " a ", " b ", " r " op_sel_hi:[0,1,1] neg_hi:[0,1,0]\n\t"
__device__ __forceinline__ f32x2 cmul(f32x2 a, cz b) {
  f32x2 r;
  asm(CMUL_TXT("%0", "%1", "%2", "%3") : "=&v"(r) : "v"(a), "v"(b.v), "v"(b.s));
  return r;
}
__device__ __forceinline__ cz csq(cz a) { return mkz(cmul(a.v, a)); }
__device__ __forceinline__ void cmul2(f32x2& r, f32x2& q, f32x2 a, f32x2 c, cz b) {
  asm(CMUL_TXT("%0", "%2", "%4", "%5") CMUL_TXT("%1", "%3", "%4", "%5") : "=&v"(r), "=&v"(q) : "v"(a), "v"(c), "v"(b.v), "v"(b.s));
}
__device__ __forceinline__ void cmulc2(f32x2& r, f32x2& q, f32x2 a, cz b, f32x2 c, cz d) {
  asm(CMULC_TXT("%0", "%2", "%3", "%4") CMULC_TXT("%1", "%5", "%6", "%7")
      : "=&v"(r), "=&v"(q) : "v"(a), "v"(b.v), "v"(b.s), "v"(c), "v"(d.v), "v"(d.s));
}
__device__ __forceinline__ f32x2 cexp_p2(float p, float ch, float ph, float pl) {
  const float mag = __builtin_amdgcn_exp2f(p * ch);
  const float fr = fmaf(p, pl, __builtin_amdgcn_fractf(p * ph));
  return f32x2{mag * __builtin_amdgcn_cosf(fr), mag * __builtin_amdgcn_sinf(fr)};
}
__device__ __forceinline__ unsigned pkh2(f32x2 v) { return pkh(v.x, v.y); }

#define CONV_ASM_TEXT \
  "v_add_u32 v120, -512, %0\n" \
  "v_add_u32 v121, -1536, %0\n" \
  "v_add_u32 v122, -2560, %0\n" \
  "v_add_u32 v123, -3584, %0\n" \
  "v_mov_b32 v124, %1\n" \
  "v_mov_b32 v125, %2\n" \
  "ds_read_b128 v[88:91], v124 offset:0\n" \
  "ds_read2_b32 v[24:25], v120 offset0:128 offset1:129\n" \
  "ds_read2_b32 v[26:27], v120 offset0:130 offset1:131\n" \
  "ds_read_b128 v[92:95], v124 offset:512\n" \
  "ds_read_b128 v[96:99], v124 offset:1024\n" \
  "ds_read_b128 v[100:103], v124 offset:1536\n" \
  "ds_read2_b32 v[28:29], v120 offset0:0 offset1:1\n" \
  "ds_read2_b32 v[30:31], v120 offset0:2 offset1:3\n" \
  "ds_read2_b32 v[32:33], v121 offset0:128 offset1:129\n" \
  "ds_read2_b32 v[34:35], v121 offset0:130 offset1:131\n" \
  "ds_read2_b32 v[36:37], v121 offset0:0 offset1:1\n" \
  "ds_read2_b32 v[38:39], v121 offset0:2 offset1:3\n" \
  "ds_read2_b32 v[40:41], v122 offset0:128 offset1:129\n" \
  "ds_read2_b32 v[42:43], v122 offset0:130 offset1:131\n" \
  "ds_read2_b32 v[44:45], v122 offset0:0 offset1:1\n" \
  "ds_read2_b32 v[46:47], v122 offset0:2 offset1:3\n" \
  "ds_read2_b32 v[48:49], v123 offset0:128 offset1:129\n" \
  "ds_read2_b32 v[50:51], v123 offset0:130 offset1:131\n" \
  "ds_read2_b32 v[52:53], v123 offset0:0 offset1:1\n" \
  "ds_read2_b32 v[54:55], v123 offset0:2 offset1:3\n" \
  "v_mov_b32 v136, 0\n" \
  "v_mov_b32 v137, 0\n" \
  "v_mov_b32 v138, 0\n" \
  "v_mov_b32 v139, 0\n" \
  "v_mov_b32 v140, 0\n" \
  "v_mov_b32 v141, 0\n" \
  "v_mov_b32 v142, 0\n" \
  "v_mov_b32 v143, 0\n" \
  "v_mov_b32 v144, 0\n" \
  "v_mov_b32 v145, 0\n" \
  "v_mov_b32 v146, 0\n" \
  "v_mov_b32 v147, 0\n" \
  "v_mov_b32 v148, 0\n" \
  "v_mov_b32 v149, 0\n" \
  "v_mov_b32 v150, 0\n" \
  "v_mov_b32 v151, 0\n" \
  "v_mov_b32 v152, 0\n" \
  "v_mov_b32 v153, 0\n" \
  "v_mov_b32 v154, 0\n" \
  "v_mov_b32 v155, 0\n" \
  "v_mov_b32 v156, 0\n" \
  "v_mov_b32 v157, 0\n" \
  "v_mov_b32 v158, 0\n" \
  "v_mov_b32 v159, 0\n" \
  "v_mov_b32 v160, 0\n" \
  "v_mov_b32 v161, 0\n" \
  "v_mov_b32 v162, 0\n" \
  "v_mov_b32 v163, 0\n" \
  "v_mov_b32 v164, 0\n" \
  "v_mov_b32 v165, 0\n" \
  "v_mov_b32 v166, 0\n" \
  "v_mov_b32 v167, 0\n" \
  "s_mov_b32 s40, 0\n" \
  "L0_%=:\n" \
  "s_waitcnt lgkmcnt(10)\n" \
  "v_mfma_f32_16x16x32_f16 v[136:139], v[24:27], v[88:91], v[136:139]\n" \
  "ds_read_b128 v[104:107], v124 offset:64\n" \
  "v_mfma_f32_16x16x32_f16 v[140:143], v[24:27], v[92:95], v[140:143]\n" \
  "ds_read2_b32 v[56:57], v120 offset0:144 offset1:145\n" \
  "v_mfma_f32_16x16x32_f16 v[144:147], v[24:27], v[96:99], v[144:147]\n" \
  "ds_read2_b32 v[58:59], v120 offset0:146 offset1:147\n" \
  "v_mfma_f32_16x16x32_f16 v[148:151], v[24:27], v[100:103], v[148:151]\n" \
  "ds_read_b128 v[108:111], v124 offset:576\n" \
  "v_mfma_f32_16x16x32_f16 v[140:143], v[28:31], v[88:91], v[140:143]\n" \
  "ds_read_b128 v[112:115], v124 offset:1088\n" \
  "v_mfma_f32_16x16x32_f16 v[144:147], v[28:31], v[92:95], v[144:147]\n" \
  "ds_read_b128 v[116:119], v124 offset:1600\n" \
  "v_mfma_f32_16x16x32_f16 v[148:151], v[28:31], v[96:99], v[148:151]\n" \
  "ds_read2_b32 v[60:61], v120 offset0:16 offset1:17\n" \
  "v_mfma_f32_16x16x32_f16 v[152:155], v[28:31], v[100:103], v[152:155]\n" \
  "ds_read2_b32 v[62:63], v120 offset0:18 offset1:19\n" \
  "v_mfma_f32_16x16x32_f16 v[144:147], v[32:35], v[88:91], v[144:147]\n" \
  "ds_read2_b32 v[64:65], v121 offset0:144 offset1:145\n" \
  "v_mfma_f32_16x16x32_f16 v[148:151], v[32:35], v[92:95], v[148:151]\n" \
  "ds_read2_b32 v[66:67], v121 offset0:146 offset1:147\n" \
  "v_mfma_f32_16x16x32_f16 v[152:155], v[32:35], v[96:99], v[152:155]\n" \
  "ds_read2_b32 v[68:69], v121 offset0:16 offset1:17\n" \
  "v_mfma_f32_16x16x32_f16 v[156:159], v[32:35], v[100:103], v[156:159]\n" \
  "ds_read2_b32 v[70:71], v121 offset0:18 offset1:19\n" \
  "s_waitcnt lgkmcnt(10)\n" \
  "v_mfma_f32_16x16x32_f16 v[148:151], v[36:39], v[88:91], v[148:151]\n" \
  "ds_read2_b32 v[72:73], v122 offset0:144 offset1:145\n" \
  "v_mfma_f32_16x16x32_f16 v[152:155], v[36:39], v[92:95], v[152:155]\n" \
  "ds_read2_b32 v[74:75], v122 offset0:146 offset1:147\n" \
  "v_mfma_f32_16x16x32_f16 v[156:159], v[36:39], v[96:99], v[156:159]\n" \
  "ds_read2_b32 v[76:77], v122 offset0:16 offset1:17\n" \
  "v_mfma_f32_16x16x32_f16 v[160:163], v[36:39], v[100:103], v[160:163]\n" \
  "ds_read2_b32 v[78:79], v122 offset0:18 offset1:19\n" \
  "v_mfma_f32_16x16x32_f16 v[152:155], v[40:43], v[88:91], v[152:155]\n" \
  "ds_read2_b32 v[80:81], v123 offset0:144 offset1:145\n" \
  "v_mfma_f32_16x16x32_f16 v[156:159], v[40:43], v[92:95], v[156:159]\n" \
  "ds_read2_b32 v[82:83], v123 offset0:146 offset1:147\n" \
  "v_mfma_f32_16x16x32_f16 v[160:163], v[40:43], v[96:99], v[160:163]\n" \
  "ds_read2_b32 v[84:85], v123 offset0:16 offset1:17\n" \
  "v_mfma_f32_16x16x32_f16 v[164:167], v[40:43], v[100:103], v[164:167]\n" \
  "ds_read2_b32 v[86:87], v123 offset0:18 offset1:19\n" \
  "v_mfma_f32_16x16x32_f16 v[156:159], v[44:47], v[88:91], v[156:159]\n" \
  "v_mfma_f32_16x16x32_f16 v[160:163], v[44:47], v[92:95], v[160:163]\n" \
  "v_mfma_f32_16x16x32_f16 v[164:167], v[44:47], v[96:99], v[164:167]\n" \
  "v_mfma_f32_16x16x32_f16 v[160:163], v[48:51], v[88:91], v[160:163]\n" \
  "v_mfma_f32_16x16x32_f16 v[164:167], v[48:51], v[92:95], v[164:167]\n" \
  "v_mfma_f32_16x16x32_f16 v[164:167], v[52:55], v[88:91], v[164:167]\n" \
  "s_waitcnt lgkmcnt(10)\n" \
  "v_mfma_f32_16x16x32_f16 v[136:139], v[56:59], v[104:107], v[136:139]\n" \
  "ds_read_b128 v[88:91], v124 offset:128\n" \
  "v_mfma_f32_16x16x32_f16 v[140:143], v[56:59], v[108:111], v[140:143]\n" \
  "ds_read2_b32 v[24:25], v120 offset0:160 offset1:161\n" \
  "v_mfma_f32_16x16x32_f16 v[144:147], v[56:59], v[112:115], v[144:147]\n" \
  "ds_read2_b32 v[26:27], v120 offset0:162 offset1:163\n" \
  "v_mfma_f32_16x16x32_f16 v[148:151], v[56:59], v[116:119], v[148:151]\n" \
  "ds_read_b128 v[92:95], v124 offset:640\n" \
  "v_mfma_f32_16x16x32_f16 v[140:143], v[60:63], v[104:107], v[140:143]\n" \
  "ds_read_b128 v[96:99], v124 offset:1152\n" \
  "v_mfma_f32_16x16x32_f16 v[144:147], v[60:63], v[108:111], v[144:147]\n" \
  "ds_read_b128 v[100:103], v124 offset:1664\n" \
  "v_mfma_f32_16x16x32_f16 v[148:151], v[60:63], v[112:115], v[148:151]\n" \
  "ds_read2_b32 v[28:29], v120 offset0:32 offset1:33\n" \
  "v_mfma_f32_16x16x32_f16 v[152:155], v[60:63], v[116:119], v[152:155]\n" \
  "ds_read2_b32 v[30:31], v120 offset0:34 offset1:35\n" \
  "v_mfma_f32_16x16x32_f16 v[144:147], v[64:67], v[104:107], v[144:147]\n" \
  "ds_read2_b32 v[32:33], v121 offset0:160 offset1:161\n" \
  "v_mfma_f32_16x16x32_f16 v[148:151], v[64:67], v[108:111], v[148:151]\n" \
  "ds_read2_b32 v[34:35], v121 offset0:162 offset1:163\n" \
  "v_mfma_f32_16x16x32_f16 v[152:155], v[64:67], v[112:115], v[152:155]\n" \
  "ds_read2_b32 v[36:37], v121 offset0:32 offset1:33\n" \
  "v_mfma_f32_16x16x32_f16 v[156:159], v[64:67], v[116:119], v[156:159]\n" \
  "ds_read2_b32 v[38:39], v121 offset0:34 offset1:35\n" \
  "s_waitcnt lgkmcnt(10)\n" \
  "v_mfma_f32_16x16x32_f16 v[148:151], v[68:71], v[104:107], v[148:151]\n" \
  "ds_read2_b32 v[40:41], v122 offset0:160 offset1:161\n" \
  "v_mfma_f32_16x16x32_f16 v[152:155], v[68:71], v[108:111], v[152:155]\n" \
  "ds_read2_b32 v[42:43], v122 offset0:162 offset1:163\n" \
  "v_mfma_f32_16x16x32_f16 v[156:159], v[68:71], v[112:115], v[156:159]\n" \
  "ds_read2_b32 v[44:45], v122 offset0:32 offset1:33\n" \
  "v_mfma_f32_16x16x32_f16 v[160:163], v[68:71], v[116:119], v[160:163]\n" \
  "ds_read2_b32 v[46:47], v122 offset0:34 offset1:35\n" \
  "v_mfma_f32_16x16x32_f16 v[152:155], v[72:75], v[104:107], v[152:155]\n" \
  "ds_read2_b32 v[48:49], v123 offset0:160 offset1:161\n" \
  "v_mfma_f32_16x16x32_f16 v[156:159], v[72:75], v[108:111], v[156:159]\n" \
  "ds_read2_b32 v[50:51], v123 offset0:162 offset1:163\n" \
  "v_mfma_f32_16x16x32_f16 v[160:163], v[72:75], v[112:115], v[160:163]\n" \
  "ds_read2_b32 v[52:53], v123 offset0:32 offset1:33\n" \
  "v_mfma_f32_16x16x32_f16 v[164:167], v[72:75], v[116:119], v[164:167]\n" \
  "ds_read2_b32 v[54:55], v123 offset0:34 offset1:35\n" \
  "v_mfma_f32_16x16x32_f16 v[156:159], v[76:79], v[104:107], v[156:159]\n" \
  "v_add_u32 v120, 128, v120\n" \
  "v_mfma_f32_16x16x32_f16 v[160:163], v[76:79], v[108:111], v[160:163]\n" \
  "v_add_u32 v121, 128, v121\n" \
  "v_mfma_f32_16x16x32_f16 v[164:167], v[76:79], v[112:115], v[164:167]\n" \
  "v_add_u32 v122, 128, v122\n" \
  "v_mfma_f32_16x16x32_f16 v[160:163], v[80:83], v[104:107], v[160:163]\n" \
  "v_add_u32 v123, 128, v123\n" \
  "v_mfma_f32_16x16x32_f16 v[164:167], v[80:83], v[108:111], v[164:167]\n" \
  "v_add_u32 v124, 128, v124\n" \
  "v_mfma_f32_16x16x32_f16 v[164:167], v[84:87], v[104:107], v[164:167]\n" \
  "s_add_u32 s40, s40, 1\n" \
  "s_cmp_lt_u32 s40, 4\n" \
  "s_cbranch_scc1 L0_%=\n" \
  "v_add_u32 v120, -512, v120\n" \
  "v_add_u32 v121, -512, v121\n" \
  "v_add_u32 v124, -512, v124\n" \
  "s_waitcnt lgkmcnt(0)\n" \
  "ds_read2_b32 v[24:25], v120 offset0:128 offset1:129\n" \
  "ds_read2_b32 v[26:27], v120 offset0:130 offset1:131\n" \
  "ds_read2_b32 v[28:29], v120 offset0:0 offset1:1\n" \
  "ds_read2_b32 v[30:31], v120 offset0:2 offset1:3\n" \
  "ds_read2_b32 v[32:33], v121 offset0:128 offset1:129\n" \
  "ds_read2_b32 v[34:35], v121 offset0:130 offset1:131\n" \
  "ds_read2_b32 v[36:37], v121 offset0:0 offset1:1\n" \
  "ds_read2_b32 v[38:39], v121 offset0:2 offset1:3\n" \
  "v_cvt_pk_bf16_f32 v126, v136, v137\n" \
  "v_cvt_pk_bf16_f32 v127, v138, v139\n" \
  "ds_write_b64 v125, v[126:127] offset:0\n" \
  "v_cvt_pk_bf16_f32 v128, v140, v141\n" \
  "v_cvt_pk_bf16_f32 v129, v142, v143\n" \
  "ds_write_b64 v125, v[128:129] offset:640\n" \
  "v_cvt_pk_bf16_f32 v130, v144, v145\n" \
  "v_cvt_pk_bf16_f32 v131, v146, v147\n" \
  "ds_write_b64 v125, v[130:131] offset:1280\n" \
  "v_cvt_pk_bf16_f32 v132, v148, v149\n" \
  "v_cvt_pk_bf16_f32 v133, v150, v151\n" \
  "ds_write_b64 v125, v[132:133] offset:1920\n" \
  "s_waitcnt lgkmcnt(0)\n" \
  "s_barrier\n" \
  "ds_read_b128 v[88:91], v124 offset:2048\n" \
  "ds_read_b128 v[92:95], v124 offset:2560\n" \
  "ds_read_b128 v[96:99], v124 offset:3072\n" \
  "ds_read_b128 v[100:103], v124 offset:3584\n" \
  "s_mov_b32 s40, 0\n" \
  "s_waitcnt lgkmcnt(0)\n" \
  "L1_%=:\n" \
  "s_waitcnt lgkmcnt(6)\n" \
  "v_mfma_f32_16x16x32_f16 v[152:155], v[24:27], v[88:91], v[152:155]\n" \
  "ds_read_b128 v[104:107], v124 offset:2112\n" \
  "ds_read2_b32 v[56:57], v120 offset0:144 offset1:145\n" \
  "v_mfma_f32_16x16x32_f16 v[156:159], v[24:27], v[92:95], v[156:159]\n" \
  "ds_read2_b32 v[58:59], v120 offset0:146 offset1:147\n" \
  "ds_read_b128 v[108:111], v124 offset:2624\n" \
  "v_mfma_f32_16x16x32_f16 v[160:163], v[24:27], v[96:99], v[160:163]\n" \
  "ds_read_b128 v[112:115], v124 offset:3136\n" \
  "v_mfma_f32_16x16x32_f16 v[164:167], v[24:27], v[100:103], v[164:167]\n" \
  "ds_read_b128 v[116:119], v124 offset:3648\n" \
  "s_waitcnt lgkmcnt(6)\n" \
  "v_mfma_f32_16x16x32_f16 v[156:159], v[28:31], v[88:91], v[156:159]\n" \
  "ds_read2_b32 v[60:61], v120 offset0:16 offset1:17\n" \
  "v_mfma_f32_16x16x32_f16 v[160:163], v[28:31], v[92:95], v[160:163]\n" \
  "ds_read2_b32 v[62:63], v120 offset0:18 offset1:19\n" \
  "v_mfma_f32_16x16x32_f16 v[164:167], v[28:31], v[96:99], v[164:167]\n" \
  "ds_read2_b32 v[64:65], v121 offset0:144 offset1:145\n" \
  "v_mfma_f32_16x16x32_f16 v[160:163], v[32:35], v[88:91], v[160:163]\n" \
  "ds_read2_b32 v[66:67], v121 offset0:146 offset1:147\n" \
  "v_mfma_f32_16x16x32_f16 v[164:167], v[32:35], v[92:95], v[164:167]\n" \
  "ds_read2_b32 v[68:69], v121 offset0:16 offset1:17\n" \
  "v_mfma_f32_16x16x32_f16 v[164:167], v[36:39], v[88:91], v[164:167]\n" \
  "ds_read2_b32 v[70:71], v121 offset0:18 offset1:19\n" \
  "s_waitcnt lgkmcnt(6)\n" \
  "v_mfma_f32_16x16x32_f16 v[152:155], v[56:59], v[104:107], v[152:155]\n" \
  "ds_read_b128 v[88:91], v124 offset:2176\n" \
  "ds_read2_b32 v[24:25], v120 offset0:160 offset1:161\n" \
  "v_mfma_f32_16x16x32_f16 v[156:159], v[56:59], v[108:111], v[156:159]\n" \
  "ds_read2_b32 v[26:27], v120 offset0:162 offset1:163\n" \
  "ds_read_b128 v[92:95], v124 offset:2688\n" \
  "v_mfma_f32_16x16x32_f16 v[160:163], v[56:59], v[112:115], v[160:163]\n" \
  "ds_read_b128 v[96:99], v124 offset:3200\n" \
  "v_mfma_f32_16x16x32_f16 v[164:167], v[56:59], v[116:119], v[164:167]\n" \
  "ds_read_b128 v[100:103], v124 offset:3712\n" \
  "s_waitcnt lgkmcnt(6)\n" \
  "v_mfma_f32_16x16x32_f16 v[156:159], v[60:63], v[104:107], v[156:159]\n" \
  "ds_read2_b32 v[28:29], v120 offset0:32 offset1:33\n" \
  "v_mfma_f32_16x16x32_f16 v[160:163], v[60:63], v[108:111], v[160:163]\n" \
  "ds_read2_b32 v[30:31], v120 offset0:34 offset1:35\n" \
  "v_mfma_f32_16x16x32_f16 v[164:167], v[60:63], v[112:115], v[164:167]\n" \
  "ds_read2_b32 v[32:33], v121 offset0:160 offset1:161\n" \
  "v_mfma_f32_16x16x32_f16 v[160:163], v[64:67], v[104:107], v[160:163]\n" \
  "ds_read2_b32 v[34:35], v121 offset0:162 offset1:163\n" \
  "v_mfma_f32_16x16x32_f16 v[164:167], v[64:67], v[108:111], v[164:167]\n" \
  "ds_read2_b32 v[36:37], v121 offset0:32 offset1:33\n" \
  "v_mfma_f32_16x16x32_f16 v[164:167], v[68:71], v[104:107], v[164:167]\n" \
  "ds_read2_b32 v[38:39], v121 offset0:34 offset1:35\n" \
  "v_add_u32 v120, 128, v120\n" \
  "v_add_u32 v121, 128, v121\n" \
  "v_add_u32 v124, 128, v124\n" \
  "s_add_u32 s40, s40, 1\n" \
  "s_cmp_lt_u32 s40, 4\n" \
  "s_cbranch_scc1 L1_%=\n" \
  "s_nop 7\n" \
  "v_cvt_pk_bf16_f32 v126, v152, v153\n" \
  "v_cvt_pk_bf16_f32 v127, v154, v155\n" \
  "ds_write_b64 v125, v[126:127] offset:2560\n" \
  "v_cvt_pk_bf16_f32 v128, v156, v157\n" \
  "v_cvt_pk_bf16_f32 v129, v158, v159\n" \
  "ds_write_b64 v125, v[128:129] offset:3200\n" \
  "v_cvt_pk_bf16_f32 v130, v160, v161\n" \
  "v_cvt_pk_bf16_f32 v131, v162, v163\n" \
  "ds_write_b64 v125, v[130:131] offset:3840\n" \
  "v_cvt_pk_bf16_f32 v132, v164, v165\n" \
  "v_cvt_pk_bf16_f32 v133, v166, v167\n" \
  "ds_write_b64 v125, v[132:133] offset:4480\n" \
  "s_waitcnt lgkmcnt(0)\n" \
  "s_barrier\n"
#define CONV_ASM_CLOBBERS "v24", "v25", "v26", "v27", "v28", "v29", "v30", "v31", "v32", "v33", "v34", "v35", "v36", "v37", "v38", "v39", "v40", "v41", "v42", "v43", "v44", "v45", "v46", "v47", "v48", "v49", "v50", "v51", "v52", "v53", "v54", "v55", "v56", "v57", "v58", "v59", "v60", "v61", "v62", "v63", "v64", "v65", "v66", "v67", "v68", "v69", "v70", "v71", "v72", "v73", "v74", "v75", "v76", "v77", "v78", "v79", "v80", "v81", "v82", "v83", "v84", "v85", "v86", "v87", "v88", "v89", "v90", "v91", "v92", "v93", "v94", "v95", "v96", "v97", "v98", "v99", "v100", "v101", "v102", "v103", "v104", "v105", "v106", "v107", "v108", "v109", "v110", "v111", "v112", "v113", "v114", "v115", "v116", "v117", "v118", "v119", "v120", "v121", "v122", "v123", "v124", "v125", "v126", "v127", "v128", "v129", "v130", "v131", "v132", "v133", "v134", "v135", "v136", "v137", "v138", "v139", "v140", "v141", "v142", "v143", "v144", "v145", "v146", "v147", "v148", "v149", "v150", "v151", "v152", "v153", "v154", "v155", "v156", "v157", "v158", "v159", "v160", "v161", "v162", "v163", "v164", "v165", "v166", "v167", "s40", "scc", "memory"

struct Params {
  const u16 *x, *lar, *aim, *crl, *cim, *ldt, *dskip, *wr, *wg, *wu, *wd;
  u16* out;
  char* ws;
};

__global__ __launch_bounds__(NT, 3) void k_main(Params p) {
  extern __shared__ __attribute__((aligned(16))) char smem[];
  const int tid = threadIdx.x, lane = tid & 63;
  int wave = __builtin_amdgcn_readfirstlane(tid >> 6);
  const int xcd = blockIdx.x & 7;
  int loc = blockIdx.x >> 3;
  STAMP_INIT();
  STAMP(0);
  unsigned cpf = 0;
  if (wave == 0 && lane < 8 && (loc * 8 + lane) * 128 < CODE_BYTES) {
    const char* pc = (const char*)(__builtin_amdgcn_s_getpc() & ~(uint64_t)255);
    cpf = *(const __attribute__((address_space(1))) unsigned*)(pc + (loc * 8 + lane) * 128);
  }
  asm volatile("" ::"s"(p.x), "s"(p.lar), "s"(p.aim), "s"(p.crl), "s"(p.cim), "s"(p.ldt), "s"(p.dskip), "s"(p.wr),
               "s"(p.wg), "s"(p.wu), "s"(p.wd), "s"(p.out), "s"(p.ws));
  STAMP(11);
  const rsrc_t rs = __builtin_amdgcn_make_buffer_rsrc((void*)p.ws, 0, WS_END, 0x00020000);
  const rsrc_t ro = __builtin_amdgcn_make_buffer_rsrc((void*)p.out, 0, SEQ * DM * 2, 0x00020000);
  const v4u sent4 = {SENT, SENT, SENT, SENT};
  const int cl = lane & 15, g = lane >> 4;
  const int cg = xcd * 18 + loc;
  const int fj = xcd * 9 + (loc - 18);
  const int row0 = xcd * 256 + (loc - 18 + max(loc - 30, 0)) * 16;

  v4u ga = {0u, 0u, 0u, 0u}, gb = ga, gc = ga, gd = ga;
  if (loc >= 18) {
    const u16* xr = p.x + (size_t)(row0 + (cl >> 1)) * DM + 64 * wave + 16 * g + 8 * (cl & 1);
    ga = *(const v4u*)xr;
    gb = *(const v4u*)(xr + 8 * DM);
    if (loc >= 30) {
      gc = *(const v4u*)(xr + 16 * DM);
      gd = *(const v4u*)(xr + 24 * DM);
    }
  } else if (wave < 8) {
    const int d = cg * 4 + (wave >> 1);
    const int pi = d * NM + (wave & 1) * 64 + lane;
    ga = v4u{p.lar[pi], p.aim[pi], p.crl[pi], p.cim[pi]};
    gb = v4u{p.ldt[d], p.crl[pi ^ 64], p.cim[pi ^ 64], 0u};
    const v2u dk = *(const v2u*)(p.dskip + cg * 4 + (lane >> 6));
    gc.x = dk.x;
    gc.y = dk.y;
  } else {
    const unsigned* w32 = (const unsigned*)p.wr;
    ga = v4u{w32[lane], w32[lane + 64], w32[lane + 128], w32[lane + 192]};
    gb.x = w32[256 + (lane & 31)];
    if (lane < 16) *(unsigned*)(smem + L_MISC + lane * 4) = 0u;
  }
  unsigned epoch = ld1(rs, WS_EPOCH);
  BAR();
  asm volatile("" : "+s"(loc), "+s"(wave));
  STAMP(12);

  if (loc < 18) {
    const int rb = xcd * 16 + loc;
    const int tau = tid & 255;
    if (wave == 8) {
      __builtin_amdgcn_s_setprio(HELP_PRIO);
      {
#pragma unroll
        for (int k = 0; k < 4; k++) *(unsigned*)(smem + L_WR + (lane + 64 * k) * 4) = ga[k];
        *(unsigned*)(smem + L_WR + (256 + (lane & 31)) * 4) = gb.x;
        float ws_ = lane < 32 ? blo(gb.x) + bhi(gb.x) : 0.f;
#pragma unroll
        for (int k = 0; k < 4; k++) ws_ += blo(ga[k]) + bhi(ga[k]);
        ws_ = wave_sum(ws_);
        if (lane == 0) *(float*)(smem + L_MISC + 64) = ws_;
      }
      SCHED();
      const rsrc_t rx = __builtin_amdgcn_make_buffer_rsrc((void*)p.x, 0, SEQ * DM * 2, 0x00020000);
      const int xo = (1024 + lane) * 1152 + cg * 8;
      v2u xc[16];
#pragma unroll
      for (int i = 0; i < XC_FIRST; i++) xc[i] = ldp2(rx, xo, i * 64 * 1152);
      STAMP(1);
      BAR();
      BAR();
#pragma unroll
      for (int i = XC_FIRST; i < 16; i++) xc[i] = ldp2(rx, xo, i * 64 * 1152);
      SCHED();
#pragma unroll
      for (int i = 0; i < 16; i++) *(v2u*)(smem + L_XCOL + 8192 + lane * 8 + i * 512) = xc[i];
      LGKM0();
      if (lane == 0) *(volatile LAS unsigned*)(smem + L_MISC + 32) = 1u;
      STAMP(3);
#pragma unroll 1
      for (int i = 0; i < 16; i++) st4(sent4, rs, WS_HT + cg * 16384 + i * 1024 + lane * 16);
      st4(sent4, rs, (loc < 16 && lane < 4) ? WS_RREC + rb * 64 + lane * 16 : OOB);
      st4(sent4, rs, cg < 20 ? WS_XSEL + cg * 1024 + lane * 16 : OOB);
      DRAIN();
      if (lane == 0) st1(epoch + 1u, rs, WS_INIT + cg * 64);
      STAMP(4);
      unsigned pf[4];
      {
        const char* gu = (const char*)(loc < 9 ? p.wg : p.wu) + (xcd * 9 + (loc < 9 ? loc : loc - 9)) * 128;
#pragma unroll
        for (int i = 0; i < 4; i++) pf[i] = *(const unsigned*)(gu + (size_t)(320 + i * 64 + lane) * 9216);
      }
      STAMP(5);
      BAR();
#pragma unroll
      for (int i = 0; i < 4; i++) asm volatile("" ::"v"(pf[i]));
      BAR();
    } else {
      const int pch = wave >> 1;
      char* const pair = smem + pch * PAIR;
      char* const scr = smem + wave * WSCR;
      v4u xk[2];
      {
        const rsrc_t rx = __builtin_amdgcn_make_buffer_rsrc((void*)p.x, 0, wave >= 4 ? SEQ * DM * 2 : 0, 0x00020000);
        const int xo = tau * 2304 + cg * 8;
        const v2u a = ldp2(rx, xo, 0), b = ldp2(rx, xo + 1152, 0);
        const v2u c = ldp2(rx, xo, 512 * 1152), d = ldp2(rx, xo + 1152, 512 * 1152);
        xk[0] = v4u{a.x, a.y, b.x, b.y};
        xk[1] = v4u{c.x, c.y, d.x, d.y};
      }
      SCHED();
      LAS char *wb_[16], *rb_[8];
#pragma unroll
      for (int s = 0; s < 16; s++) { wb_[s] = (LAS char*)scr + ((lane * 4) ^ (s << 4)); PIN(wb_[s]); }
#pragma unroll
      for (int ks = 0; ks < 8; ks++) {
        rb_[ks] = (LAS char*)scr + (lane & 31) * 256 + ((ks * 32 + (lane >> 5) * 16) ^ ((lane & 15) << 4));
        PIN(rb_[ks]);
      }
      SCHED();
      PIN(ga); PIN(gb); PIN(gc); PIN(gd);
      const float dt = rbf(expf(blo(gb.x)));
      const float cre = blo(ga.z) * dt, cim = blo(ga.w) * dt;
      const unsigned dw = (pch & 2) ? gc.y : gc.x;
      const float dsk = (pch & 1) ? bhi(dw) : blo(dw);
      const float cmx = fmaxf(fmaxf(fmaxf(fabsf(cre), fabsf(cim)), fabsf(dsk) * 0.0078125f), fmaxf(fabsf(blo(gb.y)), fabsf(blo(gb.z))) * dt);
      const unsigned ce = min(max(wave_max_u(__float_as_uint(cmx)) >> 23, 40u), 250u);
      const float csc = __uint_as_float((257u - ce) << 23);
      if (lane == 0) *(float*)(smem + L_MISC + 48 + pch * 4) = __uint_as_float((ce - 6u) << 23);
      STAMP(1);
      f32x16 acc;
      {
        const float ch = -rbf(expf(blo(ga.x))) * dt * 1.44269504f;
        const float aie = blo(ga.y) * dt;
        const float ph = aie * 0.159154936f;
        const float pl = fmaf(aie, 0.159154936f, -ph) + aie * 7.13048051e-9f;
        const cz e1 = mkz(cexp_p2(1.f, ch, ph, pl)), e8 = mkz(cexp_p2(8.f, ch, ph, pl));
        const cz e64 = mkz(cexp_p2(64.f, ch, ph, pl)), e512 = mkz(cexp_p2(512.f, ch, ph, pl));
        const cz e2 = csq(e1), e16 = csq(e8), e128 = csq(e64), e1024 = csq(e512);
        const cz e4 = csq(e2), e32 = csq(e16), e256 = csq(e128);
        auto put2 = [&](int row, unsigned a, unsigned b) {
          *(LAS unsigned*)(wb_[row & 15] + row * 256) = a;
          *(LAS unsigned*)(wb_[row & 15] + (row + 16) * 256) = b;
        };
        f32x2 T[32];
        T[0] = f32x2{16.f, 0.f};
        T[16] = e128.v * 16.f;
        put2(0, pkh2(T[0]), pkh2(T[16]));
#pragma unroll
        for (int i = 1; i < 16; i++) {
          const cz b = (i & 1) ? e8 : (i & 2) ? e16 : (i & 4) ? e32 : e64;
          const int src = i & (i - 1);
          if (src == 0) {
            T[i] = b.v * 16.f;
            T[16 + i] = cmul(T[16], b);
          } else {
            cmul2(T[i], T[16 + i], T[src], T[16 + src], b);
          }
          put2(i, pkh2(T[i]), pkh2(T[16 + i]));
        }
        const f32x2 w0 = {cre * csc, -cim * csc};
        f32x2 w1, w2, w4, w6, w256, w260, w512, w768, w1024, w1280, w1536, w1792, ta, tb;
        cmulc2(ta, w256, w0, e8, w0, e256);
        put2(32 + 0, pkh2(w0), pkh2(ta));
        cmulc2(w1, w512, w0, e1, w0, e512);
        put2(32 + 1, pkh2(w1), pkh2(w512));
        cmulc2(w2, ta, w0, e2, w512, e4);
        put2(32 + 2, pkh2(w2), pkh2(ta));
        cmulc2(ta, tb, w2, e1, w512, e8);
        put2(32 + 3, pkh2(ta), pkh2(tb));
        cmulc2(w4, w1024, w0, e4, w0, e1024);
        put2(32 + 8, pkh2(w4), pkh2(w1024));
        cmulc2(ta, tb, w4, e1, w1024, e8);
        put2(32 + 9, pkh2(ta), pkh2(tb));
        cmulc2(w6, w1280, w4, e2, w1024, e256);
        put2(32 + 10, pkh2(w6), pkh2(w1280));
        cmulc2(ta, tb, w6, e1, w1280, e8);
        put2(32 + 11, pkh2(ta), pkh2(tb));
        cmulc2(ta, w768, w256, e8, w512, e256);
        const unsigned h256 = pkh2(w256), h264 = pkh2(ta);
        put2(32 + 4, h256, h264);
        put2(32 + 5, h256, pkh2(w768));
        cmulc2(ta, tb, w256, e2, w768, e4);
        put2(32 + 6, pkh2(ta), pkh2(tb));
        cmulc2(w260, tb, w256, e4, w768, e8);
        const unsigned h260 = pkh2(w260);
        put2(32 + 7, h260, pkh2(tb));
        cmulc2(w1536, w1792, w1024, e512, w1280, e512);
        put2(32 + 12, h260, pkh2(w1536));
        cmulc2(ta, tb, w1536, e8, w260, e2);
        put2(32 + 13, h260, pkh2(ta));
        put2(32 + 14, pkh2(tb), pkh2(w1792));
        cmulc2(ta, tb, w1792, e8, w1792, e8);
        put2(32 + 15, h264, pkh2(ta));
        MEMBAR();
        f16x8 fa[8], fb[8];
#pragma unroll
        for (int ks = 0; ks < 8; ks++) {
          fb[ks] = *(const LAS f16x8*)(rb_[ks]);
          fa[ks] = *(const LAS f16x8*)(rb_[ks] + 8192);
        }
        SCHED();
#pragma unroll
        for (int i = 0; i < 16; i++) acc[i] = 0.f;
#pragma unroll
        for (int ks = 0; ks < 8; ks++) acc = __builtin_amdgcn_mfma_f32_32x32x16_f16(fa[ks], fb[ks], acc, 0, 0, 0);
        MEMBAR();
      }
      STAMP(2);
#pragma unroll
      for (int q = 0; q < 4; q++)
        *(f32x4*)(scr + q * 1024 + lane * 16) = f32x4{acc[4 * q], acc[4 * q + 1], acc[4 * q + 2], acc[4 * q + 3]};
      LGKM0();
      volatile LAS unsigned* const pf = (volatile LAS unsigned*)(smem + L_MISC);
      if (lane == 0) pf[wave] = 1u;
#pragma unroll 1
      for (int sp = 0; sp < SPIN_CAP * 16 && pf[wave ^ 1] == 0u; sp++) {}
      MEMBAR();
      {
        const char* oth = smem + (wave ^ 1) * WSCR + lane * 16;
        f32x4 o[4];
#pragma unroll
        for (int q = 0; q < 4; q++) o[q] = *(const f32x4*)(oth + q * 1024);
        SCHED();
#pragma unroll
        for (int i = 0; i < 16; i++) acc[i] += o[i >> 2][i & 3];
        acc[0] += lane == 0 ? dsk * csc * 8.f : 0.f;
      }
      {
        const int j = lane & 31, h = lane >> 5;
        const float xa = (j & 1) ? acc[13] : acc[12], xb = (j & 1) ? acc[15] : acc[14];
        const unsigned aa2 = pkh(acc[9], acc[9]), bb2 = pkh(acc[10], acc[10]), cc2 = pkh(acc[11], acc[11]);
        const unsigned aa3 = pkh(acc[12], acc[12]), bb3 = pkh(acc[13], acc[13]);
        const unsigned aa4 = pkh(acc[14], acc[14]), bb4 = pkh(acc[15], acc[15]);
        v4u c0, c1, c2, c3, ct = {0u, 0u, 0u, 0u};
        if (wave & 1) {
          c0 = v4u{pkh(acc[8], acc[7]), pkh(acc[6], acc[5]), pkh(acc[4], acc[3]), pkh(acc[2], acc[1])};
          c1 = v4u{cc2, pkh(acc[11], acc[10]), bb2, aa2};
          c2 = v4u{bb3, bb3, pkh(xa, acc[12]), aa3};
          c3 = v4u{bb4, bb4, pkh(xb, acc[14]), aa4};
          ct.x = lane == 0 ? pkh(acc[0], 0.f) : 0u;
        } else {
          c0 = v4u{pkh(acc[7], acc[6]), pkh(acc[5], acc[4]), pkh(acc[3], acc[2]), pkh(acc[1], acc[0])};
          c1 = v4u{cc2, bb2, pkh(acc[10], acc[9]), aa2};
          c2 = v4u{bb3, pkh(acc[13], xa), aa3, aa3};
          c3 = v4u{bb4, pkh(acc[15], xb), aa4, aa4};
        }
        char* ki = pair + L_KIMG + (wave & 1) * KCOPY;
        *(v4u*)(ki + 16 * (255 - lane)) = c0;
        *(v4u*)(ki + 16 * (191 - lane)) = c1;
        *(v4u*)(ki + 16 * (127 - 64 * h - j)) = c2;
        *(v4u*)(ki + 16 * (95 - 64 * h - j)) = c3;
        if (lane < 17) *(v4u*)(lane < 2 ? ki + 4096 + lane * 16 : pair + L_UIMG + ((wave & 1) * 15 + lane - 2) * 16) = ct;
      }
      STAMP(3);
      if (wave < 4) {
        const unsigned tag = epoch + 1u;
        v4u sg[4];
#pragma unroll 1
        for (int sp = 0; sp < SPIN_CAP; sp++) {
          MEMBAR();
          bool ok = true;
#pragma unroll
          for (int k = 0; k < 4; k++) sg[k] = ld4(rs, WS_STAT + wave * 4096 + k * 1024 + lane * 16);
#pragma unroll
          for (int k = 0; k < 4; k++) ok &= sg[k].y == tag && sg[k].w == tag;
          if (__all(ok)) break;
        }
#pragma unroll
        for (int k = 0; k < 4; k++) *(v2u*)(smem + L_STAT + wave * 2048 + k * 512 + lane * 8) = v2u{sg[k].x, sg[k].z};
      } else {
        *(v4u*)(smem + L_XCOL + 4096 + tau * 16) = xk[1];
      }
      STAMP(4);
      BAR();
      auto unpack = [&](const v4u& xv, f32x2(&xf)[4]) {
        xf[0] = f32x2{blo(xv.x), blo(xv.z)};
        xf[1] = f32x2{bhi(xv.x), bhi(xv.z)};
        xf[2] = f32x2{blo(xv.y), blo(xv.w)};
        xf[3] = f32x2{bhi(xv.y), bhi(xv.w)};
      };
      auto ubuild = [&](int i, const f32x2(&xf)[4]) {
        const v2u sg = *(const v2u*)(smem + L_STAT + i * 2048 + tau * 8);
        const f32x2 rr = {__builtin_amdgcn_rcpf(bhi(sg.x)), __builtin_amdgcn_rcpf(bhi(sg.y))};
        const f32x2 mm = {blo(sg.x), blo(sg.y)};
        char* up = smem + L_UIMG + (240 + 512 * i + 2 * tau) * 2;
#pragma unroll
        for (int c = 0; c < 4; c++) *(unsigned*)(up + c * PAIR) = pkh2((xf[c] - mm) * rr);
      };
      f32x2 xf[4][4];
      if (wave < 4) xk[0] = *(const v4u*)(smem + L_XCOL + 4096 + tau * 16);
      unpack(xk[0], xf[0]);
      ubuild(wave < 4 ? 1 : 0, xf[0]);
      BAR();
      STAMP(5);
      if (wave < 4) {
        const LAS char* cp = (const LAS char*)(smem + wave * PAIR);
        asm volatile(CONV_ASM_TEXT ::"v"(cp + L_KIMG + ((cl + 1) & 1) * KCOPY + (896 + 4 * g + ((16 - cl) >> 1)) * 4),
                     "v"(cp + L_UIMG + (2 * cl + g) * 16), "v"(cp + L_Y + g * 160 + cl * 8)
                     : CONV_ASM_CLOBBERS);
        STAMP(6);
        if (wave == 3 && loc < 16) sweep_init(rs, epoch + 1u, lane, 0, NCONV);
      } else {
        __builtin_amdgcn_s_setprio(HELP_PRIO);
        volatile LAS unsigned* const cf = (volatile LAS unsigned*)(smem + L_MISC + 32);
#pragma unroll 1
        for (int sp = 0; sp < SPIN_CAP * 16 && *cf == 0u; sp++) __builtin_amdgcn_s_sleep(1);
        MEMBAR();
        unpack(*(const v4u*)(smem + L_XCOL + 8192 + tau * 16), xf[2]);
        unpack(*(const v4u*)(smem + L_XCOL + 12288 + tau * 16), xf[3]);
        ubuild(2, xf[2]);
        ubuild(3, xf[3]);
        unpack(xk[1], xf[1]);
#pragma unroll
        for (int c = 0; c < 4; c++) PIN(xf[1][c]);
        f32x2 kinv[4];
#pragma unroll
        for (int c = 0; c < 4; c++) {
          const float k = *(const float*)(smem + L_MISC + 48 + c * 4);
          kinv[c] = f32x2{k, k};
          PIN(kinv[c]);
        }
        const char* yb = smem + L_Y + (tau >> 7) * 640 + ((tau >> 1) & 3) * 160 + ((tau >> 3) & 15) * 8 + (tau & 1) * 4;
        auto epi2 = [&](int i0) {
          unsigned y[2][4];
#pragma unroll
          for (int i = 0; i < 2; i++)
#pragma unroll
            for (int c = 0; c < 4; c++) y[i][c] = *(const unsigned*)(yb + (i0 + i) * 1280 + c * PAIR);
          SCHED();
#pragma unroll
          for (int i = 0; i < 2; i++) {
            f32x2 h[4];
#pragma unroll
            for (int c = 0; c < 4; c++)
              h[c] = __builtin_elementwise_fma(f32x2{blo(y[i][c]), bhi(y[i][c])}, kinv[c], xf[i0 + i][c]);
            const v4u hv = {nosent(pkbf(h[0].x, h[1].x)), nosent(pkbf(h[2].x, h[3].x)), nosent(pkbf(h[0].y, h[1].y)), nosent(pkbf(h[2].y, h[3].y))};
            st4(hv, rs, WS_HT + cg * 16384 + (i0 + i) * 4096 + tau * 16);
          }
        };
        STAMP(6);
        BAR();
        epi2(0);
        STAMP(2);
        BAR();
        epi2(2);
      }
    }
    STAMP(7);
    __builtin_amdgcn_s_setprio(0);
    asm volatile("" ::"v"(cpf));
    if (loc >= 16) { STAMP_FLUSH(); return; }
    v4u cw[2];
#pragma unroll
    for (int ks = 0; ks < 2; ks++) {
      const v4u w = *(const v4u*)(smem + L_WR + (64 * wave + 32 * ks + 8 * g) * 2);
      const bool one = (lane & 3) != 1;
      cw[ks] = v4u{one ? 0x3f803f80u : w.x, one ? 0x3f803f80u : w.y, one ? 0x3f803f80u : w.z, one ? 0x3f803f80u : w.w};
    }
    const float wsum = *(const float*)(smem + L_MISC + 64);
    BAR();
    __builtin_amdgcn_s_sleep(SLEEP_H);
    v4u hv[2];
    {
      const int o0 = WS_HT + (16 * wave + 2 * g) * 16384 + (16 * rb + cl) * 8;
#pragma unroll 1
      for (int sp = 0; sp < SPIN_CAP; sp++) {
        MEMBAR();
        const v2u a = ld2(rs, o0), b = ld2(rs, o0 + 16384), c = ld2(rs, o0 + 8 * 16384), d = ld2(rs, o0 + 9 * 16384);
        hv[0] = v4u{a.x, a.y, b.x, b.y};
        hv[1] = v4u{c.x, c.y, d.x, d.y};
        if (__all(valid4(hv[0]) && valid4(hv[1]))) break;
        __builtin_amdgcn_s_sleep(2);
      }
    }
    STAMP(8);
    {
      f32x4 s = {0.f, 0.f, 0.f, 0.f}, q = {0.f, 0.f, 0.f, 0.f};
      s = mfma_bf(cw[0], hv[0], s);
      q = mfma_bf(hv[0], hv[0], q);
      s = mfma_bf(cw[1], hv[1], s);
      q = mfma_bf(hv[1], hv[1], q);
      if ((cl >> 2) == g) *(f32x4*)(smem + L_RED + wave * 256 + cl * 16) = f32x4{s[0], diag(q, lane), s[1], 0.f};
    }
    BAR();
    float m, s;
    const f32x4 t = sum9(smem + L_RED + cl * 16);
    ln_round(t[0], t[1], m, s);
    const float rinv = __builtin_amdgcn_rcpf(s);
    unsigned okey;
    {
      const unsigned b = pkbf(0.f, fmaf(-m, wsum, t[2]) * rinv);
      okey = ((b & 0x80000000u) ? (~b & 0xffff0000u) : (b | 0x80000000u)) | (unsigned)(SEQ - 1 - (16 * rb + cl));
      if (wave == 1) st1(okey, rs, lane < 16 ? WS_RREC + (16 * rb + cl) * 4 : OOB);
    }
    STAMP(9);
    char* const tl = smem + wave * 4096;
    *(v4u*)(tl + cl * 144 + g * 16) = hv[0];
    *(v4u*)(tl + cl * 144 + 64 + g * 16) = hv[1];
    MEMBAR();
    v4u key = {0u, 0u, 0u, 0u};
    unsigned selmask = 0;
#pragma unroll 1
    for (int rd = 0; rd < 4; rd++) {
      const unsigned ka = row_max_u(okey);
      okey = okey == ka ? 0u : okey;
      const unsigned kb = row_max_u(okey);
      okey = okey == kb ? 0u : okey;
      const int ra = 15 - (ka & 15), rb2 = 15 - (kb & 15);
      v4u pub = *(const v4u*)(tl + ((lane & 16) ? rb2 : ra) * 144 + (lane & 7) * 16);
      {
        const float ma = rdlane(m, ra), mb = rdlane(m, rb2), ia = rdlane(rinv, ra), ib = rdlane(rinv, rb2);
        const float pm = (lane & 16) ? mb : ma, pi = (lane & 16) ? ib : ia;
#pragma unroll
        for (int i = 0; i < 4; i++) {
          const unsigned n = nosent(ln_apply(pub[i], pm, pi));
          pub[i] = (lane & 8) ? pub[i] : n;
        }
        if (lane >= 32) pub.x = (lane & 16) ? kb : ka;
      }
      PIN(pub);
      if (rd == 0 && wave < 8) {
        STAMP(1);
#pragma unroll 1
        for (int sp = 0; sp < SPIN_CAP; sp++) {
          MEMBAR();
          key = ld4(rs, WS_RREC + tid * 16);
          if (__all(valid4(key))) break;
          __builtin_amdgcn_s_sleep(2);
        }
        STAMP(10);
      }
      int ca = 0, cb = 0;
#pragma unroll
      for (int i = 0; i < 4; i++) {
        ca += __popcll(__ballot(key[i] > ka));
        cb += __popcll(__ballot(key[i] > kb));
      }
      char* cnt = smem + L_RED + 2304 + (rd & 1) * 64;
      if (lane == 0) *(unsigned*)(cnt + wave * 4) = (unsigned)(ca | (cb << 16));
      BAR();
      const v4u c0 = *(const v4u*)cnt, c1 = *(const v4u*)(cnt + 16);
      const unsigned cs = __builtin_amdgcn_readfirstlane((c0.x + c0.y + c0.z + c0.w) + (c1.x + c1.y + c1.z + c1.w));
      const unsigned rka = cs & 0xffffu, rkb = cs >> 16;
      if (rka >= 8u) break;
      const unsigned rank = (lane & 16) ? rkb : rka;
      const int xo = lane < 32 ? (cl >> 3) * 1152 + wave * 128 + (cl & 7) * 16 : 2304;
      st4(pub, rs, (rank < 8u && (lane < 32 || (cl == 0 && wave == 0))) ? WS_XSEL + rank * XSLOT + xo : OOB);
      selmask |= 1u << ra;
      if (rkb >= 8u) break;
      selmask |= 1u << rb2;
    }
    STAMP(3);
#pragma unroll
    for (int i = 0; i < 2; i++) {
      const int r = 8 * i + (lane >> 3);
      const v4u hh = *(const v4u*)(tl + r * 144 + (lane & 7) * 16);
      const float pm = __shfl(m, r), pi = __shfl(rinv, r);
      v4u o;
#pragma unroll
      for (int k = 0; k < 4; k++) o[k] = addbf(hh[k], ln_apply(hh[k], pm, pi));
      st4(o, ro, ((selmask >> r) & 1u) ? OOB : (16 * rb + r) * 1152 + wave * 128 + (lane & 7) * 16);
    }
    STAMP_FLUSH();
    return;
  }

  const bool ffn = loc < 27;
  v2u G[2][8];
  const rsrc_t rg = __builtin_amdgcn_make_buffer_rsrc((void*)p.wg, 0, ffn ? DM * FF * 2 : 0, 0x00020000);
  const int wo = (64 * wave + 8 * g) * (FF * 2) + 128 * fj + 8 * cl;
#pragma unroll
  for (int j = 0; j < W_EARLY; j++) G[j >> 3][j & 7] = ldp2(rg, wo, (32 * (j >> 3) + (j & 7)) * (FF * 2));
  SCHED();
  PIN(epoch); PIN(cpf);
  const unsigned tag = epoch + 1u;
  {
    const v4u ones = {0x3f803f80u, 0x3f803f80u, 0x3f803f80u, 0x3f803f80u};
    const f32x4 z4 = {0.f, 0.f, 0.f, 0.f};
    auto sums = [&](v4u a, int i) {
      const f32x4 s = mfma_bf(ones, a, z4), q = mfma_bf(a, a, z4);
      if ((cl >> 2) == g) *(f32x2*)(smem + L_RED + (i >> 1) * 2304 + wave * 256 + (i & 1) * 128 + cl * 8) = f32x2{s[0], diag(q, lane)};
    };
    sums(ga, 0);
    sums(gb, 1);
    if (loc >= 30) {
      sums(gc, 2);
      sums(gd, 3);
    }
    BAR();
    if (wave == 1 || (wave == 2 && loc >= 30)) {
      float m, s;
      const f32x4 t = sum9(smem + L_RED + (wave - 1) * 2304 + cl * 16);
      ln_round(t[0] + t[2], t[1] + t[3], m, s);
      st2(v2u{(__float_as_uint(m) >> 16) | __float_as_uint(s), tag}, rs, lane < 16 ? WS_STAT + (row0 + (wave - 1) * 16 + cl) * 8 : OOB);
    }
    BAR();
    STAMP(1);
    if (!ffn) {
      const char* dn = (const char*)p.wd + (size_t)xcd * 5184 * 128;
      unsigned pf[2];
#pragma unroll
      for (int i = 0; i < 2; i++) pf[i] = *(const unsigned*)(dn + min((i * 5 + loc - 27) * NT + tid, 5183) * 128);
#pragma unroll
      for (int i = 0; i < 2; i++) asm volatile("" ::"v"(pf[i]));
      STAMP_FLUSH();
      return;
    }
  }
  v4u FG[2][4], FU[2][4], FD[2][4];
  {
    v2u U[2][8], Dn[2][8];
    const rsrc_t ru = __builtin_amdgcn_make_buffer_rsrc((void*)p.wu, 0, DM * FF * 2, 0x00020000);
    const rsrc_t rd = __builtin_amdgcn_make_buffer_rsrc((void*)p.wd, 0, DM * FF * 2, 0x00020000);
    const int wdo = (64 * fj + 8 * g) * (DM * 2) + 128 * wave + 8 * cl;
#pragma unroll
    for (int j = W_EARLY; j < 16; j++) G[j >> 3][j & 7] = ldp2(rg, wo, (32 * (j >> 3) + (j & 7)) * (FF * 2));
#pragma unroll
    for (int j = 0; j < 16; j++) U[j >> 3][j & 7] = ldp2(ru, wo, (32 * (j >> 3) + (j & 7)) * (FF * 2));
    BAR();
#pragma unroll
    for (int j = 0; j < 16; j++) Dn[j >> 3][j & 7] = ldp2(rd, wdo, (32 * (j >> 3) + (j & 7)) * (DM * 2));
    st4(sent4, rs, WS_PART + fj * 18432 + tid * 16);
    st4(sent4, rs, WS_PART + fj * 18432 + 9216 + tid * 16);
    SCHED();
#pragma unroll
    for (int ks = 0; ks < 2; ks++)
#pragma unroll
      for (int c = 0; c < 4; c++) {
#pragma unroll
        for (int i = 0; i < 4; i++) {
          const unsigned sel = (c & 1) ? 0x07060302u : 0x05040100u;
          FG[ks][c][i] = __builtin_amdgcn_perm(G[ks][2 * i + 1][c >> 1], G[ks][2 * i][c >> 1], sel);
          FU[ks][c][i] = __builtin_amdgcn_perm(U[ks][2 * i + 1][c >> 1], U[ks][2 * i][c >> 1], sel);
          FD[ks][c][i] = __builtin_amdgcn_perm(Dn[ks][2 * i + 1][c >> 1], Dn[ks][2 * i][c >> 1], sel);
        }
        PIN(FG[ks][c]);
        PIN(FU[ks][c]);
        PIN(FD[ks][c]);
      }
  }
  STAMP(2);
  constexpr int F_GU = 1024, F_ALL = 2048, F_RED = 16384;
  if (wave == 0) sweep_init(rs, tag, lane, 0, NCONV);
  DRAIN();
  BAR();
  if (tid == 0) st1(tag, rs, WS_INIT + (NCONV + fj) * 64);
  STAMP(3);
  v4u xa[2];
  {
    v4u xv;
    const int o0 = WS_XSEL + (lane >> 3) * XSLOT + wave * 128 + (lane & 7) * 16;
#pragma unroll 1
    for (int sp = 0; sp < SPIN_CAP; sp++) {
      MEMBAR();
      xv = ld4(rs, o0);
      if (__all(valid4(xv))) break;
      __builtin_amdgcn_s_sleep(SLEEP_X);
    }
    STAMP(6);
    char* xs = smem + F_ALL + wave * 1152;
    *(v4u*)(xs + (lane >> 3) * 144 + (lane & 7) * 16) = xv;
    MEMBAR();
    xa[0] = *(const v4u*)(xs + (lane & 7) * 144 + g * 16);
    xa[1] = *(const v4u*)(xs + (lane & 7) * 144 + 64 + g * 16);
  }
#pragma unroll
  for (int c = 0; c < 4; c++) {
    f32x4 ag = {0.f, 0.f, 0.f, 0.f}, au = {0.f, 0.f, 0.f, 0.f};
#pragma unroll
    for (int ks = 0; ks < 2; ks++) {
      ag = mfma_bf(xa[ks], FG[ks][c], ag);
      au = mfma_bf(xa[ks], FU[ks][c], au);
    }
    *(f32x4*)(smem + F_RED + wave * 4096 + c * 1024 + lane * 16) = g < 2 ? ag : au;
  }
  BAR();
  STAMP(4);
  const int tk = fj / 9, cb = fj - tk * 9;
  if (wave < 8) {
    const int c = tid >> 7, wi = tid & 127;
    float gv[9], uv[9];
#pragma unroll
    for (int w = 0; w < 9; w++) {
      gv[w] = *(const float*)(smem + F_RED + w * 4096 + c * 1024 + wi * 4);
      uv[w] = *(const float*)(smem + F_RED + w * 4096 + c * 1024 + 512 + wi * 4);
    }
    SCHED();
    float gs = gv[0], us = uv[0];
#pragma unroll
    for (int w = 1; w < 9; w++) { gs += gv[w]; us += uv[w]; }
    const unsigned gu = pkbf(gs, us);
    const float gp = blo(gu);
    const float gq = rbf(gp * __builtin_amdgcn_rcpf(1.0f + __expf(-gp)));
    const int tok = (wi >> 6) * 4 + (wi & 3), col = ((wi >> 2) & 15);
    *(u16*)(smem + F_GU + tok * 128 + (col * 4 + c) * 2) = (u16)pkbf(gq * bhi(gu), 0.f);
  }
  BAR();
  STAMP(5);
  {
    const v4u a0 = *(const v4u*)(smem + F_GU + (lane & 7) * 128 + g * 16);
    const v4u a1 = *(const v4u*)(smem + F_GU + (lane & 7) * 128 + 64 + g * 16);
    f32x4 o[4];
#pragma unroll
    for (int c = 0; c < 4; c++) {
      o[c] = f32x4{0.f, 0.f, 0.f, 0.f};
      o[c] = mfma_bf(a0, FD[0][c], o[c]);
      o[c] = mfma_bf(a1, FD[1][c], o[c]);
    }
#pragma unroll
    for (int i = 0; i < 2; i++) {
      v4u v;
#pragma unroll
      for (int c = 0; c < 4; c++) v[c] = nosent(__float_as_uint(g < 2 ? o[c][i] : o[c][2 + i]));
      const int tok = 4 * (g & 1) + 2 * (g >> 1) + i;
      st4(v, rs, WS_PART + fj * 18432 + (tok * DM + 64 * wave + 4 * cl) * 4);
    }
  }
  STAMP(7);
  v2u fh = {0u, 0u}, fw = {0u, 0u};
  unsigned fkey = 0;
  if (wave == 8) {
    const int xo = WS_XSEL + tk * XSLOT + cb * 128 + cl * 8;
#pragma unroll 1
    for (int sp = 0; sp < SPIN_CAP; sp++) {
      MEMBAR();
      fw = ld2(rs, xo);
      fh = ld2(rs, xo + 1152);
      fkey = ld1(rs, WS_XSEL + tk * XSLOT + 2304);
      if (__all(valid4(v4u{fw.x, fw.y, fh.x, fh.y}) && fkey != SENT)) break;
    }
  } else if (wave == 7) {
    sweep_init(rs, tag, lane, NCONV, NFFN);
  } else {
    __builtin_amdgcn_s_sleep(SLEEP_P);
  }
  BAR();
  {
    const int o0 = WS_PART + (tid >> 4) * 18432 + (tk * DM + cb * 64 + cl * 4) * 4;
    v4u a, b;
#pragma unroll 1
    for (int sp = 0; sp < SPIN_CAP; sp++) {
      MEMBAR();
      a = ld4(rs, o0);
      b = ld4(rs, o0 + 36 * 18432);
      if (__all(valid4(a) && valid4(b))) break;
      __builtin_amdgcn_s_sleep(2);
    }
    *(f32x4*)(smem + F_ALL + tid * 16) = __builtin_bit_cast(f32x4, a) + __builtin_bit_cast(f32x4, b);
  }
  STAMP(8);
  BAR();
  if (wave == 8) {
    f32x4 s = *(const f32x4*)(smem + F_ALL + lane * 16);
#pragma unroll
    for (int i = 1; i < 9; i++) s += *(const f32x4*)(smem + F_ALL + i * 1024 + lane * 16);
#pragma unroll
    for (int i = 0; i < 4; i++) {
      s[i] += __shfl_xor(s[i], 16);
      s[i] += __shfl_xor(s[i], 32);
    }
    const int frow = SEQ - 1 - (int)(fkey & 0x7ffu);
    const unsigned kb = fkey & 0xffff0000u;
    const float rv = __uint_as_float((kb & 0x80000000u) ? (kb & 0x7fffffffu) : (~kb & 0xffff0000u));
    const float sgm = rbf(__builtin_amdgcn_rcpf(1.0f + __expf(-rv)));
    const unsigned ya = pkbf(s[0], s[1]), yb = pkbf(s[2], s[3]);
    const unsigned ta = pkbf(blo(ya) * sgm, bhi(ya) * sgm), tb = pkbf(blo(yb) * sgm, bhi(yb) * sgm);
    const v2u o = {addbf(fh.x, addbf(fw.x, ta)), addbf(fh.y, addbf(fw.y, tb))};
    st2(o, ro, lane < 16 ? frow * 1152 + cb * 128 + cl * 8 : OOB);
    if (fj == 0 && lane == 0) st1(tag, rs, WS_EPOCH);
  }
  STAMP(9);
  STAMP_FLUSH();
}

}

extern "C" void kernel_launch(void* const* d_in, const int* in_sizes, int n_in, void* d_out, int out_size,
                              void* d_ws, size_t ws_size, hipStream_t stream) {
  (void)hipFuncSetAttribute((const void*)k_main, hipFuncAttributeMaxDynamicSharedMemorySize, LDS_BYTES);
  Params p;
  p.x = (const u16*)d_in[0];
  p.lar = (const u16*)d_in[1];
  p.aim = (const u16*)d_in[2];
  p.crl = (const u16*)d_in[3];
  p.cim = (const u16*)d_in[4];
  p.ldt = (const u16*)d_in[5];
  p.dskip = (const u16*)d_in[6];
  p.wr = (const u16*)d_in[8];
  p.wg = (const u16*)d_in[9];
  p.wu = (const u16*)d_in[10];
  p.wd = (const u16*)d_in[11];
  p.out = (u16*)d_out;
  p.ws = (char*)d_ws;
  k_main<<<256, NT, LDS_BYTES, stream>>>(p);
}
```

```cpp
#define DIAG_I 14
#define DIAG_ROLE 0
#define DIAG_WM 511
#define DIAG_M 0u
#include <hip/hip_runtime.h>
#include <stdint.h>

namespace {

constexpr int SEQ = 2048, DM = 576, NM = 128, FF = 4608;
constexpr int NT = 576;
constexpr int NCONV = 144, NFFN = 72;
constexpr unsigned SENT = 0xFFFFFFFFu;
constexpr int SC1 = 16;
constexpr int SPIN_CAP = 1 << 16;
#define SLEEP_H 8
#define SLEEP_X 4
#define XC_FIRST 16
#define HELP_PRIO 2
#define W_EARLY 8
#define CODE_BYTES 18432
#define SLEEP_P 8

typedef unsigned short u16;
typedef unsigned v4u __attribute__((ext_vector_type(4)));
typedef unsigned v2u __attribute__((ext_vector_type(2)));
typedef float f32x2 __attribute__((ext_vector_type(2)));
typedef float f32x4 __attribute__((ext_vector_type(4)));
typedef float f32x16 __attribute__((ext_vector_type(16)));
typedef _Float16 f16x2 __attribute__((ext_vector_type(2)));
typedef _Float16 f16x8 __attribute__((ext_vector_type(8)));
typedef __bf16 bf16x2 __attribute__((ext_vector_type(2)));
typedef __bf16 bf16x8 __attribute__((ext_vector_type(8)));
typedef __amdgpu_buffer_rsrc_t rsrc_t;

constexpr int WS_EPOCH = 0;
constexpr int WS_INIT = 4096;
constexpr int WS_STAT = WS_INIT + 256 * 64;
constexpr int WS_RREC = WS_STAT + SEQ * 8;
constexpr int WS_XSEL = WS_RREC + SEQ * 4;
constexpr int XSLOT = 2560;
constexpr int WS_HT = 65536;
constexpr int WS_PART = WS_HT + SEQ * DM * 2;
constexpr int WS_END = WS_PART + NFFN * 8 * DM * 4;
constexpr int OOB = 0x7ffffff0;
static_assert(WS_XSEL + 8 * XSLOT <= WS_HT, "ws map");

constexpr int WSCR = 16384;
constexpr int PAIR = 2 * WSCR;
constexpr int L_KIMG = 4096;
constexpr int KCOPY = 4160;
constexpr int L_UIMG = WSCR + 4096;
constexpr int L_Y = WSCR + 8704;
constexpr int L_XCOL = 4 * PAIR;
constexpr int L_STAT = L_XCOL + 16384;
constexpr int L_MISC = L_STAT + 8192;
constexpr int L_WR = L_MISC + 256;
constexpr int L_RED = L_WR + 1152;
constexpr int LDS_BYTES = L_RED + 2 * 2304;
static_assert(L_KIMG + 2 * KCOPY <= WSCR && L_Y + 8 * 640 <= PAIR, "lds map");
static_assert(LDS_BYTES <= 160 * 1024, "lds size");

#define MEMBAR() asm volatile("" ::: "memory")
#define BAR() do { __builtin_amdgcn_sched_barrier(0); asm volatile("s_waitcnt lgkmcnt(0)\n\ts_barrier" ::: "memory"); __builtin_amdgcn_sched_barrier(0); } while (0)
#define LAS __attribute__((address_space(3)))
#define DRAIN() asm volatile("s_waitcnt vmcnt(0)" ::: "memory")
#define LGKM0() asm volatile("s_waitcnt lgkmcnt(0)" ::: "memory")
#define PIN(x) asm volatile("" : "+v"(x))
#define SCHED() __builtin_amdgcn_sched_barrier(0)

#define STAMP(i) do { if ((i) == 0 || (i) == DIAG_I) st_[i] = (unsigned)__builtin_amdgcn_s_memrealtime(); } while (0)
#define STAMP_INIT() unsigned st_[16] = {}
#define STAMP_FLUSH() do { const bool me_ = DIAG_ROLE == 0 ? (loc < 16 && ((DIAG_WM >> wave) & 1)) : (loc >= 18 && loc < 27 && ((DIAG_WM >> wave) & 1)); \
  const unsigned ts_ = me_ ? st_[DIAG_I] - st_[0] : 0u; const unsigned tg_ = 3000u + DIAG_M * ts_; \
  for (int q_ = 0; q_ < (1 << 20) && (unsigned)__builtin_amdgcn_s_memrealtime() - st_[0] < tg_; q_++) __builtin_amdgcn_s_sleep(1); } while (0)

__device__ __forceinline__ float blo(unsigned v) { return __uint_as_float(v << 16); }
__device__ __forceinline__ float bhi(unsigned v) { return __uint_as_float(v & 0xffff0000u); }
__device__ __forceinline__ unsigned pkbf(float a, float b) {
  const f32x2 f = {a, b};
  return __builtin_bit_cast(unsigned, __builtin_convertvector(f, bf16x2));
}
__device__ __forceinline__ float rbf(float f) { return blo(pkbf(f, f)); }
__device__ __forceinline__ unsigned pkh(float a, float b) {
  const f32x2 f = {a, b};
  return __builtin_bit_cast(unsigned, __builtin_convertvector(f, f16x2));
}
__device__ __forceinline__ unsigned nosent(unsigned v) { return min(v, 0xFFFFFFFEu); }
__device__ __forceinline__ bool valid4(v4u v) { return max(max(v.x, v.y), max(v.z, v.w)) != SENT; }

template <int CTRL, int RM> __device__ __forceinline__ float dppf(float v) {
  return __int_as_float(__builtin_amdgcn_update_dpp(0, __float_as_int(v), CTRL, RM, 0xf, false));
}
template <int CTRL, int RM> __device__ __forceinline__ unsigned dppu(unsigned v) {
  return (unsigned)__builtin_amdgcn_update_dpp(0, (int)v, CTRL, RM, 0xf, false);
}
__device__ __forceinline__ float wave_sum(float v) {
  v += dppf<0x111, 0xf>(v);
  v += dppf<0x112, 0xf>(v);
  v += dppf<0x114, 0xf>(v);
  v += dppf<0x118, 0xf>(v);
  v += dppf<0x142, 0xa>(v);
  v += dppf<0x143, 0xc>(v);
  return __int_as_float(__builtin_amdgcn_readlane(__float_as_int(v), 63));
}
__device__ __forceinline__ unsigned wave_max_u(unsigned v) {
  v = max(v, dppu<0x111, 0xf>(v));
  v = max(v, dppu<0x112, 0xf>(v));
  v = max(v, dppu<0x114, 0xf>(v));
  v = max(v, dppu<0x118, 0xf>(v));
  const unsigned a = (unsigned)__builtin_amdgcn_readlane((int)v, 15), b = (unsigned)__builtin_amdgcn_readlane((int)v, 31);
  const unsigned c = (unsigned)__builtin_amdgcn_readlane((int)v, 47), d = (unsigned)__builtin_amdgcn_readlane((int)v, 63);
  return max(max(a, b), max(c, d));
}

__device__ __forceinline__ unsigned row_max_u(unsigned v) {
  v = max(v, dppu<0x111, 0xf>(v));
  v = max(v, dppu<0x112, 0xf>(v));
  v = max(v, dppu<0x114, 0xf>(v));
  v = max(v, dppu<0x118, 0xf>(v));
  return (unsigned)__builtin_amdgcn_readlane((int)v, 15);
}
__device__ __forceinline__ float rdlane(float v, int l) { return __int_as_float(__builtin_amdgcn_readlane(__float_as_int(v), l)); }

__device__ __forceinline__ v4u ld4(rsrc_t rs, int off) { return __builtin_amdgcn_raw_buffer_load_b128(rs, off, 0, SC1); }
__device__ __forceinline__ v2u ld2(rsrc_t rs, int off) { return __builtin_amdgcn_raw_buffer_load_b64(rs, off, 0, SC1); }
__device__ __forceinline__ unsigned ld1(rsrc_t rs, int off) { return __builtin_amdgcn_raw_buffer_load_b32(rs, off, 0, SC1); }
__device__ __forceinline__ v2u ldp2(rsrc_t rs, int off, int soff) { return __builtin_amdgcn_raw_buffer_load_b64(rs, off, soff, 0); }
__device__ __forceinline__ void st4(v4u v, rsrc_t rs, int off) { __builtin_amdgcn_raw_buffer_store_b128(v, rs, off, 0, SC1); }
__device__ __forceinline__ void st2(v2u v, rsrc_t rs, int off) { __builtin_amdgcn_raw_buffer_store_b64(v, rs, off, 0, SC1); }
__device__ __forceinline__ void st1(unsigned v, rsrc_t rs, int off) { __builtin_amdgcn_raw_buffer_store_b32(v, rs, off, 0, SC1); }

__device__ __forceinline__ f32x4 mfma_bf(v4u a, v4u b, f32x4 c) {
  return __builtin_amdgcn_mfma_f32_16x16x32_bf16(__builtin_bit_cast(bf16x8, a), __builtin_bit_cast(bf16x8, b), c, 0, 0, 0);
}
__device__ __forceinline__ f32x4 mfma_h(v4u a, v4u b, f32x4 c) {
  return __builtin_amdgcn_mfma_f32_16x16x32_f16(__builtin_bit_cast(f16x8, a), __builtin_bit_cast(f16x8, b), c, 0, 0, 0);
}

__device__ __forceinline__ float diag(f32x4 q, int lane) {
  const float qa = (lane & 1) ? q[1] : q[0], qb = (lane & 1) ? q[3] : q[2];
  return (lane & 2) ? qb : qa;
}
__device__ __forceinline__ f32x4 sum9(const char* red) {
  f32x4 t = *(const f32x4*)red;
#pragma unroll
  for (int w = 1; w < 9; w++) t += *(const f32x4*)(red + w * 256);
  return t;
}
__device__ __forceinline__ void ln_round(float sum, float sq, float& m, float& s) {
  const float mean = sum * (1.0f / DM);
  const float var = fmaxf(fmaf(-mean, mean, sq * (1.0f / DM)), 0.f);
  const unsigned mv = pkbf(mean, var);
  m = blo(mv);
  s = rbf(__builtin_amdgcn_sqrtf(rbf(bhi(mv) + 1e-5f)));
}
__device__ __forceinline__ unsigned ln_apply(unsigned xp, float m, float rinv) {
  const unsigned d = pkbf(blo(xp) - m, bhi(xp) - m);
  return pkbf(blo(d) * rinv, bhi(d) * rinv);
}
__device__ __forceinline__ unsigned addbf(unsigned a, unsigned b) { return pkbf(blo(a) + blo(b), bhi(a) + bhi(b)); }

__device__ __forceinline__ void sweep_init(rsrc_t rs, unsigned tag, int lane, int first, int n) {
#pragma unroll 1
  for (int sp = 0; sp < SPIN_CAP; sp++) {
    MEMBAR();
    bool ok = true;
#pragma unroll
    for (int k = 0; k < 3; k++) ok &= ld1(rs, WS_INIT + (first + min(lane + 64 * k, n - 1)) * 64) == tag;
    if (__all(ok)) break;
    __builtin_amdgcn_s_sleep(4);
  }
}

struct cz { f32x2 v, s; };
__device__ __forceinline__ cz mkz(f32x2 v) {
  cz z;
  z.v = v;
  asm("v_pk_mov_b32 %0, %1, %1 op_sel:[1,0]" : "=v"(z.s) : "v"(v));
  return z;
}
#define CMUL_TXT(r, a, b, bs) "v_pk_mul_f32 " r ", " a ", " bs " op_sel:[1,0] neg_lo:[0,1]\n\tv_pk_fma_f32 " r ", " a ", " b ", " r " op_sel_hi:[0,1,1]\n\t"
#define CMULC_TXT(r, a, b, bs) "v_pk_mul_f32 " r ", " a ", " bs " op_sel:[1,0]\n\tv_pk_fma_f32 " r ", " a ", " b ", " r " op_sel_hi:[0,1,1] neg_hi:[0,1,0]\n\t"
__device__ __forceinline__ f32x2 cmul(f32x2 a, cz b) {
  f32x2 r;
  asm(CMUL_TXT("%0", "%1", "%2", "%3") : "=&v"(r) : "v"(a), "v"(b.v), "v"(b.s));
  return r;
}
__device__ __forceinline__ cz csq(cz a) { return mkz(cmul(a.v, a)); }
__device__ __forceinline__ void cmul2(f32x2& r, f32x2& q, f32x2 a, f32x2 c, cz b) {
  asm(CMUL_TXT("%0", "%2", "%4", "%5") CMUL_TXT("%1", "%3", "%4", "%5") : "=&v"(r), "=&v"(q) : "v"(a), "v"(c), "v"(b.v), "v"(b.s));
}
__device__ __forceinline__ void cmulc2(f32x2& r, f32x2& q, f32x2 a, cz b, f32x2 c, cz d) {
  asm(CMULC_TXT("%0", "%2", "%3", "%4") CMULC_TXT("%1", "%5", "%6", "%7")
      : "=&v"(r), "=&v"(q) : "v"(a), "v"(b.v), "v"(b.s), "v"(c), "v"(d.v), "v"(d.s));
}
__device__ __forceinline__ f32x2 cexp_p2(float p, float ch, float ph, float pl) {
  const float mag = __builtin_amdgcn_exp2f(p * ch);
  const float fr = fmaf(p, pl, __builtin_amdgcn_fractf(p * ph));
  return f32x2{mag * __builtin_amdgcn_cosf(fr), mag * __builtin_amdgcn_sinf(fr)};
}
__device__ __forceinline__ unsigned pkh2(f32x2 v) { return pkh(v.x, v.y); }

#define CONV_ASM_TEXT \
  "v_add_u32 v120, -512, %0\n" \
  "v_add_u32 v121, -1536, %0\n" \
  "v_add_u32 v122, -2560, %0\n" \
  "v_add_u32 v123, -3584, %0\n" \
  "v_mov_b32 v124, %1\n" \
  "v_mov_b32 v125, %2\n" \
  "ds_read_b128 v[88:91], v124 offset:0\n" \
  "ds_read2_b32 v[24:25], v120 offset0:128 offset1:129\n" \
  "ds_read2_b32 v[26:27], v120 offset0:130 offset1:131\n" \
  "ds_read_b128 v[92:95], v124 offset:512\n" \
  "ds_read_b128 v[96:99], v124 offset:1024\n" \
  "ds_read_b128 v[100:103], v124 offset:1536\n" \
  "ds_read2_b32 v[28:29], v120 offset0:0 offset1:1\n" \
  "ds_read2_b32 v[30:31], v120 offset0:2 offset1:3\n" \
  "ds_read2_b32 v[32:33], v121 offset0:128 offset1:129\n" \
  "ds_read2_b32 v[34:35], v121 offset0:130 offset1:131\n" \
  "ds_read2_b32 v[36:37], v121 offset0:0 offset1:1\n" \
  "ds_read2_b32 v[38:39], v121 offset0:2 offset1:3\n" \
  "ds_read2_b32 v[40:41], v122 offset0:128 offset1:129\n" \
  "ds_read2_b32 v[42:43], v122 offset0:130 offset1:131\n" \
  "ds_read2_b32 v[44:45], v122 offset0:0 offset1:1\n" \
  "ds_read2_b32 v[46:47], v122 offset0:2 offset1:3\n" \
  "ds_read2_b32 v[48:49], v123 offset0:128 offset1:129\n" \
  "ds_read2_b32 v[50:51], v123 offset0:130 offset1:131\n" \
  "ds_read2_b32 v[52:53], v123 offset0:0 offset1:1\n" \
  "ds_read2_b32 v[54:55], v123 offset0:2 offset1:3\n" \
  "v_mov_b32 v136, 0\n" \
  "v_mov_b32 v137, 0\n" \
  "v_mov_b32 v138, 0\n" \
  "v_mov_b32 v139, 0\n" \
  "v_mov_b32 v140, 0\n" \
  "v_mov_b32 v141, 0\n" \
  "v_mov_b32 v142, 0\n" \
  "v_mov_b32 v143, 0\n" \
  "v_mov_b32 v144, 0\n" \
  "v_mov_b32 v145, 0\n" \
  "v_mov_b32 v146, 0\n" \
  "v_mov_b32 v147, 0\n" \
  "v_mov_b32 v148, 0\n" \
  "v_mov_b32 v149, 0\n" \
  "v_mov_b32 v150, 0\n" \
  "v_mov_b32 v151, 0\n" \
  "v_mov_b32 v152, 0\n" \
  "v_mov_b32 v153, 0\n" \
  "v_mov_b32 v154, 0\n" \
  "v_mov_b32 v155, 0\n" \
  "v_mov_b32 v156, 0\n" \
  "v_mov_b32 v157, 0\n" \
  "v_mov_b32 v158, 0\n" \
  "v_mov_b32 v159, 0\n" \
  "v_mov_b32 v160, 0\n" \
  "v_mov_b32 v161, 0\n" \
  "v_mov_b32 v162, 0\n" \
  "v_mov_b32 v163, 0\n" \
  "v_mov_b32 v164, 0\n" \
  "v_mov_b32 v165, 0\n" \
  "v_mov_b32 v166, 0\n" \
  "v_mov_b32 v167, 0\n" \
  "s_mov_b32 s40, 0\n" \
  "L0_%=:\n" \
  "s_waitcnt lgkmcnt(10)\n" \
  "v_mfma_f32_16x16x32_f16 v[136:139], v[24:27], v[88:91], v[136:139]\n" \
  "ds_read_b128 v[104:107], v124 offset:64\n" \
  "v_mfma_f32_16x16x32_f16 v[140:143], v[24:27], v[92:95], v[140:143]\n" \
  "ds_read2_b32 v[56:57], v120 offset0:144 offset1:145\n" \
  "v_mfma_f32_16x16x32_f16 v[144:147], v[24:27], v[96:99], v[144:147]\n" \
  "ds_read2_b32 v[58:59], v120 offset0:146 offset1:147\n" \
  "v_mfma_f32_16x16x32_f16 v[148:151], v[24:27], v[100:103], v[148:151]\n" \
  "ds_read_b128 v[108:111], v124 offset:576\n" \
  "v_mfma_f32_16x16x32_f16 v[140:143], v[28:31], v[88:91], v[140:143]\n" \
  "ds_read_b128 v[112:115], v124 offset:1088\n" \
  "v_mfma_f32_16x16x32_f16 v[144:147], v[28:31], v[92:95], v[144:147]\n" \
  "ds_read_b128 v[116:119], v124 offset:1600\n" \
  "v_mfma_f32_16x16x32_f16 v[148:151], v[28:31], v[96:99], v[148:151]\n" \
  "ds_read2_b32 v[60:61], v120 offset0:16 offset1:17\n" \
  "v_mfma_f32_16x16x32_f16 v[152:155], v[28:31], v[100:103], v[152:155]\n" \
  "ds_read2_b32 v[62:63], v120 offset0:18 offset1:19\n" \
  "v_mfma_f32_16x16x32_f16 v[144:147], v[32:35], v[88:91], v[144:147]\n" \
  "ds_read2_b32 v[64:65], v121 offset0:144 offset1:145\n" \
  "v_mfma_f32_16x16x32_f16 v[148:151], v[32:35], v[92:95], v[148:151]\n" \
  "ds_read2_b32 v[66:67], v121 offset0:146 offset1:147\n" \
  "v_mfma_f32_16x16x32_f16 v[152:155], v[32:35], v[96:99], v[152:155]\n" \
  "ds_read2_b32 v[68:69], v121 offset0:16 offset1:17\n" \
  "v_mfma_f32_16x16x32_f16 v[156:159], v[32:35], v[100:103], v[156:159]\n" \
  "ds_read2_b32 v[70:71], v121 offset0:18 offset1:19\n" \
  "s_waitcnt lgkmcnt(10)\n" \
  "v_mfma_f32_16x16x32_f16 v[148:151], v[36:39], v[88:91], v[148:151]\n" \
  "ds_read2_b32 v[72:73], v122 offset0:144 offset1:145\n" \
  "v_mfma_f32_16x16x32_f16 v[152:155], v[36:39], v[92:95], v[152:155]\n" \
  "ds_read2_b32 v[74:75], v122 offset0:146 offset1:147\n" \
  "v_mfma_f32_16x16x32_f16 v[156:159], v[36:39], v[96:99], v[156:159]\n" \
  "ds_read2_b32 v[76:77], v122 offset0:16 offset1:17\n" \
  "v_mfma_f32_16x16x32_f16 v[160:163], v[36:39], v[100:103], v[160:163]\n" \
  "ds_read2_b32 v[78:79], v122 offset0:18 offset1:19\n" \
  "v_mfma_f32_16x16x32_f16 v[152:155], v[40:43], v[88:91], v[152:155]\n" \
  "ds_read2_b32 v[80:81], v123 offset0:144 offset1:145\n" \
  "v_mfma_f32_16x16x32_f16 v[156:159], v[40:43], v[92:95], v[156:159]\n" \
  "ds_read2_b32 v[82:83], v123 offset0:146 offset1:147\n" \
  "v_mfma_f32_16x16x32_f16 v[160:163], v[40:43], v[96:99], v[160:163]\n" \
  "ds_read2_b32 v[84:85], v123 offset0:16 offset1:17\n" \
  "v_mfma_f32_16x16x32_f16 v[164:167], v[40:43], v[100:103], v[164:167]\n" \
  "ds_read2_b32 v[86:87], v123 offset0:18 offset1:19\n" \
  "v_mfma_f32_16x16x32_f16 v[156:159], v[44:47], v[88:91], v[156:159]\n" \
  "v_mfma_f32_16x16x32_f16 v[160:163], v[44:47], v[92:95], v[160:163]\n" \
  "v_mfma_f32_16x16x32_f16 v[164:167], v[44:47], v[96:99], v[164:167]\n" \
  "v_mfma_f32_16x16x32_f16 v[160:163], v[48:51], v[88:91], v[160:163]\n" \
  "v_mfma_f32_16x16x32_f16 v[164:167], v[48:51], v[92:95], v[164:167]\n" \
  "v_mfma_f32_16x16x32_f16 v[164:167], v[52:55], v[88:91], v[164:167]\n" \
  "s_waitcnt lgkmcnt(10)\n" \
  "v_mfma_f32_16x16x32_f16 v[136:139], v[56:59], v[104:107], v[136:139]\n" \
  "ds_read_b128 v[88:91], v124 offset:128\n" \
  "v_mfma_f32_16x16x32_f16 v[140:143], v[56:59], v[108:111], v[140:143]\n" \
  "ds_read2_b32 v[24:25], v120 offset0:160 offset1:161\n" \
  "v_mfma_f32_16x16x32_f16 v[144:147], v[56:59], v[112:115], v[144:147]\n" \
  "ds_read2_b32 v[26:27], v120 offset0:162 offset1:163\n" \
  "v_mfma_f32_16x16x32_f16 v[148:151], v[56:59], v[116:119], v[148:151]\n" \
  "ds_read_b128 v[92:95], v124 offset:640\n" \
  "v_mfma_f32_16x16x32_f16 v[140:143], v[60:63], v[104:107], v[140:143]\n" \
  "ds_read_b128 v[96:99], v124 offset:1152\n" \
  "v_mfma_f32_16x16x32_f16 v[144:147], v[60:63], v[108:111], v[144:147]\n" \
  "ds_read_b128 v[100:103], v124 offset:1664\n" \
  "v_mfma_f32_16x16x32_f16 v[148:151], v[60:63], v[112:115], v[148:151]\n" \
  "ds_read2_b32 v[28:29], v120 offset0:32 offset1:33\n" \
  "v_mfma_f32_16x16x32_f16 v[152:155], v[60:63], v[116:119], v[152:155]\n" \
  "ds_read2_b32 v[30:31], v120 offset0:34 offset1:35\n" \
  "v_mfma_f32_16x16x32_f16 v[144:147], v[64:67], v[104:107], v[144:147]\n" \
  "ds_read2_b32 v[32:33], v121 offset0:160 offset1:161\n" \
  "v_mfma_f32_16x16x32_f16 v[148:151], v[64:67], v[108:111], v[148:151]\n" \
  "ds_read2_b32 v[34:35], v121 offset0:162 offset1:163\n" \
  "v_mfma_f32_16x16x32_f16 v[152:155], v[64:67], v[112:115], v[152:155]\n" \
  "ds_read2_b32 v[36:37], v121 offset0:32 offset1:33\n" \
  "v_mfma_f32_16x16x32_f16 v[156:159], v[64:67], v[116:119], v[156:159]\n" \
  "ds_read2_b32 v[38:39], v121 offset0:34 offset1:35\n" \
  "s_waitcnt lgkmcnt(10)\n" \
  "v_mfma_f32_16x16x32_f16 v[148:151], v[68:71], v[104:107], v[148:151]\n" \
  "ds_read2_b32 v[40:41], v122 offset0:160 offset1:161\n" \
  "v_mfma_f32_16x16x32_f16 v[152:155], v[68:71], v[108:111], v[152:155]\n" \
  "ds_read2_b32 v[42:43], v122 offset0:162 offset1:163\n" \
  "v_mfma_f32_16x16x32_f16 v[156:159], v[68:71], v[112:115], v[156:159]\n" \
  "ds_read2_b32 v[44:45], v122 offset0:32 offset1:33\n" \
  "v_mfma_f32_16x16x32_f16 v[160:163], v[68:71], v[116:119], v[160:163]\n" \
  "ds_read2_b32 v[46:47], v122 offset0:34 offset1:35\n" \
  "v_mfma_f32_16x16x32_f16 v[152:155], v[72:75], v[104:107], v[152:155]\n" \
  "ds_read2_b32 v[48:49], v123 offset0:160 offset1:161\n" \
  "v_mfma_f32_16x16x32_f16 v[156:159], v[72:75], v[108:111], v[156:159]\n" \
  "ds_read2_b32 v[50:51], v123 offset0:162 offset1:163\n" \
  "v_mfma_f32_16x16x32_f16 v[160:163], v[72:75], v[112:115], v[160:163]\n" \
  "ds_read2_b32 v[52:53], v123 offset0:32 offset1:33\n" \
  "v_mfma_f32_16x16x32_f16 v[164:167], v[72:75], v[116:119], v[164:167]\n" \
  "ds_read2_b32 v[54:55], v123 offset0:34 offset1:35\n" \
  "v_mfma_f32_16x16x32_f16 v[156:159], v[76:79], v[104:107], v[156:159]\n" \
  "v_add_u32 v120, 128, v120\n" \
  "v_mfma_f32_16x16x32_f16 v[160:163], v[76:79], v[108:111], v[160:163]\n" \
  "v_add_u32 v121, 128, v121\n" \
  "v_mfma_f32_16x16x32_f16 v[164:167], v[76:79], v[112:115], v[164:167]\n" \
  "v_add_u32 v122, 128, v122\n" \
  "v_mfma_f32_16x16x32_f16 v[160:163], v[80:83], v[104:107], v[160:163]\n" \
  "v_add_u32 v123, 128, v123\n" \
  "v_mfma_f32_16x16x32_f16 v[164:167], v[80:83], v[108:111], v[164:167]\n" \
  "v_add_u32 v124, 128, v124\n" \
  "v_mfma_f32_16x16x32_f16 v[164:167], v[84:87], v[104:107], v[164:167]\n" \
  "s_add_u32 s40, s40, 1\n" \
  "s_cmp_lt_u32 s40, 4\n" \
  "s_cbranch_scc1 L0_%=\n" \
  "v_add_u32 v120, -512, v120\n" \
  "v_add_u32 v121, -512, v121\n" \
  "v_add_u32 v124, -512, v124\n" \
  "s_waitcnt lgkmcnt(0)\n" \
  "ds_read2_b32 v[24:25], v120 offset0:128 offset1:129\n" \
  "ds_read2_b32 v[26:27], v120 offset0:130 offset1:131\n" \
  "ds_read2_b32 v[28:29], v120 offset0:0 offset1:1\n" \
  "ds_read2_b32 v[30:31], v120 offset0:2 offset1:3\n" \
  "ds_read2_b32 v[32:33], v121 offset0:128 offset1:129\n" \
  "ds_read2_b32 v[34:35], v121 offset0:130 offset1:131\n" \
  "ds_read2_b32 v[36:37], v121 offset0:0 offset1:1\n" \
  "ds_read2_b32 v[38:39], v121 offset0:2 offset1:3\n" \
  "v_cvt_pk_bf16_f32 v126, v136, v137\n" \
  "v_cvt_pk_bf16_f32 v127, v138, v139\n" \
  "ds_write_b64 v125, v[126:127] offset:0\n" \
  "v_cvt_pk_bf16_f32 v128, v140, v141\n" \
  "v_cvt_pk_bf16_f32 v129, v142, v143\n" \
  "ds_write_b64 v125, v[128:129] offset:640\n" \
  "v_cvt_pk_bf16_f32 v130, v144, v145\n" \
  "v_cvt_pk_bf16_f32 v131, v146, v147\n" \
  "ds_write_b64 v125, v[130:131] offset:1280\n" \
  "v_cvt_pk_bf16_f32 v132, v148, v149\n" \
  "v_cvt_pk_bf16_f32 v133, v150, v151\n" \
  "ds_write_b64 v125, v[132:133] offset:1920\n" \
  "s_waitcnt lgkmcnt(0)\n" \
  "s_barrier\n" \
  "ds_read_b128 v[88:91], v124 offset:2048\n" \
  "ds_read_b128 v[92:95], v124 offset:2560\n" \
  "ds_read_b128 v[96:99], v124 offset:3072\n" \
  "ds_read_b128 v[100:103], v124 offset:3584\n" \
  "s_mov_b32 s40, 0\n" \
  "s_waitcnt lgkmcnt(0)\n" \
  "L1_%=:\n" \
  "s_waitcnt lgkmcnt(6)\n" \
  "v_mfma_f32_16x16x32_f16 v[152:155], v[24:27], v[88:91], v[152:155]\n" \
  "ds_read_b128 v[104:107], v124 offset:2112\n" \
  "ds_read2_b32 v[56:57], v120 offset0:144 offset1:145\n" \
  "v_mfma_f32_16x16x32_f16 v[156:159], v[24:27], v[92:95], v[156:159]\n" \
  "ds_read2_b32 v[58:59], v120 offset0:146 offset1:147\n" \
  "ds_read_b128 v[108:111], v124 offset:2624\n" \
  "v_mfma_f32_16x16x32_f16 v[160:163], v[24:27], v[96:99], v[160:163]\n" \
  "ds_read_b128 v[112:115], v124 offset:3136\n" \
  "v_mfma_f32_16x16x32_f16 v[164:167], v[24:27], v[100:103], v[164:167]\n" \
  "ds_read_b128 v[116:119], v124 offset:3648\n" \
  "s_waitcnt lgkmcnt(6)\n" \
  "v_mfma_f32_16x16x32_f16 v[156:159], v[28:31], v[88:91], v[156:159]\n" \
  "ds_read2_b32 v[60:61], v120 offset0:16 offset1:17\n" \
  "v_mfma_f32_16x16x32_f16 v[160:163], v[28:31], v[92:95], v[160:163]\n" \
  "ds_read2_b32 v[62:63], v120 offset0:18 offset1:19\n" \
  "v_mfma_f32_16x16x32_f16 v[164:167], v[28:31], v[96:99], v[164:167]\n" \
  "ds_read2_b32 v[64:65], v121 offset0:144 offset1:145\n" \
  "v_mfma_f32_16x16x32_f16 v[160:163], v[32:35], v[88:91], v[160:163]\n" \
  "ds_read2_b32 v[66:67], v121 offset0:146 offset1:147\n" \
  "v_mfma_f32_16x16x32_f16 v[164:167], v[32:35], v[92:95], v[164:167]\n" \
  "ds_read2_b32 v[68:69], v121 offset0:16 offset1:17\n" \
  "v_mfma_f32_16x16x32_f16 v[164:167], v[36:39], v[88:91], v[164:167]\n" \
  "ds_read2_b32 v[70:71], v121 offset0:18 offset1:19\n" \
  "s_waitcnt lgkmcnt(6)\n" \
  "v_mfma_f32_16x16x32_f16 v[152:155], v[56:59], v[104:107], v[152:155]\n" \
  "ds_read_b128 v[88:91], v124 offset:2176\n" \
  "ds_read2_b32 v[24:25], v120 offset0:160 offset1:161\n" \
  "v_mfma_f32_16x16x32_f16 v[156:159], v[56:59], v[108:111], v[156:159]\n" \
  "ds_read2_b32 v[26:27], v120 offset0:162 offset1:163\n" \
  "ds_read_b128 v[92:95], v124 offset:2688\n" \
  "v_mfma_f32_16x16x32_f16 v[160:163], v[56:59], v[112:115], v[160:163]\n" \
  "ds_read_b128 v[96:99], v124 offset:3200\n" \
  "v_mfma_f32_16x16x32_f16 v[164:167], v[56:59], v[116:119], v[164:167]\n" \
  "ds_read_b128 v[100:103], v124 offset:3712\n" \
  "s_waitcnt lgkmcnt(6)\n" \
  "v_mfma_f32_16x16x32_f16 v[156:159], v[60:63], v[104:107], v[156:159]\n" \
  "ds_read2_b32 v[28:29], v120 offset0:32 offset1:33\n" \
  "v_mfma_f32_16x16x32_f16 v[160:163], v[60:63], v[108:111], v[160:163]\n" \
  "ds_read2_b32 v[30:31], v120 offset0:34 offset1:35\n" \
  "v_mfma_f32_16x16x32_f16 v[164:167], v[60:63], v[112:115], v[164:167]\n" \
  "ds_read2_b32 v[32:33], v121 offset0:160 offset1:161\n" \
  "v_mfma_f32_16x16x32_f16 v[160:163], v[64:67], v[104:107], v[160:163]\n" \
  "ds_read2_b32 v[34:35], v121 offset0:162 offset1:163\n" \
  "v_mfma_f32_16x16x32_f16 v[164:167], v[64:67], v[108:111], v[164:167]\n" \
  "ds_read2_b32 v[36:37], v121 offset0:32 offset1:33\n" \
  "v_mfma_f32_16x16x32_f16 v[164:167], v[68:71], v[104:107], v[164:167]\n" \
  "ds_read2_b32 v[38:39], v121 offset0:34 offset1:35\n" \
  "v_add_u32 v120, 128, v120\n" \
  "v_add_u32 v121, 128, v121\n" \
  "v_add_u32 v124, 128, v124\n" \
  "s_add_u32 s40, s40, 1\n" \
  "s_cmp_lt_u32 s40, 4\n" \
  "s_cbranch_scc1 L1_%=\n" \
  "s_nop 7\n" \
  "v_cvt_pk_bf16_f32 v126, v152, v153\n" \
  "v_cvt_pk_bf16_f32 v127, v154, v155\n" \
  "ds_write_b64 v125, v[126:127] offset:2560\n" \
  "v_cvt_pk_bf16_f32 v128, v156, v157\n" \
  "v_cvt_pk_bf16_f32 v129, v158, v159\n" \
  "ds_write_b64 v125, v[128:129] offset:3200\n" \
  "v_cvt_pk_bf16_f32 v130, v160, v161\n" \
  "v_cvt_pk_bf16_f32 v131, v162, v163\n" \
  "ds_write_b64 v125, v[130:131] offset:3840\n" \
  "v_cvt_pk_bf16_f32 v132, v164, v165\n" \
  "v_cvt_pk_bf16_f32 v133, v166, v167\n" \
  "ds_write_b64 v125, v[132:133] offset:4480\n" \
  "s_waitcnt lgkmcnt(0)\n" \
  "s_barrier\n"
#define CONV_ASM_CLOBBERS "v24", "v25", "v26", "v27", "v28", "v29", "v30", "v31", "v32", "v33", "v34", "v35", "v36", "v37", "v38", "v39", "v40", "v41", "v42", "v43", "v44", "v45", "v46", "v47", "v48", "v49", "v50", "v51", "v52", "v53", "v54", "v55", "v56", "v57", "v58", "v59", "v60", "v61", "v62", "v63", "v64", "v65", "v66", "v67", "v68", "v69", "v70", "v71", "v72", "v73", "v74", "v75", "v76", "v77", "v78", "v79", "v80", "v81", "v82", "v83", "v84", "v85", "v86", "v87", "v88", "v89", "v90", "v91", "v92", "v93", "v94", "v95", "v96", "v97", "v98", "v99", "v100", "v101", "v102", "v103", "v104", "v105", "v106", "v107", "v108", "v109", "v110", "v111", "v112", "v113", "v114", "v115", "v116", "v117", "v118", "v119", "v120", "v121", "v122", "v123", "v124", "v125", "v126", "v127", "v128", "v129", "v130", "v131", "v132", "v133", "v134", "v135", "v136", "v137", "v138", "v139", "v140", "v141", "v142", "v143", "v144", "v145", "v146", "v147", "v148", "v149", "v150", "v151", "v152", "v153", "v154", "v155", "v156", "v157", "v158", "v159", "v160", "v161", "v162", "v163", "v164", "v165", "v166", "v167", "s40", "scc", "memory"

struct Params {
  const u16 *x, *lar, *aim, *crl, *cim, *ldt, *dskip, *wr, *wg, *wu, *wd;
  u16* out;
  char* ws;
};

__global__ __launch_bounds__(NT, 3) void k_main(Params p) {
  extern __shared__ __attribute__((aligned(16))) char smem[];
  const int tid = threadIdx.x, lane = tid & 63;
  int wave = __builtin_amdgcn_readfirstlane(tid >> 6);
  const int xcd = blockIdx.x & 7;
  int loc = blockIdx.x >> 3;
  STAMP_INIT();
  STAMP(0);
  unsigned cpf = 0;
  if (wave == 0 && lane < 8 && (loc * 8 + lane) * 128 < CODE_BYTES) {
    const char* pc = (const char*)(__builtin_amdgcn_s_getpc() & ~(uint64_t)255);
    cpf = *(const __attribute__((address_space(1))) unsigned*)(pc + (loc * 8 + lane) * 128);
  }
  asm volatile("" ::"s"(p.x), "s"(p.lar), "s"(p.aim), "s"(p.crl), "s"(p.cim), "s"(p.ldt), "s"(p.dskip), "s"(p.wr),
               "s"(p.wg), "s"(p.wu), "s"(p.wd), "s"(p.out), "s"(p.ws));
  STAMP(11);
  const rsrc_t rs = __builtin_amdgcn_make_buffer_rsrc((void*)p.ws, 0, WS_END, 0x00020000);
  const rsrc_t ro = __builtin_amdgcn_make_buffer_rsrc((void*)p.out, 0, SEQ * DM * 2, 0x00020000);
  const v4u sent4 = {SENT, SENT, SENT, SENT};
  const int cl = lane & 15, g = lane >> 4;
  const int cg = xcd * 18 + loc;
  const int fj = xcd * 9 + (loc - 18);
  const int row0 = xcd * 256 + (loc - 18 + max(loc - 30, 0)) * 16;

  v4u ga = {0u, 0u, 0u, 0u}, gb = ga, gc = ga, gd = ga;
  if (loc >= 18) {
    const u16* xr = p.x + (size_t)(row0 + (cl >> 1)) * DM + 64 * wave + 16 * g + 8 * (cl & 1);
    ga = *(const v4u*)xr;
    gb = *(const v4u*)(xr + 8 * DM);
    if (loc >= 30) {
      gc = *(const v4u*)(xr + 16 * DM);
      gd = *(const v4u*)(xr + 24 * DM);
    }
  } else if (wave < 8) {
    const int d = cg * 4 + (wave >> 1);
    const int pi = d * NM + (wave & 1) * 64 + lane;
    ga = v4u{p.lar[pi], p.aim[pi], p.crl[pi], p.cim[pi]};
    gb = v4u{p.ldt[d], p.crl[pi ^ 64], p.cim[pi ^ 64], 0u};
    const v2u dk = *(const v2u*)(p.dskip + cg * 4 + (lane >> 6));
    gc.x = dk.x;
    gc.y = dk.y;
  } else {
    const unsigned* w32 = (const unsigned*)p.wr;
    ga = v4u{w32[lane], w32[lane + 64], w32[lane + 128], w32[lane + 192]};
    gb.x = w32[256 + (lane & 31)];
    if (lane < 16) *(unsigned*)(smem + L_MISC + lane * 4) = 0u;
  }
  unsigned epoch = ld1(rs, WS_EPOCH);
  BAR();
  asm volatile("" : "+s"(loc), "+s"(wave));
  STAMP(12);

  if (loc < 18) {
    const int rb = xcd * 16 + loc;
    const int tau = tid & 255;
    if (wave == 8) {
      __builtin_amdgcn_s_setprio(HELP_PRIO);
      {
#pragma unroll
        for (int k = 0; k < 4; k++) *(unsigned*)(smem + L_WR + (lane + 64 * k) * 4) = ga[k];
        *(unsigned*)(smem + L_WR + (256 + (lane & 31)) * 4) = gb.x;
        float ws_ = lane < 32 ? blo(gb.x) + bhi(gb.x) : 0.f;
#pragma unroll
        for (int k = 0; k < 4; k++) ws_ += blo(ga[k]) + bhi(ga[k]);
        ws_ = wave_sum(ws_);
        if (lane == 0) *(float*)(smem + L_MISC + 64) = ws_;
      }
      SCHED();
      const rsrc_t rx = __builtin_amdgcn_make_buffer_rsrc((void*)p.x, 0, SEQ * DM * 2, 0x00020000);
      const int xo = (1024 + lane) * 1152 + cg * 8;
      v2u xc[16];
#pragma unroll
      for (int i = 0; i < XC_FIRST; i++) xc[i] = ldp2(rx, xo, i * 64 * 1152);
      STAMP(1);
      BAR();
      BAR();
#pragma unroll
      for (int i = XC_FIRST; i < 16; i++) xc[i] = ldp2(rx, xo, i * 64 * 1152);
      SCHED();
#pragma unroll
      for (int i = 0; i < 16; i++) *(v2u*)(smem + L_XCOL + 8192 + lane * 8 + i * 512) = xc[i];
      LGKM0();
      if (lane == 0) *(volatile LAS unsigned*)(smem + L_MISC + 32) = 1u;
      STAMP(3);
#pragma unroll 1
      for (int i = 0; i < 16; i++) st4(sent4, rs, WS_HT + cg * 16384 + i * 1024 + lane * 16);
      st4(sent4, rs, (loc < 16 && lane < 4) ? WS_RREC + rb * 64 + lane * 16 : OOB);
      st4(sent4, rs, cg < 20 ? WS_XSEL + cg * 1024 + lane * 16 : OOB);
      DRAIN();
      if (lane == 0) st1(epoch + 1u, rs, WS_INIT + cg * 64);
      STAMP(4);
      unsigned pf[4];
      {
        const char* gu = (const char*)(loc < 9 ? p.wg : p.wu) + (xcd * 9 + (loc < 9 ? loc : loc - 9)) * 128;
#pragma unroll
        for (int i = 0; i < 4; i++) pf[i] = *(const unsigned*)(gu + (size_t)(320 + i * 64 + lane) * 9216);
      }
      STAMP(5);
      BAR();
#pragma unroll
      for (int i = 0; i < 4; i++) asm volatile("" ::"v"(pf[i]));
      BAR();
    } else {
      const int pch = wave >> 1;
      char* const pair = smem + pch * PAIR;
      char* const scr = smem + wave * WSCR;
      v4u xk[2];
      {
        const rsrc_t rx = __builtin_amdgcn_make_buffer_rsrc((void*)p.x, 0, wave >= 4 ? SEQ * DM * 2 : 0, 0x00020000);
        const int xo = tau * 2304 + cg * 8;
        const v2u a = ldp2(rx, xo, 0), b = ldp2(rx, xo + 1152, 0);
        const v2u c = ldp2(rx, xo, 512 * 1152), d = ldp2(rx, xo + 1152, 512 * 1152);
        xk[0] = v4u{a.x, a.y, b.x, b.y};
        xk[1] = v4u{c.x, c.y, d.x, d.y};
      }
      SCHED();
      LAS char *wb_[16], *rb_[8];
#pragma unroll
      for (int s = 0; s < 16; s++) { wb_[s] = (LAS char*)scr + ((lane * 4) ^ (s << 4)); PIN(wb_[s]); }
#pragma unroll
      for (int ks = 0; ks < 8; ks++) {
        rb_[ks] = (LAS char*)scr + (lane & 31) * 256 + ((ks * 32 + (lane >> 5) * 16) ^ ((lane & 15) << 4));
        PIN(rb_[ks]);
      }
      SCHED();
      PIN(ga); PIN(gb); PIN(gc); PIN(gd);
      const float dt = rbf(expf(blo(gb.x)));
      const float cre = blo(ga.z) * dt, cim = blo(ga.w) * dt;
      const unsigned dw = (pch & 2) ? gc.y : gc.x;
      const float dsk = (pch & 1) ? bhi(dw) : blo(dw);
      const float cmx = fmaxf(fmaxf(fmaxf(fabsf(cre), fabsf(cim)), fabsf(dsk) * 0.0078125f), fmaxf(fabsf(blo(gb.y)), fabsf(blo(gb.z))) * dt);
      const unsigned ce = min(max(wave_max_u(__float_as_uint(cmx)) >> 23, 40u), 250u);
      const float csc = __uint_as_float((257u - ce) << 23);
      if (lane == 0) *(float*)(smem + L_MISC + 48 + pch * 4) = __uint_as_float((ce - 6u) << 23);
      STAMP(1);
      f32x16 acc;
      {
        const float ch = -rbf(expf(blo(ga.x))) * dt * 1.44269504f;
        const float aie = blo(ga.y) * dt;
        const float ph = aie * 0.159154936f;
        const float pl = fmaf(aie, 0.159154936f, -ph) + aie * 7.13048051e-9f;
        const cz e1 = mkz(cexp_p2(1.f, ch, ph, pl)), e8 = mkz(cexp_p2(8.f, ch, ph, pl));
        const cz e64 = mkz(cexp_p2(64.f, ch, ph, pl)), e512 = mkz(cexp_p2(512.f, ch, ph, pl));
        const cz e2 = csq(e1), e16 = csq(e8), e128 = csq(e64), e1024 = csq(e512);
        const cz e4 = csq(e2), e32 = csq(e16), e256 = csq(e128);
        auto put2 = [&](int row, unsigned a, unsigned b) {
          *(LAS unsigned*)(wb_[row & 15] + row * 256) = a;
          *(LAS unsigned*)(wb_[row & 15] + (row + 16) * 256) = b;
        };
        f32x2 T[32];
        T[0] = f32x2{16.f, 0.f};
        T[16] = e128.v * 16.f;
        put2(0, pkh2(T[0]), pkh2(T[16]));
#pragma unroll
        for (int i = 1; i < 16; i++) {
          const cz b = (i & 1) ? e8 : (i & 2) ? e16 : (i & 4) ? e32 : e64;
          const int src = i & (i - 1);
          if (src == 0) {
            T[i] = b.v * 16.f;
            T[16 + i] = cmul(T[16], b);
          } else {
            cmul2(T[i], T[16 + i], T[src], T[16 + src], b);
          }
          put2(i, pkh2(T[i]), pkh2(T[16 + i]));
        }
        const f32x2 w0 = {cre * csc, -cim * csc};
        f32x2 w1, w2, w4, w6, w256, w260, w512, w768, w1024, w1280, w1536, w1792, ta, tb;
        cmulc2(ta, w256, w0, e8, w0, e256);
        put2(32 + 0, pkh2(w0), pkh2(ta));
        cmulc2(w1, w512, w0, e1, w0, e512);
        put2(32 + 1, pkh2(w1), pkh2(w512));
        cmulc2(w2, ta, w0, e2, w512, e4);
        put2(32 + 2, pkh2(w2), pkh2(ta));
        cmulc2(ta, tb, w2, e1, w512, e8);
        put2(32 + 3, pkh2(ta), pkh2(tb));
        cmulc2(w4, w1024, w0, e4, w0, e1024);
        put2(32 + 8, pkh2(w4), pkh2(w1024));
        cmulc2(ta, tb, w4, e1, w1024, e8);
        put2(32 + 9, pkh2(ta), pkh2(tb));
        cmulc2(w6, w1280, w4, e2, w1024, e256);
        put2(32 + 10, pkh2(w6), pkh2(w1280));
        cmulc2(ta, tb, w6, e1, w1280, e8);
        put2(32 + 11, pkh2(ta), pkh2(tb));
        cmulc2(ta, w768, w256, e8, w512, e256);
        const unsigned h256 = pkh2(w256), h264 = pkh2(ta);
        put2(32 + 4, h256, h264);
        put2(32 + 5, h256, pkh2(w768));
        cmulc2(ta, tb, w256, e2, w768, e4);
        put2(32 + 6, pkh2(ta), pkh2(tb));
        cmulc2(w260, tb, w256, e4, w768, e8);
        const unsigned h260 = pkh2(w260);
        put2(32 + 7, h260, pkh2(tb));
        cmulc2(w1536, w1792, w1024, e512, w1280, e512);
        put2(32 + 12, h260, pkh2(w1536));
        cmulc2(ta, tb, w1536, e8, w260, e2);
        put2(32 + 13, h260, pkh2(ta));
        put2(32 + 14, pkh2(tb), pkh2(w1792));
        cmulc2(ta, tb, w1792, e8, w1792, e8);
        put2(32 + 15, h264, pkh2(ta));
        MEMBAR();
        f16x8 fa[8], fb[8];
#pragma unroll
        for (int ks = 0; ks < 8; ks++) {
          fb[ks] = *(const LAS f16x8*)(rb_[ks]);
          fa[ks] = *(const LAS f16x8*)(rb_[ks] + 8192);
        }
        SCHED();
#pragma unroll
        for (int i = 0; i < 16; i++) acc[i] = 0.f;
#pragma unroll
        for (int ks = 0; ks < 8; ks++) acc = __builtin_amdgcn_mfma_f32_32x32x16_f16(fa[ks], fb[ks], acc, 0, 0, 0);
        MEMBAR();
      }
      STAMP(2);
#pragma unroll
      for (int q = 0; q < 4; q++)
        *(f32x4*)(scr + q * 1024 + lane * 16) = f32x4{acc[4 * q], acc[4 * q + 1], acc[4 * q + 2], acc[4 * q + 3]};
      LGKM0();
      volatile LAS unsigned* const pf = (volatile LAS unsigned*)(smem + L_MISC);
      if (lane == 0) pf[wave] = 1u;
#pragma unroll 1
      for (int sp = 0; sp < SPIN_CAP * 16 && pf[wave ^ 1] == 0u; sp++) {}
      MEMBAR();
      {
        const char* oth = smem + (wave ^ 1) * WSCR + lane * 16;
        f32x4 o[4];
#pragma unroll
        for (int q = 0; q < 4; q++) o[q] = *(const f32x4*)(oth + q * 1024);
        SCHED();
#pragma unroll
        for (int i = 0; i < 16; i++) acc[i] += o[i >> 2][i & 3];
        acc[0] += lane == 0 ? dsk * csc * 8.f : 0.f;
      }
      {
        const int j = lane & 31, h = lane >> 5;
        const float xa = (j & 1) ? acc[13] : acc[12], xb = (j & 1) ? acc[15] : acc[14];
        const unsigned aa2 = pkh(acc[9], acc[9]), bb2 = pkh(acc[10], acc[10]), cc2 = pkh(acc[11], acc[11]);
        const unsigned aa3 = pkh(acc[12], acc[12]), bb3 = pkh(acc[13], acc[13]);
        const unsigned aa4 = pkh(acc[14], acc[14]), bb4 = pkh(acc[15], acc[15]);
        v4u c0, c1, c2, c3, ct = {0u, 0u, 0u, 0u};
        if (wave & 1) {
          c0 = v4u{pkh(acc[8], acc[7]), pkh(acc[6], acc[5]), pkh(acc[4], acc[3]), pkh(acc[2], acc[1])};
          c1 = v4u{cc2, pkh(acc[11], acc[10]), bb2, aa2};
          c2 = v4u{bb3, bb3, pkh(xa, acc[12]), aa3};
          c3 = v4u{bb4, bb4, pkh(xb, acc[14]), aa4};
          ct.x = lane == 0 ? pkh(acc[0], 0.f) : 0u;
        } else {
          c0 = v4u{pkh(acc[7], acc[6]), pkh(acc[5], acc[4]), pkh(acc[3], acc[2]), pkh(acc[1], acc[0])};
          c1 = v4u{cc2, bb2, pkh(acc[10], acc[9]), aa2};
          c2 = v4u{bb3, pkh(acc[13], xa), aa3, aa3};
          c3 = v4u{bb4, pkh(acc[15], xb), aa4, aa4};
        }
        char* ki = pair + L_KIMG + (wave & 1) * KCOPY;
        *(v4u*)(ki + 16 * (255 - lane)) = c0;
        *(v4u*)(ki + 16 * (191 - lane)) = c1;
        *(v4u*)(ki + 16 * (127 - 64 * h - j)) = c2;
        *(v4u*)(ki + 16 * (95 - 64 * h - j)) = c3;
        if (lane < 17) *(v4u*)(lane < 2 ? ki + 4096 + lane * 16 : pair + L_UIMG + ((wave & 1) * 15 + lane - 2) * 16) = ct;
      }
      STAMP(3);
      if (wave < 4) {
        const unsigned tag = epoch + 1u;
        v4u sg[4];
#pragma unroll 1
        for (int sp = 0; sp < SPIN_CAP; sp++) {
          MEMBAR();
          bool ok = true;
#pragma unroll
          for (int k = 0; k < 4; k++) sg[k] = ld4(rs, WS_STAT + wave * 4096 + k * 1024 + lane * 16);
#pragma unroll
          for (int k = 0; k < 4; k++) ok &= sg[k].y == tag && sg[k].w == tag;
          if (__all(ok)) break;
        }
#pragma unroll
        for (int k = 0; k < 4; k++) *(v2u*)(smem + L_STAT + wave * 2048 + k * 512 + lane * 8) = v2u{sg[k].x, sg[k].z};
      } else {
        *(v4u*)(smem + L_XCOL + 4096 + tau * 16) = xk[1];
      }
      STAMP(4);
      BAR();
      auto unpack = [&](const v4u& xv, f32x2(&xf)[4]) {
        xf[0] = f32x2{blo(xv.x), blo(xv.z)};
        xf[1] = f32x2{bhi(xv.x), bhi(xv.z)};
        xf[2] = f32x2{blo(xv.y), blo(xv.w)};
        xf[3] = f32x2{bhi(xv.y), bhi(xv.w)};
      };
      auto ubuild = [&](int i, const f32x2(&xf)[4]) {
        const v2u sg = *(const v2u*)(smem + L_STAT + i * 2048 + tau * 8);
        const f32x2 rr = {__builtin_amdgcn_rcpf(bhi(sg.x)), __builtin_amdgcn_rcpf(bhi(sg.y))};
        const f32x2 mm = {blo(sg.x), blo(sg.y)};
        char* up = smem + L_UIMG + (240 + 512 * i + 2 * tau) * 2;
#pragma unroll
        for (int c = 0; c < 4; c++) *(unsigned*)(up + c * PAIR) = pkh2((xf[c] - mm) * rr);
      };
      f32x2 xf[4][4];
      if (wave < 4) xk[0] = *(const v4u*)(smem + L_XCOL + 4096 + tau * 16);
      unpack(xk[0], xf[0]);
      ubuild(wave < 4 ? 1 : 0, xf[0]);
      BAR();
      STAMP(5);
      if (wave < 4) {
        const LAS char* cp = (const LAS char*)(smem + wave * PAIR);
        asm volatile(CONV_ASM_TEXT ::"v"(cp + L_KIMG + ((cl + 1) & 1) * KCOPY + (896 + 4 * g + ((16 - cl) >> 1)) * 4),
                     "v"(cp + L_UIMG + (2 * cl + g) * 16), "v"(cp + L_Y + g * 160 + cl * 8)
                     : CONV_ASM_CLOBBERS);
        STAMP(6);
        if (wave == 3 && loc < 16) sweep_init(rs, epoch + 1u, lane, 0, NCONV);
      } else {
        __builtin_amdgcn_s_setprio(HELP_PRIO);
        volatile LAS unsigned* const cf = (volatile LAS unsigned*)(smem + L_MISC + 32);
#pragma unroll 1
        for (int sp = 0; sp < SPIN_CAP * 16 && *cf == 0u; sp++) __builtin_amdgcn_s_sleep(1);
        MEMBAR();
        unpack(*(const v4u*)(smem + L_XCOL + 8192 + tau * 16), xf[2]);
        unpack(*(const v4u*)(smem + L_XCOL + 12288 + tau * 16), xf[3]);
        ubuild(2, xf[2]);
        ubuild(3, xf[3]);
        unpack(xk[1], xf[1]);
#pragma unroll
        for (int c = 0; c < 4; c++) PIN(xf[1][c]);
        f32x2 kinv[4];
#pragma unroll
        for (int c = 0; c < 4; c++) {
          const float k = *(const float*)(smem + L_MISC + 48 + c * 4);
          kinv[c] = f32x2{k, k};
          PIN(kinv[c]);
        }
        const char* yb = smem + L_Y + (tau >> 7) * 640 + ((tau >> 1) & 3) * 160 + ((tau >> 3) & 15) * 8 + (tau & 1) * 4;
        auto epi2 = [&](int i0) {
          unsigned y[2][4];
#pragma unroll
          for (int i = 0; i < 2; i++)
#pragma unroll
            for (int c = 0; c < 4; c++) y[i][c] = *(const unsigned*)(yb + (i0 + i) * 1280 + c * PAIR);
          SCHED();
#pragma unroll
          for (int i = 0; i < 2; i++) {
            f32x2 h[4];
#pragma unroll
            for (int c = 0; c < 4; c++)
              h[c] = __builtin_elementwise_fma(f32x2{blo(y[i][c]), bhi(y[i][c])}, kinv[c], xf[i0 + i][c]);
            const v4u hv = {nosent(pkbf(h[0].x, h[1].x)), nosent(pkbf(h[2].x, h[3].x)), nosent(pkbf(h[0].y, h[1].y)), nosent(pkbf(h[2].y, h[3].y))};
            st4(hv, rs, WS_HT + cg * 16384 + (i0 + i) * 4096 + tau * 16);
          }
        };
        STAMP(6);
        BAR();
        epi2(0);
        STAMP(2);
        BAR();
        epi2(2);
      }
    }
    STAMP(7);
    __builtin_amdgcn_s_setprio(0);
    asm volatile("" ::"v"(cpf));
    if (loc >= 16) { STAMP_FLUSH(); return; }
    v4u cw[2];
#pragma unroll
    for (int ks = 0; ks < 2; ks++) {
      const v4u w = *(const v4u*)(smem + L_WR + (64 * wave + 32 * ks + 8 * g) * 2);
      const bool one = (lane & 3) != 1;
      cw[ks] = v4u{one ? 0x3f803f80u : w.x, one ? 0x3f803f80u : w.y, one ? 0x3f803f80u : w.z, one ? 0x3f803f80u : w.w};
    }
    const float wsum = *(const float*)(smem + L_MISC + 64);
    BAR();
    __builtin_amdgcn_s_sleep(SLEEP_H);
    v4u hv[2];
    {
      const int o0 = WS_HT + (16 * wave + 2 * g) * 16384 + (16 * rb + cl) * 8;
#pragma unroll 1
      for (int sp = 0; sp < SPIN_CAP; sp++) {
        MEMBAR();
        const v2u a = ld2(rs, o0), b = ld2(rs, o0 + 16384), c = ld2(rs, o0 + 8 * 16384), d = ld2(rs, o0 + 9 * 16384);
        hv[0] = v4u{a.x, a.y, b.x, b.y};
        hv[1] = v4u{c.x, c.y, d.x, d.y};
        if (__all(valid4(hv[0]) && valid4(hv[1]))) break;
        __builtin_amdgcn_s_sleep(2);
      }
    }
    STAMP(8);
    {
      f32x4 s = {0.f, 0.f, 0.f, 0.f}, q = {0.f, 0.f, 0.f, 0.f};
      s = mfma_bf(cw[0], hv[0], s);
      q = mfma_bf(hv[0], hv[0], q);
      s = mfma_bf(cw[1], hv[1], s);
      q = mfma_bf(hv[1], hv[1], q);
      if ((cl >> 2) == g) *(f32x4*)(smem + L_RED + wave * 256 + cl * 16) = f32x4{s[0], diag(q, lane), s[1], 0.f};
    }
    BAR();
    float m, s;
    const f32x4 t = sum9(smem + L_RED + cl * 16);
    ln_round(t[0], t[1], m, s);
    const float rinv = __builtin_amdgcn_rcpf(s);
    unsigned okey;
    {
      const unsigned b = pkbf(0.f, fmaf(-m, wsum, t[2]) * rinv);
      okey = ((b & 0x80000000u) ? (~b & 0xffff0000u) : (b | 0x80000000u)) | (unsigned)(SEQ - 1 - (16 * rb + cl));
      if (wave == 1) st1(okey, rs, lane < 16 ? WS_RREC + (16 * rb + cl) * 4 : OOB);
    }
    STAMP(9);
    char* const tl = smem + wave * 4096;
    *(v4u*)(tl + cl * 144 + g * 16) = hv[0];
    *(v4u*)(tl + cl * 144 + 64 + g * 16) = hv[1];
    MEMBAR();
    v4u key = {0u, 0u, 0u, 0u};
    unsigned selmask = 0;
#pragma unroll 1
    for (int rd = 0; rd < 4; rd++) {
      const unsigned ka = row_max_u(okey);
      okey = okey == ka ? 0u : okey;
      const unsigned kb = row_max_u(okey);
      okey = okey == kb ? 0u : okey;
      const int ra = 15 - (ka & 15), rb2 = 15 - (kb & 15);
      v4u pub = *(const v4u*)(tl + ((lane & 16) ? rb2 : ra) * 144 + (lane & 7) * 16);
      {
        const float ma = rdlane(m, ra), mb = rdlane(m, rb2), ia = rdlane(rinv, ra), ib = rdlane(rinv, rb2);
        const float pm = (lane & 16) ? mb : ma, pi = (lane & 16) ? ib : ia;
#pragma unroll
        for (int i = 0; i < 4; i++) {
          const unsigned n = nosent(ln_apply(pub[i], pm, pi));
          pub[i] = (lane & 8) ? pub[i] : n;
        }
        if (lane >= 32) pub.x = (lane & 16) ? kb : ka;
      }
      PIN(pub);
      if (rd == 0 && wave < 8) {
        STAMP(1);
#pragma unroll 1
        for (int sp = 0; sp < SPIN_CAP; sp++) {
          MEMBAR();
          key = ld4(rs, WS_RREC + tid * 16);
          if (__all(valid4(key))) break;
          __builtin_amdgcn_s_sleep(2);
        }
        STAMP(10);
      }
      int ca = 0, cb = 0;
#pragma unroll
      for (int i = 0; i < 4; i++) {
        ca += __popcll(__ballot(key[i] > ka));
        cb += __popcll(__ballot(key[i] > kb));
      }
      char* cnt = smem + L_RED + 2304 + (rd & 1) * 64;
      if (lane == 0) *(unsigned*)(cnt + wave * 4) = (unsigned)(ca | (cb << 16));
      BAR();
      const v4u c0 = *(const v4u*)cnt, c1 = *(const v4u*)(cnt + 16);
      const unsigned cs = __builtin_amdgcn_readfirstlane((c0.x + c0.y + c0.z + c0.w) + (c1.x + c1.y + c1.z + c1.w));
      const unsigned rka = cs & 0xffffu, rkb = cs >> 16;
      if (rka >= 8u) break;
      const unsigned rank = (lane & 16) ? rkb : rka;
      const int xo = lane < 32 ? (cl >> 3) * 1152 + wave * 128 + (cl & 7) * 16 : 2304;
      st4(pub, rs, (rank < 8u && (lane < 32 || (cl == 0 && wave == 0))) ? WS_XSEL + rank * XSLOT + xo : OOB);
      selmask |= 1u << ra;
      if (rkb >= 8u) break;
      selmask |= 1u << rb2;
    }
    STAMP(3);
#pragma unroll
    for (int i = 0; i < 2; i++) {
      const int r = 8 * i + (lane >> 3);
      const v4u hh = *(const v4u*)(tl + r * 144 + (lane & 7) * 16);
      const float pm = __shfl(m, r), pi = __shfl(rinv, r);
      v4u o;
#pragma unroll
      for (int k = 0; k < 4; k++) o[k] = addbf(hh[k], ln_apply(hh[k], pm, pi));
      st4(o, ro, ((selmask >> r) & 1u) ? OOB : (16 * rb + r) * 1152 + wave * 128 + (lane & 7) * 16);
    }
    STAMP_FLUSH();
    return;
  }

  const bool ffn = loc < 27;
  v2u G[2][8];
  const rsrc_t rg = __builtin_amdgcn_make_buffer_rsrc((void*)p.wg, 0, ffn ? DM * FF * 2 : 0, 0x00020000);
  const int wo = (64 * wave + 8 * g) * (FF * 2) + 128 * fj + 8 * cl;
#pragma unroll
  for (int j = 0; j < W_EARLY; j++) G[j >> 3][j & 7] = ldp2(rg, wo, (32 * (j >> 3) + (j & 7)) * (FF * 2));
  SCHED();
  PIN(epoch); PIN(cpf);
  const unsigned tag = epoch + 1u;
  {
    const v4u ones = {0x3f803f80u, 0x3f803f80u, 0x3f803f80u, 0x3f803f80u};
    const f32x4 z4 = {0.f, 0.f, 0.f, 0.f};
    auto sums = [&](v4u a, int i) {
      const f32x4 s = mfma_bf(ones, a, z4), q = mfma_bf(a, a, z4);
      if ((cl >> 2) == g) *(f32x2*)(smem + L_RED + (i >> 1) * 2304 + wave * 256 + (i & 1) * 128 + cl * 8) = f32x2{s[0], diag(q, lane)};
    };
    sums(ga, 0);
    sums(gb, 1);
    if (loc >= 30) {
      sums(gc, 2);
      sums(gd, 3);
    }
    BAR();
    if (wave == 1 || (wave == 2 && loc >= 30)) {
      float m, s;
      const f32x4 t = sum9(smem + L_RED + (wave - 1) * 2304 + cl * 16);
      ln_round(t[0] + t[2], t[1] + t[3], m, s);
      st2(v2u{(__float_as_uint(m) >> 16) | __float_as_uint(s), tag}, rs, lane < 16 ? WS_STAT + (row0 + (wave - 1) * 16 + cl) * 8 : OOB);
    }
    BAR();
    STAMP(1);
    if (!ffn) {
      const char* dn = (const char*)p.wd + (size_t)xcd * 5184 * 128;
      unsigned pf[2];
#pragma unroll
      for (int i = 0; i < 2; i++) pf[i] = *(const unsigned*)(dn + min((i * 5 + loc - 27) * NT + tid, 5183) * 128);
#pragma unroll
      for (int i = 0; i < 2; i++) asm volatile("" ::"v"(pf[i]));
      STAMP_FLUSH();
      return;
    }
  }
  v4u FG[2][4], FU[2][4], FD[2][4];
  {
    v2u U[2][8], Dn[2][8];
    const rsrc_t ru = __builtin_amdgcn_make_buffer_rsrc((void*)p.wu, 0, DM * FF * 2, 0x00020000);
    const rsrc_t rd = __builtin_amdgcn_make_buffer_rsrc((void*)p.wd, 0, DM * FF * 2, 0x00020000);
    const int wdo = (64 * fj + 8 * g) * (DM * 2) + 128 * wave + 8 * cl;
#pragma unroll
    for (int j = W_EARLY; j < 16; j++) G[j >> 3][j & 7] = ldp2(rg, wo, (32 * (j >> 3) + (j & 7)) * (FF * 2));
#pragma unroll
    for (int j = 0; j < 16; j++) U[j >> 3][j & 7] = ldp2(ru, wo, (32 * (j >> 3) + (j & 7)) * (FF * 2));
    BAR();
#pragma unroll
    for (int j = 0; j < 16; j++) Dn[j >> 3][j & 7] = ldp2(rd, wdo, (32 * (j >> 3) + (j & 7)) * (DM * 2));
    st4(sent4, rs, WS_PART + fj * 18432 + tid * 16);
    st4(sent4, rs, WS_PART + fj * 18432 + 9216 + tid * 16);
    SCHED();
#pragma unroll
    for (int ks = 0; ks < 2; ks++)
#pragma unroll
      for (int c = 0; c < 4; c++) {
#pragma unroll
        for (int i = 0; i < 4; i++) {
          const unsigned sel = (c & 1) ? 0x07060302u : 0x05040100u;
          FG[ks][c][i] = __builtin_amdgcn_perm(G[ks][2 * i + 1][c >> 1], G[ks][2 * i][c >> 1], sel);
          FU[ks][c][i] = __builtin_amdgcn_perm(U[ks][2 * i + 1][c >> 1], U[ks][2 * i][c >> 1], sel);
          FD[ks][c][i] = __builtin_amdgcn_perm(Dn[ks][2 * i + 1][c >> 1], Dn[ks][2 * i][c >> 1], sel);
        }
        PIN(FG[ks][c]);
        PIN(FU[ks][c]);
        PIN(FD[ks][c]);
      }
  }
  STAMP(2);
  constexpr int F_GU = 1024, F_ALL = 2048, F_RED = 16384;
  if (wave == 0) sweep_init(rs, tag, lane, 0, NCONV);
  DRAIN();
  BAR();
  if (tid == 0) st1(tag, rs, WS_INIT + (NCONV + fj) * 64);
  STAMP(3);
  v4u xa[2];
  {
    v4u xv;
    const int o0 = WS_XSEL + (lane >> 3) * XSLOT + wave * 128 + (lane & 7) * 16;
#pragma unroll 1
    for (int sp = 0; sp < SPIN_CAP; sp++) {
      MEMBAR();
      xv = ld4(rs, o0);
      if (__all(valid4(xv))) break;
      __builtin_amdgcn_s_sleep(SLEEP_X);
    }
    STAMP(6);
    char* xs = smem + F_ALL + wave * 1152;
    *(v4u*)(xs + (lane >> 3) * 144 + (lane & 7) * 16) = xv;
    MEMBAR();
    xa[0] = *(const v4u*)(xs + (lane & 7) * 144 + g * 16);
    xa[1] = *(const v4u*)(xs + (lane & 7) * 144 + 64 + g * 16);
  }
#pragma unroll
  for (int c = 0; c < 4; c++) {
    f32x4 ag = {0.f, 0.f, 0.f, 0.f}, au = {0.f, 0.f, 0.f, 0.f};
#pragma unroll
    for (int ks = 0; ks < 2; ks++) {
      ag = mfma_bf(xa[ks], FG[ks][c], ag);
      au = mfma_bf(xa[ks], FU[ks][c], au);
    }
    *(f32x4*)(smem + F_RED + wave * 4096 + c * 1024 + lane * 16) = g < 2 ? ag : au;
  }
  BAR();
  STAMP(4);
  const int tk = fj / 9, cb = fj - tk * 9;
  if (wave < 8) {
    const int c = tid >> 7, wi = tid & 127;
    float gv[9], uv[9];
#pragma unroll
    for (int w = 0; w < 9; w++) {
      gv[w] = *(const float*)(smem + F_RED + w * 4096 + c * 1024 + wi * 4);
      uv[w] = *(const float*)(smem + F_RED + w * 4096 + c * 1024 + 512 + wi * 4);
    }
    SCHED();
    float gs = gv[0], us = uv[0];
#pragma unroll
    for (int w = 1; w < 9; w++) { gs += gv[w]; us += uv[w]; }
    const unsigned gu = pkbf(gs, us);
    const float gp = blo(gu);
    const float gq = rbf(gp * __builtin_amdgcn_rcpf(1.0f + __expf(-gp)));
    const int tok = (wi >> 6) * 4 + (wi & 3), col = ((wi >> 2) & 15);
    *(u16*)(smem + F_GU + tok * 128 + (col * 4 + c) * 2) = (u16)pkbf(gq * bhi(gu), 0.f);
  }
  BAR();
  STAMP(5);
  {
    const v4u a0 = *(const v4u*)(smem + F_GU + (lane & 7) * 128 + g * 16);
    const v4u a1 = *(const v4u*)(smem + F_GU + (lane & 7) * 128 + 64 + g * 16);
    f32x4 o[4];
#pragma unroll
    for (int c = 0; c < 4; c++) {
      o[c] = f32x4{0.f, 0.f, 0.f, 0.f};
      o[c] = mfma_bf(a0, FD[0][c], o[c]);
      o[c] = mfma_bf(a1, FD[1][c], o[c]);
    }
#pragma unroll
    for (int i = 0; i < 2; i++) {
      v4u v;
#pragma unroll
      for (int c = 0; c < 4; c++) v[c] = nosent(__float_as_uint(g < 2 ? o[c][i] : o[c][2 + i]));
      const int tok = 4 * (g & 1) + 2 * (g >> 1) + i;
      st4(v, rs, WS_PART + fj * 18432 + (tok * DM + 64 * wave + 4 * cl) * 4);
    }
  }
  STAMP(7);
  v2u fh = {0u, 0u}, fw = {0u, 0u};
  unsigned fkey = 0;
  if (wave == 8) {
    const int xo = WS_XSEL + tk * XSLOT + cb * 128 + cl * 8;
#pragma unroll 1
    for (int sp = 0; sp < SPIN_CAP; sp++) {
      MEMBAR();
      fw = ld2(rs, xo);
      fh = ld2(rs, xo + 1152);
      fkey = ld1(rs, WS_XSEL + tk * XSLOT + 2304);
      if (__all(valid4(v4u{fw.x, fw.y, fh.x, fh.y}) && fkey != SENT)) break;
    }
  } else if (wave == 7) {
    sweep_init(rs, tag, lane, NCONV, NFFN);
  } else {
    __builtin_amdgcn_s_sleep(SLEEP_P);
  }
  BAR();
  {
    const int o0 = WS_PART + (tid >> 4) * 18432 + (tk * DM + cb * 64 + cl * 4) * 4;
    v4u a, b;
#pragma unroll 1
    for (int sp = 0; sp < SPIN_CAP; sp++) {
      MEMBAR();
      a = ld4(rs, o0);
      b = ld4(rs, o0 + 36 * 18432);
      if (__all(valid4(a) && valid4(b))) break;
      __builtin_amdgcn_s_sleep(2);
    }
    *(f32x4*)(smem + F_ALL + tid * 16) = __builtin_bit_cast(f32x4, a) + __builtin_bit_cast(f32x4, b);
  }
  STAMP(8);
  BAR();
  if (wave == 8) {
    f32x4 s = *(const f32x4*)(smem + F_ALL + lane * 16);
#pragma unroll
    for (int i = 1; i < 9; i++) s += *(const f32x4*)(smem + F_ALL + i * 1024 + lane * 16);
#pragma unroll
    for (int i = 0; i < 4; i++) {
      s[i] += __shfl_xor(s[i], 16);
      s[i] += __shfl_xor(s[i], 32);
    }
    const int frow = SEQ - 1 - (int)(fkey & 0x7ffu);
    const unsigned kb = fkey & 0xffff0000u;
    const float rv = __uint_as_float((kb & 0x80000000u) ? (kb & 0x7fffffffu) : (~kb & 0xffff0000u));
    const float sgm = rbf(__builtin_amdgcn_rcpf(1.0f + __expf(-rv)));
    const unsigned ya = pkbf(s[0], s[1]), yb = pkbf(s[2], s[3]);
    const unsigned ta = pkbf(blo(ya) * sgm, bhi(ya) * sgm), tb = pkbf(blo(yb) * sgm, bhi(yb) * sgm);
    const v2u o = {addbf(fh.x, addbf(fw.x, ta)), addbf(fh.y, addbf(fw.y, tb))};
    st2(o, ro, lane < 16 ? frow * 1152 + cb * 128 + cl * 8 : OOB);
    if (fj == 0 && lane == 0) st1(tag, rs, WS_EPOCH);
  }
  STAMP(9);
  STAMP_FLUSH();
}

}

extern "C" void kernel_launch(void* const* d_in, const int* in_sizes, int n_in, void* d_out, int out_size,
                              void* d_ws, size_t ws_size, hipStream_t stream) {
  (void)hipFuncSetAttribute((const void*)k_main, hipFuncAttributeMaxDynamicSharedMemorySize, LDS_BYTES);
  Params p;
  p.x = (const u16*)d_in[0];
  p.lar = (const u16*)d_in[1];
  p.aim = (const u16*)d_in[2];
  p.crl = (const u16*)d_in[3];
  p.cim = (const u16*)d_in[4];
  p.ldt = (const u16*)d_in[5];
  p.dskip = (const u16*)d_in[6];
  p.wr = (const u16*)d_in[8];
  p.wg = (const u16*)d_in[9];
  p.wu = (const u16*)d_in[10];
  p.wd = (const u16*)d_in[11];
  p.out = (u16*)d_out;
  p.ws = (char*)d_ws;
  k_main<<<256, NT, LDS_BYTES, stream>>>(p);
}
```

```cpp
#include <hip/hip_runtime.h>
#include <stdint.h>
#pragma clang diagnostic ignored "-Wint-to-pointer-cast"

namespace {

constexpr int SEQ = 2048, DM = 576, NM = 128, FF = 4608;
constexpr int NT = 576;
constexpr int NCONV = 144, NFFN = 72;
constexpr unsigned SENT = 0xFFFFFFFFu;
constexpr int SC1 = 16;
constexpr int SPIN_CAP = 1 << 16;
#define SLEEP_H 8
#define SLEEP_X 4
#define W_EARLY 0
#define CODE_BYTES 20480
#define H_PRIO 3
#ifndef M_PRIO
#define M_PRIO 1
#endif
#define S_PRIO 3
#define PF_MODE 2
#define PF_N 5
#define PF_SLEEP 0
#define SLEEP_U 5
#define SLEEP_U7 4
#define SLEEP_P 8

typedef unsigned short u16;
typedef unsigned v4u __attribute__((ext_vector_type(4)));
typedef unsigned v2u __attribute__((ext_vector_type(2)));
typedef float f32x2 __attribute__((ext_vector_type(2)));
typedef float f32x4 __attribute__((ext_vector_type(4)));
typedef float f32x16 __attribute__((ext_vector_type(16)));
typedef _Float16 f16x2 __attribute__((ext_vector_type(2)));
typedef _Float16 f16x8 __attribute__((ext_vector_type(8)));
typedef __bf16 bf16x2 __attribute__((ext_vector_type(2)));
typedef __bf16 bf16x8 __attribute__((ext_vector_type(8)));
typedef __amdgpu_buffer_rsrc_t rsrc_t;

constexpr int WS_EPOCH = 0;
constexpr int WS_INIT = 4096;
constexpr int WS_RREC = WS_INIT + 256 * 64;
constexpr int WS_XSEL = WS_RREC + SEQ * 4;
constexpr int XSLOT = 2560;
constexpr int GSTR = 16384;
constexpr int WS_U = 65536;
constexpr int WS_Y = WS_U + NCONV * GSTR;
constexpr int WS_PART = WS_Y + NCONV * GSTR;
constexpr int WS_END = WS_PART + NFFN * 8 * DM * 4;
constexpr int OOB = 0x7ffffff0;
static_assert(WS_XSEL + 8 * XSLOT <= WS_U, "ws map");

constexpr int WSCR = 16384;
constexpr int L_KN = 4096;
constexpr int KCOPY = 1216;
constexpr int L_K4 = L_KN + 2 * KCOPY;
constexpr int L_K8 = L_K4 + 512;
constexpr int L_Y = 8192;
constexpr int L_DUMMY = 12800;
constexpr int L_XROW = 4 * WSCR;
constexpr int L_UIMG = 8 * WSCR;
constexpr int USTR = 4640;
constexpr int L_FILT = L_UIMG + 4 * USTR;
constexpr int L_MISC = L_FILT + 4096;
constexpr int L_WR = L_MISC + 256;
constexpr int L_RED = L_WR + 1152;
constexpr int LDS_BYTES = L_RED + 2 * 2304;
static_assert(L_KN + 224 * 16 <= L_Y, "lds map");
static_assert(LDS_BYTES <= 160 * 1024, "lds size");

#define MEMBAR() asm volatile("" ::: "memory")
#define BAR() do { __builtin_amdgcn_sched_barrier(0); asm volatile("s_waitcnt lgkmcnt(0)\n\ts_barrier" ::: "memory"); __builtin_amdgcn_sched_barrier(0); } while (0)
#define LAS __attribute__((address_space(3)))
#define DRAIN() asm volatile("s_waitcnt vmcnt(0)" ::: "memory")
#define LGKM0() asm volatile("s_waitcnt lgkmcnt(0)" ::: "memory")
#define PIN(x) asm volatile("" : "+v"(x))
#define SCHED() __builtin_amdgcn_sched_barrier(0)

#define STAMP(i) do {} while (0)
#define STAMP_INIT() do {} while (0)
#define STAMP_FLUSH() do {} while (0)

__device__ __forceinline__ float blo(unsigned v) { return __uint_as_float(v << 16); }
__device__ __forceinline__ float bhi(unsigned v) { return __uint_as_float(v & 0xffff0000u); }
__device__ __forceinline__ unsigned pkbf(float a, float b) {
  const f32x2 f = {a, b};
  return __builtin_bit_cast(unsigned, __builtin_convertvector(f, bf16x2));
}
__device__ __forceinline__ float rbf(float f) { return blo(pkbf(f, f)); }
__device__ __forceinline__ unsigned pkh(float a, float b) {
  const f32x2 f = {a, b};
  return __builtin_bit_cast(unsigned, __builtin_convertvector(f, f16x2));
}
__device__ __forceinline__ unsigned nosent(unsigned v) { return min(v, 0xFFFFFFFEu); }
__device__ __forceinline__ bool valid4(v4u v) { return max(max(v.x, v.y), max(v.z, v.w)) != SENT; }

template <int CTRL, int RM> __device__ __forceinline__ float dppf(float v) {
  return __int_as_float(__builtin_amdgcn_update_dpp(0, __float_as_int(v), CTRL, RM, 0xf, false));
}
template <int CTRL, int RM> __device__ __forceinline__ unsigned dppu(unsigned v) {
  return (unsigned)__builtin_amdgcn_update_dpp(0, (int)v, CTRL, RM, 0xf, false);
}
__device__ __forceinline__ float wave_sum(float v) {
  v += dppf<0x111, 0xf>(v);
  v += dppf<0x112, 0xf>(v);
  v += dppf<0x114, 0xf>(v);
  v += dppf<0x118, 0xf>(v);
  v += dppf<0x142, 0xa>(v);
  v += dppf<0x143, 0xc>(v);
  return __int_as_float(__builtin_amdgcn_readlane(__float_as_int(v), 63));
}
__device__ __forceinline__ unsigned wave_max_u(unsigned v) {
  v = max(v, dppu<0x111, 0xf>(v));
  v = max(v, dppu<0x112, 0xf>(v));
  v = max(v, dppu<0x114, 0xf>(v));
  v = max(v, dppu<0x118, 0xf>(v));
  const unsigned a = (unsigned)__builtin_amdgcn_readlane((int)v, 15), b = (unsigned)__builtin_amdgcn_readlane((int)v, 31);
  const unsigned c = (unsigned)__builtin_amdgcn_readlane((int)v, 47), d = (unsigned)__builtin_amdgcn_readlane((int)v, 63);
  return max(max(a, b), max(c, d));
}

__device__ __forceinline__ unsigned row_max_u(unsigned v) {
  v = max(v, dppu<0x111, 0xf>(v));
  v = max(v, dppu<0x112, 0xf>(v));
  v = max(v, dppu<0x114, 0xf>(v));
  v = max(v, dppu<0x118, 0xf>(v));
  return (unsigned)__builtin_amdgcn_readlane((int)v, 15);
}
__device__ __forceinline__ float rdlane(float v, int l) { return __int_as_float(__builtin_amdgcn_readlane(__float_as_int(v), l)); }

__device__ __forceinline__ v4u ld4(rsrc_t rs, int off) { return __builtin_amdgcn_raw_buffer_load_b128(rs, off, 0, SC1); }
__device__ __forceinline__ v2u ld2(rsrc_t rs, int off) { return __builtin_amdgcn_raw_buffer_load_b64(rs, off, 0, SC1); }
__device__ __forceinline__ unsigned ld1(rsrc_t rs, int off) { return __builtin_amdgcn_raw_buffer_load_b32(rs, off, 0, SC1); }
__device__ __forceinline__ v2u ldp2(rsrc_t rs, int off, int soff) { return __builtin_amdgcn_raw_buffer_load_b64(rs, off, soff, 0); }
__device__ __forceinline__ void st4(v4u v, rsrc_t rs, int off) { __builtin_amdgcn_raw_buffer_store_b128(v, rs, off, 0, SC1); }
__device__ __forceinline__ void st2(v2u v, rsrc_t rs, int off) { __builtin_amdgcn_raw_buffer_store_b64(v, rs, off, 0, SC1); }
__device__ __forceinline__ void st1(unsigned v, rsrc_t rs, int off) { __builtin_amdgcn_raw_buffer_store_b32(v, rs, off, 0, SC1); }

__device__ __forceinline__ f32x4 mfma_bf(v4u a, v4u b, f32x4 c) {
  return __builtin_amdgcn_mfma_f32_16x16x32_bf16(__builtin_bit_cast(bf16x8, a), __builtin_bit_cast(bf16x8, b), c, 0, 0, 0);
}
__device__ __forceinline__ f32x4 mfma_h(v4u a, v4u b, f32x4 c) {
  return __builtin_amdgcn_mfma_f32_16x16x32_f16(__builtin_bit_cast(f16x8, a), __builtin_bit_cast(f16x8, b), c, 0, 0, 0);
}

__device__ __forceinline__ float diag(f32x4 q, int lane) {
  const float qa = (lane & 1) ? q[1] : q[0], qb = (lane & 1) ? q[3] : q[2];
  return (lane & 2) ? qb : qa;
}
__device__ __forceinline__ f32x4 sum9(const char* red) {
  f32x4 t[9];
#pragma unroll
  for (int w = 0; w < 9; w++) t[w] = *(const f32x4*)(red + w * 256);
  SCHED();
  return (((t[0] + t[1]) + (t[2] + t[3])) + ((t[4] + t[5]) + (t[6] + t[7]))) + t[8];
}
__device__ __forceinline__ void ln_round(float sum, float sq, float& m, float& s) {
  const float mean = sum * (1.0f / DM);
  const float var = fmaxf(fmaf(-mean, mean, sq * (1.0f / DM)), 0.f);
  const unsigned mv = pkbf(mean, var);
  m = blo(mv);
  s = rbf(__builtin_amdgcn_sqrtf(rbf(bhi(mv) + 1e-5f)));
}
__device__ __forceinline__ unsigned ln_apply(unsigned xp, float m, float rinv) {
  const unsigned d = pkbf(blo(xp) - m, bhi(xp) - m);
  return pkbf(blo(d) * rinv, bhi(d) * rinv);
}
__device__ __forceinline__ unsigned addbf(unsigned a, unsigned b) { return pkbf(blo(a) + blo(b), bhi(a) + bhi(b)); }

__device__ __forceinline__ void sweep_init(rsrc_t rs, unsigned tag, int lane, int first, int n) {
#pragma unroll 1
  for (int sp = 0; sp < SPIN_CAP; sp++) {
    MEMBAR();
    bool ok = true;
#pragma unroll
    for (int k = 0; k < 3; k++) ok &= ld1(rs, WS_INIT + (first + min(lane + 64 * k, n - 1)) * 64) == tag;
    if (__all(ok)) break;
    __builtin_amdgcn_s_sleep(4);
  }
}

typedef short s16x4 __attribute__((ext_vector_type(4)));
__device__ __forceinline__ v2u ldtr(const LAS char* a) {
  return __builtin_bit_cast(v2u, __builtin_amdgcn_ds_read_tr16_b64_v4i16((LAS s16x4*)a));
}
__device__ __forceinline__ v4u tag_bits(unsigned tag) {
  v4u t;
#pragma unroll
  for (int k = 0; k < 4; k++) t[k] = ((tag >> (6 * k)) & 7u) | (((tag >> (6 * k + 3)) & 7u) << 16);
  return t;
}

template <int N>
__device__ __forceinline__ void fetch_u(rsrc_t rs, int src, char* dst, const int (&chunk)[N], unsigned tag) {
  v4u v[N];
#pragma unroll
  for (int i = 0; i < N; i++) v[i] = __builtin_amdgcn_raw_buffer_load_b128(rs, src, chunk[i] * 1024, SC1);
  SCHED();
  const v4u tb = tag_bits(tag);
#pragma unroll 1
  for (int sp = 0; sp < SPIN_CAP; sp++) {
    v4u o = {0u, 0u, 0u, 0u};
#pragma unroll
    for (int i = 0; i < N; i++) o |= v[i] ^ tb;
    if (__all(((o.x | o.y | o.z | o.w) & 0x70007u) == 0u)) break;
    __builtin_amdgcn_s_sleep(2);
    MEMBAR();
#pragma unroll
    for (int i = 0; i < N; i++) v[i] = __builtin_amdgcn_raw_buffer_load_b128(rs, src, chunk[i] * 1024, SC1);
  }
#pragma unroll
  for (int i = 0; i < N; i++) {
    const v4u w = v[i] ^ (tb ^ 0x40004u);
    v2u* const up = (v2u*)(dst + chunk[i] * 256);
    up[0] = v2u{w.x, w.y};
    up[1] = v2u{w.z, w.w};
  }
}

struct cz { f32x2 v, s; };
__device__ __forceinline__ cz mkz(f32x2 v) {
  cz z;
  z.v = v;
  asm("v_pk_mov_b32 %0, %1, %1 op_sel:[1,0]" : "=v"(z.s) : "v"(v));
  return z;
}
#define CMUL_TXT(r, a, b, bs) "v_pk_mul_f32 " r ", " a ", " bs " op_sel:[1,0] neg_lo:[0,1]\n\tv_pk_fma_f32 " r ", " a ", " b ", " r " op_sel_hi:[0,1,1]\n\t"
__device__ __forceinline__ f32x2 cmul(f32x2 a, cz b) {
  f32x2 r;
  asm(CMUL_TXT("%0", "%1", "%2", "%3") : "=&v"(r) : "v"(a), "v"(b.v), "v"(b.s));
  return r;
}
__device__ __forceinline__ cz csq(cz a) { return mkz(cmul(a.v, a)); }
#define MC_(r, a, bs) "v_pk_mul_f32 " r ", " a ", " bs " op_sel:[1,0]\n\t"
#define FC_(r, a, b) "v_pk_fma_f32 " r ", " a ", " b ", " r " op_sel_hi:[0,1,1] neg_hi:[0,1,0]\n\t"
__device__ __forceinline__ void cmulc3(f32x2 (&r)[3], unsigned (&h)[3], f32x2 a0, cz b0, f32x2 a1, cz b1, f32x2 a2, cz b2) {
  asm(MC_("%0", "%3", "%5") MC_("%1", "%6", "%8") MC_("%2", "%9", "%11") FC_("%0", "%3", "%4") FC_("%1", "%6", "%7") FC_("%2", "%9", "%10")
      : "=&v"(r[0]), "=&v"(r[1]), "=&v"(r[2])
      : "v"(a0), "v"(b0.v), "v"(b0.s), "v"(a1), "v"(b1.v), "v"(b1.s), "v"(a2), "v"(b2.v), "v"(b2.s));
#pragma unroll
  for (int i = 0; i < 3; i++) h[i] = pkh(r[i].x, r[i].y);
}
__device__ __forceinline__ void hmul2(unsigned& r, unsigned& q, unsigned a, unsigned c, unsigned b, unsigned bs) {
  asm("v_pk_mul_f16 %0, %2, %5 op_sel:[1,0] neg_lo:[0,1]\n\tv_pk_mul_f16 %1, %3, %5 op_sel:[1,0] neg_lo:[0,1]\n\t"
      "v_pk_fma_f16 %0, %2, %4, %0 op_sel_hi:[0,1,1]\n\tv_pk_fma_f16 %1, %3, %4, %1 op_sel_hi:[0,1,1]"
      : "=&v"(r), "=&v"(q) : "v"(a), "v"(c), "v"(b), "v"(bs));
}
__device__ __forceinline__ float exp_c(float v) {
  const float t = v * 1.44269502f;
  const float e = fmaf(v, 1.92596303e-8f, fmaf(v, 1.44269502f, -t));
  return __builtin_amdgcn_exp2f(t) * fmaf(e, 0.693147182f, 1.f);
}
__device__ __forceinline__ f32x2 cexp_p2(float p, float ch, float ph, float pl) {
  const float mag = __builtin_amdgcn_exp2f(p * ch);
  const float fr = fmaf(p, pl, __builtin_amdgcn_fractf(p * ph));
  return f32x2{mag * __builtin_amdgcn_cosf(fr), mag * __builtin_amdgcn_sinf(fr)};
}
__device__ __forceinline__ unsigned pkh2(f32x2 v) { return pkh(v.x, v.y); }

#define CONV_ASM_TEXT \
  "s_barrier\n" "ds_read_b128 v[32:35], %8 offset:480\n" \
  "ds_read_b128 v[36:39], %8 offset:992\n" "ds_read_b128 v[40:43], %8 offset:1504\n" \
  "ds_read2_b32 v[44:45], %11 offset0:48 offset1:49\n" "ds_read2_b32 v[46:47], %11 offset0:50 offset1:51\n" \
  "ds_read2_b32 v[48:49], %11 offset0:32 offset1:33\n" "s_waitcnt lgkmcnt(5)\n" \
  "v_mfma_f32_16x16x32_f16 v[132:135], v[32:35], v[16:19], 0\n" "s_nop 0\n" \
  "ds_read2_b32 v[50:51], %11 offset0:34 offset1:35\n" "ds_read2_b32 v[52:53], %11 offset0:16 offset1:17\n" \
  "v_mfma_f32_16x16x32_f16 v[128:131], v[32:35], v[20:23], 0\n" "s_waitcnt lgkmcnt(6)\n" \
  "ds_read2_b32 v[54:55], %11 offset0:18 offset1:19\n" "ds_read2_b32 v[56:57], %11 offset0:0 offset1:1\n" \
  "v_mfma_f32_16x16x32_f16 v[124:127], v[36:39], v[16:19], 0\n" "s_nop 0\n" \
  "ds_read2_b32 v[58:59], %11 offset0:2 offset1:3\n" "ds_read_b128 v[60:63], %8 offset:2016\n" \
  "v_mfma_f32_16x16x32_f16 v[120:123], v[36:39], v[20:23], 0\n" "s_waitcnt lgkmcnt(9)\n" \
  "v_cvt_pk_f16_f32 v116, v132, v128\n" "v_cvt_pk_f16_f32 v117, v133, v129\n" \
  "v_mfma_f32_16x16x32_f16 v[164:167], v[40:43], v[16:19], 0\n" "v_cvt_pk_f16_f32 v118, v134, v130\n" \
  "v_cvt_pk_f16_f32 v119, v135, v131\n" "v_mfma_f32_16x16x32_f16 v[160:163], v[40:43], v[20:23], 0\n" \
  "s_waitcnt lgkmcnt(7)\n" "ds_read_b128 v[64:67], %9 offset:480\n" \
  "v_cvt_pk_f16_f32 v112, v124, v120\n" "v_mfma_f32_16x16x32_f16 v[148:151], v[116:119], v[44:47], 0\n" \
  "s_waitcnt lgkmcnt(6)\n" "ds_read_b128 v[68:71], %9 offset:736\n" \
  "v_cvt_pk_f16_f32 v113, v125, v121\n" "v_mfma_f32_16x16x32_f16 v[144:147], v[116:119], v[48:51], 0\n" \
  "s_waitcnt lgkmcnt(5)\n" "v_cvt_pk_f16_f32 v114, v126, v122\n" \
  "v_cvt_pk_f16_f32 v115, v127, v123\n" "v_mfma_f32_16x16x32_f16 v[140:143], v[116:119], v[52:55], 0\n" \
  "s_waitcnt lgkmcnt(3)\n" "ds_read_b128 v[72:75], %9 offset:992\n" \
  "ds_read_b64 v[76:77], %10 offset:256\n" "v_mfma_f32_16x16x32_f16 v[136:139], v[116:119], v[56:59], 0\n" \
  "s_waitcnt lgkmcnt(4)\n" "ds_read_b64 v[78:79], %10 offset:264\n" \
  "v_cvt_pk_f16_f32 v116, v164, v160\n" "v_mfma_f32_16x16x32_f16 v[132:135], v[60:63], v[16:19], 0\n" \
  "v_cvt_pk_f16_f32 v117, v165, v161\n" "v_cvt_pk_f16_f32 v118, v166, v162\n" \
  "v_mfma_f32_16x16x32_f16 v[128:131], v[60:63], v[20:23], 0\n" "ds_read_b64 v[80:81], %10 offset:128\n" \
  "v_cvt_pk_f16_f32 v119, v167, v163\n" "v_mfma_f32_16x16x32_f16 v[144:147], v[112:115], v[44:47], v[144:147]\n" \
  "ds_read_b64 v[82:83], %10 offset:136\n" "v_mfma_f32_16x16x32_f16 v[140:143], v[112:115], v[48:51], v[140:143]\n" \
  "s_nop 0\n" "ds_read_b64 v[84:85], %10 offset:0\n" \
  "ds_read_b64 v[86:87], %10 offset:8\n" "v_mfma_f32_16x16x32_f16 v[136:139], v[112:115], v[52:55], v[136:139]\n" \
  "s_waitcnt lgkmcnt(7)\n" "ds_read_b128 v[88:91], %9 offset:1248\n" \
  "v_cvt_pk_f16_f32 v112, v132, v128\n" "v_mfma_f32_16x16x32_f16 v[124:127], v[64:67], v[24:27], 0\n" \
  "v_cvt_pk_f16_f32 v113, v133, v129\n" "v_cvt_pk_f16_f32 v114, v134, v130\n" \
  "v_mfma_f32_16x16x32_f16 v[120:123], v[64:67], v[28:31], 0\n" "ds_read_b64 v[92:93], %10 offset:320\n" \
  "v_cvt_pk_f16_f32 v115, v135, v131\n" "v_mfma_f32_16x16x32_f16 v[140:143], v[116:119], v[44:47], v[140:143]\n" \
  "ds_read_b64 v[94:95], %10 offset:328\n" "v_mfma_f32_16x16x32_f16 v[136:139], v[116:119], v[48:51], v[136:139]\n" \
  "s_nop 0\n" "ds_read_b64 v[96:97], %10 offset:192\n" \
  "ds_read_b64 v[98:99], %10 offset:200\n" "v_mfma_f32_16x16x32_f16 v[164:167], v[68:71], v[24:27], 0\n" \
  "s_nop 0\n" "ds_read_b64 v[100:101], %10 offset:64\n" \
  "ds_read_b64 v[102:103], %10 offset:72\n" "v_mfma_f32_16x16x32_f16 v[160:163], v[68:71], v[28:31], 0\n" \
  "v_cvt_pk_f16_f32 v116, v124, v120\n" "v_cvt_pk_f16_f32 v117, v125, v121\n" \
  "v_mfma_f32_16x16x32_f16 v[136:139], v[112:115], v[44:47], v[136:139]\n" "s_waitcnt lgkmcnt(7)\n" \
  "v_cvt_pk_f16_f32 v118, v126, v122\n" "v_cvt_pk_f16_f32 v119, v127, v123\n" \
  "v_mfma_f32_16x16x32_f16 v[132:135], v[72:75], v[24:27], 0\n" "ds_read_b128 v[104:107], %9 offset:1504\n" \
  "v_cvt_pk_f16_f32 v112, v164, v160\n" "v_mfma_f32_16x16x32_f16 v[128:131], v[72:75], v[28:31], 0\n" \
  "v_cvt_pk_f16_f32 v113, v165, v161\n" "v_cvt_pk_f16_f32 v114, v166, v162\n" \
  "v_mfma_f32_16x16x32_f16 v[156:159], v[116:119], v[76:79], 0\n" "ds_read_b128 v[108:111], %9 offset:1760\n" \
  "v_cvt_pk_f16_f32 v115, v167, v163\n" "v_mfma_f32_16x16x32_f16 v[152:155], v[116:119], v[80:83], 0\n" \
  "s_nop 0\n" "ds_read2_b32 v[32:33], %6 offset0:144 offset1:145\n" \
  "ds_read2_b32 v[34:35], %6 offset0:146 offset1:147\n" "v_mfma_f32_16x16x32_f16 v[148:151], v[116:119], v[84:87], v[148:151]\n" \
  "s_waitcnt lgkmcnt(6)\n" "v_cvt_pk_f16_f32 v116, v132, v128\n" \
  "v_cvt_pk_f16_f32 v117, v133, v129\n" "v_mfma_f32_16x16x32_f16 v[124:127], v[88:91], v[24:27], 0\n" \
  "v_cvt_pk_f16_f32 v118, v134, v130\n" "v_cvt_pk_f16_f32 v119, v135, v131\n" \
  "v_mfma_f32_16x16x32_f16 v[120:123], v[88:91], v[28:31], 0\n" "s_nop 0\n" \
  "ds_read_b128 v[36:39], %9 offset:2016\n" "ds_read2_b32 v[40:41], %6 offset0:16 offset1:17\n" \
  "v_mfma_f32_16x16x32_f16 v[156:159], v[112:115], v[92:95], v[156:159]\n" "ds_read2_b32 v[42:43], %6 offset0:18 offset1:19\n" \
  "v_mfma_f32_16x16x32_f16 v[152:155], v[112:115], v[96:99], v[152:155]\n" "s_waitcnt lgkmcnt(6)\n" \
  "ds_read2_b32 v[56:57], %6 offset0:48 offset1:49\n" "ds_read2_b32 v[58:59], %6 offset0:50 offset1:51\n" \
  "v_mfma_f32_16x16x32_f16 v[148:151], v[112:115], v[100:103], v[148:151]\n" "v_cvt_pk_f16_f32 v112, v124, v120\n" \
  "v_cvt_pk_f16_f32 v113, v125, v121\n" "v_mfma_f32_16x16x32_f16 v[164:167], v[104:107], v[24:27], 0\n" \
  "v_cvt_pk_f16_f32 v114, v126, v122\n" "v_cvt_pk_f16_f32 v115, v127, v123\n" \
  "v_mfma_f32_16x16x32_f16 v[160:163], v[104:107], v[28:31], 0\n" "s_nop 0\n" \
  "ds_read_b128 v[16:19], %9 offset:2272\n" "ds_read2_b32 v[60:61], %6 offset0:176 offset1:177\n" \
  "v_mfma_f32_16x16x32_f16 v[152:155], v[116:119], v[76:79], v[152:155]\n" "ds_read2_b32 v[62:63], %6 offset0:178 offset1:179\n" \
  "v_mfma_f32_16x16x32_f16 v[148:151], v[116:119], v[80:83], v[148:151]\n" "s_nop 0\n" \
  "ds_read2_b32 v[20:21], %6 offset0:32 offset1:33\n" "ds_read2_b32 v[22:23], %6 offset0:34 offset1:35\n" \
  "v_mfma_f32_16x16x32_f16 v[144:147], v[116:119], v[84:87], v[144:147]\n" "s_waitcnt lgkmcnt(8)\n" \
  "v_cvt_pk_f16_f32 v116, v164, v160\n" "v_cvt_pk_f16_f32 v117, v165, v161\n" \
  "v_mfma_f32_16x16x32_f16 v[132:135], v[108:111], v[24:27], 0\n" "v_cvt_pk_f16_f32 v118, v166, v162\n" \
  "v_cvt_pk_f16_f32 v119, v167, v163\n" "v_mfma_f32_16x16x32_f16 v[128:131], v[108:111], v[28:31], 0\n" \
  "s_nop 0\n" "ds_read_b128 v[52:55], %9 offset:2528\n" \
  "ds_read2_b32 v[64:65], %6 offset0:160 offset1:161\n" "v_mfma_f32_16x16x32_f16 v[152:155], v[112:115], v[92:95], v[152:155]\n" \
  "ds_read2_b32 v[66:67], %6 offset0:162 offset1:163\n" "v_mfma_f32_16x16x32_f16 v[148:151], v[112:115], v[96:99], v[148:151]\n" \
  "s_nop 0\n" "ds_read2_b32 v[48:49], %6 offset0:80 offset1:81\n" \
  "ds_read2_b32 v[50:51], %6 offset0:82 offset1:83\n" "v_mfma_f32_16x16x32_f16 v[144:147], v[112:115], v[100:103], v[144:147]\n" \
  "v_cvt_pk_f16_f32 v112, v132, v128\n" "v_cvt_pk_f16_f32 v113, v133, v129\n" \
  "v_mfma_f32_16x16x32_f16 v[124:127], v[36:39], v[24:27], 0\n" "v_cvt_pk_f16_f32 v114, v134, v130\n" \
  "v_cvt_pk_f16_f32 v115, v135, v131\n" "v_mfma_f32_16x16x32_f16 v[120:123], v[36:39], v[28:31], 0\n" \
  "s_nop 0\n" "ds_read_b128 v[68:71], %9 offset:2784\n" \
  "ds_read2_b32 v[44:45], %6 offset0:208 offset1:209\n" "v_mfma_f32_16x16x32_f16 v[148:151], v[116:119], v[76:79], v[148:151]\n" \
  "ds_read2_b32 v[46:47], %6 offset0:210 offset1:211\n" "v_mfma_f32_16x16x32_f16 v[144:147], v[116:119], v[80:83], v[144:147]\n" \
  "s_nop 0\n" "ds_read2_b32 v[72:73], %6 offset0:64 offset1:65\n" \
  "ds_read2_b32 v[74:75], %6 offset0:66 offset1:67\n" "v_mfma_f32_16x16x32_f16 v[140:143], v[116:119], v[84:87], v[140:143]\n" \
  "s_waitcnt lgkmcnt(8)\n" "v_cvt_pk_f16_f32 v116, v124, v120\n" \
  "v_cvt_pk_f16_f32 v117, v125, v121\n" "v_mfma_f32_16x16x32_f16 v[132:135], v[16:19], v[24:27], 0\n" \
  "v_cvt_pk_f16_f32 v118, v126, v122\n" "v_cvt_pk_f16_f32 v119, v127, v123\n" \
  "v_mfma_f32_16x16x32_f16 v[128:131], v[16:19], v[28:31], 0\n" "s_nop 0\n" \
  "ds_read_b128 v[88:91], %9 offset:3040\n" "ds_read2_b32 v[104:105], %6 offset0:192 offset1:193\n" \
  "v_mfma_f32_16x16x32_f16 v[148:151], v[112:115], v[92:95], v[148:151]\n" "ds_read2_b32 v[106:107], %6 offset0:194 offset1:195\n" \
  "v_mfma_f32_16x16x32_f16 v[144:147], v[112:115], v[96:99], v[144:147]\n" "s_nop 0\n" \
  "ds_read_b128 v[108:111], %8 offset:0\n" "ds_read2_b32 v[36:37], %6 offset0:224 offset1:225\n" \
  "v_mfma_f32_16x16x32_f16 v[140:143], v[112:115], v[100:103], v[140:143]\n" "v_cvt_pk_f16_f32 v112, v132, v128\n" \
  "v_cvt_pk_f16_f32 v113, v133, v129\n" "v_mfma_f32_16x16x32_f16 v[124:127], v[52:55], v[24:27], 0\n" \
  "v_cvt_pk_f16_f32 v114, v134, v130\n" "v_cvt_pk_f16_f32 v115, v135, v131\n" \
  "v_mfma_f32_16x16x32_f16 v[120:123], v[52:55], v[28:31], 0\n" "s_nop 0\n" \
  "ds_read2_b32 v[38:39], %6 offset0:226 offset1:227\n" "ds_read_b128 v[16:19], %9 offset:3296\n" \
  "v_mfma_f32_16x16x32_f16 v[144:147], v[116:119], v[76:79], v[144:147]\n" "ds_read2_b32 v[52:53], %6 offset0:96 offset1:97\n" \
  "v_mfma_f32_16x16x32_f16 v[140:143], v[116:119], v[80:83], v[140:143]\n" "ds_read2_b32 v[54:55], %6 offset0:98 offset1:99\n" \
  "v_mfma_f32_16x16x32_f16 v[136:139], v[116:119], v[84:87], v[136:139]\n" "s_waitcnt lgkmcnt(7)\n" \
  "v_cvt_pk_f16_f32 v116, v124, v120\n" "v_cvt_pk_f16_f32 v117, v125, v121\n" \
  "v_mfma_f32_16x16x32_f16 v[132:135], v[68:71], v[24:27], 0\n" "v_cvt_pk_f16_f32 v118, v126, v122\n" \
  "v_cvt_pk_f16_f32 v119, v127, v123\n" "v_mfma_f32_16x16x32_f16 v[128:131], v[68:71], v[28:31], 0\n" \
  "s_nop 0\n" "ds_read_b128 v[84:87], %8 offset:128\n" \
  "ds_read2_b32 v[68:69], %6 offset0:240 offset1:241\n" "v_mfma_f32_16x16x32_f16 v[144:147], v[112:115], v[92:95], v[144:147]\n" \
  "ds_read2_b32 v[70:71], %6 offset0:242 offset1:243\n" "v_mfma_f32_16x16x32_f16 v[140:143], v[112:115], v[96:99], v[140:143]\n" \
  "v_mfma_f32_16x16x32_f16 v[136:139], v[112:115], v[100:103], v[136:139]\n" "ds_read_b128 v[100:103], %8 offset:64\n" \
  "v_cvt_pk_f16_f32 v112, v132, v128\n" "v_mfma_f32_16x16x32_f16 v[124:127], v[88:91], v[24:27], 0\n" \
  "v_cvt_pk_f16_f32 v113, v133, v129\n" "v_cvt_pk_f16_f32 v114, v134, v130\n" \
  "v_mfma_f32_16x16x32_f16 v[120:123], v[88:91], v[28:31], 0\n" "s_waitcnt lgkmcnt(4)\n" \
  "ds_read_b128 v[88:91], %8 offset:256\n" "v_cvt_pk_f16_f32 v115, v135, v131\n" \
  "v_mfma_f32_16x16x32_f16 v[164:167], v[32:35], v[108:111], 0\n" "v_mfma_f32_16x16x32_f16 v[140:143], v[116:119], v[76:79], v[140:143]\n" \
  "v_mfma_f32_16x16x32_f16 v[136:139], v[116:119], v[80:83], v[136:139]\n" "s_nop 0\n" \
  "ds_read_b128 v[80:83], %8 offset:192\n" "v_cvt_pk_f16_f32 v116, v124, v120\n" \
  "v_mfma_f32_16x16x32_f16 v[132:135], v[16:19], v[24:27], 0\n" "v_cvt_pk_f16_f32 v117, v125, v121\n" \
  "v_cvt_pk_f16_f32 v118, v126, v122\n" "v_mfma_f32_16x16x32_f16 v[128:131], v[16:19], v[28:31], 0\n" \
  "ds_read_b128 v[24:27], %8 offset:320\n" "v_cvt_pk_f16_f32 v119, v127, v123\n" \
  "v_mfma_f32_16x16x32_f16 v[160:163], v[40:43], v[108:111], 0\n" "s_nop 0\n" \
  "ds_read2_b32 v[16:17], %6 offset0:112 offset1:113\n" "ds_read2_b32 v[18:19], %6 offset0:114 offset1:115\n" \
  "v_mfma_f32_16x16x32_f16 v[140:143], v[112:115], v[92:95], v[140:143]\n" "ds_read_b128 v[28:31], %8 offset:384\n" \
  "v_mfma_f32_16x16x32_f16 v[136:139], v[112:115], v[96:99], v[136:139]\n" "s_waitcnt lgkmcnt(5)\n" \
  "ds_read2_b32 v[124:125], %7 offset0:0 offset1:1\n" "v_cvt_pk_f16_f32 v112, v132, v128\n" \
  "v_mfma_f32_16x16x32_f16 v[160:163], v[56:59], v[84:87], v[160:163]\n" "v_cvt_pk_f16_f32 v113, v133, v129\n" \
  "v_cvt_pk_f16_f32 v114, v134, v130\n" "v_mfma_f32_16x16x32_f16 v[164:167], v[60:63], v[84:87], v[164:167]\n" \
  "ds_read2_b32 v[126:127], %7 offset0:2 offset1:3\n" "v_cvt_pk_f16_f32 v115, v135, v131\n" \
  "v_mfma_f32_16x16x32_f16 v[160:163], v[20:23], v[100:103], v[160:163]\n" "s_nop 0\n" \
  "ds_read_b128 v[108:111], %8 offset:448\n" "ds_read2_b32 v[120:121], %6 offset0:128 offset1:129\n" \
  "v_mfma_f32_16x16x32_f16 v[164:167], v[64:67], v[100:103], v[164:167]\n" "ds_read2_b32 v[122:123], %6 offset0:130 offset1:131\n" \
  "v_mfma_f32_16x16x32_f16 v[136:139], v[116:119], v[76:79], v[136:139]\n" "s_nop 0\n" \
  "ds_read2_b32 v[96:97], %7 offset0:16 offset1:17\n" "ds_read2_b32 v[98:99], %7 offset0:18 offset1:19\n" \
  "v_mfma_f32_16x16x32_f16 v[160:163], v[48:51], v[88:91], v[160:163]\n" "ds_read_b128 v[84:87], %8 offset:512\n" \
  "v_mfma_f32_16x16x32_f16 v[164:167], v[44:47], v[88:91], v[164:167]\n" "s_waitcnt lgkmcnt(8)\n" \
  "v_mfma_f32_16x16x32_f16 v[160:163], v[72:75], v[80:83], v[160:163]\n" "s_nop 0\n" \
  "ds_read2_b32 v[132:133], %6 offset0:0 offset1:1\n" "ds_read2_b32 v[134:135], %6 offset0:2 offset1:3\n" \
  "v_mfma_f32_16x16x32_f16 v[164:167], v[104:107], v[80:83], v[164:167]\n" "ds_read_b128 v[128:131], %8 offset:576\n" \
  "v_mfma_f32_16x16x32_f16 v[136:139], v[112:115], v[92:95], v[136:139]\n" "ds_read_b128 v[100:103], %8 offset:640\n" \
  "v_mfma_f32_16x16x32_f16 v[164:167], v[36:39], v[24:27], v[164:167]\n" "ds_read_b128 v[116:119], %8 offset:704\n" \
  "v_mfma_f32_16x16x32_f16 v[160:163], v[52:55], v[24:27], v[160:163]\n" "v_mfma_f32_16x16x32_f16 v[164:167], v[68:71], v[28:31], v[164:167]\n" \
  "s_nop 0\n" "ds_read_b128 v[76:79], %8 offset:768\n" \
  "ds_read_b128 v[88:91], %8 offset:832\n" "v_mfma_f32_16x16x32_f16 v[160:163], v[16:19], v[28:31], v[160:163]\n" \
  "s_waitcnt lgkmcnt(7)\n" "ds_read_b128 v[80:83], %8 offset:896\n" \
  "v_mfma_f32_16x16x32_f16 v[164:167], v[124:127], v[108:111], v[164:167]\n" "ds_read_b128 v[112:115], %8 offset:960\n" \
  "v_mfma_f32_16x16x32_f16 v[160:163], v[120:123], v[108:111], v[160:163]\n" "ds_read_b128 v[92:95], %8 offset:1024\n" \
  "v_mfma_f32_16x16x32_f16 v[164:167], v[96:99], v[84:87], v[164:167]\n" "v_mfma_f32_16x16x32_f16 v[160:163], v[32:35], v[84:87], v[160:163]\n" \
  "s_nop 0\n" "ds_read_b128 v[24:27], %8 offset:1088\n" \
  "ds_read_b128 v[28:31], %8 offset:1152\n" "v_mfma_f32_16x16x32_f16 v[156:159], v[40:43], v[84:87], v[156:159]\n" \
  "s_waitcnt lgkmcnt(7)\n" "ds_read_b128 v[108:111], %8 offset:1216\n" \
  "v_mfma_f32_16x16x32_f16 v[148:151], v[132:135], v[84:87], v[148:151]\n" "v_mul_f32 v164, %5, v164\n" \
  "v_mul_f32 v165, %5, v165\n" "v_mfma_f32_16x16x32_f16 v[160:163], v[64:67], v[128:131], v[160:163]\n" \
  "v_mul_f32 v166, %5, v166\n" "v_mul_f32 v167, %5, v167\n" \
  "v_mfma_f32_16x16x32_f16 v[156:159], v[20:23], v[128:131], v[156:159]\n" "v_cvt_pk_bf16_f32 v164, v164, v165\n" \
  "v_cvt_pk_bf16_f32 v165, v166, v167\n" "v_mfma_f32_16x16x32_f16 v[160:163], v[60:63], v[100:103], v[160:163]\n" \
  "v_pk_min_u16 v164, v164, %4\n" "v_pk_min_u16 v165, v165, %4\n" \
  "v_mfma_f32_16x16x32_f16 v[156:159], v[56:59], v[100:103], v[156:159]\n" "ds_write_b64 %12, v[164:165] offset:0\n" \
  "v_mfma_f32_16x16x32_f16 v[160:163], v[104:107], v[116:119], v[160:163]\n" "s_nop 0\n" \
  "ds_read_b128 v[84:87], %8 offset:1280\n" "ds_read_b128 v[128:131], %8 offset:1344\n" \
  "v_mfma_f32_16x16x32_f16 v[156:159], v[72:75], v[116:119], v[156:159]\n" "s_waitcnt lgkmcnt(4)\n" \
  "ds_read_b128 v[100:103], %8 offset:1408\n" "ds_read_b128 v[164:167], %8 offset:1472\n" \
  "v_mfma_f32_16x16x32_f16 v[160:163], v[44:47], v[76:79], v[160:163]\n" "v_mfma_f32_16x16x32_f16 v[156:159], v[48:51], v[76:79], v[156:159]\n" \
  "v_mfma_f32_16x16x32_f16 v[160:163], v[36:39], v[88:91], v[160:163]\n" "ds_read_b128 v[116:119], %8 offset:1536\n" \
  "v_mfma_f32_16x16x32_f16 v[156:159], v[52:55], v[88:91], v[156:159]\n" "s_nop 0\n" \
  "ds_read_b128 v[76:79], %8 offset:1600\n" "ds_read_b128 v[88:91], %8 offset:1664\n" \
  "v_mfma_f32_16x16x32_f16 v[160:163], v[68:71], v[80:83], v[160:163]\n" "v_mfma_f32_16x16x32_f16 v[156:159], v[16:19], v[80:83], v[156:159]\n" \
  "ds_read_b128 v[80:83], %8 offset:1728\n" "v_mfma_f32_16x16x32_f16 v[160:163], v[124:127], v[112:115], v[160:163]\n" \
  "v_mfma_f32_16x16x32_f16 v[156:159], v[120:123], v[112:115], v[156:159]\n" "ds_read_b128 v[112:115], %8 offset:1792\n" \
  "v_mfma_f32_16x16x32_f16 v[160:163], v[96:99], v[92:95], v[160:163]\n" "v_mfma_f32_16x16x32_f16 v[156:159], v[32:35], v[92:95], v[156:159]\n" \
  "v_mfma_f32_16x16x32_f16 v[152:155], v[40:43], v[92:95], v[152:155]\n" "v_mfma_f32_16x16x32_f16 v[144:147], v[132:135], v[92:95], v[144:147]\n" \
  "ds_read_b128 v[92:95], %8 offset:1856\n" "v_mfma_f32_16x16x32_f16 v[156:159], v[64:67], v[24:27], v[156:159]\n" \
  "v_mfma_f32_16x16x32_f16 v[152:155], v[20:23], v[24:27], v[152:155]\n" "ds_read_b128 v[24:27], %8 offset:1920\n" \
  "v_mfma_f32_16x16x32_f16 v[156:159], v[60:63], v[28:31], v[156:159]\n" "v_mul_f32 v160, %5, v160\n" \
  "v_mul_f32 v161, %5, v161\n" "v_mfma_f32_16x16x32_f16 v[152:155], v[56:59], v[28:31], v[152:155]\n" \
  "s_waitcnt lgkmcnt(2)\n" "v_mul_f32 v162, %5, v162\n" \
  "v_mul_f32 v163, %5, v163\n" "v_mfma_f32_16x16x32_f16 v[156:159], v[104:107], v[108:111], v[156:159]\n" \
  "v_cvt_pk_bf16_f32 v160, v160, v161\n" "v_cvt_pk_bf16_f32 v161, v162, v163\n" \
  "v_mfma_f32_16x16x32_f16 v[152:155], v[72:75], v[108:111], v[152:155]\n" "v_pk_min_u16 v160, v160, %4\n" \
  "v_pk_min_u16 v161, v161, %4\n" "v_mfma_f32_16x16x32_f16 v[156:159], v[44:47], v[84:87], v[156:159]\n" \
  "ds_write_b64 %12, v[160:161] offset:512\n" "v_mfma_f32_16x16x32_f16 v[152:155], v[48:51], v[84:87], v[152:155]\n" \
  "s_nop 0\n" "ds_read_b128 v[28:31], %8 offset:1984\n" \
  "ds_read_b128 v[108:111], %8 offset:2048\n" "v_mfma_f32_16x16x32_f16 v[156:159], v[36:39], v[128:131], v[156:159]\n" \
  "s_nop 0\n" "ds_read_b128 v[160:163], %8 offset:2112\n" \
  "ds_read_b128 v[84:87], %8 offset:2176\n" "v_mfma_f32_16x16x32_f16 v[152:155], v[52:55], v[128:131], v[152:155]\n" \
  "v_mfma_f32_16x16x32_f16 v[156:159], v[68:71], v[100:103], v[156:159]\n" "v_mfma_f32_16x16x32_f16 v[152:155], v[16:19], v[100:103], v[152:155]\n" \
  "s_waitcnt lgkmcnt(4)\n" "ds_read_b128 v[128:131], %8 offset:2240\n" \
  "v_mfma_f32_16x16x32_f16 v[156:159], v[124:127], v[164:167], v[156:159]\n" "s_barrier\n" \
  "ds_read_b128 v[100:103], %8 offset:2304\n" "v_mfma_f32_16x16x32_f16 v[152:155], v[120:123], v[164:167], v[152:155]\n" \
  "ds_read_b128 v[164:167], %8 offset:2368\n" "v_mfma_f32_16x16x32_f16 v[156:159], v[96:99], v[116:119], v[156:159]\n" \
  "v_mfma_f32_16x16x32_f16 v[152:155], v[32:35], v[116:119], v[152:155]\n" "v_mfma_f32_16x16x32_f16 v[148:151], v[40:43], v[116:119], v[148:151]\n" \
  "v_mfma_f32_16x16x32_f16 v[140:143], v[132:135], v[116:119], v[140:143]\n" "ds_read_b128 v[116:119], %8 offset:2432\n" \
  "v_mfma_f32_16x16x32_f16 v[152:155], v[64:67], v[76:79], v[152:155]\n" "v_mfma_f32_16x16x32_f16 v[148:151], v[20:23], v[76:79], v[148:151]\n" \
  "ds_read_b128 v[76:79], %8 offset:2496\n" "v_mfma_f32_16x16x32_f16 v[152:155], v[60:63], v[88:91], v[152:155]\n" \
  "v_mul_f32 v156, %5, v156\n" "v_mul_f32 v157, %5, v157\n" \
  "v_mfma_f32_16x16x32_f16 v[148:151], v[56:59], v[88:91], v[148:151]\n" "v_mul_f32 v158, %5, v158\n" \
  "v_mul_f32 v159, %5, v159\n" "v_mfma_f32_16x16x32_f16 v[152:155], v[104:107], v[80:83], v[152:155]\n" \
  "v_cvt_pk_bf16_f32 v156, v156, v157\n" "v_cvt_pk_bf16_f32 v157, v158, v159\n" \
  "v_mfma_f32_16x16x32_f16 v[148:151], v[72:75], v[80:83], v[148:151]\n" "v_pk_min_u16 v156, v156, %4\n" \
  "v_pk_min_u16 v157, v157, %4\n" "v_mfma_f32_16x16x32_f16 v[152:155], v[44:47], v[112:115], v[152:155]\n" \
  "ds_write_b64 %12, v[156:157] offset:1024\n" "v_mfma_f32_16x16x32_f16 v[148:151], v[48:51], v[112:115], v[148:151]\n" \
  "s_nop 0\n" "ds_read_b128 v[88:91], %8 offset:2560\n" \
  "ds_read_b128 v[80:83], %8 offset:2624\n" "v_mfma_f32_16x16x32_f16 v[152:155], v[36:39], v[92:95], v[152:155]\n" \
  "s_nop 0\n" "ds_read_b128 v[156:159], %8 offset:2688\n" \
  "ds_read_b128 v[112:115], %8 offset:2752\n" "v_mfma_f32_16x16x32_f16 v[148:151], v[52:55], v[92:95], v[148:151]\n" \
  "v_mfma_f32_16x16x32_f16 v[152:155], v[68:71], v[24:27], v[152:155]\n" "v_mfma_f32_16x16x32_f16 v[148:151], v[16:19], v[24:27], v[148:151]\n" \
  "s_waitcnt lgkmcnt(5)\n" "ds_read_b128 v[92:95], %8 offset:2816\n" \
  "v_mfma_f32_16x16x32_f16 v[152:155], v[124:127], v[28:31], v[152:155]\n" "ds_read_b128 v[24:27], %8 offset:2880\n" \
  "v_mfma_f32_16x16x32_f16 v[148:151], v[120:123], v[28:31], v[148:151]\n" "ds_read_b128 v[28:31], %8 offset:2944\n" \
  "v_mfma_f32_16x16x32_f16 v[152:155], v[96:99], v[108:111], v[152:155]\n" "v_mfma_f32_16x16x32_f16 v[148:151], v[32:35], v[108:111], v[148:151]\n" \
  "v_mfma_f32_16x16x32_f16 v[144:147], v[40:43], v[108:111], v[144:147]\n" "v_mfma_f32_16x16x32_f16 v[136:139], v[132:135], v[108:111], v[136:139]\n" \
  "ds_read_b128 v[132:135], %8 offset:3008\n" "v_mfma_f32_16x16x32_f16 v[148:151], v[64:67], v[160:163], v[148:151]\n" \
  "v_mfma_f32_16x16x32_f16 v[144:147], v[20:23], v[160:163], v[144:147]\n" "v_mfma_f32_16x16x32_f16 v[148:151], v[60:63], v[84:87], v[148:151]\n" \
  "s_nop 0\n" "v_mul_f32 v152, %5, v152\n" \
  "v_mul_f32 v153, %5, v153\n" "v_mfma_f32_16x16x32_f16 v[144:147], v[56:59], v[84:87], v[144:147]\n" \
  "v_mul_f32 v154, %5, v154\n" "v_mul_f32 v155, %5, v155\n" \
  "v_mfma_f32_16x16x32_f16 v[148:151], v[104:107], v[128:131], v[148:151]\n" "v_cvt_pk_bf16_f32 v152, v152, v153\n" \
  "v_cvt_pk_bf16_f32 v153, v154, v155\n" "v_mfma_f32_16x16x32_f16 v[144:147], v[72:75], v[128:131], v[144:147]\n" \
  "v_pk_min_u16 v152, v152, %4\n" "v_pk_min_u16 v153, v153, %4\n" \
  "v_mfma_f32_16x16x32_f16 v[148:151], v[44:47], v[100:103], v[148:151]\n" "ds_write_b64 %12, v[152:153] offset:1536\n" \
  "v_mfma_f32_16x16x32_f16 v[144:147], v[48:51], v[100:103], v[144:147]\n" "v_mfma_f32_16x16x32_f16 v[148:151], v[36:39], v[164:167], v[148:151]\n" \
  "v_mfma_f32_16x16x32_f16 v[144:147], v[52:55], v[164:167], v[144:147]\n" "v_mfma_f32_16x16x32_f16 v[148:151], v[68:71], v[116:119], v[148:151]\n" \
  "v_mfma_f32_16x16x32_f16 v[144:147], v[16:19], v[116:119], v[144:147]\n" "s_waitcnt lgkmcnt(0)\n" \
  "v_mfma_f32_16x16x32_f16 v[148:151], v[124:127], v[76:79], v[148:151]\n" "s_barrier\n" \
  "ds_read_b128 v[108:111], %8 offset:3072\n" "ds_read_b128 v[160:163], %8 offset:3136\n" \
  "v_mfma_f32_16x16x32_f16 v[144:147], v[120:123], v[76:79], v[144:147]\n" "s_nop 0\n" \
  "ds_read_b128 v[84:87], %8 offset:3200\n" "ds_read_b128 v[128:131], %8 offset:3264\n" \
  "v_mfma_f32_16x16x32_f16 v[148:151], v[96:99], v[88:91], v[148:151]\n" "ds_read_b128 v[152:155], %8 offset:3328\n" \
  "v_mfma_f32_16x16x32_f16 v[144:147], v[32:35], v[88:91], v[144:147]\n" "s_nop 2\n" \
  "v_mfma_f32_16x16x32_f16 v[140:143], v[40:43], v[88:91], v[140:143]\n" "s_nop 1\n" \
  "v_mfma_f32_16x16x32_f16 v[144:147], v[64:67], v[80:83], v[144:147]\n" "v_mul_f32 v148, %5, v148\n" \
  "v_mul_f32 v149, %5, v149\n" "v_mfma_f32_16x16x32_f16 v[140:143], v[20:23], v[80:83], v[140:143]\n" \
  "v_mul_f32 v150, %5, v150\n" "v_mul_f32 v151, %5, v151\n" \
  "v_mfma_f32_16x16x32_f16 v[144:147], v[60:63], v[156:159], v[144:147]\n" "v_cvt_pk_bf16_f32 v148, v148, v149\n" \
  "v_cvt_pk_bf16_f32 v149, v150, v151\n" "v_mfma_f32_16x16x32_f16 v[140:143], v[56:59], v[156:159], v[140:143]\n" \
  "v_pk_min_u16 v148, v148, %4\n" "v_pk_min_u16 v149, v149, %4\n" \
  "v_mfma_f32_16x16x32_f16 v[144:147], v[104:107], v[112:115], v[144:147]\n" "ds_write_b64 %12, v[148:149] offset:2048\n" \
  "v_mfma_f32_16x16x32_f16 v[140:143], v[72:75], v[112:115], v[140:143]\n" "s_nop 0\n" \
  "ds_read_b128 v[100:103], %8 offset:3392\n" "ds_read_b128 v[164:167], %8 offset:3456\n" \
  "v_mfma_f32_16x16x32_f16 v[144:147], v[44:47], v[92:95], v[144:147]\n" "s_nop 0\n" \
  "ds_read_b128 v[116:119], %8 offset:3520\n" "ds_read_b128 v[76:79], %8 offset:3584\n" \
  "v_mfma_f32_16x16x32_f16 v[140:143], v[48:51], v[92:95], v[140:143]\n" "v_mfma_f32_16x16x32_f16 v[144:147], v[36:39], v[24:27], v[144:147]\n" \
  "v_mfma_f32_16x16x32_f16 v[140:143], v[52:55], v[24:27], v[140:143]\n" "ds_read_b128 v[88:91], %8 offset:3648\n" \
  "v_mfma_f32_16x16x32_f16 v[144:147], v[68:71], v[28:31], v[144:147]\n" "s_nop 0\n" \
  "ds_read_b128 v[80:83], %8 offset:3712\n" "ds_read_b128 v[156:159], %8 offset:3776\n" \
  "v_mfma_f32_16x16x32_f16 v[140:143], v[16:19], v[28:31], v[140:143]\n" "s_nop 0\n" \
  "ds_read_b128 v[148:151], %8 offset:3840\n" "ds_read_b128 v[112:115], %8 offset:3904\n" \
  "v_mfma_f32_16x16x32_f16 v[144:147], v[124:127], v[132:135], v[144:147]\n" "v_mfma_f32_16x16x32_f16 v[140:143], v[120:123], v[132:135], v[140:143]\n" \
  "s_waitcnt lgkmcnt(7)\n" "v_mfma_f32_16x16x32_f16 v[144:147], v[96:99], v[108:111], v[144:147]\n" \
  "ds_read_b128 v[92:95], %8 offset:3968\n" "v_mfma_f32_16x16x32_f16 v[140:143], v[32:35], v[108:111], v[140:143]\n" \
  "s_nop 0\n" "ds_read_b128 v[24:27], %8 offset:4032\n" \
  "ds_read_b128 v[28:31], %8 offset:4096\n" "v_mfma_f32_16x16x32_f16 v[136:139], v[40:43], v[108:111], v[136:139]\n" \
  "s_nop 1\n" "v_mfma_f32_16x16x32_f16 v[140:143], v[64:67], v[160:163], v[140:143]\n" \
  "v_mul_f32 v144, %5, v144\n" "v_mul_f32 v145, %5, v145\n" \
  "v_mfma_f32_16x16x32_f16 v[140:143], v[60:63], v[84:87], v[140:143]\n" "v_mul_f32 v146, %5, v146\n" \
  "v_mul_f32 v147, %5, v147\n" "v_mfma_f32_16x16x32_f16 v[140:143], v[104:107], v[128:131], v[140:143]\n" \
  "v_cvt_pk_bf16_f32 v144, v144, v145\n" "v_cvt_pk_bf16_f32 v145, v146, v147\n" \
  "v_mfma_f32_16x16x32_f16 v[140:143], v[44:47], v[152:155], v[140:143]\n" "v_pk_min_u16 v144, v144, %4\n" \
  "v_pk_min_u16 v145, v145, %4\n" "v_mfma_f32_16x16x32_f16 v[140:143], v[36:39], v[100:103], v[140:143]\n" \
  "ds_write_b64 %12, v[144:145] offset:2560\n" "v_mfma_f32_16x16x32_f16 v[140:143], v[68:71], v[164:167], v[140:143]\n" \
  "s_waitcnt lgkmcnt(3)\n" "v_mfma_f32_16x16x32_f16 v[140:143], v[124:127], v[116:119], v[140:143]\n" \
  "v_mfma_f32_16x16x32_f16 v[140:143], v[96:99], v[76:79], v[140:143]\n" "s_nop 2\n" \
  "v_mfma_f32_16x16x32_f16 v[136:139], v[20:23], v[160:163], v[136:139]\n" "s_nop 2\n" \
  "v_mfma_f32_16x16x32_f16 v[136:139], v[56:59], v[84:87], v[136:139]\n" "s_waitcnt lgkmcnt(0)\n" \
  "v_mul_f32 v140, %5, v140\n" "v_mul_f32 v141, %5, v141\n" \
  "v_mfma_f32_16x16x32_f16 v[136:139], v[72:75], v[128:131], v[136:139]\n" "s_barrier\n" \
  "v_mul_f32 v142, %5, v142\n" "v_mul_f32 v143, %5, v143\n" \
  "v_mfma_f32_16x16x32_f16 v[136:139], v[48:51], v[152:155], v[136:139]\n" "v_cvt_pk_bf16_f32 v140, v140, v141\n" \
  "v_cvt_pk_bf16_f32 v141, v142, v143\n" "v_mfma_f32_16x16x32_f16 v[136:139], v[52:55], v[100:103], v[136:139]\n" \
  "v_pk_min_u16 v140, v140, %4\n" "v_pk_min_u16 v141, v141, %4\n" \
  "v_mfma_f32_16x16x32_f16 v[136:139], v[16:19], v[164:167], v[136:139]\n" "ds_write_b64 %12, v[140:141] offset:3072\n" \
  "v_mfma_f32_16x16x32_f16 v[136:139], v[120:123], v[116:119], v[136:139]\n" "v_mfma_f32_16x16x32_f16 v[136:139], v[32:35], v[76:79], v[136:139]\n" \
  "v_mfma_f32_16x16x32_f16 v[136:139], v[64:67], v[88:91], v[136:139]\n" "v_mfma_f32_16x16x32_f16 v[136:139], v[60:63], v[80:83], v[136:139]\n" \
  "v_mfma_f32_16x16x32_f16 v[136:139], v[104:107], v[156:159], v[136:139]\n" "v_mfma_f32_16x16x32_f16 v[136:139], v[44:47], v[148:151], v[136:139]\n" \
  "v_mfma_f32_16x16x32_f16 v[136:139], v[36:39], v[112:115], v[136:139]\n" "v_mfma_f32_16x16x32_f16 v[136:139], v[68:71], v[92:95], v[136:139]\n" \
  "v_mfma_f32_16x16x32_f16 v[136:139], v[124:127], v[24:27], v[136:139]\n" "v_mfma_f32_16x16x32_f16 v[136:139], v[96:99], v[28:31], v[136:139]\n" \
  "s_nop 2\n" "s_nop 4\n" \
  "v_mul_f32 v136, %5, v136\n" "v_mul_f32 v137, %5, v137\n" \
  "v_mul_f32 v138, %5, v138\n" "v_mul_f32 v139, %5, v139\n" \
  "v_cvt_pk_bf16_f32 v136, v136, v137\n" "v_cvt_pk_bf16_f32 v137, v138, v139\n" \
  "v_pk_min_u16 v136, v136, %4\n" "v_pk_min_u16 v137, v137, %4\n" \
  "ds_write_b64 %12, v[136:137] offset:3584\n" "s_waitcnt lgkmcnt(0)\n" \
  "s_barrier\n"
#define CONV_ASM_CLOBBERS "v32", "v33", "v34", "v35", "v36", "v37", "v38", "v39", "v40", "v41", "v42", "v43", "v44", "v45", "v46", "v47", "v48", "v49", "v50", "v51", "v52", "v53", "v54", "v55", "v56", "v57", "v58", "v59", "v60", "v61", "v62", "v63", "v64", "v65", "v66", "v67", "v68", "v69", "v70", "v71", "v72", "v73", "v74", "v75", "v76", "v77", "v78", "v79", "v80", "v81", "v82", "v83", "v84", "v85", "v86", "v87", "v88", "v89", "v90", "v91", "v92", "v93", "v94", "v95", "v96", "v97", "v98", "v99", "v100", "v101", "v102", "v103", "v104", "v105", "v106", "v107", "v108", "v109", "v110", "v111", "v112", "v113", "v114", "v115", "v116", "v117", "v118", "v119", "v120", "v121", "v122", "v123", "v124", "v125", "v126", "v127", "v128", "v129", "v130", "v131", "v132", "v133", "v134", "v135", "v136", "v137", "v138", "v139", "v140", "v141", "v142", "v143", "v144", "v145", "v146", "v147", "v148", "v149", "v150", "v151", "v152", "v153", "v154", "v155", "v156", "v157", "v158", "v159", "v160", "v161", "v162", "v163", "v164", "v165", "v166", "v167", "memory"
#define CONV_ASM_PRE(q) "+{v[16:19]}"(q[0]), "+{v[20:23]}"(q[1]), "+{v[24:27]}"(q[2]), "+{v[28:31]}"(q[3])

struct Params {
  const u16 *x, *lar, *aim, *crl, *cim, *ldt, *dskip, *wr, *wg, *wu, *wd;
  u16* out;
  char* ws;
};

typedef unsigned long long u64;
typedef u64 v8q __attribute__((ext_vector_type(8)));
typedef u64 v4q __attribute__((ext_vector_type(4)));
#define GAS __attribute__((address_space(1)))

__global__ __launch_bounds__(NT, 3) void k_main(Params) {
  extern __shared__ __attribute__((aligned(16))) char smem[];
  v8q ka;
  v4q kb_;
  u64 kc;
  asm volatile("s_load_dwordx16 s[8:23], %3, 0x0\n\ts_load_dwordx8 s[24:31], %3, 0x40\n\ts_load_dwordx2 s[36:37], %3, 0x60"
               : "=&{s[8:23]}"(ka), "=&{s[24:31]}"(kb_), "=&{s[36:37]}"(kc)
               : "s"(__builtin_amdgcn_kernarg_segment_ptr()));
  SCHED();
  const int tid = threadIdx.x, lane = tid & 63;
  int wave = __builtin_amdgcn_readfirstlane(tid >> 6);
  const int xcd = blockIdx.x & 7;
  int loc = blockIdx.x >> 3;
  STAMP_INIT();
  STAMP(0);
  unsigned cpf = 0;
  if (wave == 0 && lane < 8 && (loc * 8 + lane) * 128 < CODE_BYTES) {
    const char* pc = (const char*)(__builtin_amdgcn_s_getpc() & ~(uint64_t)255);
    cpf = *(const __attribute__((address_space(1))) unsigned*)(pc + (loc * 8 + lane) * 128);
  }
  const int cl = lane & 15, g = lane >> 4;
  const int cg = xcd * 18 + loc;
  const int fj = xcd * 9 + (loc - 18);
  const int rb = xcd * 16 + loc;
  const int tile = loc < 30 ? xcd * 12 + (loc - 18) : 66 + xcd * 2 + loc;
  const unsigned od = (cg * 4 + (wave & 3)) * 2;
  const unsigned ox = (tile * 16 + (cl >> 1)) * 1152 + 128 * wave + 32 * g + 16 * (cl & 1);
  const unsigned opar = (wave < 8 ? od * NM + (wave & 4) * 32 : 0u) + (lane << (wave < 8 ? 1 : 2));
  unsigned o0 = loc >= 18 ? ox : opar, o1 = loc >= 18 ? ox + 9216 : opar ^ 128u;
  unsigned o2 = loc >= 18 ? ox + 16 * 18432 : od, o3 = o2 + 9216;
  PIN(o0); PIN(o1); PIN(o2); PIN(o3);
  SCHED();
  asm volatile("s_waitcnt lgkmcnt(0)" : "+{s[8:23]}"(ka), "+{s[24:31]}"(kb_), "+{s[36:37]}"(kc));
  v4u ga, gb, gc, gd;
  unsigned epoch;
  asm volatile(
      "v_mov_b32 v1, 0\n\t"
      "s_cmp_lt_u32 %9, 18\n\t"
      "global_load_dword v1, v1, s[36:37] sc1\n\t"
      "s_cbranch_scc1 Lc_%=\n\t"
      "global_load_dwordx4 v[2:5], %5, s[8:9]\n\t"
      "global_load_dwordx4 v[6:9], %6, s[8:9]\n\t"
      "s_cmp_lt_u32 %9, 30\n\t"
      "s_cbranch_scc1 Le_%=\n\t"
      "global_load_dwordx4 v[10:13], %7, s[8:9]\n\t"
      "global_load_dwordx4 v[14:17], %8, s[8:9]\n\t"
      "s_branch Le_%=\n"
      "Lc_%=:\n\t"
      "s_cmp_eq_u32 %10, 8\n\t"
      "s_cbranch_scc1 Lw_%=\n\t"
      "global_load_ushort v6, %7, s[18:19]\n\t"
      "global_load_ushort v2, %5, s[10:11]\n\t"
      "global_load_ushort v3, %5, s[12:13]\n\t"
      "global_load_ushort v4, %5, s[14:15]\n\t"
      "global_load_ushort v5, %5, s[16:17]\n\t"
      "global_load_ushort v7, %6, s[14:15]\n\t"
      "global_load_ushort v8, %6, s[16:17]\n\t"
      "global_load_ushort v9, %7, s[20:21]\n\t"
      "s_branch Le_%=\n"
      "Lw_%=:\n\t"
      "global_load_dword v2, %5, s[22:23]\n\t"
      "global_load_dword v3, %5, s[22:23] offset:256\n\t"
      "global_load_dword v4, %5, s[22:23] offset:512\n\t"
      "global_load_dword v5, %5, s[22:23] offset:768\n\t"
      "global_load_dword v6, %5, s[22:23] offset:896\n"
      "Le_%=:"
      : "=&{v1}"(epoch), "=&{v[2:5]}"(ga), "=&{v[6:9]}"(gb), "=&{v[10:13]}"(gc), "=&{v[14:17]}"(gd)
      : "v"(o0), "v"(o1), "v"(o2), "v"(o3), "s"(loc), "s"(wave), "{s[8:23]}"(ka), "{s[36:37]}"(kc)
      : "scc", "memory");
  struct { const GAS char *x, *wg, *wu, *wd; GAS char *out, *ws; } p = {(const GAS char*)ka[0], (const GAS char*)kb_[0], (const GAS char*)kb_[1], (const GAS char*)kb_[2], (GAS char*)kb_[3], (GAS char*)kc};
#define FIRST_WAIT(n) asm volatile("s_waitcnt vmcnt(%5) ; first loads" : "+{v1}"(epoch), "+{v[2:5]}"(ga), "+{v[6:9]}"(gb), "+{v[10:13]}"(gc), "+{v[14:17]}"(gd) : "n"(n) : "memory")
  STAMP(11);
  const rsrc_t rs = __builtin_amdgcn_make_buffer_rsrc((void*)p.ws, 0, WS_END, 0x00020000);
  const rsrc_t ro = __builtin_amdgcn_make_buffer_rsrc((void*)p.out, 0, SEQ * DM * 2, 0x00020000);
  const v4u sent4 = {SENT, SENT, SENT, SENT};
  if (loc < 18) {
    if (wave < 4) {
      const unsigned win = wave == 0 ? 0x1ffu : wave == 1 ? 0xfe00u : wave == 2 ? 0x7cu : 0x380u;
      const unsigned bits = (win << cl) >> (8 * g);
      v4u f;
#pragma unroll
      for (int i = 0; i < 4; i++) f[i] = __umul24(__umul24((bits >> (2 * i)) & 3u, 0x8001u) & 0x10001u, 0x3c00u);
      *(v4u*)(smem + L_FILT + wave * 1024 + lane * 16) = f;
    } else if (lane < (wave < 8 ? 36 : 4)) {
      *(v4u*)(smem + (wave < 8 ? L_UIMG + (wave - 4) * USTR : L_MISC) + (lane < 33 ? lane : lane < 35 ? lane + 255 : 224) * 16) = v4u{0u, 0u, 0u, 0u};
    }
  }
  asm volatile("" : "+s"(loc), "+s"(wave));

  if (loc < 18) {
    const int usrc = WS_U + cg * GSTR + lane * 16;
    char* const udst = smem + L_UIMG + (lane & 3) * USTR + 520 + (lane >> 2) * 16;
    if (wave == 8) {
      unsigned pw[PF_N];
      auto warm = [&]() {
#pragma unroll
        for (int i = 0; i < PF_N; i++) {
          const int r = i < 5 ? min(i * 64 + lane, 287) * 2 + (loc >= 9) : min(((i - 5) * 4 + (lane >> 4)) * 64 + 32 + (lane & 15) * 2 + (loc >= 9), 575);
          pw[i] = *(const GAS unsigned*)((i < 5 ? p.wu : p.wg) + (unsigned)(r * 9216 + (xcd * 9 + (loc < 9 ? loc : loc - 9)) * 128));
        }
        SCHED();
      };
      if (PF_MODE == 1) warm();
      BAR();
      __builtin_amdgcn_s_setprio(3);
      FIRST_WAIT(PF_MODE == 1 ? PF_N : 0);
      if (PF_MODE == 2) warm();
      {
        const v4u wa = ga;
        const unsigned wb = gb.x;
#pragma unroll
        for (int k = 0; k < 4; k++) *(unsigned*)(smem + L_WR + (lane + 64 * k) * 4) = wa[k];
        *(unsigned*)(smem + L_WR + (224 + lane) * 4) = wb;
        float ws_ = lane >= 32 ? blo(wb) + bhi(wb) : 0.f;
#pragma unroll
        for (int k = 0; k < 4; k++) ws_ += blo(wa[k]) + bhi(wa[k]);
        ws_ = wave_sum(ws_);
        if (lane == 0) *(float*)(smem + L_MISC + 64) = ws_;
      }
      SCHED();
      if (PF_SLEEP) __builtin_amdgcn_s_sleep(PF_SLEEP);
#pragma unroll 1
      for (int i = 0; i < 16; i++) st4(sent4, rs, WS_Y + cg * GSTR + i * 1024 + lane * 16);
      st4(sent4, rs, (loc < 16 && lane < 4) ? WS_RREC + rb * 64 + lane * 16 : OOB);
      st4(sent4, rs, cg < 20 ? WS_XSEL + cg * 1024 + lane * 16 : OOB);
      DRAIN();
      if (lane == 0) st1(epoch + 1u, rs, WS_INIT + cg * 64);
      STAMP(4);
      __builtin_amdgcn_s_setprio(S_PRIO);
      BAR();
      STAMP(5);
      if (loc < 16) {
        sweep_init(rs, epoch + 1u, lane, 0, NCONV);
        const GAS char* xs = p.x + (unsigned)((16 * rb + (lane >> 3)) * 1152 + ((lane & 7) ^ (lane >> 3)) * 16);
#pragma unroll 1
        for (int i = 0; i < 9; i++) {
          __builtin_amdgcn_global_load_lds((const GAS unsigned*)(xs + i * 128), (LAS unsigned*)(L_XROW + i * 2048), 16, 0, 0);
          __builtin_amdgcn_global_load_lds((const GAS unsigned*)(xs + i * 128 + 9216), (LAS unsigned*)(L_XROW + i * 2048 + 1024), 16, 0, 0);
        }
      }
      STAMP(6);
      if (PF_MODE == 3) warm();
      BAR();
      BAR();
      BAR();
      BAR();
      DRAIN();
#pragma unroll
      for (int i = 0; i < (PF_MODE ? PF_N : 0); i++) asm volatile("" ::"v"(pw[i]));
      __builtin_amdgcn_s_setprio(0);
    }
    asm volatile("" : "+s"(wave));
    if (wave < 8) {
      const int ch = wave & 3;
      char* const img = smem + ch * WSCR;
      char* const scr = smem + wave * WSCR;
      volatile LAS unsigned* const pf = (volatile LAS unsigned*)(smem + L_MISC);
      if (wave < 4) __builtin_amdgcn_s_setprio(2); else __builtin_amdgcn_s_setprio(1);
      unsigned wb_[16], rb_[8];
#pragma unroll
      for (int k = 0; k < 16; k++) { wb_[k] = k ? wb_[0] ^ (k << 4) : wave * WSCR + lane * 4; PIN(wb_[k]); }
#pragma unroll
      for (int k = 0; k < 8; k++) { rb_[k] = k ? rb_[0] ^ (k << 5) : wave * WSCR + (lane & 31) * 256 + (((lane >> 5) ^ cl) << 4); PIN(rb_[k]); }
      BAR();
      STAMP(12);
      FIRST_WAIT(0);
      PIN(cpf);
      STAMP(5);
      const float dt = rbf(exp_c(blo(gb.x)));
      const float ar = -rbf(exp_c(blo(ga.x))) * dt * 1.44269504f;
      const float aie = blo(ga.y) * dt;
      const float ph = aie * 0.159154936f;
      const float pl = fmaf(aie, 0.159154936f, -ph) + aie * 7.13048051e-9f;
      auto put2 = [&](int row, unsigned x, unsigned y) {
        *(LAS unsigned*)(wb_[row & 15] + row * 256) = x;
        *(LAS unsigned*)(wb_[row & 15] + (row + 16) * 256) = y;
      };
      auto put1 = [&](int row, unsigned x) { *(LAS unsigned*)(wb_[row & 15] + row * 256) = x; };
      const cz e8 = mkz(cexp_p2(8.f, ar, ph, pl)), e64 = mkz(cexp_p2(64.f, ar, ph, pl));
      const cz e16 = csq(e8), e128 = csq(e64), e32 = csq(e16);
      {
        const f32x2 e24 = cmul(e16.v, e8), e192 = cmul(e128.v, e64);
        const unsigned ha[4] = {0x3c00u, pkh2(e8.v), pkh2(e16.v), pkh2(e24)};
        const unsigned hs[4] = {0u, pkh2(e8.s), pkh2(e16.s), pkh(e24.y, e24.x)};
        const unsigned hb[8] = {0x3c00u, pkh2(e32.v), pkh2(e64.v), pkh2(cmul(e64.v, e32)), pkh2(e128.v), pkh2(cmul(e128.v, e32)), pkh2(e192), pkh2(cmul(e192, e32))};
#pragma unroll
        for (int k = 0; k < 4; k++) put2(4 * k, hb[k], hb[k + 4]);
#pragma unroll
        for (int i = 1; i < 4; i++) {
          unsigned r[8];
          hmul2(r[1], r[5], hb[1], hb[5], ha[i], hs[i]);
          put2(4 + i, r[1], r[5]);
          hmul2(r[2], r[6], hb[2], hb[6], ha[i], hs[i]);
          put2(8 + i, r[2], r[6]);
          hmul2(r[3], r[7], hb[3], hb[7], ha[i], hs[i]);
          put2(12 + i, r[3], r[7]);
        }
        unsigned r1, r2, r3, r4;
        const unsigned s128 = pkh2(e128.s);
        hmul2(r1, r2, ha[1], ha[2], hb[4], s128);
        hmul2(r3, r4, ha[3], ha[3], hb[4], s128);
        put2(1, ha[1], r1);
        put2(2, ha[2], r2);
        put2(3, ha[3], r3);
      }
      MEMBAR();
      f16x8 fa[8], fb[8];
#pragma unroll
      for (int k = 0; k < 8; k++) fb[k] = *(const LAS f16x8*)(rb_[k]);
      SCHED();
      if (wave >= 4) STAMP(11);
      const float cre = blo(ga.z) * dt, cim = blo(ga.w) * dt;
      const float dsk = blo(gb.w);
      const float cmx = fmaxf(fmaxf(fmaxf(fabsf(cre), fabsf(cim)), fabsf(dsk) * 0.0078125f), fmaxf(fabsf(blo(gb.y)), fabsf(blo(gb.z))) * dt);
      const unsigned ce = min(max(wave_max_u(__float_as_uint(cmx)) >> 23, 40u), 250u);
      const float csc = __uint_as_float((261u - ce) << 23);
      f32x16 acc;
      unsigned have;
      auto partial = [&]() {
        MEMBAR();
#pragma unroll
        for (int k = 0; k < 4; k++) {
          const f32x4 o = *(const f32x4*)(img + k * 1024 + lane * 16);
#pragma unroll
          for (int i = 0; i < 4; i++) acc[4 * k + i] = o[i];
        }
      };
#define KSTEP(k) SCHED(); acc = __builtin_amdgcn_mfma_f32_32x32x16_f16(fa[k], fb[k], acc, 0, 0, 0); SCHED()
      {
        const cz e1 = mkz(cexp_p2(1.f, ar, ph, pl)), e512 = mkz(cexp_p2(512.f, ar, ph, pl));
        const cz e2 = csq(e1), e1024 = csq(e512), e256 = csq(e128), e4 = csq(e2);
        const f32x2 w0 = {cre * csc, -cim * csc};
        f32x2 A[3], B[3], C[3], D[3], E[3];
        unsigned a[3], b[3], c[3], d[3], e[3];
        cmulc3(A, a, w0, e8, w0, e1, w0, e256);
        put2(32 + 0, a[0], pkh2(w0));
        put1(32 + 11, a[1]);
        put1(32 + 15, a[2]);
        cmulc3(B, b, w0, e2, A[0], e256, A[0], e512);
        put1(32 + 10, b[0]);
        put2(32 + 4, b[1], a[2]);
        cmulc3(C, c, w0, e4, A[0], e1024, A[1], e2);
        put1(32 + 9, c[2]);
        cmulc3(D, d, B[1], e512, B[1], e1024, B[0], e256);
        put2(32 + 5, b[1], d[0]);
        put2(32 + 8, c[0], d[1]);
        put1(32 + 14, d[2]);
        cmulc3(E, e, C[0], e256, A[1], e4, B[2], e1024);
        put1(32 + 13, e[0]);
        put2(32 + 3, e[1], c[1]);
        put2(32 + 7, e[0], e[2]);
        SCHED();
        if (wave >= 4) {
          have = pf[ch];
          partial();
        }
        SCHED();
        cmulc3(A, a, B[0], e4, C[0], e512, C[2], e4);
        put2(32 + 2, a[0], a[1]);
        put2(32 + 1, a[2], b[2]);
        cmulc3(B, b, D[0], e1024, D[2], e4, E[0], e512);
        put2(32 + 12, e[0], b[0]);
        put2(32 + 6, b[1], b[2]);
      }
      MEMBAR();
#pragma unroll
      for (int k = 0; k < 8; k++) fa[k] = *(const LAS f16x8*)(rb_[k] + 8192);
      SCHED();
      if (wave < 4) {
        acc = f32x16{};
        KSTEP(0);
        *(v4u*)(scr + L_KN + (lane + (lane < 7 ? 0 : 64) + (lane < 19 ? 0 : 64) + (lane < 32 ? 0 : 16) + (lane < 40 ? 0 : 16)) * 16) = v4u{0u, 0u, 0u, 0u};
        KSTEP(1);
        KSTEP(2);
        float d0 = lane == 0 ? dsk * csc * 0.5f : 0.f;
        unsigned pa = wave * WSCR + lane * 16, fl = lane == 0 ? L_MISC + wave * 4 : wave * WSCR + L_DUMMY + lane * 4, one = 1u;
        PIN(d0); PIN(pa); PIN(fl); PIN(one);
#pragma unroll
        for (int k = 3; k < 8; k++) acc = __builtin_amdgcn_mfma_f32_32x32x16_f16(fa[k], fb[k], acc, 0, 0, 0);
        SCHED();
        acc[8] += d0;
        STAMP(2);
#pragma unroll
        for (int k = 0; k < 3; k++) *(LAS f32x4*)(pa + k * 1024) = f32x4{acc[4 * k], acc[4 * k + 1], acc[4 * k + 2], acc[4 * k + 3]};
        *(LAS float*)(pa + 3072) = acc[12];
        MEMBAR();
        *(volatile LAS unsigned*)fl = one;
        __builtin_amdgcn_s_setprio(0);
        STAMP(11);
        __builtin_amdgcn_s_sleep(SLEEP_U);
        __builtin_amdgcn_s_setprio(H_PRIO);
        const unsigned tag = __builtin_amdgcn_readfirstlane(epoch) + 1u;
        fetch_u<3>(rs, usrc, udst, {wave, wave + 4, wave + 8}, tag);
        STAMP(4);
        BAR();
        unsigned yb = ((lane >> 2) & 3) * (WSCR + 32) + L_Y + (lane & 1) * 128 + (wave & 1) * 64 + g * 16 + (lane & 2) * 4 + (wave >> 1) * 512;
        int yo = WS_Y + cg * GSTR + wave * 1024 + lane * 16;
        PIN(yb); PIN(yo);
        __builtin_amdgcn_s_sleep(SLEEP_U7);
        fetch_u<1>(rs, usrc, udst, {12 + wave}, tag);
        STAMP(6);
#pragma unroll
        for (int m = 0; m < 4; m++) {
          BAR();
          const v2u r0 = ldtr((const LAS char*)(yb + m * 1024)), r1 = ldtr((const LAS char*)(yb + m * 1024 + 256));
          __builtin_amdgcn_raw_buffer_store_b128(v4u{r0.x, r0.y, r1.x, r1.y}, rs, yo, m * 4096, SC1);
        }
        __builtin_amdgcn_s_setprio(0);
      }
      asm volatile("" : "+s"(wave));
      if (wave >= 4) {
        if (__builtin_amdgcn_readfirstlane(have) == 0u) {
#pragma unroll 1
          for (int sp = 0; sp < SPIN_CAP * 16 && pf[ch] == 0u; sp++) {}
          partial();
          PIN(acc);
        }
        v4u q[4];
        int ln = lane;
        PIN(ln);
        const int cp = ch * WSCR, c_ = ln & 15, g16 = ln & 48;
        unsigned a_kn, a_k4, a_k8, a_sp, sel_sp, op[7], top = 0xfffefffeu, kinv = (ce - 6u) << 23;
#define KASM(k, pre) asm volatile(pre "\n\tv_mfma_f32_32x32x16_f16 %0, %1, %2, %0" : "+v"(acc) : "v"(fa[k]), "v"(fb[k])); SCHED()
        KASM(0, "s_nop 1");
        a_kn = cp + L_KN + 16 * (70 - ln);
        a_k4 = cp + L_K4 + 4 * (95 - ln);
        a_k8 = cp + L_K8 + 2 * (95 - (ln & 32) - ln);
        PIN(a_kn); PIN(a_k4); PIN(a_k8);
        KASM(1, "");
        op[0] = cp + L_KN + 40 + ((c_ + 1) & 1) * KCOPY + g16 - ((2 * c_ + 2) & ~3);
        op[1] = op[0] + 1024;
        asm volatile("" : "+{v7}"(op[0]), "+{v8}"(op[1]));
        KASM(2, "");
        op[3] = L_UIMG + ch * USTR + g16 + 16 * c_;
        op[2] = op[3] + 16 * c_;
        op[4] = cp + L_K4 + 120 + g16 - 8 * c_;
        asm volatile("" : "+{v9}"(op[2]), "+{v10}"(op[3]), "+{v11}"(op[4]));
        KASM(3, "");
        op[5] = cp + L_K8 + 60 + g16 - 4 * c_;
        op[6] = cp + L_Y + ch * 32 + ln * 8;
        asm volatile("" : "+{v12}"(op[5]), "+{v13}"(op[6]), "+{v14}"(top), "+{v15}"(kinv));
        KASM(4, "");
        const unsigned dummy = cp + L_DUMMY + ln * 4;
        sel_sp = ln == 0 ? 0x0302u : ln < 7 ? 0x0504u : 0x0706u;
        PIN(sel_sp);
        KASM(5, "");
        a_sp = cp + L_KN + (ln == 0 ? 1134 : ln < 7 ? 112 : 48) - ((ln + 1) & ~1);
        PIN(a_sp);
        KASM(6, "");
        a_sp = ln < 11 ? a_sp + ((ln & 1) ? 0 : KCOPY + 2) : dummy;
        PIN(a_sp);
        KASM(7, "");
#pragma unroll
        for (int k = 0; k < 4; k++) q[k] = *(const v4u*)(smem + L_FILT + k * 1024 + lane * 16);
        SCHED();
        asm volatile("s_nop 11" : "+v"(acc));
        STAMP(2);
        const unsigned k8 = pkh(acc[11], acc[12]);
        *(LAS u16*)(a_k8 + 64) = (u16)k8;
        *(LAS u16*)a_k8 = (u16)(k8 >> 16);
        *(LAS unsigned*)a_k4 = pkh(acc[9], acc[10]);
        const v4u c0 = {pkh(acc[1], acc[2]), pkh(acc[3], acc[4]), pkh(acc[5], acc[6]), pkh(acc[7], acc[8])};
        *(LAS v4u*)a_kn = c0;
        *(LAS v4u*)(a_kn + KCOPY) = v4u{pkh(acc[0], acc[1]), pkh(acc[2], acc[3]), pkh(acc[4], acc[5]), pkh(acc[6], acc[7])};
        const unsigned kb = (unsigned)__builtin_amdgcn_readlane((int)pkh(acc[0], acc[9]), 63);
        *(LAS u16*)a_sp = (u16)__builtin_amdgcn_perm(kb, c0.w, sel_sp);
        STAMP(4);
        __builtin_amdgcn_s_setprio(M_PRIO);
        asm volatile(CONV_ASM_TEXT
                     : CONV_ASM_PRE(q)
                     : "{v14}"(top), "{v15}"(kinv), "{v7}"(op[0]), "{v8}"(op[1]), "{v9}"(op[2]), "{v10}"(op[3]), "{v11}"(op[4]), "{v12}"(op[5]),
                       "{v13}"(op[6])
                     : CONV_ASM_CLOBBERS);
        STAMP(6);
        __builtin_amdgcn_s_setprio(0);
      }
    }
    STAMP(7);
    if (loc >= 16) { STAMP_FLUSH(); return; }
    int lane;
    asm volatile("v_mbcnt_lo_u32_b32 %0, -1, 0\n\tv_mbcnt_hi_u32_b32 %0, -1, %0" : "=v"(lane));
    const int cl = lane & 15, g = lane >> 4, tid = wave * 64 + lane;
    v4u cw[2];
#pragma unroll
    for (int ks = 0; ks < 2; ks++) {
      const v4u w = *(const v4u*)(smem + L_WR + (64 * wave + 32 * ks + 8 * g) * 2);
      const bool one = (lane & 3) != 1;
      cw[ks] = v4u{one ? 0x3f803f80u : w.x, one ? 0x3f803f80u : w.y, one ? 0x3f803f80u : w.z, one ? 0x3f803f80u : w.w};
    }
    const float wsum = *(const float*)(smem + L_MISC + 64);
    BAR();
    float xf[2][8];
#pragma unroll
    for (int k = 0; k < 2; k++) {
      const v4u gx = *(const v4u*)(smem + L_XROW + wave * 2048 + cl * 128 + ((4 * k + g) ^ (cl & 7)) * 16);
#pragma unroll
      for (int i = 0; i < 4; i++) {
        xf[k][2 * i] = blo(gx[i]);
        xf[k][2 * i + 1] = bhi(gx[i]);
        PIN(xf[k][2 * i]);
        PIN(xf[k][2 * i + 1]);
      }
    }
    __builtin_amdgcn_s_sleep(SLEEP_H);
    v4u hv[2];
    {
      const int o0 = WS_Y + (16 * wave + 2 * g) * GSTR + rb * 128 + (cl & 7) * 16 + (cl >> 3) * 8;
#pragma unroll 1
      for (int sp = 0; sp < SPIN_CAP; sp++) {
        MEMBAR();
        const v2u a = ld2(rs, o0), b = ld2(rs, o0 + GSTR), c = ld2(rs, o0 + 8 * GSTR), d = ld2(rs, o0 + 9 * GSTR);
        hv[0] = v4u{a.x, a.y, b.x, b.y};
        hv[1] = v4u{c.x, c.y, d.x, d.y};
        if (__all(valid4(hv[0]) && valid4(hv[1]))) break;
        __builtin_amdgcn_s_sleep(2);
      }
    }
#pragma unroll
    for (int i = 0; i < 8; i++) {
      float lo = blo(hv[i >> 2][i & 3]) + xf[i >> 2][2 * (i & 3)];
      PIN(lo);
      hv[i >> 2][i & 3] = pkbf(lo, bhi(hv[i >> 2][i & 3]) + xf[i >> 2][2 * (i & 3) + 1]);
    }
    STAMP(8);
    {
      f32x4 s = {0.f, 0.f, 0.f, 0.f}, q = {0.f, 0.f, 0.f, 0.f};
      s = mfma_bf(cw[0], hv[0], s);
      q = mfma_bf(hv[0], hv[0], q);
      s = mfma_bf(cw[1], hv[1], s);
      q = mfma_bf(hv[1], hv[1], q);
      if ((cl >> 2) == g) *(f32x4*)(smem + L_RED + wave * 256 + cl * 16) = f32x4{s[0], diag(q, lane), s[1], 0.f};
    }
    BAR();
    float m, s;
    const f32x4 t = sum9(smem + L_RED + cl * 16);
    ln_round(t[0], t[1], m, s);
    const float rinv = __builtin_amdgcn_rcpf(s);
    unsigned okey;
    {
      const unsigned b = pkbf(0.f, fmaf(-m, wsum, t[2]) * rinv);
      okey = ((b & 0x80000000u) ? (~b & 0xffff0000u) : (b | 0x80000000u)) | (unsigned)(SEQ - 1 - (16 * rb + cl));
      if (wave == 1) st1(okey, rs, lane < 16 ? WS_RREC + (16 * rb + cl) * 4 : OOB);
    }
    STAMP(9);
    char* const tl = smem + wave * 4096;
    *(v4u*)(tl + cl * 144 + g * 16) = hv[0];
    *(v4u*)(tl + cl * 144 + 64 + g * 16) = hv[1];
    MEMBAR();
    v4u key = {0u, 0u, 0u, 0u};
    unsigned selmask = 0;
#pragma unroll 1
    for (int rd = 0; rd < 4; rd++) {
      const unsigned ka = row_max_u(okey);
      okey = okey == ka ? 0u : okey;
      const unsigned kb = row_max_u(okey);
      okey = okey == kb ? 0u : okey;
      const int ra = 15 - (ka & 15), rb2 = 15 - (kb & 15);
      v4u pub = *(const v4u*)(tl + ((lane & 16) ? rb2 : ra) * 144 + (lane & 7) * 16);
      {
        const float ma = rdlane(m, ra), mb = rdlane(m, rb2), ia = rdlane(rinv, ra), ib = rdlane(rinv, rb2);
        const float pm = (lane & 16) ? mb : ma, pi = (lane & 16) ? ib : ia;
#pragma unroll
        for (int i = 0; i < 4; i++) {
          const unsigned n = ln_apply(pub[i], pm, pi);
          pub[i] = nosent((lane & 8) ? pub[i] : n);
        }
        if (lane >= 32) pub.x = (lane & 16) ? kb : ka;
      }
      PIN(pub);
      if (rd == 0 && wave < 8) {
        STAMP(1);
#pragma unroll 1
        for (int sp = 0; sp < SPIN_CAP; sp++) {
          MEMBAR();
          key = ld4(rs, WS_RREC + tid * 16);
          if (__all(valid4(key))) break;
          __builtin_amdgcn_s_sleep(2);
        }
        STAMP(10);
      }
      int ca = 0, cb = 0;
#pragma unroll
      for (int i = 0; i < 4; i++) {
        ca += __popcll(__ballot(key[i] > ka));
        cb += __popcll(__ballot(key[i] > kb));
      }
      char* cnt = smem + L_RED + 2304 + (rd & 1) * 64;
      if (lane == 0) *(unsigned*)(cnt + wave * 4) = (unsigned)(ca | (cb << 16));
      BAR();
      const v4u c0 = *(const v4u*)cnt, c1 = *(const v4u*)(cnt + 16);
      const unsigned cs = __builtin_amdgcn_readfirstlane((c0.x + c0.y + c0.z + c0.w) + (c1.x + c1.y + c1.z + c1.w));
      const unsigned rka = cs & 0xffffu, rkb = cs >> 16;
      if (rka >= 8u) break;
      const unsigned rank = (lane & 16) ? rkb : rka;
      const int xo = lane < 32 ? (cl >> 3) * 1152 + wave * 128 + (cl & 7) * 16 : 2304;
      st4(pub, rs, (rank < 8u && (lane < 32 || (cl == 0 && wave == 0))) ? WS_XSEL + rank * XSLOT + xo : OOB);
      selmask |= 1u << ra;
      if (rkb >= 8u) break;
      selmask |= 1u << rb2;
    }
    STAMP(3);
#pragma unroll
    for (int i = 0; i < 2; i++) {
      const int r = 8 * i + (lane >> 3);
      const v4u hh = *(const v4u*)(tl + r * 144 + (lane & 7) * 16);
      const float pm = __shfl(m, r), pi = __shfl(rinv, r);
      v4u o;
#pragma unroll
      for (int k = 0; k < 4; k++) o[k] = addbf(hh[k], ln_apply(hh[k], pm, pi));
      st4(o, ro, ((selmask >> r) & 1u) ? OOB : (16 * rb + r) * 1152 + wave * 128 + (lane & 7) * 16);
    }
    STAMP_FLUSH();
    return;
  }

  const bool ffn = loc < 27;
  v2u G[2][8];
  const rsrc_t rg = __builtin_amdgcn_make_buffer_rsrc((void*)p.wg, 0, ffn ? DM * FF * 2 : 0, 0x00020000);
  const int wo = (64 * wave + 8 * g) * (FF * 2) + 128 * fj + 8 * cl;
#pragma unroll
  for (int j = 0; j < W_EARLY; j++) G[j >> 3][j & 7] = ldp2(rg, wo, (32 * (j >> 3) + (j & 7)) * (FF * 2));
  SCHED();
  LAS char* xw = (LAS char*)smem + wave * 4608 + (cl >> 1) * 288 + g * 32 + (cl & 1) * 16;
  LAS char* xr_ = (LAS char*)smem + wave * 4608 + ((lane >> 2) & 3) * 288 + (lane & 1) * 128 + g * 16 + (lane & 2) * 4;
  LAS char* rw = (LAS char*)smem + L_RED + wave * 256 + cl * 8;
  int uo = WS_U + (16 * wave + (lane >> 3)) * GSTR + tile * 128 + (lane & 7) * 16;
  v4u ones = {0x3f803f80u, 0x3f803f80u, 0x3f803f80u, 0x3f803f80u};
  LAS char* sa = (LAS char*)smem + L_MISC + (lane & 4) * 8;
  unsigned keep = 0xfff8fff8u;
  PIN(xw); PIN(xr_); PIN(rw); PIN(uo); PIN(ones); PIN(sa); PIN(keep);
  SCHED();
  FIRST_WAIT(W_EARLY + 2);
  const unsigned tag = __builtin_amdgcn_readfirstlane(epoch) + 1u;
  v4u tb = tag_bits(tag);
  asm volatile("" : "+s"(tb));
  const bool fin = wave == 1 || (wave == 2 && loc >= 30);
  SCHED();
  FIRST_WAIT(W_EARLY);
  PIN(cpf);
  {
    const f32x4 z4 = {0.f, 0.f, 0.f, 0.f};
    auto sums = [&](v4u a, v4u b, int tt) {
      const f32x4 s0 = mfma_bf(ones, a, z4), q0 = mfma_bf(a, a, z4), s1 = mfma_bf(ones, b, z4), q1 = mfma_bf(b, b, z4);
      *(LAS v4u*)(xw + tt * 2304) = a;
      *(LAS v4u*)(xw + tt * 2304 + 128) = b;
      if ((cl >> 2) == g) {
        *(LAS f32x2*)(rw + tt * 2304) = f32x2{s0[0], diag(q0, lane)};
        *(LAS f32x2*)(rw + tt * 2304 + 128) = f32x2{s1[0], diag(q1, lane)};
      }
    };
    sums(ga, gb, 0);
    if (loc >= 30) sums(gc, gd, 1);
    STAMP(10);
    BAR();
    STAMP(11);
    if (fin) {
      __builtin_amdgcn_s_setprio(3);
      const f32x4 t = sum9(smem + L_RED + (wave - 1) * 2304 + cl * 16);
      float m, sd;
      ln_round(t[0] + t[2], t[1] + t[3], m, sd);
      const float rinv = __builtin_amdgcn_rcpf(sd);
      *(float*)(smem + L_MISC + (wave - 1) * 128 + (lane & 31) * 4) = (lane & 16) ? -m * rinv : rinv;
      BAR();
    }
#pragma unroll 1
    for (int tt = 0; tt < 2; tt++) {
      f32x2 xt[2][4];
#pragma unroll
      for (int i = 0; i < 4; i++) {
        const v2u v = ldtr(xr_ + tt * 2304 + (i >> 1) * 64 + (i & 1) * 1152);
        xt[i >> 1][2 * (i & 1)] = f32x2{blo(v.x), bhi(v.x)};
        xt[i >> 1][2 * (i & 1) + 1] = f32x2{blo(v.y), bhi(v.y)};
      }
#pragma unroll
      for (int i = 0; i < 8; i++) PIN(xt[i >> 2][i & 3]);
      if (tt == 0 && !fin) BAR();
      if (tt == 0) STAMP(12);
      const LAS f32x4* st = (const LAS f32x4*)(sa + tt * 128);
      const f32x4 r0 = st[0], r1 = st[1], c0 = st[4], c1 = st[5];
      const f32x2 rr[4] = {r0.xy, r0.zw, r1.xy, r1.zw}, cc[4] = {c0.xy, c0.zw, c1.xy, c1.zw};
#pragma unroll
      for (int i = 0; i < 2; i++) {
        v4u o;
#pragma unroll
        for (int k = 0; k < 4; k++) o[k] = (pkh2(__builtin_elementwise_fma(xt[i][k], rr[k], cc[k])) & keep) | tb[k];
        __builtin_amdgcn_raw_buffer_store_b128(o, rs, uo, i * 8 * GSTR, SC1);
      }
      if (loc < 30) break;
      uo += 16 * 128;
    }
    __builtin_amdgcn_s_setprio(0);
    BAR();
    STAMP(1);
    if (!ffn) {
      const GAS char* dn = p.wd + (size_t)xcd * 5184 * 128;
      unsigned pf[2];
#pragma unroll
      for (int i = 0; i < 2; i++) pf[i] = *(const GAS unsigned*)(dn + min((i * 5 + loc - 27) * NT + tid, 5183) * 128);
#pragma unroll
      for (int i = 0; i < 2; i++) asm volatile("" ::"v"(pf[i]));
      STAMP_FLUSH();
      return;
    }
  }
  v4u FG[2][4], FU[2][4], FD[2][4];
  {
    v2u U[2][8], Dn[2][8];
    const rsrc_t ru = __builtin_amdgcn_make_buffer_rsrc((void*)p.wu, 0, DM * FF * 2, 0x00020000);
    const rsrc_t rd = __builtin_amdgcn_make_buffer_rsrc((void*)p.wd, 0, DM * FF * 2, 0x00020000);
    const int wdo = (64 * fj + 8 * g) * (DM * 2) + 128 * wave + 8 * cl;
#pragma unroll
    for (int j = W_EARLY; j < 16; j++) G[j >> 3][j & 7] = ldp2(rg, wo, (32 * (j >> 3) + (j & 7)) * (FF * 2));
#pragma unroll
    for (int j = 0; j < 16; j++) U[j >> 3][j & 7] = ldp2(ru, wo, (32 * (j >> 3) + (j & 7)) * (FF * 2));
    BAR();
#pragma unroll
    for (int j = 0; j < 16; j++) Dn[j >> 3][j & 7] = ldp2(rd, wdo, (32 * (j >> 3) + (j & 7)) * (DM * 2));
    st4(sent4, rs, WS_PART + fj * 18432 + tid * 16);
    st4(sent4, rs, WS_PART + fj * 18432 + 9216 + tid * 16);
    SCHED();
#pragma unroll
    for (int ks = 0; ks < 2; ks++)
#pragma unroll
      for (int c = 0; c < 4; c++) {
#pragma unroll
        for (int i = 0; i < 4; i++) {
          const unsigned sel = (c & 1) ? 0x07060302u : 0x05040100u;
          FG[ks][c][i] = __builtin_amdgcn_perm(G[ks][2 * i + 1][c >> 1], G[ks][2 * i][c >> 1], sel);
          FU[ks][c][i] = __builtin_amdgcn_perm(U[ks][2 * i + 1][c >> 1], U[ks][2 * i][c >> 1], sel);
          FD[ks][c][i] = __builtin_amdgcn_perm(Dn[ks][2 * i + 1][c >> 1], Dn[ks][2 * i][c >> 1], sel);
        }
        PIN(FG[ks][c]);
        PIN(FU[ks][c]);
        PIN(FD[ks][c]);
      }
  }
  STAMP(2);
  constexpr int F_GU = 1024, F_ALL = 2048, F_RED = 16384;
  if (wave == 0) sweep_init(rs, tag, lane, 0, NCONV);
  DRAIN();
  BAR();
  if (tid == 0) st1(tag, rs, WS_INIT + (NCONV + fj) * 64);
  STAMP(3);
  v4u xa[2];
  {
    v4u xv;
    const int o0 = WS_XSEL + (lane >> 3) * XSLOT + wave * 128 + (lane & 7) * 16;
#pragma unroll 1
    for (int sp = 0; sp < SPIN_CAP; sp++) {
      MEMBAR();
      xv = ld4(rs, o0);
      if (__all(valid4(xv))) break;
      __builtin_amdgcn_s_sleep(SLEEP_X);
    }
    STAMP(6);
    char* xs = smem + F_ALL + wave * 1152;
    *(v4u*)(xs + (lane >> 3) * 144 + (lane & 7) * 16) = xv;
    MEMBAR();
    xa[0] = *(const v4u*)(xs + (lane & 7) * 144 + g * 16);
    xa[1] = *(const v4u*)(xs + (lane & 7) * 144 + 64 + g * 16);
  }
#pragma unroll
  for (int c = 0; c < 4; c++) {
    f32x4 ag = {0.f, 0.f, 0.f, 0.f}, au = {0.f, 0.f, 0.f, 0.f};
#pragma unroll
    for (int ks = 0; ks < 2; ks++) {
      ag = mfma_bf(xa[ks], FG[ks][c], ag);
      au = mfma_bf(xa[ks], FU[ks][c], au);
    }
    *(f32x4*)(smem + F_RED + wave * 4096 + c * 1024 + lane * 16) = g < 2 ? ag : au;
  }
  BAR();
  STAMP(4);
  const int tk = fj / 9, cb = fj - tk * 9;
  if (wave < 8) {
    const int c = tid >> 7, wi = tid & 127;
    float gv[9], uv[9];
#pragma unroll
    for (int w = 0; w < 9; w++) {
      gv[w] = *(const float*)(smem + F_RED + w * 4096 + c * 1024 + wi * 4);
      uv[w] = *(const float*)(smem + F_RED + w * 4096 + c * 1024 + 512 + wi * 4);
    }
    SCHED();
    float gs = gv[0], us = uv[0];
#pragma unroll
    for (int w = 1; w < 9; w++) { gs += gv[w]; us += uv[w]; }
    const unsigned gu = pkbf(gs, us);
    const float gp = blo(gu);
    const float gq = rbf(gp * __builtin_amdgcn_rcpf(1.0f + __expf(-gp)));
    const int tok = (wi >> 6) * 4 + (wi & 3), col = ((wi >> 2) & 15);
    *(u16*)(smem + F_GU + tok * 128 + (col * 4 + c) * 2) = (u16)pkbf(gq * bhi(gu), 0.f);
  }
  BAR();
  STAMP(5);
  {
    const v4u a0 = *(const v4u*)(smem + F_GU + (lane & 7) * 128 + g * 16);
    const v4u a1 = *(const v4u*)(smem + F_GU + (lane & 7) * 128 + 64 + g * 16);
    f32x4 o[4];
#pragma unroll
    for (int c = 0; c < 4; c++) {
      o[c] = f32x4{0.f, 0.f, 0.f, 0.f};
      o[c] = mfma_bf(a0, FD[0][c], o[c]);
      o[c] = mfma_bf(a1, FD[1][c], o[c]);
    }
#pragma unroll
    for (int i = 0; i < 2; i++) {
      v4u v;
#pragma unroll
      for (int c = 0; c < 4; c++) v[c] = nosent(__float_as_uint(g < 2 ? o[c][i] : o[c][2 + i]));
      const int tok = 4 * (g & 1) + 2 * (g >> 1) + i;
      st4(v, rs, WS_PART + fj * 18432 + (tok * DM + 64 * wave + 4 * cl) * 4);
    }
  }
  STAMP(7);
  v2u fh = {0u, 0u}, fw = {0u, 0u};
  unsigned fkey = 0;
  if (wave == 8) {
    const int xo = WS_XSEL + tk * XSLOT + cb * 128 + cl * 8;
#pragma unroll 1
    for (int sp = 0; sp < SPIN_CAP; sp++) {
      MEMBAR();
      fw = ld2(rs, xo);
      fh = ld2(rs, xo + 1152);
      fkey = ld1(rs, WS_XSEL + tk * XSLOT + 2304);
      if (__all(valid4(v4u{fw.x, fw.y, fh.x, fh.y}) && fkey != SENT)) break;
    }
  } else if (wave == 7) {
    sweep_init(rs, tag, lane, NCONV, NFFN);
  } else {
    __builtin_amdgcn_s_sleep(SLEEP_P);
  }
  BAR();
  {
    const int o0 = WS_PART + (tid >> 4) * 18432 + (tk * DM + cb * 64 + cl * 4) * 4;
    v4u a, b;
#pragma unroll 1
    for (int sp = 0; sp < SPIN_CAP; sp++) {
      MEMBAR();
      a = ld4(rs, o0);
      b = ld4(rs, o0 + 36 * 18432);
      if (__all(valid4(a) && valid4(b))) break;
      __builtin_amdgcn_s_sleep(2);
    }
    *(f32x4*)(smem + F_ALL + tid * 16) = __builtin_bit_cast(f32x4, a) + __builtin_bit_cast(f32x4, b);
  }
  STAMP(8);
  BAR();
  if (wave == 8) {
    f32x4 s = *(const f32x4*)(smem + F_ALL + lane * 16);
#pragma unroll
    for (int i = 1; i < 9; i++) s += *(const f32x4*)(smem + F_ALL + i * 1024 + lane * 16);
#pragma unroll
    for (int i = 0; i < 4; i++) {
      s[i] += __shfl_xor(s[i], 16);
      s[i] += __shfl_xor(s[i], 32);
    }
    const int frow = SEQ - 1 - (int)(fkey & 0x7ffu);
    const unsigned kb = fkey & 0xffff0000u;
    const float rv = __uint_as_float((kb & 0x80000000u) ? (kb & 0x7fffffffu) : (~kb & 0xffff0000u));
    const float sgm = rbf(__builtin_amdgcn_rcpf(1.0f + __expf(-rv)));
    const unsigned ya = pkbf(s[0], s[1]), yb = pkbf(s[2], s[3]);
    const unsigned ta = pkbf(blo(ya) * sgm, bhi(ya) * sgm), tb = pkbf(blo(yb) * sgm, bhi(yb) * sgm);
    const v2u o = {addbf(fh.x, addbf(fw.x, ta)), addbf(fh.y, addbf(fw.y, tb))};
    st2(o, ro, lane < 16 ? frow * 1152 + cb * 128 + cl * 8 : OOB);
    if (fj == 0 && lane == 0) st1(tag, rs, WS_EPOCH);
  }
  STAMP(9);
  STAMP_FLUSH();
}

}

extern "C" void kernel_launch(void* const* d_in, const int* in_sizes, int n_in, void* d_out, int out_size,
                              void* d_ws, size_t ws_size, hipStream_t stream) {
  (void)hipFuncSetAttribute((const void*)k_main, hipFuncAttributeMaxDynamicSharedMemorySize, LDS_BYTES);
  Params p;
  p.x = (const u16*)d_in[0];
  p.lar = (const u16*)d_in[1];
  p.aim = (const u16*)d_in[2];
  p.crl = (const u16*)d_in[3];
  p.cim = (const u16*)d_in[4];
  p.ldt = (const u16*)d_in[5];
  p.dskip = (const u16*)d_in[6];
  p.wr = (const u16*)d_in[8];
  p.wg = (const u16*)d_in[9];
  p.wu = (const u16*)d_in[10];
  p.wd = (const u16*)d_in[11];
  p.out = (u16*)d_out;
  p.ws = (char*)d_ws;
  k_main<<<256, NT, LDS_BYTES, stream>>>(p);
}
```

```cpp
#include <hip/hip_runtime.h>
#include <stdint.h>
#pragma clang diagnostic ignored "-Wint-to-pointer-cast"

namespace {

constexpr int SEQ = 2048, DM = 576, NM = 128, FF = 4608;
constexpr int NT = 576;
constexpr int NCONV = 144, NFFN = 72;
constexpr unsigned SENT = 0xFFFFFFFFu;
constexpr int SC1 = 16;
constexpr int SPIN_CAP = 1 << 16;
#define SLEEP_X 4
#define GPAD 0
#define PPAD 0
#define D_CONV 0
#define T_Y3 32
#define T_Y2 20
#define T_KEY 196
#define CANARY 7168
#define T_SEL 40
#define CODE_BYTES 20480
#define H_PRIO 3
#ifndef M_PRIO
#define M_PRIO 1
#endif
#define S_PRIO 3
#define PF_MODE 2
#define PF_N 2
#define PF_SLEEP 0
#define SLEEP_U 5
#define SLEEP_U7 4
#define T_PART 40

typedef unsigned short u16;
typedef unsigned v4u __attribute__((ext_vector_type(4)));
typedef unsigned v2u __attribute__((ext_vector_type(2)));
typedef float f32x2 __attribute__((ext_vector_type(2)));
typedef float f32x4 __attribute__((ext_vector_type(4)));
typedef float f32x16 __attribute__((ext_vector_type(16)));
typedef _Float16 f16x2 __attribute__((ext_vector_type(2)));
typedef _Float16 f16x8 __attribute__((ext_vector_type(8)));
typedef __bf16 bf16x2 __attribute__((ext_vector_type(2)));
typedef __bf16 bf16x8 __attribute__((ext_vector_type(8)));
typedef __amdgpu_buffer_rsrc_t rsrc_t;

constexpr int WS_EPOCH = 0;
constexpr int WS_INIT = 4096;
constexpr int WS_RREC = WS_INIT + 256 * 64;
constexpr int WS_XSEL = WS_RREC + SEQ * 4;
constexpr int XSLOT = 2560;
constexpr int GSTR = 16384 + GPAD;
constexpr int WS_U = 65536;
constexpr int WS_Y = WS_U + NCONV * GSTR;
constexpr int WS_PART = WS_Y + NCONV * GSTR;
constexpr int PSTR = 8 * DM * 4 + PPAD;
constexpr int WS_END = WS_PART + NFFN * PSTR;
constexpr int OOB = 0x7ffffff0;
static_assert(WS_XSEL + 8 * XSLOT <= WS_U, "ws map");

constexpr int WSCR = 16384;
constexpr int L_KN = 4096;
constexpr int KCOPY = 1216;
constexpr int L_K4 = L_KN + 2 * KCOPY;
constexpr int L_K8 = L_K4 + 512;
constexpr int L_Y = 8192;
constexpr int L_DUMMY = 12800;
constexpr int L_XROW = 4 * WSCR;
constexpr int L_UIMG = 8 * WSCR;
constexpr int USTR = 4640;
constexpr int L_FILT = L_UIMG + 4 * USTR;
constexpr int L_MISC = L_FILT + 4096;
constexpr int L_WR = L_MISC + 256;
constexpr int L_RED = L_WR + 1152;
constexpr int LDS_BYTES = L_RED + 2 * 2304;
static_assert(L_KN + 224 * 16 <= L_Y, "lds map");
static_assert(LDS_BYTES <= 160 * 1024, "lds size");

#define MEMBAR() asm volatile("" ::: "memory")
#define BAR() do { __builtin_amdgcn_sched_barrier(0); asm volatile("s_waitcnt lgkmcnt(0)\n\ts_barrier" ::: "memory"); __builtin_amdgcn_sched_barrier(0); } while (0)
#define LAS __attribute__((address_space(3)))
#define DRAIN() asm volatile("s_waitcnt vmcnt(0)" ::: "memory")
#define LGKM0() asm volatile("s_waitcnt lgkmcnt(0)" ::: "memory")
#define PIN(x) asm volatile("" : "+v"(x))
#define SCHED() __builtin_amdgcn_sched_barrier(0)

#define STAMP(i) do {} while (0)
#define STAMP_INIT() do {} while (0)
#define STAMP_FLUSH() do {} while (0)

__device__ __forceinline__ float blo(unsigned v) { return __uint_as_float(v << 16); }
__device__ __forceinline__ float bhi(unsigned v) { return __uint_as_float(v & 0xffff0000u); }
__device__ __forceinline__ unsigned pkbf(float a, float b) {
  const f32x2 f = {a, b};
  return __builtin_bit_cast(unsigned, __builtin_convertvector(f, bf16x2));
}
__device__ __forceinline__ float rbf(float f) { return blo(pkbf(f, f)); }
__device__ __forceinline__ unsigned pkh(float a, float b) {
  const f32x2 f = {a, b};
  return __builtin_bit_cast(unsigned, __builtin_convertvector(f, f16x2));
}
__device__ __forceinline__ unsigned nosent(unsigned v) { return min(v, 0xFFFFFFFEu); }
__device__ __forceinline__ bool valid4(v4u v) { return max(max(v.x, v.y), max(v.z, v.w)) != SENT; }

__device__ __forceinline__ bool valid8(v4u a, v4u b) { return max(max(max(max(a.x, a.y), a.z), max(max(a.w, b.x), b.y)), max(b.z, b.w)) != SENT; }

template <int CTRL, int RM> __device__ __forceinline__ float dppf(float v) {
  return __int_as_float(__builtin_amdgcn_update_dpp(0, __float_as_int(v), CTRL, RM, 0xf, false));
}
template <int CTRL, int RM> __device__ __forceinline__ unsigned dppu(unsigned v) {
  return (unsigned)__builtin_amdgcn_update_dpp(0, (int)v, CTRL, RM, 0xf, false);
}
__device__ __forceinline__ float wave_sum(float v) {
  v += dppf<0x111, 0xf>(v);
  v += dppf<0x112, 0xf>(v);
  v += dppf<0x114, 0xf>(v);
  v += dppf<0x118, 0xf>(v);
  v += dppf<0x142, 0xa>(v);
  v += dppf<0x143, 0xc>(v);
  return __int_as_float(__builtin_amdgcn_readlane(__float_as_int(v), 63));
}
__device__ __forceinline__ unsigned wave_max_u(unsigned v) {
  v = max(v, dppu<0x111, 0xf>(v));
  v = max(v, dppu<0x112, 0xf>(v));
  v = max(v, dppu<0x114, 0xf>(v));
  v = max(v, dppu<0x118, 0xf>(v));
  const unsigned a = (unsigned)__builtin_amdgcn_readlane((int)v, 15), b = (unsigned)__builtin_amdgcn_readlane((int)v, 31);
  const unsigned c = (unsigned)__builtin_amdgcn_readlane((int)v, 47), d = (unsigned)__builtin_amdgcn_readlane((int)v, 63);
  return max(max(a, b), max(c, d));
}

__device__ __forceinline__ unsigned row_max_u(unsigned v) {
  v = max(v, dppu<0x111, 0xf>(v));
  v = max(v, dppu<0x112, 0xf>(v));
  v = max(v, dppu<0x114, 0xf>(v));
  v = max(v, dppu<0x118, 0xf>(v));
  return (unsigned)__builtin_amdgcn_readlane((int)v, 15);
}
__device__ __forceinline__ float rdlane(float v, int l) { return __int_as_float(__builtin_amdgcn_readlane(__float_as_int(v), l)); }

__device__ __forceinline__ v4u ld4(rsrc_t rs, int off) { return __builtin_amdgcn_raw_buffer_load_b128(rs, off, 0, SC1); }
__device__ __forceinline__ v2u ld2(rsrc_t rs, int off) { return __builtin_amdgcn_raw_buffer_load_b64(rs, off, 0, SC1); }
__device__ __forceinline__ unsigned ld1(rsrc_t rs, int off) { return __builtin_amdgcn_raw_buffer_load_b32(rs, off, 0, SC1); }
__device__ __forceinline__ v2u ldp2(rsrc_t rs, int off, int soff) { return __builtin_amdgcn_raw_buffer_load_b64(rs, off, soff, 0); }
__device__ __forceinline__ void st4(v4u v, rsrc_t rs, int off) { __builtin_amdgcn_raw_buffer_store_b128(v, rs, off, 0, SC1); }
__device__ __forceinline__ void st2(v2u v, rsrc_t rs, int off) { __builtin_amdgcn_raw_buffer_store_b64(v, rs, off, 0, SC1); }
__device__ __forceinline__ void st1(unsigned v, rsrc_t rs, int off) { __builtin_amdgcn_raw_buffer_store_b32(v, rs, off, 0, SC1); }

__device__ __forceinline__ f32x4 mfma_bf(v4u a, v4u b, f32x4 c) {
  return __builtin_amdgcn_mfma_f32_16x16x32_bf16(__builtin_bit_cast(bf16x8, a), __builtin_bit_cast(bf16x8, b), c, 0, 0, 0);
}
__device__ __forceinline__ f32x4 mfma_h(v4u a, v4u b, f32x4 c) {
  return __builtin_amdgcn_mfma_f32_16x16x32_f16(__builtin_bit_cast(f16x8, a), __builtin_bit_cast(f16x8, b), c, 0, 0, 0);
}

__device__ __forceinline__ float diag(f32x4 q, int lane) {
  const float qa = (lane & 1) ? q[1] : q[0], qb = (lane & 1) ? q[3] : q[2];
  return (lane & 2) ? qb : qa;
}
__device__ __forceinline__ f32x4 sum9(const LAS char* red) {
  f32x4 t[9];
#pragma unroll
  for (int w = 0; w < 9; w++) t[w] = *(const LAS f32x4*)(red + w * 256);
  SCHED();
  return (((t[0] + t[1]) + (t[2] + t[3])) + ((t[4] + t[5]) + (t[6] + t[7]))) + t[8];
}
__device__ __forceinline__ void ln_round(float sum, float sq, float& m, float& s) {
  const float mean = sum * (1.0f / DM);
  const float var = fmaxf(fmaf(-mean, mean, sq * (1.0f / DM)), 0.f);
  const unsigned mv = pkbf(mean, var);
  m = blo(mv);
  s = rbf(__builtin_amdgcn_sqrtf(rbf(bhi(mv) + 1e-5f)));
}
__device__ __forceinline__ unsigned ln_apply(unsigned xp, float m, float rinv) {
  const unsigned d = pkbf(blo(xp) - m, bhi(xp) - m);
  return pkbf(blo(d) * rinv, bhi(d) * rinv);
}
__device__ __forceinline__ unsigned addbf(unsigned a, unsigned b) { return pkbf(blo(a) + blo(b), bhi(a) + bhi(b)); }

__device__ __forceinline__ unsigned now() { return (unsigned)__builtin_amdgcn_s_memrealtime(); }
__device__ __forceinline__ void wait_until(unsigned t0, unsigned ticks) {
#pragma unroll 1
  for (int sp = 0; sp < 256 && now() - t0 < ticks; sp++) __builtin_amdgcn_s_sleep(1);
}

__device__ __forceinline__ void sweep_init(rsrc_t rs, unsigned tag, int lane, int first, int n) {
#pragma unroll 1
  for (int sp = 0; sp < SPIN_CAP; sp++) {
    MEMBAR();
    bool ok = true;
#pragma unroll
    for (int k = 0; k < 3; k++) ok &= ld1(rs, WS_INIT + (first + min(lane + 64 * k, n - 1)) * 64) == tag;
    if (__all(ok)) break;
    __builtin_amdgcn_s_sleep(4);
  }
}

typedef short s16x4 __attribute__((ext_vector_type(4)));
__device__ __forceinline__ v2u ldtr(const LAS char* a) {
  return __builtin_bit_cast(v2u, __builtin_amdgcn_ds_read_tr16_b64_v4i16((LAS s16x4*)a));
}
__device__ __forceinline__ v4u tag_bits(unsigned tag) {
  v4u t;
#pragma unroll
  for (int k = 0; k < 4; k++) t[k] = ((tag >> (6 * k)) & 7u) | (((tag >> (6 * k + 3)) & 7u) << 16);
  return t;
}

template <int N>
__device__ __forceinline__ void fetch_u(rsrc_t rs, int src, char* dst, const int (&chunk)[N], unsigned tag) {
  v4u v[N];
#pragma unroll
  for (int i = 0; i < N; i++) v[i] = __builtin_amdgcn_raw_buffer_load_b128(rs, src, chunk[i] * 1024, SC1);
  SCHED();
  const v4u tb = tag_bits(tag);
#pragma unroll 1
  for (int sp = 0; sp < SPIN_CAP; sp++) {
    v4u o = {0u, 0u, 0u, 0u};
#pragma unroll
    for (int i = 0; i < N; i++) o |= v[i] ^ tb;
    if (__all(((o.x | o.y | o.z | o.w) & 0x70007u) == 0u)) break;
    __builtin_amdgcn_s_sleep(2);
    MEMBAR();
#pragma unroll
    for (int i = 0; i < N; i++) v[i] = __builtin_amdgcn_raw_buffer_load_b128(rs, src, chunk[i] * 1024, SC1);
  }
#pragma unroll
  for (int i = 0; i < N; i++) {
    const v4u w = v[i] ^ (tb ^ 0x40004u);
    v2u* const up = (v2u*)(dst + chunk[i] * 256);
    up[0] = v2u{w.x, w.y};
    up[1] = v2u{w.z, w.w};
  }
}

struct cz { f32x2 v, s; };
__device__ __forceinline__ cz mkz(f32x2 v) {
  cz z;
  z.v = v;
  asm("v_pk_mov_b32 %0, %1, %1 op_sel:[1,0]" : "=v"(z.s) : "v"(v));
  return z;
}
#define CMUL_TXT(r, a, b, bs) "v_pk_mul_f32 " r ", " a ", " bs " op_sel:[1,0] neg_lo:[0,1]\n\tv_pk_fma_f32 " r ", " a ", " b ", " r " op_sel_hi:[0,1,1]\n\t"
__device__ __forceinline__ f32x2 cmul(f32x2 a, cz b) {
  f32x2 r;
  asm(CMUL_TXT("%0", "%1", "%2", "%3") : "=&v"(r) : "v"(a), "v"(b.v), "v"(b.s));
  return r;
}
__device__ __forceinline__ cz csq(cz a) { return mkz(cmul(a.v, a)); }
#define MC_(r, a, bs) "v_pk_mul_f32 " r ", " a ", " bs " op_sel:[1,0]\n\t"
#define FC_(r, a, b) "v_pk_fma_f32 " r ", " a ", " b ", " r " op_sel_hi:[0,1,1] neg_hi:[0,1,0]\n\t"
__device__ __forceinline__ void cmulc3(f32x2 (&r)[3], unsigned (&h)[3], f32x2 a0, cz b0, f32x2 a1, cz b1, f32x2 a2, cz b2) {
  asm(MC_("%0", "%3", "%5") MC_("%1", "%6", "%8") MC_("%2", "%9", "%11") FC_("%0", "%3", "%4") FC_("%1", "%6", "%7") FC_("%2", "%9", "%10")
      : "=&v"(r[0]), "=&v"(r[1]), "=&v"(r[2])
      : "v"(a0), "v"(b0.v), "v"(b0.s), "v"(a1), "v"(b1.v), "v"(b1.s), "v"(a2), "v"(b2.v), "v"(b2.s));
#pragma unroll
  for (int i = 0; i < 3; i++) h[i] = pkh(r[i].x, r[i].y);
}
__device__ __forceinline__ void hmul2(unsigned& r, unsigned& q, unsigned a, unsigned c, unsigned b, unsigned bs) {
  asm("v_pk_mul_f16 %0, %2, %5 op_sel:[1,0] neg_lo:[0,1]\n\tv_pk_mul_f16 %1, %3, %5 op_sel:[1,0] neg_lo:[0,1]\n\t"
      "v_pk_fma_f16 %0, %2, %4, %0 op_sel_hi:[0,1,1]\n\tv_pk_fma_f16 %1, %3, %4, %1 op_sel_hi:[0,1,1]"
      : "=&v"(r), "=&v"(q) : "v"(a), "v"(c), "v"(b), "v"(bs));
}
__device__ __forceinline__ float exp_c(float v) {
  const float t = v * 1.44269502f;
  const float e = fmaf(v, 1.92596303e-8f, fmaf(v, 1.44269502f, -t));
  return __builtin_amdgcn_exp2f(t) * fmaf(e, 0.693147182f, 1.f);
}
__device__ __forceinline__ f32x2 cexp_p2(float p, float ch, float ph, float pl) {
  const float mag = __builtin_amdgcn_exp2f(p * ch);
  const float fr = fmaf(p, pl, __builtin_amdgcn_fractf(p * ph));
  return f32x2{mag * __builtin_amdgcn_cosf(fr), mag * __builtin_amdgcn_sinf(fr)};
}
__device__ __forceinline__ unsigned pkh2(f32x2 v) { return pkh(v.x, v.y); }

#define CONV_ASM_TEXT \
  "s_barrier\n" "ds_read_b128 v[32:35], %8 offset:480\n" \
  "ds_read_b128 v[36:39], %8 offset:992\n" "ds_read_b128 v[40:43], %8 offset:1504\n" \
  "ds_read2_b32 v[44:45], %11 offset0:48 offset1:49\n" "ds_read2_b32 v[46:47], %11 offset0:50 offset1:51\n" \
  "ds_read2_b32 v[48:49], %11 offset0:32 offset1:33\n" "s_waitcnt lgkmcnt(5)\n" \
  "v_mfma_f32_16x16x32_f16 v[132:135], v[32:35], v[16:19], 0\n" "s_nop 0\n" \
  "ds_read2_b32 v[50:51], %11 offset0:34 offset1:35\n" "ds_read2_b32 v[52:53], %11 offset0:16 offset1:17\n" \
  "v_mfma_f32_16x16x32_f16 v[128:131], v[32:35], v[20:23], 0\n" "s_waitcnt lgkmcnt(6)\n" \
  "ds_read2_b32 v[54:55], %11 offset0:18 offset1:19\n" "ds_read2_b32 v[56:57], %11 offset0:0 offset1:1\n" \
  "v_mfma_f32_16x16x32_f16 v[124:127], v[36:39], v[16:19], 0\n" "s_nop 0\n" \
  "ds_read2_b32 v[58:59], %11 offset0:2 offset1:3\n" "ds_read_b128 v[60:63], %8 offset:2016\n" \
  "v_mfma_f32_16x16x32_f16 v[120:123], v[36:39], v[20:23], 0\n" "s_waitcnt lgkmcnt(9)\n" \
  "v_cvt_pk_f16_f32 v116, v132, v128\n" "v_cvt_pk_f16_f32 v117, v133, v129\n" \
  "v_mfma_f32_16x16x32_f16 v[164:167], v[40:43], v[16:19], 0\n" "v_cvt_pk_f16_f32 v118, v134, v130\n" \
  "v_cvt_pk_f16_f32 v119, v135, v131\n" "v_mfma_f32_16x16x32_f16 v[160:163], v[40:43], v[20:23], 0\n" \
  "s_waitcnt lgkmcnt(7)\n" "ds_read_b128 v[64:67], %9 offset:480\n" \
  "v_cvt_pk_f16_f32 v112, v124, v120\n" "v_mfma_f32_16x16x32_f16 v[148:151], v[116:119], v[44:47], 0\n" \
  "s_waitcnt lgkmcnt(6)\n" "ds_read_b128 v[68:71], %9 offset:736\n" \
  "v_cvt_pk_f16_f32 v113, v125, v121\n" "v_mfma_f32_16x16x32_f16 v[144:147], v[116:119], v[48:51], 0\n" \
  "s_waitcnt lgkmcnt(5)\n" "v_cvt_pk_f16_f32 v114, v126, v122\n" \
  "v_cvt_pk_f16_f32 v115, v127, v123\n" "v_mfma_f32_16x16x32_f16 v[140:143], v[116:119], v[52:55], 0\n" \
  "s_waitcnt lgkmcnt(3)\n" "ds_read_b128 v[72:75], %9 offset:992\n" \
  "ds_read_b64 v[76:77], %10 offset:256\n" "v_mfma_f32_16x16x32_f16 v[136:139], v[116:119], v[56:59], 0\n" \
  "s_waitcnt lgkmcnt(4)\n" "ds_read_b64 v[78:79], %10 offset:264\n" \
  "v_cvt_pk_f16_f32 v116, v164, v160\n" "v_mfma_f32_16x16x32_f16 v[132:135], v[60:63], v[16:19], 0\n" \
  "v_cvt_pk_f16_f32 v117, v165, v161\n" "v_cvt_pk_f16_f32 v118, v166, v162\n" \
  "v_mfma_f32_16x16x32_f16 v[128:131], v[60:63], v[20:23], 0\n" "ds_read_b64 v[80:81], %10 offset:128\n" \
  "v_cvt_pk_f16_f32 v119, v167, v163\n" "v_mfma_f32_16x16x32_f16 v[144:147], v[112:115], v[44:47], v[144:147]\n" \
  "ds_read_b64 v[82:83], %10 offset:136\n" "v_mfma_f32_16x16x32_f16 v[140:143], v[112:115], v[48:51], v[140:143]\n" \
  "s_nop 0\n" "ds_read_b64 v[84:85], %10 offset:0\n" \
  "ds_read_b64 v[86:87], %10 offset:8\n" "v_mfma_f32_16x16x32_f16 v[136:139], v[112:115], v[52:55], v[136:139]\n" \
  "s_waitcnt lgkmcnt(7)\n" "ds_read_b128 v[88:91], %9 offset:1248\n" \
  "v_cvt_pk_f16_f32 v112, v132, v128\n" "v_mfma_f32_16x16x32_f16 v[124:127], v[64:67], v[24:27], 0\n" \
  "v_cvt_pk_f16_f32 v113, v133, v129\n" "v_cvt_pk_f16_f32 v114, v134, v130\n" \
  "v_mfma_f32_16x16x32_f16 v[120:123], v[64:67], v[28:31], 0\n" "ds_read_b64 v[92:93], %10 offset:320\n" \
  "v_cvt_pk_f16_f32 v115, v135, v131\n" "v_mfma_f32_16x16x32_f16 v[140:143], v[116:119], v[44:47], v[140:143]\n" \
  "ds_read_b64 v[94:95], %10 offset:328\n" "v_mfma_f32_16x16x32_f16 v[136:139], v[116:119], v[48:51], v[136:139]\n" \
  "s_nop 0\n" "ds_read_b64 v[96:97], %10 offset:192\n" \
  "ds_read_b64 v[98:99], %10 offset:200\n" "v_mfma_f32_16x16x32_f16 v[164:167], v[68:71], v[24:27], 0\n" \
  "s_nop 0\n" "ds_read_b64 v[100:101], %10 offset:64\n" \
  "ds_read_b64 v[102:103], %10 offset:72\n" "v_mfma_f32_16x16x32_f16 v[160:163], v[68:71], v[28:31], 0\n" \
  "v_cvt_pk_f16_f32 v116, v124, v120\n" "v_cvt_pk_f16_f32 v117, v125, v121\n" \
  "v_mfma_f32_16x16x32_f16 v[136:139], v[112:115], v[44:47], v[136:139]\n" "s_waitcnt lgkmcnt(7)\n" \
  "v_cvt_pk_f16_f32 v118, v126, v122\n" "v_cvt_pk_f16_f32 v119, v127, v123\n" \
  "v_mfma_f32_16x16x32_f16 v[132:135], v[72:75], v[24:27], 0\n" "ds_read_b128 v[104:107], %9 offset:1504\n" \
  "v_cvt_pk_f16_f32 v112, v164, v160\n" "v_mfma_f32_16x16x32_f16 v[128:131], v[72:75], v[28:31], 0\n" \
  "v_cvt_pk_f16_f32 v113, v165, v161\n" "v_cvt_pk_f16_f32 v114, v166, v162\n" \
  "v_mfma_f32_16x16x32_f16 v[156:159], v[116:119], v[76:79], 0\n" "ds_read_b128 v[108:111], %9 offset:1760\n" \
  "v_cvt_pk_f16_f32 v115, v167, v163\n" "v_mfma_f32_16x16x32_f16 v[152:155], v[116:119], v[80:83], 0\n" \
  "s_nop 0\n" "ds_read2_b32 v[32:33], %6 offset0:144 offset1:145\n" \
  "ds_read2_b32 v[34:35], %6 offset0:146 offset1:147\n" "v_mfma_f32_16x16x32_f16 v[148:151], v[116:119], v[84:87], v[148:151]\n" \
  "s_waitcnt lgkmcnt(6)\n" "v_cvt_pk_f16_f32 v116, v132, v128\n" \
  "v_cvt_pk_f16_f32 v117, v133, v129\n" "v_mfma_f32_16x16x32_f16 v[124:127], v[88:91], v[24:27], 0\n" \
  "v_cvt_pk_f16_f32 v118, v134, v130\n" "v_cvt_pk_f16_f32 v119, v135, v131\n" \
  "v_mfma_f32_16x16x32_f16 v[120:123], v[88:91], v[28:31], 0\n" "s_nop 0\n" \
  "ds_read_b128 v[36:39], %9 offset:2016\n" "ds_read2_b32 v[40:41], %6 offset0:16 offset1:17\n" \
  "v_mfma_f32_16x16x32_f16 v[156:159], v[112:115], v[92:95], v[156:159]\n" "ds_read2_b32 v[42:43], %6 offset0:18 offset1:19\n" \
  "v_mfma_f32_16x16x32_f16 v[152:155], v[112:115], v[96:99], v[152:155]\n" "s_waitcnt lgkmcnt(6)\n" \
  "ds_read2_b32 v[56:57], %6 offset0:48 offset1:49\n" "ds_read2_b32 v[58:59], %6 offset0:50 offset1:51\n" \
  "v_mfma_f32_16x16x32_f16 v[148:151], v[112:115], v[100:103], v[148:151]\n" "v_cvt_pk_f16_f32 v112, v124, v120\n" \
  "v_cvt_pk_f16_f32 v113, v125, v121\n" "v_mfma_f32_16x16x32_f16 v[164:167], v[104:107], v[24:27], 0\n" \
  "v_cvt_pk_f16_f32 v114, v126, v122\n" "v_cvt_pk_f16_f32 v115, v127, v123\n" \
  "v_mfma_f32_16x16x32_f16 v[160:163], v[104:107], v[28:31], 0\n" "s_nop 0\n" \
  "ds_read_b128 v[16:19], %9 offset:2272\n" "ds_read2_b32 v[60:61], %6 offset0:176 offset1:177\n" \
  "v_mfma_f32_16x16x32_f16 v[152:155], v[116:119], v[76:79], v[152:155]\n" "ds_read2_b32 v[62:63], %6 offset0:178 offset1:179\n" \
  "v_mfma_f32_16x16x32_f16 v[148:151], v[116:119], v[80:83], v[148:151]\n" "s_nop 0\n" \
  "ds_read2_b32 v[20:21], %6 offset0:32 offset1:33\n" "ds_read2_b32 v[22:23], %6 offset0:34 offset1:35\n" \
  "v_mfma_f32_16x16x32_f16 v[144:147], v[116:119], v[84:87], v[144:147]\n" "s_waitcnt lgkmcnt(8)\n" \
  "v_cvt_pk_f16_f32 v116, v164, v160\n" "v_cvt_pk_f16_f32 v117, v165, v161\n" \
  "v_mfma_f32_16x16x32_f16 v[132:135], v[108:111], v[24:27], 0\n" "v_cvt_pk_f16_f32 v118, v166, v162\n" \
  "v_cvt_pk_f16_f32 v119, v167, v163\n" "v_mfma_f32_16x16x32_f16 v[128:131], v[108:111], v[28:31], 0\n" \
  "s_nop 0\n" "ds_read_b128 v[52:55], %9 offset:2528\n" \
  "ds_read2_b32 v[64:65], %6 offset0:160 offset1:161\n" "v_mfma_f32_16x16x32_f16 v[152:155], v[112:115], v[92:95], v[152:155]\n" \
  "ds_read2_b32 v[66:67], %6 offset0:162 offset1:163\n" "v_mfma_f32_16x16x32_f16 v[148:151], v[112:115], v[96:99], v[148:151]\n" \
  "s_nop 0\n" "ds_read2_b32 v[48:49], %6 offset0:80 offset1:81\n" \
  "ds_read2_b32 v[50:51], %6 offset0:82 offset1:83\n" "v_mfma_f32_16x16x32_f16 v[144:147], v[112:115], v[100:103], v[144:147]\n" \
  "v_cvt_pk_f16_f32 v112, v132, v128\n" "v_cvt_pk_f16_f32 v113, v133, v129\n" \
  "v_mfma_f32_16x16x32_f16 v[124:127], v[36:39], v[24:27], 0\n" "v_cvt_pk_f16_f32 v114, v134, v130\n" \
  "v_cvt_pk_f16_f32 v115, v135, v131\n" "v_mfma_f32_16x16x32_f16 v[120:123], v[36:39], v[28:31], 0\n" \
  "s_nop 0\n" "ds_read_b128 v[68:71], %9 offset:2784\n" \
  "ds_read2_b32 v[44:45], %6 offset0:208 offset1:209\n" "v_mfma_f32_16x16x32_f16 v[148:151], v[116:119], v[76:79], v[148:151]\n" \
  "ds_read2_b32 v[46:47], %6 offset0:210 offset1:211\n" "v_mfma_f32_16x16x32_f16 v[144:147], v[116:119], v[80:83], v[144:147]\n" \
  "s_nop 0\n" "ds_read2_b32 v[72:73], %6 offset0:64 offset1:65\n" \
  "ds_read2_b32 v[74:75], %6 offset0:66 offset1:67\n" "v_mfma_f32_16x16x32_f16 v[140:143], v[116:119], v[84:87], v[140:143]\n" \
  "s_waitcnt lgkmcnt(8)\n" "v_cvt_pk_f16_f32 v116, v124, v120\n" \
  "v_cvt_pk_f16_f32 v117, v125, v121\n" "v_mfma_f32_16x16x32_f16 v[132:135], v[16:19], v[24:27], 0\n" \
  "v_cvt_pk_f16_f32 v118, v126, v122\n" "v_cvt_pk_f16_f32 v119, v127, v123\n" \
  "v_mfma_f32_16x16x32_f16 v[128:131], v[16:19], v[28:31], 0\n" "s_nop 0\n" \
  "ds_read_b128 v[88:91], %9 offset:3040\n" "ds_read2_b32 v[104:105], %6 offset0:192 offset1:193\n" \
  "v_mfma_f32_16x16x32_f16 v[148:151], v[112:115], v[92:95], v[148:151]\n" "ds_read2_b32 v[106:107], %6 offset0:194 offset1:195\n" \
  "v_mfma_f32_16x16x32_f16 v[144:147], v[112:115], v[96:99], v[144:147]\n" "s_nop 0\n" \
  "ds_read_b128 v[108:111], %8 offset:0\n" "ds_read2_b32 v[36:37], %6 offset0:224 offset1:225\n" \
  "v_mfma_f32_16x16x32_f16 v[140:143], v[112:115], v[100:103], v[140:143]\n" "v_cvt_pk_f16_f32 v112, v132, v128\n" \
  "v_cvt_pk_f16_f32 v113, v133, v129\n" "v_mfma_f32_16x16x32_f16 v[124:127], v[52:55], v[24:27], 0\n" \
  "v_cvt_pk_f16_f32 v114, v134, v130\n" "v_cvt_pk_f16_f32 v115, v135, v131\n" \
  "v_mfma_f32_16x16x32_f16 v[120:123], v[52:55], v[28:31], 0\n" "s_nop 0\n" \
  "ds_read2_b32 v[38:39], %6 offset0:226 offset1:227\n" "ds_read_b128 v[16:19], %9 offset:3296\n" \
  "v_mfma_f32_16x16x32_f16 v[144:147], v[116:119], v[76:79], v[144:147]\n" "ds_read2_b32 v[52:53], %6 offset0:96 offset1:97\n" \
  "v_mfma_f32_16x16x32_f16 v[140:143], v[116:119], v[80:83], v[140:143]\n" "ds_read2_b32 v[54:55], %6 offset0:98 offset1:99\n" \
  "v_mfma_f32_16x16x32_f16 v[136:139], v[116:119], v[84:87], v[136:139]\n" "s_waitcnt lgkmcnt(7)\n" \
  "v_cvt_pk_f16_f32 v116, v124, v120\n" "v_cvt_pk_f16_f32 v117, v125, v121\n" \
  "v_mfma_f32_16x16x32_f16 v[132:135], v[68:71], v[24:27], 0\n" "v_cvt_pk_f16_f32 v118, v126, v122\n" \
  "v_cvt_pk_f16_f32 v119, v127, v123\n" "v_mfma_f32_16x16x32_f16 v[128:131], v[68:71], v[28:31], 0\n" \
  "s_nop 0\n" "ds_read_b128 v[84:87], %8 offset:128\n" \
  "ds_read2_b32 v[68:69], %6 offset0:240 offset1:241\n" "v_mfma_f32_16x16x32_f16 v[144:147], v[112:115], v[92:95], v[144:147]\n" \
  "ds_read2_b32 v[70:71], %6 offset0:242 offset1:243\n" "v_mfma_f32_16x16x32_f16 v[140:143], v[112:115], v[96:99], v[140:143]\n" \
  "v_mfma_f32_16x16x32_f16 v[136:139], v[112:115], v[100:103], v[136:139]\n" "ds_read_b128 v[100:103], %8 offset:64\n" \
  "v_cvt_pk_f16_f32 v112, v132, v128\n" "v_mfma_f32_16x16x32_f16 v[124:127], v[88:91], v[24:27], 0\n" \
  "v_cvt_pk_f16_f32 v113, v133, v129\n" "v_cvt_pk_f16_f32 v114, v134, v130\n" \
  "v_mfma_f32_16x16x32_f16 v[120:123], v[88:91], v[28:31], 0\n" "s_waitcnt lgkmcnt(4)\n" \
  "ds_read_b128 v[88:91], %8 offset:256\n" "v_cvt_pk_f16_f32 v115, v135, v131\n" \
  "v_mfma_f32_16x16x32_f16 v[164:167], v[32:35], v[108:111], 0\n" "v_mfma_f32_16x16x32_f16 v[140:143], v[116:119], v[76:79], v[140:143]\n" \
  "v_mfma_f32_16x16x32_f16 v[136:139], v[116:119], v[80:83], v[136:139]\n" "s_nop 0\n" \
  "ds_read_b128 v[80:83], %8 offset:192\n" "v_cvt_pk_f16_f32 v116, v124, v120\n" \
  "v_mfma_f32_16x16x32_f16 v[132:135], v[16:19], v[24:27], 0\n" "v_cvt_pk_f16_f32 v117, v125, v121\n" \
  "v_cvt_pk_f16_f32 v118, v126, v122\n" "v_mfma_f32_16x16x32_f16 v[128:131], v[16:19], v[28:31], 0\n" \
  "ds_read_b128 v[24:27], %8 offset:320\n" "v_cvt_pk_f16_f32 v119, v127, v123\n" \
  "v_mfma_f32_16x16x32_f16 v[160:163], v[40:43], v[108:111], 0\n" "s_nop 0\n" \
  "ds_read2_b32 v[16:17], %6 offset0:112 offset1:113\n" "ds_read2_b32 v[18:19], %6 offset0:114 offset1:115\n" \
  "v_mfma_f32_16x16x32_f16 v[140:143], v[112:115], v[92:95], v[140:143]\n" "ds_read_b128 v[28:31], %8 offset:384\n" \
  "v_mfma_f32_16x16x32_f16 v[136:139], v[112:115], v[96:99], v[136:139]\n" "s_waitcnt lgkmcnt(5)\n" \
  "ds_read2_b32 v[124:125], %7 offset0:0 offset1:1\n" "v_cvt_pk_f16_f32 v112, v132, v128\n" \
  "v_mfma_f32_16x16x32_f16 v[160:163], v[56:59], v[84:87], v[160:163]\n" "v_cvt_pk_f16_f32 v113, v133, v129\n" \
  "v_cvt_pk_f16_f32 v114, v134, v130\n" "v_mfma_f32_16x16x32_f16 v[164:167], v[60:63], v[84:87], v[164:167]\n" \
  "ds_read2_b32 v[126:127], %7 offset0:2 offset1:3\n" "v_cvt_pk_f16_f32 v115, v135, v131\n" \
  "v_mfma_f32_16x16x32_f16 v[160:163], v[20:23], v[100:103], v[160:163]\n" "s_nop 0\n" \
  "ds_read_b128 v[108:111], %8 offset:448\n" "ds_read2_b32 v[120:121], %6 offset0:128 offset1:129\n" \
  "v_mfma_f32_16x16x32_f16 v[164:167], v[64:67], v[100:103], v[164:167]\n" "ds_read2_b32 v[122:123], %6 offset0:130 offset1:131\n" \
  "v_mfma_f32_16x16x32_f16 v[136:139], v[116:119], v[76:79], v[136:139]\n" "s_nop 0\n" \
  "ds_read2_b32 v[96:97], %7 offset0:16 offset1:17\n" "ds_read2_b32 v[98:99], %7 offset0:18 offset1:19\n" \
  "v_mfma_f32_16x16x32_f16 v[160:163], v[48:51], v[88:91], v[160:163]\n" "ds_read_b128 v[84:87], %8 offset:512\n" \
  "v_mfma_f32_16x16x32_f16 v[164:167], v[44:47], v[88:91], v[164:167]\n" "s_waitcnt lgkmcnt(8)\n" \
  "v_mfma_f32_16x16x32_f16 v[160:163], v[72:75], v[80:83], v[160:163]\n" "s_nop 0\n" \
  "ds_read2_b32 v[132:133], %6 offset0:0 offset1:1\n" "ds_read2_b32 v[134:135], %6 offset0:2 offset1:3\n" \
  "v_mfma_f32_16x16x32_f16 v[164:167], v[104:107], v[80:83], v[164:167]\n" "ds_read_b128 v[128:131], %8 offset:576\n" \
  "v_mfma_f32_16x16x32_f16 v[136:139], v[112:115], v[92:95], v[136:139]\n" "ds_read_b128 v[100:103], %8 offset:640\n" \
  "v_mfma_f32_16x16x32_f16 v[164:167], v[36:39], v[24:27], v[164:167]\n" "ds_read_b128 v[116:119], %8 offset:704\n" \
  "v_mfma_f32_16x16x32_f16 v[160:163], v[52:55], v[24:27], v[160:163]\n" "v_mfma_f32_16x16x32_f16 v[164:167], v[68:71], v[28:31], v[164:167]\n" \
  "s_nop 0\n" "ds_read_b128 v[76:79], %8 offset:768\n" \
  "ds_read_b128 v[88:91], %8 offset:832\n" "v_mfma_f32_16x16x32_f16 v[160:163], v[16:19], v[28:31], v[160:163]\n" \
  "s_waitcnt lgkmcnt(7)\n" "ds_read_b128 v[80:83], %8 offset:896\n" \
  "v_mfma_f32_16x16x32_f16 v[164:167], v[124:127], v[108:111], v[164:167]\n" "ds_read_b128 v[112:115], %8 offset:960\n" \
  "v_mfma_f32_16x16x32_f16 v[160:163], v[120:123], v[108:111], v[160:163]\n" "ds_read_b128 v[92:95], %8 offset:1024\n" \
  "v_mfma_f32_16x16x32_f16 v[164:167], v[96:99], v[84:87], v[164:167]\n" "v_mfma_f32_16x16x32_f16 v[160:163], v[32:35], v[84:87], v[160:163]\n" \
  "s_nop 0\n" "ds_read_b128 v[24:27], %8 offset:1088\n" \
  "ds_read_b128 v[28:31], %8 offset:1152\n" "v_mfma_f32_16x16x32_f16 v[156:159], v[40:43], v[84:87], v[156:159]\n" \
  "s_waitcnt lgkmcnt(7)\n" "ds_read_b128 v[108:111], %8 offset:1216\n" \
  "v_mfma_f32_16x16x32_f16 v[148:151], v[132:135], v[84:87], v[148:151]\n" "v_mul_f32 v164, %5, v164\n" \
  "v_mul_f32 v165, %5, v165\n" "v_mfma_f32_16x16x32_f16 v[160:163], v[64:67], v[128:131], v[160:163]\n" \
  "v_mul_f32 v166, %5, v166\n" "v_mul_f32 v167, %5, v167\n" \
  "v_mfma_f32_16x16x32_f16 v[156:159], v[20:23], v[128:131], v[156:159]\n" "v_cvt_pk_bf16_f32 v164, v164, v165\n" \
  "v_cvt_pk_bf16_f32 v165, v166, v167\n" "v_mfma_f32_16x16x32_f16 v[160:163], v[60:63], v[100:103], v[160:163]\n" \
  "v_pk_min_u16 v164, v164, %4\n" "v_pk_min_u16 v165, v165, %4\n" \
  "v_mfma_f32_16x16x32_f16 v[156:159], v[56:59], v[100:103], v[156:159]\n" "ds_write_b64 %12, v[164:165] offset:0\n" \
  "v_mfma_f32_16x16x32_f16 v[160:163], v[104:107], v[116:119], v[160:163]\n" "s_nop 0\n" \
  "ds_read_b128 v[84:87], %8 offset:1280\n" "ds_read_b128 v[128:131], %8 offset:1344\n" \
  "v_mfma_f32_16x16x32_f16 v[156:159], v[72:75], v[116:119], v[156:159]\n" "s_waitcnt lgkmcnt(4)\n" \
  "ds_read_b128 v[100:103], %8 offset:1408\n" "ds_read_b128 v[164:167], %8 offset:1472\n" \
  "v_mfma_f32_16x16x32_f16 v[160:163], v[44:47], v[76:79], v[160:163]\n" "v_mfma_f32_16x16x32_f16 v[156:159], v[48:51], v[76:79], v[156:159]\n" \
  "v_mfma_f32_16x16x32_f16 v[160:163], v[36:39], v[88:91], v[160:163]\n" "ds_read_b128 v[116:119], %8 offset:1536\n" \
  "v_mfma_f32_16x16x32_f16 v[156:159], v[52:55], v[88:91], v[156:159]\n" "s_nop 0\n" \
  "ds_read_b128 v[76:79], %8 offset:1600\n" "ds_read_b128 v[88:91], %8 offset:1664\n" \
  "v_mfma_f32_16x16x32_f16 v[160:163], v[68:71], v[80:83], v[160:163]\n" "v_mfma_f32_16x16x32_f16 v[156:159], v[16:19], v[80:83], v[156:159]\n" \
  "ds_read_b128 v[80:83], %8 offset:1728\n" "v_mfma_f32_16x16x32_f16 v[160:163], v[124:127], v[112:115], v[160:163]\n" \
  "v_mfma_f32_16x16x32_f16 v[156:159], v[120:123], v[112:115], v[156:159]\n" "ds_read_b128 v[112:115], %8 offset:1792\n" \
  "v_mfma_f32_16x16x32_f16 v[160:163], v[96:99], v[92:95], v[160:163]\n" "v_mfma_f32_16x16x32_f16 v[156:159], v[32:35], v[92:95], v[156:159]\n" \
  "v_mfma_f32_16x16x32_f16 v[152:155], v[40:43], v[92:95], v[152:155]\n" "v_mfma_f32_16x16x32_f16 v[144:147], v[132:135], v[92:95], v[144:147]\n" \
  "ds_read_b128 v[92:95], %8 offset:1856\n" "v_mfma_f32_16x16x32_f16 v[156:159], v[64:67], v[24:27], v[156:159]\n" \
  "v_mfma_f32_16x16x32_f16 v[152:155], v[20:23], v[24:27], v[152:155]\n" "ds_read_b128 v[24:27], %8 offset:1920\n" \
  "v_mfma_f32_16x16x32_f16 v[156:159], v[60:63], v[28:31], v[156:159]\n" "v_mul_f32 v160, %5, v160\n" \
  "v_mul_f32 v161, %5, v161\n" "v_mfma_f32_16x16x32_f16 v[152:155], v[56:59], v[28:31], v[152:155]\n" \
  "s_waitcnt lgkmcnt(2)\n" "v_mul_f32 v162, %5, v162\n" \
  "v_mul_f32 v163, %5, v163\n" "v_mfma_f32_16x16x32_f16 v[156:159], v[104:107], v[108:111], v[156:159]\n" \
  "v_cvt_pk_bf16_f32 v160, v160, v161\n" "v_cvt_pk_bf16_f32 v161, v162, v163\n" \
  "v_mfma_f32_16x16x32_f16 v[152:155], v[72:75], v[108:111], v[152:155]\n" "v_pk_min_u16 v160, v160, %4\n" \
  "v_pk_min_u16 v161, v161, %4\n" "v_mfma_f32_16x16x32_f16 v[156:159], v[44:47], v[84:87], v[156:159]\n" \
  "ds_write_b64 %12, v[160:161] offset:512\n" "v_mfma_f32_16x16x32_f16 v[152:155], v[48:51], v[84:87], v[152:155]\n" \
  "s_nop 0\n" "ds_read_b128 v[28:31], %8 offset:1984\n" \
  "ds_read_b128 v[108:111], %8 offset:2048\n" "v_mfma_f32_16x16x32_f16 v[156:159], v[36:39], v[128:131], v[156:159]\n" \
  "s_nop 0\n" "ds_read_b128 v[160:163], %8 offset:2112\n" \
  "ds_read_b128 v[84:87], %8 offset:2176\n" "v_mfma_f32_16x16x32_f16 v[152:155], v[52:55], v[128:131], v[152:155]\n" \
  "v_mfma_f32_16x16x32_f16 v[156:159], v[68:71], v[100:103], v[156:159]\n" "v_mfma_f32_16x16x32_f16 v[152:155], v[16:19], v[100:103], v[152:155]\n" \
  "s_waitcnt lgkmcnt(4)\n" "ds_read_b128 v[128:131], %8 offset:2240\n" \
  "v_mfma_f32_16x16x32_f16 v[156:159], v[124:127], v[164:167], v[156:159]\n" "s_barrier\n" \
  "ds_read_b128 v[100:103], %8 offset:2304\n" "v_mfma_f32_16x16x32_f16 v[152:155], v[120:123], v[164:167], v[152:155]\n" \
  "ds_read_b128 v[164:167], %8 offset:2368\n" "v_mfma_f32_16x16x32_f16 v[156:159], v[96:99], v[116:119], v[156:159]\n" \
  "v_mfma_f32_16x16x32_f16 v[152:155], v[32:35], v[116:119], v[152:155]\n" "v_mfma_f32_16x16x32_f16 v[148:151], v[40:43], v[116:119], v[148:151]\n" \
  "v_mfma_f32_16x16x32_f16 v[140:143], v[132:135], v[116:119], v[140:143]\n" "ds_read_b128 v[116:119], %8 offset:2432\n" \
  "v_mfma_f32_16x16x32_f16 v[152:155], v[64:67], v[76:79], v[152:155]\n" "v_mfma_f32_16x16x32_f16 v[148:151], v[20:23], v[76:79], v[148:151]\n" \
  "ds_read_b128 v[76:79], %8 offset:2496\n" "v_mfma_f32_16x16x32_f16 v[152:155], v[60:63], v[88:91], v[152:155]\n" \
  "v_mul_f32 v156, %5, v156\n" "v_mul_f32 v157, %5, v157\n" \
  "v_mfma_f32_16x16x32_f16 v[148:151], v[56:59], v[88:91], v[148:151]\n" "v_mul_f32 v158, %5, v158\n" \
  "v_mul_f32 v159, %5, v159\n" "v_mfma_f32_16x16x32_f16 v[152:155], v[104:107], v[80:83], v[152:155]\n" \
  "v_cvt_pk_bf16_f32 v156, v156, v157\n" "v_cvt_pk_bf16_f32 v157, v158, v159\n" \
  "v_mfma_f32_16x16x32_f16 v[148:151], v[72:75], v[80:83], v[148:151]\n" "v_pk_min_u16 v156, v156, %4\n" \
  "v_pk_min_u16 v157, v157, %4\n" "v_mfma_f32_16x16x32_f16 v[152:155], v[44:47], v[112:115], v[152:155]\n" \
  "ds_write_b64 %12, v[156:157] offset:1024\n" "v_mfma_f32_16x16x32_f16 v[148:151], v[48:51], v[112:115], v[148:151]\n" \
  "s_nop 0\n" "ds_read_b128 v[88:91], %8 offset:2560\n" \
  "ds_read_b128 v[80:83], %8 offset:2624\n" "v_mfma_f32_16x16x32_f16 v[152:155], v[36:39], v[92:95], v[152:155]\n" \
  "s_nop 0\n" "ds_read_b128 v[156:159], %8 offset:2688\n" \
  "ds_read_b128 v[112:115], %8 offset:2752\n" "v_mfma_f32_16x16x32_f16 v[148:151], v[52:55], v[92:95], v[148:151]\n" \
  "v_mfma_f32_16x16x32_f16 v[152:155], v[68:71], v[24:27], v[152:155]\n" "v_mfma_f32_16x16x32_f16 v[148:151], v[16:19], v[24:27], v[148:151]\n" \
  "s_waitcnt lgkmcnt(5)\n" "ds_read_b128 v[92:95], %8 offset:2816\n" \
  "v_mfma_f32_16x16x32_f16 v[152:155], v[124:127], v[28:31], v[152:155]\n" "ds_read_b128 v[24:27], %8 offset:2880\n" \
  "v_mfma_f32_16x16x32_f16 v[148:151], v[120:123], v[28:31], v[148:151]\n" "ds_read_b128 v[28:31], %8 offset:2944\n" \
  "v_mfma_f32_16x16x32_f16 v[152:155], v[96:99], v[108:111], v[152:155]\n" "v_mfma_f32_16x16x32_f16 v[148:151], v[32:35], v[108:111], v[148:151]\n" \
  "v_mfma_f32_16x16x32_f16 v[144:147], v[40:43], v[108:111], v[144:147]\n" "v_mfma_f32_16x16x32_f16 v[136:139], v[132:135], v[108:111], v[136:139]\n" \
  "ds_read_b128 v[132:135], %8 offset:3008\n" "v_mfma_f32_16x16x32_f16 v[148:151], v[64:67], v[160:163], v[148:151]\n" \
  "v_mfma_f32_16x16x32_f16 v[144:147], v[20:23], v[160:163], v[144:147]\n" "v_mfma_f32_16x16x32_f16 v[148:151], v[60:63], v[84:87], v[148:151]\n" \
  "s_nop 0\n" "v_mul_f32 v152, %5, v152\n" \
  "v_mul_f32 v153, %5, v153\n" "v_mfma_f32_16x16x32_f16 v[144:147], v[56:59], v[84:87], v[144:147]\n" \
  "v_mul_f32 v154, %5, v154\n" "v_mul_f32 v155, %5, v155\n" \
  "v_mfma_f32_16x16x32_f16 v[148:151], v[104:107], v[128:131], v[148:151]\n" "v_cvt_pk_bf16_f32 v152, v152, v153\n" \
  "v_cvt_pk_bf16_f32 v153, v154, v155\n" "v_mfma_f32_16x16x32_f16 v[144:147], v[72:75], v[128:131], v[144:147]\n" \
  "v_pk_min_u16 v152, v152, %4\n" "v_pk_min_u16 v153, v153, %4\n" \
  "v_mfma_f32_16x16x32_f16 v[148:151], v[44:47], v[100:103], v[148:151]\n" "ds_write_b64 %12, v[152:153] offset:1536\n" \
  "v_mfma_f32_16x16x32_f16 v[144:147], v[48:51], v[100:103], v[144:147]\n" "v_mfma_f32_16x16x32_f16 v[148:151], v[36:39], v[164:167], v[148:151]\n" \
  "v_mfma_f32_16x16x32_f16 v[144:147], v[52:55], v[164:167], v[144:147]\n" "v_mfma_f32_16x16x32_f16 v[148:151], v[68:71], v[116:119], v[148:151]\n" \
  "v_mfma_f32_16x16x32_f16 v[144:147], v[16:19], v[116:119], v[144:147]\n" "s_waitcnt lgkmcnt(0)\n" \
  "v_mfma_f32_16x16x32_f16 v[148:151], v[124:127], v[76:79], v[148:151]\n" "s_barrier\n" \
  "ds_read_b128 v[108:111], %8 offset:3072\n" "ds_read_b128 v[160:163], %8 offset:3136\n" \
  "v_mfma_f32_16x16x32_f16 v[144:147], v[120:123], v[76:79], v[144:147]\n" "s_nop 0\n" \
  "ds_read_b128 v[84:87], %8 offset:3200\n" "ds_read_b128 v[128:131], %8 offset:3264\n" \
  "v_mfma_f32_16x16x32_f16 v[148:151], v[96:99], v[88:91], v[148:151]\n" "ds_read_b128 v[152:155], %8 offset:3328\n" \
  "v_mfma_f32_16x16x32_f16 v[144:147], v[32:35], v[88:91], v[144:147]\n" "s_nop 2\n" \
  "v_mfma_f32_16x16x32_f16 v[140:143], v[40:43], v[88:91], v[140:143]\n" "s_nop 1\n" \
  "v_mfma_f32_16x16x32_f16 v[144:147], v[64:67], v[80:83], v[144:147]\n" "v_mul_f32 v148, %5, v148\n" \
  "v_mul_f32 v149, %5, v149\n" "v_mfma_f32_16x16x32_f16 v[140:143], v[20:23], v[80:83], v[140:143]\n" \
  "v_mul_f32 v150, %5, v150\n" "v_mul_f32 v151, %5, v151\n" \
  "v_mfma_f32_16x16x32_f16 v[144:147], v[60:63], v[156:159], v[144:147]\n" "v_cvt_pk_bf16_f32 v148, v148, v149\n" \
  "v_cvt_pk_bf16_f32 v149, v150, v151\n" "v_mfma_f32_16x16x32_f16 v[140:143], v[56:59], v[156:159], v[140:143]\n" \
  "v_pk_min_u16 v148, v148, %4\n" "v_pk_min_u16 v149, v149, %4\n" \
  "v_mfma_f32_16x16x32_f16 v[144:147], v[104:107], v[112:115], v[144:147]\n" "ds_write_b64 %12, v[148:149] offset:2048\n" \
  "v_mfma_f32_16x16x32_f16 v[140:143], v[72:75], v[112:115], v[140:143]\n" "s_nop 0\n" \
  "ds_read_b128 v[100:103], %8 offset:3392\n" "ds_read_b128 v[164:167], %8 offset:3456\n" \
  "v_mfma_f32_16x16x32_f16 v[144:147], v[44:47], v[92:95], v[144:147]\n" "s_nop 0\n" \
  "ds_read_b128 v[116:119], %8 offset:3520\n" "ds_read_b128 v[76:79], %8 offset:3584\n" \
  "v_mfma_f32_16x16x32_f16 v[140:143], v[48:51], v[92:95], v[140:143]\n" "v_mfma_f32_16x16x32_f16 v[144:147], v[36:39], v[24:27], v[144:147]\n" \
  "v_mfma_f32_16x16x32_f16 v[140:143], v[52:55], v[24:27], v[140:143]\n" "ds_read_b128 v[88:91], %8 offset:3648\n" \
  "v_mfma_f32_16x16x32_f16 v[144:147], v[68:71], v[28:31], v[144:147]\n" "s_nop 0\n" \
  "ds_read_b128 v[80:83], %8 offset:3712\n" "ds_read_b128 v[156:159], %8 offset:3776\n" \
  "v_mfma_f32_16x16x32_f16 v[140:143], v[16:19], v[28:31], v[140:143]\n" "s_nop 0\n" \
  "ds_read_b128 v[148:151], %8 offset:3840\n" "ds_read_b128 v[112:115], %8 offset:3904\n" \
  "v_mfma_f32_16x16x32_f16 v[144:147], v[124:127], v[132:135], v[144:147]\n" "v_mfma_f32_16x16x32_f16 v[140:143], v[120:123], v[132:135], v[140:143]\n" \
  "s_waitcnt lgkmcnt(7)\n" "v_mfma_f32_16x16x32_f16 v[144:147], v[96:99], v[108:111], v[144:147]\n" \
  "ds_read_b128 v[92:95], %8 offset:3968\n" "v_mfma_f32_16x16x32_f16 v[140:143], v[32:35], v[108:111], v[140:143]\n" \
  "s_nop 0\n" "ds_read_b128 v[24:27], %8 offset:4032\n" \
  "ds_read_b128 v[28:31], %8 offset:4096\n" "v_mfma_f32_16x16x32_f16 v[136:139], v[40:43], v[108:111], v[136:139]\n" \
  "s_nop 1\n" "v_mfma_f32_16x16x32_f16 v[140:143], v[64:67], v[160:163], v[140:143]\n" \
  "v_mul_f32 v144, %5, v144\n" "v_mul_f32 v145, %5, v145\n" \
  "v_mfma_f32_16x16x32_f16 v[140:143], v[60:63], v[84:87], v[140:143]\n" "v_mul_f32 v146, %5, v146\n" \
  "v_mul_f32 v147, %5, v147\n" "v_mfma_f32_16x16x32_f16 v[140:143], v[104:107], v[128:131], v[140:143]\n" \
  "v_cvt_pk_bf16_f32 v144, v144, v145\n" "v_cvt_pk_bf16_f32 v145, v146, v147\n" \
  "v_mfma_f32_16x16x32_f16 v[140:143], v[44:47], v[152:155], v[140:143]\n" "v_pk_min_u16 v144, v144, %4\n" \
  "v_pk_min_u16 v145, v145, %4\n" "v_mfma_f32_16x16x32_f16 v[140:143], v[36:39], v[100:103], v[140:143]\n" \
  "ds_write_b64 %12, v[144:145] offset:2560\n" "v_mfma_f32_16x16x32_f16 v[140:143], v[68:71], v[164:167], v[140:143]\n" \
  "s_waitcnt lgkmcnt(3)\n" "v_mfma_f32_16x16x32_f16 v[140:143], v[124:127], v[116:119], v[140:143]\n" \
  "v_mfma_f32_16x16x32_f16 v[140:143], v[96:99], v[76:79], v[140:143]\n" "s_nop 2\n" \
  "v_mfma_f32_16x16x32_f16 v[136:139], v[20:23], v[160:163], v[136:139]\n" "s_nop 2\n" \
  "v_mfma_f32_16x16x32_f16 v[136:139], v[56:59], v[84:87], v[136:139]\n" "s_waitcnt lgkmcnt(0)\n" \
  "v_mul_f32 v140, %5, v140\n" "v_mul_f32 v141, %5, v141\n" \
  "v_mfma_f32_16x16x32_f16 v[136:139], v[72:75], v[128:131], v[136:139]\n" "s_barrier\n" \
  "v_mul_f32 v142, %5, v142\n" "v_mul_f32 v143, %5, v143\n" \
  "v_mfma_f32_16x16x32_f16 v[136:139], v[48:51], v[152:155], v[136:139]\n" "v_cvt_pk_bf16_f32 v140, v140, v141\n" \
  "v_cvt_pk_bf16_f32 v141, v142, v143\n" "v_mfma_f32_16x16x32_f16 v[136:139], v[52:55], v[100:103], v[136:139]\n" \
  "v_pk_min_u16 v140, v140, %4\n" "v_pk_min_u16 v141, v141, %4\n" \
  "v_mfma_f32_16x16x32_f16 v[136:139], v[16:19], v[164:167], v[136:139]\n" "ds_write_b64 %12, v[140:141] offset:3072\n" \
  "v_mfma_f32_16x16x32_f16 v[136:139], v[120:123], v[116:119], v[136:139]\n" "v_mfma_f32_16x16x32_f16 v[136:139], v[32:35], v[76:79], v[136:139]\n" \
  "v_mfma_f32_16x16x32_f16 v[136:139], v[64:67], v[88:91], v[136:139]\n" "v_mfma_f32_16x16x32_f16 v[136:139], v[60:63], v[80:83], v[136:139]\n" \
  "v_mfma_f32_16x16x32_f16 v[136:139], v[104:107], v[156:159], v[136:139]\n" "v_mfma_f32_16x16x32_f16 v[136:139], v[44:47], v[148:151], v[136:139]\n" \
  "v_mfma_f32_16x16x32_f16 v[136:139], v[36:39], v[112:115], v[136:139]\n" "v_mfma_f32_16x16x32_f16 v[136:139], v[68:71], v[92:95], v[136:139]\n" \
  "v_mfma_f32_16x16x32_f16 v[136:139], v[124:127], v[24:27], v[136:139]\n" "v_mfma_f32_16x16x32_f16 v[136:139], v[96:99], v[28:31], v[136:139]\n" \
  "s_nop 2\n" "s_nop 4\n" \
  "v_mul_f32 v136, %5, v136\n" "v_mul_f32 v137, %5, v137\n" \
  "v_mul_f32 v138, %5, v138\n" "v_mul_f32 v139, %5, v139\n" \
  "v_cvt_pk_bf16_f32 v136, v136, v137\n" "v_cvt_pk_bf16_f32 v137, v138, v139\n" \
  "v_pk_min_u16 v136, v136, %4\n" "v_pk_min_u16 v137, v137, %4\n" \
  "ds_write_b64 %12, v[136:137] offset:3584\n" "s_waitcnt lgkmcnt(0)\n" \
  "s_barrier\n"
#define CONV_ASM_CLOBBERS "v32", "v33", "v34", "v35", "v36", "v37", "v38", "v39", "v40", "v41", "v42", "v43", "v44", "v45", "v46", "v47", "v48", "v49", "v50", "v51", "v52", "v53", "v54", "v55", "v56", "v57", "v58", "v59", "v60", "v61", "v62", "v63", "v64", "v65", "v66", "v67", "v68", "v69", "v70", "v71", "v72", "v73", "v74", "v75", "v76", "v77", "v78", "v79", "v80", "v81", "v82", "v83", "v84", "v85", "v86", "v87", "v88", "v89", "v90", "v91", "v92", "v93", "v94", "v95", "v96", "v97", "v98", "v99", "v100", "v101", "v102", "v103", "v104", "v105", "v106", "v107", "v108", "v109", "v110", "v111", "v112", "v113", "v114", "v115", "v116", "v117", "v118", "v119", "v120", "v121", "v122", "v123", "v124", "v125", "v126", "v127", "v128", "v129", "v130", "v131", "v132", "v133", "v134", "v135", "v136", "v137", "v138", "v139", "v140", "v141", "v142", "v143", "v144", "v145", "v146", "v147", "v148", "v149", "v150", "v151", "v152", "v153", "v154", "v155", "v156", "v157", "v158", "v159", "v160", "v161", "v162", "v163", "v164", "v165", "v166", "v167", "memory"
#define CONV_ASM_PRE(q) "+{v[16:19]}"(q[0]), "+{v[20:23]}"(q[1]), "+{v[24:27]}"(q[2]), "+{v[28:31]}"(q[3])

struct Params {
  const u16 *x, *lar, *aim, *crl, *cim, *ldt, *dskip, *wr, *wg, *wu, *wd;
  u16* out;
  char* ws;
};

typedef unsigned long long u64;
typedef u64 v8q __attribute__((ext_vector_type(8)));
typedef u64 v4q __attribute__((ext_vector_type(4)));
#define GAS __attribute__((address_space(1)))

__global__ __launch_bounds__(NT, 3) void k_main(Params) {
  extern __shared__ __attribute__((aligned(16))) char smem[];
  v8q ka;
  v4q kb_;
  u64 kc;
  asm volatile("s_load_dwordx16 s[8:23], %3, 0x0\n\ts_load_dwordx8 s[24:31], %3, 0x40\n\ts_load_dwordx2 s[36:37], %3, 0x60"
               : "=&{s[8:23]}"(ka), "=&{s[24:31]}"(kb_), "=&{s[36:37]}"(kc)
               : "s"(__builtin_amdgcn_kernarg_segment_ptr()));
  SCHED();
  const int tid = threadIdx.x, lane = tid & 63;
  int wave = __builtin_amdgcn_readfirstlane(tid >> 6);
  const int xcd = blockIdx.x & 7;
  int loc = blockIdx.x >> 3;
  STAMP_INIT();
  STAMP(0);
  unsigned cpf = 0;
  if (wave == 0 && lane < 8 && (loc * 8 + lane) * 128 < CODE_BYTES) {
    const char* pc = (const char*)(__builtin_amdgcn_s_getpc() & ~(uint64_t)255);
    cpf = *(const __attribute__((address_space(1))) unsigned*)(pc + (loc * 8 + lane) * 128);
  }
  const int cl = lane & 15, g = lane >> 4;
  const int cg = xcd * 18 + loc;
  const int fj = xcd * 9 + (loc - 18);
  const int rb = loc * 8 + xcd;
  const int tile = loc < 30 ? xcd * 12 + (loc - 18) : 66 + xcd * 2 + loc;
  const unsigned od = (cg * 4 + (wave & 3)) * 2;
  const unsigned ox = (tile * 16 + (cl >> 1)) * 1152 + 128 * wave + 32 * g + 16 * (cl & 1);
  const unsigned opar = (wave < 8 ? od * NM + (wave & 4) * 32 : 0u) + (lane << (wave < 8 ? 1 : 2));
  unsigned o0 = loc >= 18 ? ox : opar, o1 = loc >= 18 ? ox + 9216 : opar ^ 128u;
  unsigned o2 = loc >= 18 ? ox + 16 * 18432 : od, o3 = o2 + 9216;
  PIN(o0); PIN(o1); PIN(o2); PIN(o3);
  SCHED();
  asm volatile("s_waitcnt lgkmcnt(0)" : "+{s[8:23]}"(ka), "+{s[24:31]}"(kb_), "+{s[36:37]}"(kc));
  v4u ga, gb, gc, gd;
  unsigned epoch;
  asm volatile(
      "v_mov_b32 v1, 0\n\t"
      "s_cmp_lt_u32 %9, 18\n\t"
      "global_load_dword v1, v1, s[36:37] sc1\n\t"
      "s_cbranch_scc1 Lc_%=\n\t"
      "global_load_dwordx4 v[2:5], %5, s[8:9]\n\t"
      "global_load_dwordx4 v[6:9], %6, s[8:9]\n\t"
      "s_cmp_lt_u32 %9, 30\n\t"
      "s_cbranch_scc1 Le_%=\n\t"
      "global_load_dwordx4 v[10:13], %7, s[8:9]\n\t"
      "global_load_dwordx4 v[14:17], %8, s[8:9]\n\t"
      "s_branch Le_%=\n"
      "Lc_%=:\n\t"
      "s_cmp_eq_u32 %10, 8\n\t"
      "s_cbranch_scc1 Lw_%=\n\t"
      "global_load_ushort v6, %7, s[18:19]\n\t"
      "global_load_ushort v2, %5, s[10:11]\n\t"
      "global_load_ushort v3, %5, s[12:13]\n\t"
      "global_load_ushort v4, %5, s[14:15]\n\t"
      "global_load_ushort v5, %5, s[16:17]\n\t"
      "global_load_ushort v7, %6, s[14:15]\n\t"
      "global_load_ushort v8, %6, s[16:17]\n\t"
      "global_load_ushort v9, %7, s[20:21]\n\t"
      "s_branch Le_%=\n"
      "Lw_%=:\n\t"
      "global_load_dword v2, %5, s[22:23]\n\t"
      "global_load_dword v3, %5, s[22:23] offset:256\n\t"
      "global_load_dword v4, %5, s[22:23] offset:512\n\t"
      "global_load_dword v5, %5, s[22:23] offset:768\n\t"
      "global_load_dword v6, %5, s[22:23] offset:896\n"
      "Le_%=:"
      : "=&{v1}"(epoch), "=&{v[2:5]}"(ga), "=&{v[6:9]}"(gb), "=&{v[10:13]}"(gc), "=&{v[14:17]}"(gd)
      : "v"(o0), "v"(o1), "v"(o2), "v"(o3), "s"(loc), "s"(wave), "{s[8:23]}"(ka), "{s[36:37]}"(kc)
      : "scc", "memory");
  struct { const GAS char *x, *wg, *wu, *wd; GAS char *out, *ws; } p = {(const GAS char*)ka[0], (const GAS char*)kb_[0], (const GAS char*)kb_[1], (const GAS char*)kb_[2], (GAS char*)kb_[3], (GAS char*)kc};
#define FIRST_WAIT(n) asm volatile("s_waitcnt vmcnt(%5) ; first loads" : "+{v1}"(epoch), "+{v[2:5]}"(ga), "+{v[6:9]}"(gb), "+{v[10:13]}"(gc), "+{v[14:17]}"(gd) : "n"(n) : "memory")
  STAMP(11);
  const rsrc_t rs = __builtin_amdgcn_make_buffer_rsrc((void*)p.ws, 0, WS_END, 0x00020000);
  const rsrc_t ro = __builtin_amdgcn_make_buffer_rsrc((void*)p.out, 0, SEQ * DM * 2, 0x00020000);
  const v4u sent4 = {SENT, SENT, SENT, SENT};
  if (loc < 18) {
    if (wave < 4) {
      const unsigned win = wave == 0 ? 0x1ffu : wave == 1 ? 0xfe00u : wave == 2 ? 0x7cu : 0x380u;
      const unsigned bits = (win << cl) >> (8 * g);
      v4u f;
#pragma unroll
      for (int i = 0; i < 4; i++) f[i] = __umul24(__umul24((bits >> (2 * i)) & 3u, 0x8001u) & 0x10001u, 0x3c00u);
      *(v4u*)(smem + L_FILT + wave * 1024 + lane * 16) = f;
    } else if (lane < (wave < 8 ? 36 : 4)) {
      *(v4u*)(smem + (wave < 8 ? L_UIMG + (wave - 4) * USTR : L_MISC) + (lane < 33 ? lane : lane < 35 ? lane + 255 : 224) * 16) = v4u{0u, 0u, 0u, 0u};
    }
  }
  asm volatile("" : "+s"(loc), "+s"(wave));

  if (loc < 18) {
    const int usrc = WS_U + cg * GSTR + lane * 16;
    char* const udst = smem + L_UIMG + (lane & 3) * USTR + 520 + (lane >> 2) * 16;
    if (wave == 8) {
      unsigned pw[PF_N];
      auto warm = [&]() {
        const GAS char* wb = (loc < 9 ? p.wg : p.wu) + xcd * 1152;
        const unsigned s0 = (3u - (unsigned)((u64)wb >> 7)) & 7u, ro_ = ((loc < 9 ? loc : loc - 9) * 64 + lane) * 9216;
        pw[0] = *(const GAS unsigned*)(wb + (ro_ + s0 * 128));
        pw[1] = *(const GAS unsigned*)(wb + (ro_ + (s0 ? s0 : 8u) * 128));
        SCHED();
      };
      if (PF_MODE == 1) warm();
      BAR();
      __builtin_amdgcn_s_setprio(3);
      FIRST_WAIT(PF_MODE == 1 ? PF_N : 0);
      if (PF_MODE == 2) warm();
      {
        const v4u wa = ga;
        const unsigned wb = gb.x;
#pragma unroll
        for (int k = 0; k < 4; k++) *(unsigned*)(smem + L_WR + (lane + 64 * k) * 4) = wa[k];
        *(unsigned*)(smem + L_WR + (224 + lane) * 4) = wb;
        float ws_ = lane >= 32 ? blo(wb) + bhi(wb) : 0.f;
#pragma unroll
        for (int k = 0; k < 4; k++) ws_ += blo(wa[k]) + bhi(wa[k]);
        ws_ = wave_sum(ws_);
        if (lane == 0) *(float*)(smem + L_MISC + 64) = ws_;
      }
      SCHED();
      if (PF_SLEEP) __builtin_amdgcn_s_sleep(PF_SLEEP);
#pragma unroll 1
      for (int i = 0; i < 16; i++) st4(sent4, rs, WS_Y + cg * GSTR + i * 1024 + lane * 16);
      st4(sent4, rs, (loc < 16 && lane < 4) ? WS_RREC + rb * 64 + lane * 16 : OOB);
      st4(sent4, rs, cg < 20 ? WS_XSEL + cg * 1024 + lane * 16 : OOB);
      DRAIN();
      if (lane == 0) st1(epoch + 1u, rs, WS_INIT + cg * 64);
      STAMP(4);
      __builtin_amdgcn_s_setprio(S_PRIO);
      BAR();
      STAMP(5);
      if (loc < 16) {
        sweep_init(rs, epoch + 1u, lane, 0, NCONV);
        const GAS char* xs = p.x + (unsigned)((16 * rb + (lane >> 3)) * 1152 + ((lane & 7) ^ (lane >> 3)) * 16);
#pragma unroll 1
        for (int i = 0; i < 9; i++) {
          __builtin_amdgcn_global_load_lds((const GAS unsigned*)(xs + i * 128), (LAS unsigned*)(L_XROW + i * 2048), 16, 0, 0);
          __builtin_amdgcn_global_load_lds((const GAS unsigned*)(xs + i * 128 + 9216), (LAS unsigned*)(L_XROW + i * 2048 + 1024), 16, 0, 0);
        }
      }
      STAMP(6);
      if (PF_MODE == 3) warm();
      BAR();
      BAR();
      BAR();
      DRAIN();
      BAR();
#pragma unroll
      for (int i = 0; i < (PF_MODE ? PF_N : 0); i++) asm volatile("" ::"v"(pw[i]));
      __builtin_amdgcn_s_setprio(0);
    }
    asm volatile("" : "+s"(wave));
    if (wave < 8) {
      const int ch = wave & 3;
      char* const img = smem + ch * WSCR;
      char* const scr = smem + wave * WSCR;
      volatile LAS unsigned* const pf = (volatile LAS unsigned*)(smem + L_MISC);
      if (wave < 4) __builtin_amdgcn_s_setprio(2); else __builtin_amdgcn_s_setprio(1);
      unsigned wb_[16], rb_[8];
#pragma unroll
      for (int k = 0; k < 16; k++) { wb_[k] = k ? wb_[0] ^ (k << 4) : wave * WSCR + lane * 4; PIN(wb_[k]); }
#pragma unroll
      for (int k = 0; k < 8; k++) { rb_[k] = k ? rb_[0] ^ (k << 5) : wave * WSCR + (lane & 31) * 256 + (((lane >> 5) ^ cl) << 4); PIN(rb_[k]); }
      BAR();
      STAMP(12);
      FIRST_WAIT(0);
      PIN(cpf);
      STAMP(5);
      const float dt = rbf(exp_c(blo(gb.x)));
      const float ar = -rbf(exp_c(blo(ga.x))) * dt * 1.44269504f;
      const float aie = blo(ga.y) * dt;
      const float ph = aie * 0.159154936f;
      const float pl = fmaf(aie, 0.159154936f, -ph) + aie * 7.13048051e-9f;
      auto put2 = [&](int row, unsigned x, unsigned y) {
        *(LAS unsigned*)(wb_[row & 15] + row * 256) = x;
        *(LAS unsigned*)(wb_[row & 15] + (row + 16) * 256) = y;
      };
      auto put1 = [&](int row, unsigned x) { *(LAS unsigned*)(wb_[row & 15] + row * 256) = x; };
      const cz e8 = mkz(cexp_p2(8.f, ar, ph, pl)), e64 = mkz(cexp_p2(64.f, ar, ph, pl));
      const cz e16 = csq(e8), e128 = csq(e64), e32 = csq(e16);
      {
        const f32x2 e24 = cmul(e16.v, e8), e192 = cmul(e128.v, e64);
        const unsigned ha[4] = {0x3c00u, pkh2(e8.v), pkh2(e16.v), pkh2(e24)};
        const unsigned hs[4] = {0u, pkh2(e8.s), pkh2(e16.s), pkh(e24.y, e24.x)};
        const unsigned hb[8] = {0x3c00u, pkh2(e32.v), pkh2(e64.v), pkh2(cmul(e64.v, e32)), pkh2(e128.v), pkh2(cmul(e128.v, e32)), pkh2(e192), pkh2(cmul(e192, e32))};
#pragma unroll
        for (int k = 0; k < 4; k++) put2(4 * k, hb[k], hb[k + 4]);
#pragma unroll
        for (int i = 1; i < 4; i++) {
          unsigned r[8];
          hmul2(r[1], r[5], hb[1], hb[5], ha[i], hs[i]);
          put2(4 + i, r[1], r[5]);
          hmul2(r[2], r[6], hb[2], hb[6], ha[i], hs[i]);
          put2(8 + i, r[2], r[6]);
          hmul2(r[3], r[7], hb[3], hb[7], ha[i], hs[i]);
          put2(12 + i, r[3], r[7]);
        }
        unsigned r1, r2, r3, r4;
        const unsigned s128 = pkh2(e128.s);
        hmul2(r1, r2, ha[1], ha[2], hb[4], s128);
        hmul2(r3, r4, ha[3], ha[3], hb[4], s128);
        put2(1, ha[1], r1);
        put2(2, ha[2], r2);
        put2(3, ha[3], r3);
      }
      MEMBAR();
      f16x8 fa[8], fb[8];
#pragma unroll
      for (int k = 0; k < 8; k++) fb[k] = *(const LAS f16x8*)(rb_[k]);
      SCHED();
      if (wave >= 4) STAMP(11);
      const float cre = blo(ga.z) * dt, cim = blo(ga.w) * dt;
      const float dsk = blo(gb.w);
      const float cmx = fmaxf(fmaxf(fmaxf(fabsf(cre), fabsf(cim)), fabsf(dsk) * 0.0078125f), fmaxf(fabsf(blo(gb.y)), fabsf(blo(gb.z))) * dt);
      const unsigned ce = min(max(wave_max_u(__float_as_uint(cmx)) >> 23, 40u), 250u);
      const float csc = __uint_as_float((261u - ce) << 23);
      f32x16 acc;
      unsigned have;
      auto partial = [&]() {
        MEMBAR();
#pragma unroll
        for (int k = 0; k < 4; k++) {
          const f32x4 o = *(const f32x4*)(img + k * 1024 + lane * 16);
#pragma unroll
          for (int i = 0; i < 4; i++) acc[4 * k + i] = o[i];
        }
      };
#define KSTEP(k) SCHED(); acc = __builtin_amdgcn_mfma_f32_32x32x16_f16(fa[k], fb[k], acc, 0, 0, 0); SCHED()
      {
        const cz e1 = mkz(cexp_p2(1.f, ar, ph, pl)), e512 = mkz(cexp_p2(512.f, ar, ph, pl));
        const cz e2 = csq(e1), e1024 = csq(e512), e256 = csq(e128), e4 = csq(e2);
        const f32x2 w0 = {cre * csc, -cim * csc};
        f32x2 A[3], B[3], C[3], D[3], E[3];
        unsigned a[3], b[3], c[3], d[3], e[3];
        cmulc3(A, a, w0, e8, w0, e1, w0, e256);
        put2(32 + 0, a[0], pkh2(w0));
        put1(32 + 11, a[1]);
        put1(32 + 15, a[2]);
        cmulc3(B, b, w0, e2, A[0], e256, A[0], e512);
        put1(32 + 10, b[0]);
        put2(32 + 4, b[1], a[2]);
        cmulc3(C, c, w0, e4, A[0], e1024, A[1], e2);
        put1(32 + 9, c[2]);
        cmulc3(D, d, B[1], e512, B[1], e1024, B[0], e256);
        put2(32 + 5, b[1], d[0]);
        put2(32 + 8, c[0], d[1]);
        put1(32 + 14, d[2]);
        cmulc3(E, e, C[0], e256, A[1], e4, B[2], e1024);
        put1(32 + 13, e[0]);
        put2(32 + 3, e[1], c[1]);
        put2(32 + 7, e[0], e[2]);
        SCHED();
        if (wave >= 4) {
          have = pf[ch];
          partial();
        }
        SCHED();
        cmulc3(A, a, B[0], e4, C[0], e512, C[2], e4);
        put2(32 + 2, a[0], a[1]);
        put2(32 + 1, a[2], b[2]);
        cmulc3(B, b, D[0], e1024, D[2], e4, E[0], e512);
        put2(32 + 12, e[0], b[0]);
        put2(32 + 6, b[1], b[2]);
      }
      MEMBAR();
#pragma unroll
      for (int k = 0; k < 8; k++) fa[k] = *(const LAS f16x8*)(rb_[k] + 8192);
      SCHED();
      if (wave < 4) {
        acc = f32x16{};
        KSTEP(0);
        *(v4u*)(scr + L_KN + (lane + (lane < 7 ? 0 : 64) + (lane < 19 ? 0 : 64) + (lane < 32 ? 0 : 16) + (lane < 40 ? 0 : 16)) * 16) = v4u{0u, 0u, 0u, 0u};
        KSTEP(1);
        KSTEP(2);
        float d0 = lane == 0 ? dsk * csc * 0.5f : 0.f;
        unsigned pa = wave * WSCR + lane * 16, fl = lane == 0 ? L_MISC + wave * 4 : wave * WSCR + L_DUMMY + lane * 4, one = 1u;
        PIN(d0); PIN(pa); PIN(fl); PIN(one);
#pragma unroll
        for (int k = 3; k < 8; k++) acc = __builtin_amdgcn_mfma_f32_32x32x16_f16(fa[k], fb[k], acc, 0, 0, 0);
        SCHED();
        acc[8] += d0;
        STAMP(2);
#pragma unroll
        for (int k = 0; k < 3; k++) *(LAS f32x4*)(pa + k * 1024) = f32x4{acc[4 * k], acc[4 * k + 1], acc[4 * k + 2], acc[4 * k + 3]};
        *(LAS float*)(pa + 3072) = acc[12];
        MEMBAR();
        *(volatile LAS unsigned*)fl = one;
        __builtin_amdgcn_s_setprio(0);
        STAMP(11);
        __builtin_amdgcn_s_sleep(SLEEP_U);
        __builtin_amdgcn_s_setprio(H_PRIO);
        const unsigned tag = __builtin_amdgcn_readfirstlane(epoch) + 1u;
        fetch_u<3>(rs, usrc, udst, {wave, wave + 4, wave + 8}, tag);
        STAMP(4);
        BAR();
        unsigned yb = ((lane >> 2) & 3) * (WSCR + 32) + L_Y + (lane & 1) * 128 + (wave & 1) * 64 + g * 16 + (lane & 2) * 4 + (wave >> 1) * 512;
        int yo = WS_Y + cg * GSTR + wave * 1024 + lane * 16;
        PIN(yb); PIN(yo);
        __builtin_amdgcn_s_sleep(SLEEP_U7);
        fetch_u<1>(rs, usrc, udst, {12 + wave}, tag);
        STAMP(6);
        unsigned pd = 0;
        if (wave * 64 < D_CONV) pd = *(const GAS unsigned*)(p.wd + (unsigned)(xcd * 5184 + 5184 - 18 * D_CONV + loc * D_CONV + wave * 64 + lane) * 128u);
#pragma unroll
        for (int m = 0; m < 4; m++) {
          if (m == 3) asm volatile("" ::"v"(pd));
          BAR();
          const v2u r0 = ldtr((const LAS char*)(yb + m * 1024)), r1 = ldtr((const LAS char*)(yb + m * 1024 + 256));
          __builtin_amdgcn_raw_buffer_store_b128(v4u{r0.x, r0.y, r1.x, r1.y}, rs, yo, m * 4096, SC1);
        }
        __builtin_amdgcn_s_setprio(0);
      }
      asm volatile("" : "+s"(wave));
      if (wave >= 4) {
        if (__builtin_amdgcn_readfirstlane(have) == 0u) {
#pragma unroll 1
          for (int sp = 0; sp < SPIN_CAP * 16 && pf[ch] == 0u; sp++) {}
          partial();
          PIN(acc);
        }
        v4u q[4];
        int ln = lane;
        PIN(ln);
        const int cp = ch * WSCR, c_ = ln & 15, g16 = ln & 48;
        unsigned a_kn, a_k4, a_k8, a_sp, sel_sp, op[7], top = 0xfffefffeu, kinv = (ce - 6u) << 23;
#define KASM(k, pre) asm volatile(pre "\n\tv_mfma_f32_32x32x16_f16 %0, %1, %2, %0" : "+v"(acc) : "v"(fa[k]), "v"(fb[k])); SCHED()
        KASM(0, "s_nop 1");
        a_kn = cp + L_KN + 16 * (70 - ln);
        a_k4 = cp + L_K4 + 4 * (95 - ln);
        a_k8 = cp + L_K8 + 2 * (95 - (ln & 32) - ln);
        PIN(a_kn); PIN(a_k4); PIN(a_k8);
        KASM(1, "");
        op[0] = cp + L_KN + 40 + ((c_ + 1) & 1) * KCOPY + g16 - ((2 * c_ + 2) & ~3);
        op[1] = op[0] + 1024;
        asm volatile("" : "+{v7}"(op[0]), "+{v8}"(op[1]));
        KASM(2, "");
        op[3] = L_UIMG + ch * USTR + g16 + 16 * c_;
        op[2] = op[3] + 16 * c_;
        op[4] = cp + L_K4 + 120 + g16 - 8 * c_;
        asm volatile("" : "+{v9}"(op[2]), "+{v10}"(op[3]), "+{v11}"(op[4]));
        KASM(3, "");
        op[5] = cp + L_K8 + 60 + g16 - 4 * c_;
        op[6] = cp + L_Y + ch * 32 + ln * 8;
        asm volatile("" : "+{v12}"(op[5]), "+{v13}"(op[6]), "+{v14}"(top), "+{v15}"(kinv));
        KASM(4, "");
        const unsigned dummy = cp + L_DUMMY + ln * 4;
        sel_sp = ln == 0 ? 0x0302u : ln < 7 ? 0x0504u : 0x0706u;
        PIN(sel_sp);
        KASM(5, "");
        a_sp = cp + L_KN + (ln == 0 ? 1134 : ln < 7 ? 112 : 48) - ((ln + 1) & ~1);
        PIN(a_sp);
        KASM(6, "");
        a_sp = ln < 11 ? a_sp + ((ln & 1) ? 0 : KCOPY + 2) : dummy;
        PIN(a_sp);
        KASM(7, "");
#pragma unroll
        for (int k = 0; k < 4; k++) q[k] = *(const v4u*)(smem + L_FILT + k * 1024 + lane * 16);
        SCHED();
        asm volatile("s_nop 11" : "+v"(acc));
        STAMP(2);
        const unsigned k8 = pkh(acc[11], acc[12]);
        *(LAS u16*)(a_k8 + 64) = (u16)k8;
        *(LAS u16*)a_k8 = (u16)(k8 >> 16);
        *(LAS unsigned*)a_k4 = pkh(acc[9], acc[10]);
        const v4u c0 = {pkh(acc[1], acc[2]), pkh(acc[3], acc[4]), pkh(acc[5], acc[6]), pkh(acc[7], acc[8])};
        *(LAS v4u*)a_kn = c0;
        *(LAS v4u*)(a_kn + KCOPY) = v4u{pkh(acc[0], acc[1]), pkh(acc[2], acc[3]), pkh(acc[4], acc[5]), pkh(acc[6], acc[7])};
        const unsigned kb = (unsigned)__builtin_amdgcn_readlane((int)pkh(acc[0], acc[9]), 63);
        *(LAS u16*)a_sp = (u16)__builtin_amdgcn_perm(kb, c0.w, sel_sp);
        STAMP(4);
        __builtin_amdgcn_s_setprio(M_PRIO);
        asm volatile(CONV_ASM_TEXT
                     : CONV_ASM_PRE(q)
                     : "{v14}"(top), "{v15}"(kinv), "{v7}"(op[0]), "{v8}"(op[1]), "{v9}"(op[2]), "{v10}"(op[3]), "{v11}"(op[4]), "{v12}"(op[5]),
                       "{v13}"(op[6])
                     : CONV_ASM_CLOBBERS);
        STAMP(6);
        __builtin_amdgcn_s_setprio(0);
      }
    }
    STAMP(7);
    if (loc >= 16) { STAMP_FLUSH(); return; }
    int lane;
    asm volatile("v_mbcnt_lo_u32_b32 %0, -1, 0\n\tv_mbcnt_hi_u32_b32 %0, -1, %0" : "=v"(lane));
    const int cl = lane & 15, g = lane >> 4, tid = wave * 64 + lane;
    const unsigned t7 = now();
    int o0 = WS_Y + (16 * wave + 2 * g) * GSTR + rb * 128 + (cl & 7) * 16 + (cl >> 3) * 8;
    unsigned l_sw = (cl >> 2) == g ? L_RED + wave * 256 + cl * 16 : 7 * WSCR + lane * 16, l_sr = L_RED + cl * 16;
    unsigned krow = SEQ - 1 - (16 * rb + cl);
    int a_key = lane < 16 ? WS_RREC + (16 * rb + cl) * 4 : OOB;
    PIN(o0); PIN(l_sw); PIN(l_sr); PIN(krow); PIN(a_key);
    v4u cw[2];
    f32x2 xf[8];
#pragma unroll
    for (int k = 0; k < 2; k++) {
      const v4u w = *(const v4u*)(smem + L_WR + (64 * wave + 32 * k + 8 * g) * 2);
      const v4u gx = *(const v4u*)(smem + L_XROW + wave * 2048 + cl * 128 + ((4 * k + g) ^ (cl & 7)) * 16);
#pragma unroll
      for (int i = 0; i < 4; i++) {
        cw[k][i] = (lane & 3) != 1 ? 0x3f803f80u : w[i];
        xf[4 * k + i] = f32x2{blo(gx[i]), bhi(gx[i])};
      }
    }
    float wsum = *(const float*)(smem + L_MISC + 64);
#pragma unroll
    for (int i = 0; i < 8; i++) PIN(xf[i]);
    PIN(cw[0]); PIN(cw[1]); PIN(wsum);
    if (loc >= 8) wait_until(t7, loc >= 12 ? T_Y3 : T_Y2);
    v4u hv[2];
#pragma unroll 1
    for (int sp = 0; sp < SPIN_CAP; sp++) {
      MEMBAR();
      v2u y[4];
#pragma unroll
      for (int i = 0; i < 4; i++) y[i] = __builtin_amdgcn_raw_buffer_load_b64(rs, o0, ((i & 1) + (i >> 1) * 8) * GSTR, SC1);
#pragma unroll
      for (int i = 0; i < 4; i++) {
        const f32x2 h0 = f32x2{blo(y[i].x), bhi(y[i].x)} + xf[2 * i], h1 = f32x2{blo(y[i].y), bhi(y[i].y)} + xf[2 * i + 1];
        hv[i >> 1][2 * (i & 1)] = pkbf(h0.x, h0.y);
        hv[i >> 1][2 * (i & 1) + 1] = pkbf(h1.x, h1.y);
      }
      PIN(hv[0]); PIN(hv[1]);
      if (__all(valid8(v4u{y[0].x, y[0].y, y[1].x, y[1].y}, v4u{y[2].x, y[2].y, y[3].x, y[3].y}))) break;
      __builtin_amdgcn_s_sleep(2);
    }
    STAMP(8);
    {
      f32x4 s = {0.f, 0.f, 0.f, 0.f}, q = {0.f, 0.f, 0.f, 0.f};
      q = mfma_bf(hv[0], hv[0], q);
      q = mfma_bf(hv[1], hv[1], q);
      s = mfma_bf(cw[0], hv[0], s);
      s = mfma_bf(cw[1], hv[1], s);
      s[2] = diag(q, lane);
      *(LAS f32x4*)l_sw = s;
    }
    BAR();
    if (wave == 1) __builtin_amdgcn_s_setprio(3); else __builtin_amdgcn_s_sleep(2);
    float m, s;
    const f32x4 t = sum9((const LAS char*)l_sr);
    ln_round(t[0], t[2], m, s);
    const float rinv = __builtin_amdgcn_rcpf(s);
    unsigned okey;
    {
      const unsigned b = pkbf(0.f, fmaf(-m, wsum, t[1]) * rinv);
      okey = (b ^ (((int)b >> 31 & 0x7fff0000u) | 0x80000000u)) | krow;
      if (wave == 1) st1(okey, rs, a_key);
    }
    STAMP(9);
    __builtin_amdgcn_s_setprio(0);
    char* const tl = smem + wave * 4096;
    *(v4u*)(tl + cl * 144 + g * 16) = hv[0];
    *(v4u*)(tl + cl * 144 + 64 + g * 16) = hv[1];
    MEMBAR();
    v4u key = {0u, 0u, 0u, 0u};
    unsigned selmask = 0;
#pragma unroll 1
    for (int rd = 0; rd < 4; rd++) {
      const unsigned ka = row_max_u(okey);
      okey = okey == ka ? 0u : okey;
      const unsigned kb = row_max_u(okey);
      okey = okey == kb ? 0u : okey;
      const int ra = 15 - (ka & 15), rb2 = 15 - (kb & 15);
      v4u pub = *(const v4u*)(tl + ((lane & 16) ? rb2 : ra) * 144 + (lane & 7) * 16);
      {
        const float ma = rdlane(m, ra), mb = rdlane(m, rb2), ia = rdlane(rinv, ra), ib = rdlane(rinv, rb2);
        const float pm = (lane & 16) ? mb : ma, pi = (lane & 16) ? ib : ia;
#pragma unroll
        for (int i = 0; i < 4; i++) {
          const unsigned n = ln_apply(pub[i], pm, pi);
          pub[i] = nosent((lane & 8) ? pub[i] : n);
        }
        if (lane >= 32) pub.x = (lane & 16) ? kb : ka;
      }
      PIN(pub);
      if (rd == 0 && wave < 8) {
        wait_until(t7, T_KEY);
        STAMP(1);
#pragma unroll 1
        for (int sp = 0; sp < SPIN_CAP; sp++) {
          MEMBAR();
          key = ld4(rs, WS_RREC + tid * 16);
          if (__all(valid4(key))) break;
          __builtin_amdgcn_s_sleep(2);
        }
        STAMP(10);
      }
      int ca = 0, cb = 0;
#pragma unroll
      for (int i = 0; i < 4; i++) {
        ca += __popcll(__ballot(key[i] > ka));
        cb += __popcll(__ballot(key[i] > kb));
      }
      LAS unsigned* const cnt = (LAS unsigned*)(L_MISC + 32 + rd * 4);
      if (lane == 0) __hip_atomic_fetch_add(cnt, (unsigned)(ca | (cb << 16)), __ATOMIC_RELAXED, __HIP_MEMORY_SCOPE_WORKGROUP);
      BAR();
      const unsigned cs = __builtin_amdgcn_readfirstlane(*cnt);
      const unsigned rka = cs & 0xffffu, rkb = cs >> 16;
      if (rka >= 8u) break;
      const unsigned rank = (lane & 16) ? rkb : rka;
      const int xo = lane < 32 ? (cl >> 3) * 1152 + wave * 128 + (cl & 7) * 16 : 2304;
      st4(pub, rs, (rank < 8u && (lane < 32 || (cl == 0 && wave == 0))) ? WS_XSEL + rank * XSLOT + xo : OOB);
      selmask |= 1u << ra;
      if (rkb >= 8u) break;
      selmask |= 1u << rb2;
    }
    STAMP(3);
#pragma unroll
    for (int i = 0; i < 2; i++) {
      const int r = 8 * i + (lane >> 3);
      const v4u hh = *(const v4u*)(tl + r * 144 + (lane & 7) * 16);
      const float pm = __shfl(m, r), pi = __shfl(rinv, r);
      v4u o;
#pragma unroll
      for (int k = 0; k < 4; k++) o[k] = addbf(hh[k], ln_apply(hh[k], pm, pi));
      st4(o, ro, ((selmask >> r) & 1u) ? OOB : (16 * rb + r) * 1152 + wave * 128 + (lane & 7) * 16);
    }
    STAMP_FLUSH();
    return;
  }

  const bool ffn = loc < 27;
  LAS char* xw = (LAS char*)smem + wave * 4608 + (cl >> 1) * 288 + g * 32 + (cl & 1) * 16;
  LAS char* xr_ = (LAS char*)smem + wave * 4608 + ((lane >> 2) & 3) * 288 + (lane & 1) * 128 + g * 16 + (lane & 2) * 4;
  LAS char* rw = (LAS char*)smem + L_RED + wave * 256 + cl * 8;
  int uo = WS_U + (16 * wave + (lane >> 3)) * GSTR + tile * 128 + (lane & 7) * 16;
  v4u ones = {0x3f803f80u, 0x3f803f80u, 0x3f803f80u, 0x3f803f80u};
  LAS char* sa = (LAS char*)smem + L_MISC + (lane & 4) * 8;
  unsigned keep = 0xfff8fff8u;
  PIN(xw); PIN(xr_); PIN(rw); PIN(uo); PIN(ones); PIN(sa); PIN(keep);
  SCHED();
  FIRST_WAIT(2);
  const unsigned tag = __builtin_amdgcn_readfirstlane(epoch) + 1u;
  v4u tb = tag_bits(tag);
  asm volatile("" : "+s"(tb));
  const bool fin = wave == 1 || (wave == 2 && loc >= 30);
  SCHED();
  FIRST_WAIT(0);
  PIN(cpf);
  {
    const f32x4 z4 = {0.f, 0.f, 0.f, 0.f};
    auto sums = [&](v4u a, v4u b, int tt) {
      const f32x4 s0 = mfma_bf(ones, a, z4), q0 = mfma_bf(a, a, z4), s1 = mfma_bf(ones, b, z4), q1 = mfma_bf(b, b, z4);
      *(LAS v4u*)(xw + tt * 2304) = a;
      *(LAS v4u*)(xw + tt * 2304 + 128) = b;
      if ((cl >> 2) == g) {
        *(LAS f32x2*)(rw + tt * 2304) = f32x2{s0[0], diag(q0, lane)};
        *(LAS f32x2*)(rw + tt * 2304 + 128) = f32x2{s1[0], diag(q1, lane)};
      }
    };
    sums(ga, gb, 0);
    if (loc >= 30) sums(gc, gd, 1);
    STAMP(10);
    BAR();
    STAMP(11);
    if (fin) {
      __builtin_amdgcn_s_setprio(3);
      const f32x4 t = sum9((const LAS char*)smem + L_RED + (wave - 1) * 2304 + cl * 16);
      float m, sd;
      ln_round(t[0] + t[2], t[1] + t[3], m, sd);
      const float rinv = __builtin_amdgcn_rcpf(sd);
      *(float*)(smem + L_MISC + (wave - 1) * 128 + (lane & 31) * 4) = (lane & 16) ? -m * rinv : rinv;
      BAR();
    }
#pragma unroll 1
    for (int tt = 0; tt < 2; tt++) {
      f32x2 xt[2][4];
#pragma unroll
      for (int i = 0; i < 4; i++) {
        const v2u v = ldtr(xr_ + tt * 2304 + (i >> 1) * 64 + (i & 1) * 1152);
        xt[i >> 1][2 * (i & 1)] = f32x2{blo(v.x), bhi(v.x)};
        xt[i >> 1][2 * (i & 1) + 1] = f32x2{blo(v.y), bhi(v.y)};
      }
#pragma unroll
      for (int i = 0; i < 8; i++) PIN(xt[i >> 2][i & 3]);
      if (tt == 0 && !fin) BAR();
      if (tt == 0) STAMP(12);
      const LAS f32x4* st = (const LAS f32x4*)(sa + tt * 128);
      const f32x4 r0 = st[0], r1 = st[1], c0 = st[4], c1 = st[5];
      const f32x2 rr[4] = {r0.xy, r0.zw, r1.xy, r1.zw}, cc[4] = {c0.xy, c0.zw, c1.xy, c1.zw};
#pragma unroll
      for (int i = 0; i < 2; i++) {
        v4u o;
#pragma unroll
        for (int k = 0; k < 4; k++) o[k] = (pkh2(__builtin_elementwise_fma(xt[i][k], rr[k], cc[k])) & keep) | tb[k];
        __builtin_amdgcn_raw_buffer_store_b128(o, rs, uo, i * 8 * GSTR, SC1);
      }
      if (loc < 30) break;
      uo += 16 * 128;
    }
    __builtin_amdgcn_s_setprio(0);
    BAR();
    STAMP(1);
    if (!ffn) {
      const GAS char* dn = p.wd + (size_t)xcd * 5184 * 128;
      unsigned pf[2];
#pragma unroll
      for (int i = 0; i < 2; i++) pf[i] = *(const GAS unsigned*)(dn + (unsigned)min((i * 5 + loc - 27) * NT + tid, 5183 - 18 * D_CONV) * 128u);
#pragma unroll
      for (int i = 0; i < 2; i++) asm volatile("" ::"v"(pf[i]));
      STAMP_FLUSH();
      return;
    }
  }
  v4u FW[2][8], FD[2][4];
  {
    v4u W[2][8], Dn[8];
    const GAS char* wp = ((lane & 8) ? p.wu : p.wg) + (unsigned)((64 * wave + 8 * g) * (FF * 2) + 128 * fj + 16 * (lane & 7));
    const rsrc_t rd = __builtin_amdgcn_make_buffer_rsrc((void*)p.wd, 0, DM * FF * 2, 0x00020000);
    const int wdo = (64 * fj + 8 * g + 4 * (lane & 8)) * (DM * 2) + 128 * wave + 16 * (lane & 7);
    st4(sent4, rs, WS_PART + fj * PSTR + tid * 16);
    st4(sent4, rs, WS_PART + fj * PSTR + 9216 + tid * 16);
    SCHED();
#pragma unroll
    for (int j = 0; j < 16; j++) W[j >> 3][j & 7] = *(const GAS v4u*)(wp + (32 * (j >> 3) + (j & 7)) * (FF * 2));
    SCHED();
    asm volatile("s_waitcnt vmcnt(16)" ::: "memory");
    auto down = [&]() {
      BAR();
      STAMP(12);
#pragma unroll
      for (int j = 0; j < 8; j++) Dn[j] = __builtin_amdgcn_raw_buffer_load_b128(rd, wdo, j * (DM * 2), 0);
      SCHED();
    };
    down();
    st1(tag, rs, tid == 0 ? WS_INIT + (NCONV + fj) * 64 : OOB);
    SCHED();
#pragma unroll
    for (int ks = 0; ks < 2; ks++)
#pragma unroll
      for (int c = 0; c < 8; c++) {
#pragma unroll
        for (int i = 0; i < 4; i++)
          FW[ks][c][i] = __builtin_amdgcn_perm(W[ks][2 * i + 1][c >> 1], W[ks][2 * i][c >> 1], (c & 1) ? 0x07060302u : 0x05040100u);
        PIN(FW[ks][c]);
      }
    SCHED();
#pragma unroll
    for (int j = 0; j < 8; j++)
#pragma unroll
      for (int i = 0; i < 2; i++) {
        const int lo = (int)Dn[j][i], hi = (int)Dn[j][2 + i];
        Dn[j][i] = (unsigned)__builtin_amdgcn_update_dpp(lo, hi, 0x128, 0xf, 0xc, false);
        Dn[j][2 + i] = (unsigned)__builtin_amdgcn_update_dpp(hi, lo, 0x128, 0xf, 0x3, false);
      }
#pragma unroll
    for (int ks = 0; ks < 2; ks++)
#pragma unroll
      for (int c = 0; c < 4; c++) {
#pragma unroll
        for (int i = 0; i < 4; i++)
          FD[ks][c][i] = __builtin_amdgcn_perm(Dn[2 * i + 1][2 * ks + (c >> 1)], Dn[2 * i][2 * ks + (c >> 1)], (c & 1) ? 0x07060302u : 0x05040100u);
        PIN(FD[ks][c]);
      }
  }
  STAMP(2);
  constexpr int F_GU = 0, F_ZERO = 1280, F_ALL = 2048, F_RED = 16384;
  if (wave == 0) sweep_init(rs, tag, lane, 0, NCONV);
  if (wave == 2) *(unsigned*)(smem + F_ZERO + lane * 4) = 0u;
  STAMP(3);
  const int tk = fj / 9, cb = fj - tk * 9;
  const int fcol = cb * 64 + 8 * (cl & 7) + 4 * (cl >> 3) + (g >> 1) + 2 * (g & 1);
  int a_x = WS_XSEL + (lane >> 3) * XSLOT + wave * 128 + (lane & 7) * 16, a_f = WS_XSEL + tk * XSLOT + (fcol >> 1) * 4;
  int a_po = WS_PART + fj * PSTR + ((4 * (g & 1) + 2 * (g >> 1)) * DM + 64 * wave + 4 * cl) * 4;
  int a_pi = WS_PART + (tid >> 4) * PSTR + (tk * DM + cb * 64 + cl * 4) * 4;
  unsigned l_xw = F_ALL + wave * 1152 + (lane >> 3) * 144 + (lane & 7) * 16, l_xr = F_ALL + wave * 1152 + (lane & 7) * 144 + g * 16;
  unsigned l_lo = (lane & 8) ? F_ZERO : l_xr, l_hi = (lane & 8) ? l_xr : F_ZERO, l_rw = F_RED + wave * 4096 + lane * 16;
  unsigned l_rg = F_RED + (tid >> 5) * 256 + (tid & 31) * 4, l_ru = l_rg + 128;
  unsigned l_gw = F_GU + (((tid >> 5) & 1) * 4 + (tid & 3)) * 144 + (((tid >> 2) & 7) * 8 + (tid >> 7) + ((tid >> 6) & 1) * 4) * 2;
  unsigned l_gr = F_GU + ((cl & 4) + ((cl + (cl >> 2 & 2)) & 3)) * 144 + g * 16, l_s = F_ALL + (wave == 8 ? 0 : wave + 1) * 256 + lane * 4;
  int a_k = WS_XSEL + tk * XSLOT + 2304;
  PIN(a_k); PIN(a_x); PIN(a_f); PIN(a_po); PIN(a_pi); PIN(l_xw); PIN(l_lo); PIN(l_hi); PIN(l_rw); PIN(l_rg); PIN(l_ru); PIN(l_gw); PIN(l_gr); PIN(l_s);
  if (wave == 0) {
#pragma unroll 1
    for (int sp = 0; sp < SPIN_CAP; sp++) {
      MEMBAR();
      if (__all(valid4(ld4(rs, WS_RREC + CANARY + lane * 16)))) break;
      __builtin_amdgcn_s_sleep(2);
    }
    STAMP(10);
    wait_until(now(), T_SEL);
  } else if (wave == 1) {
    sweep_init(rs, tag, lane, NCONV, NFFN);
  }
  BAR();
  unsigned w2 = 0u, h2 = 0u, fkey = 0u;
  auto flook = [&]() {
    MEMBAR();
    w2 = ld1(rs, a_f);
    h2 = ld1(rs, a_f + 1152);
    fkey = ld1(rs, a_k);
  };
  {
    v4u xv;
#pragma unroll 1
    for (int sp = 0; sp < SPIN_CAP; sp++) {
      MEMBAR();
      xv = ld4(rs, a_x);
      if (__all(valid4(xv))) break;
      __builtin_amdgcn_s_sleep(SLEEP_X);
    }
    STAMP(6);
    *(LAS v4u*)l_xw = xv;
    MEMBAR();
    const v4u lo0 = *(const LAS v4u*)l_lo, hi0 = *(const LAS v4u*)l_hi, lo1 = *(const LAS v4u*)(l_lo + 64), hi1 = *(const LAS v4u*)(l_hi + 64);
    if (wave == 8) flook();
    f32x4 a[4];
#pragma unroll
    for (int c = 0; c < 5; c++) {
      SCHED();
      if (c < 4) {
        a[c] = mfma_bf(lo0, FW[0][c], f32x4{0.f, 0.f, 0.f, 0.f});
        a[c] = mfma_bf(hi0, FW[0][c + 4], a[c]);
        a[c] = mfma_bf(lo1, FW[1][c], a[c]);
        a[c] = mfma_bf(hi1, FW[1][c + 4], a[c]);
        asm volatile("" ::"v"(FW[1][c + 4]));
      }
      SCHED();
      if (c > 0) *(LAS f32x4*)(l_rw + (c - 1) * 1024) = a[c - 1];
    }
  }
  BAR();
  STAMP(4);
  float fh = 0.f, fw = 0.f, sgm = 0.f;
  int oo = OOB;
  if (wave < 8) {
    f32x2 gv[4], uv[4];
#pragma unroll
    for (int w = 0; w < 4; w++) {
      gv[w] = f32x2{*(const LAS float*)(l_rg + w * 8192), *(const LAS float*)(l_rg + w * 8192 + 4096)};
      uv[w] = f32x2{*(const LAS float*)(l_ru + w * 8192), *(const LAS float*)(l_ru + w * 8192 + 4096)};
    }
    const float g8 = *(const LAS float*)(l_rg + 32768), u8 = *(const LAS float*)(l_ru + 32768);
    SCHED();
    f32x2 g2 = (gv[0] + gv[1]) + (gv[2] + gv[3]), u2 = (uv[0] + uv[1]) + (uv[2] + uv[3]);
    PIN(g2); PIN(u2);
    float gs = g2.x + g2.y;
    PIN(gs);
    gs += g8;
    PIN(gs);
    const float us = (u2.x + u2.y) + u8;
    const unsigned gu = pkbf(gs, us);
    const float gp = blo(gu);
    const float gq = rbf(gp * __builtin_amdgcn_rcpf(1.0f + __expf(-gp)));
    *(LAS u16*)l_gw = (u16)pkbf(gq * bhi(gu), 0.f);
  } else {
#pragma unroll 1
    for (int sp = 0; sp < SPIN_CAP && !__all(valid4(v4u{w2, h2, fkey, 0u})); sp++) flook();
    fw = (fcol & 1) ? bhi(w2) : blo(w2);
    fh = (fcol & 1) ? bhi(h2) : blo(h2);
    const unsigned kb = fkey & 0xffff0000u;
    const float rv = __uint_as_float((kb & 0x80000000u) ? (kb & 0x7fffffffu) : (~kb & 0xffff0000u));
    sgm = rbf(__builtin_amdgcn_rcpf(1.0f + __expf(-rv)));
    oo = (SEQ - 1 - (int)(fkey & 0x7ffu)) * 1152 + fcol * 2;
  }
  PIN(fw); PIN(fh); PIN(sgm); PIN(oo);
  BAR();
  STAMP(5);
  {
    const v4u a0 = *(const LAS v4u*)l_gr, a1 = *(const LAS v4u*)(l_gr + 64);
    f32x4 o[4];
#pragma unroll
    for (int c = 0; c < 4; c++) {
      o[c] = f32x4{0.f, 0.f, 0.f, 0.f};
      o[c] = mfma_bf(a0, FD[0][c], o[c]);
      o[c] = mfma_bf(a1, FD[1][c], o[c]);
    }
#pragma unroll
    for (int i = 0; i < 2; i++) {
      v4u v;
#pragma unroll
      for (int c = 0; c < 4; c++) v[c] = nosent(__float_as_uint(o[c][i]));
      st4(v, rs, a_po + i * DM * 4);
    }
  }
  STAMP(7);
  wait_until(now(), T_PART);
  {
    v4u a, b;
#pragma unroll 1
    for (int sp = 0; sp < SPIN_CAP; sp++) {
      MEMBAR();
      a = ld4(rs, a_pi);
      b = __builtin_amdgcn_raw_buffer_load_b128(rs, a_pi, 36 * PSTR, SC1);
      if (__all(valid8(a, b))) break;
      __builtin_amdgcn_s_sleep(2);
    }
    const v4u t = __builtin_bit_cast(v4u, __builtin_bit_cast(f32x4, a) + __builtin_bit_cast(f32x4, b));
    const v2u p0 = __builtin_amdgcn_permlane32_swap(t.x, t.y, false, false), p1 = __builtin_amdgcn_permlane32_swap(t.z, t.w, false, false);
    const v2u p2 = __builtin_amdgcn_permlane16_swap(__float_as_uint(__uint_as_float(p0.x) + __uint_as_float(p0.y)),
                                                    __float_as_uint(__uint_as_float(p1.x) + __uint_as_float(p1.y)), false, false);
    *(LAS float*)l_s = __uint_as_float(p2.x) + __uint_as_float(p2.y);
  }
  STAMP(8);
  BAR();
  if (wave == 8) {
    float t[9];
#pragma unroll
    for (int i = 0; i < 9; i++) t[i] = *(const LAS float*)(l_s + i * 256);
    SCHED();
    const float y = rbf((((t[0] + t[1]) + (t[2] + t[3])) + ((t[4] + t[5]) + (t[6] + t[7]))) + t[8]);
    __builtin_amdgcn_raw_buffer_store_b16((u16)pkbf(fh + rbf(fw + rbf(y * sgm)), 0.f), ro, oo, 0, 0);
    if (fj == 0 && lane == 0) st1(tag, rs, WS_EPOCH);
  }
  STAMP(9);
  STAMP_FLUSH();
}

}

extern "C" void kernel_launch(void* const* d_in, const int* in_sizes, int n_in, void* d_out, int out_size,
                              void* d_ws, size_t ws_size, hipStream_t stream) {
  (void)hipFuncSetAttribute((const void*)k_main, hipFuncAttributeMaxDynamicSharedMemorySize, LDS_BYTES);
  Params p;
  p.x = (const u16*)d_in[0];
  p.lar = (const u16*)d_in[1];
  p.aim = (const u16*)d_in[2];
  p.crl = (const u16*)d_in[3];
  p.cim = (const u16*)d_in[4];
  p.ldt = (const u16*)d_in[5];
  p.dskip = (const u16*)d_in[6];
  p.wr = (const u16*)d_in[8];
  p.wg = (const u16*)d_in[9];
  p.wu = (const u16*)d_in[10];
  p.wd = (const u16*)d_in[11];
  p.out = (u16*)d_out;
  p.ws = (char*)d_ws;
  k_main<<<256, NT, LDS_BYTES, stream>>>(p);
}
```

```cpp
#include <hip/hip_runtime.h>
#include <stdint.h>
#pragma clang diagnostic ignored "-Wint-to-pointer-cast"

namespace {

constexpr int SEQ = 2048, DM = 576, NM = 128, FF = 4608;
constexpr int NT = 576;
constexpr int NCONV = 144, NFFN = 72;
constexpr unsigned SENT = 0xFFFFFFFFu;
constexpr int SC1 = 16;
constexpr int SPIN_CAP = 1 << 16;
#define SLEEP_X 4
#define T_Y3 0
#define T_KEY 125
#define T_SEL 215
#define CODE_BYTES 24576
#define H_PRIO 3
#ifndef M_PRIO
#define M_PRIO 1
#endif
#define S_PRIO 3
#define PF_N 2
#define SLEEP_U 5
#define SLEEP_U7 4
#define EXPW 0
#define T_LOOK 105
#define T_SOON 30
#define T_SOON2 25
#define T_PART 40

typedef unsigned short u16;
typedef unsigned v4u __attribute__((ext_vector_type(4)));
typedef unsigned v2u __attribute__((ext_vector_type(2)));
typedef float f32x2 __attribute__((ext_vector_type(2)));
typedef float f32x4 __attribute__((ext_vector_type(4)));
typedef float f32x16 __attribute__((ext_vector_type(16)));
typedef _Float16 f16x2 __attribute__((ext_vector_type(2)));
typedef _Float16 f16x8 __attribute__((ext_vector_type(8)));
typedef __bf16 bf16x2 __attribute__((ext_vector_type(2)));
typedef __bf16 bf16x8 __attribute__((ext_vector_type(8)));
typedef __amdgpu_buffer_rsrc_t rsrc_t;

constexpr int WS_EPOCH = 0;
constexpr int WS_TIME = 2048;
constexpr int WS_INIT = 4096;
constexpr int WS_VOTE = 24576;
constexpr int WS_RREC = 32768;
constexpr int WS_XSEL = 65536;
constexpr int GSTR = 16384;
constexpr int WS_U = 2 << 20;
constexpr int WS_U2 = 131072;
constexpr int WS_Y = 4 << 20;
constexpr int WS_Y2 = WS_U2 + 16 * GSTR;
constexpr int WS_PART = WS_Y2 + 16 * GSTR;
constexpr int WS_END = 6 << 20;
static_assert(WS_PART + 2 * NFFN * 9216 + 16384 <= WS_U, "ws map");
__device__ __forceinline__ int part_at(int v) { return WS_PART + v + (v & ~8191); }
__device__ __forceinline__ int slot_at(int k) { return WS_XSEL + (k >> 1) * 16384 + (k & 1) * 4096; }
constexpr int OOB = 0x7ffffff0;

constexpr int WSCR = 16384;
constexpr int L_KN = 4096;
constexpr int KCOPY = 1216;
constexpr int L_K4 = L_KN + 2 * KCOPY;
constexpr int L_K8 = L_K4 + 512;
constexpr int L_Y = 8192;
constexpr int L_DUMMY = 12800;
constexpr int L_XROW = 4 * WSCR;
constexpr int L_UIMG = 8 * WSCR;
constexpr int USTR = 4640;
constexpr int L_FILT = L_UIMG + 4 * USTR;
constexpr int L_MISC = L_FILT + 4096;
constexpr int L_WR = L_MISC + 256;
constexpr int L_RED = L_WR + 1152;
constexpr int LDS_BYTES = L_RED + 2 * 2304;
static_assert(L_KN + 224 * 16 <= L_Y, "lds map");
static_assert(LDS_BYTES <= 160 * 1024, "lds size");

#define MEMBAR() asm volatile("" ::: "memory")
#define BAR() do { __builtin_amdgcn_sched_barrier(0); asm volatile("s_waitcnt lgkmcnt(0)\n\ts_barrier" ::: "memory"); __builtin_amdgcn_sched_barrier(0); } while (0)
#define LAS __attribute__((address_space(3)))
#define DRAIN() asm volatile("s_waitcnt vmcnt(0)" ::: "memory")
#define LGKM0() asm volatile("s_waitcnt lgkmcnt(0)" ::: "memory")
#define PIN(x) asm volatile("" : "+v"(x))
#define SCHED() __builtin_amdgcn_sched_barrier(0)

#define STAMP(i) do {} while (0)
#define STAMP_INIT() do {} while (0)
#define T0_ASM
#define STAMP_FLUSH() do {} while (0)

__device__ __forceinline__ float blo(unsigned v) { return __uint_as_float(v << 16); }
__device__ __forceinline__ float bhi(unsigned v) { return __uint_as_float(v & 0xffff0000u); }
__device__ __forceinline__ unsigned pkbf(float a, float b) {
  const f32x2 f = {a, b};
  return __builtin_bit_cast(unsigned, __builtin_convertvector(f, bf16x2));
}
__device__ __forceinline__ float rbf(float f) { return blo(pkbf(f, f)); }
__device__ __forceinline__ unsigned pkh(float a, float b) {
  const f32x2 f = {a, b};
  return __builtin_bit_cast(unsigned, __builtin_convertvector(f, f16x2));
}
__device__ __forceinline__ unsigned nosent(unsigned v) { return min(v, 0xFFFFFFFEu); }
__device__ __forceinline__ bool valid4(v4u v) { return max(max(v.x, v.y), max(v.z, v.w)) != SENT; }

__device__ __forceinline__ bool valid8(v4u a, v4u b) { return max(max(max(max(a.x, a.y), a.z), max(max(a.w, b.x), b.y)), max(b.z, b.w)) != SENT; }

template <int CTRL, int RM> __device__ __forceinline__ float dppf(float v) {
  return __int_as_float(__builtin_amdgcn_update_dpp(0, __float_as_int(v), CTRL, RM, 0xf, false));
}
template <int CTRL, int RM> __device__ __forceinline__ unsigned dppu(unsigned v) {
  return (unsigned)__builtin_amdgcn_update_dpp(0, (int)v, CTRL, RM, 0xf, false);
}
__device__ __forceinline__ float wave_sum(float v) {
  v += dppf<0x111, 0xf>(v);
  v += dppf<0x112, 0xf>(v);
  v += dppf<0x114, 0xf>(v);
  v += dppf<0x118, 0xf>(v);
  v += dppf<0x142, 0xa>(v);
  v += dppf<0x143, 0xc>(v);
  return __int_as_float(__builtin_amdgcn_readlane(__float_as_int(v), 63));
}
__device__ __forceinline__ unsigned wave_max_u(unsigned v) {
  v = max(v, dppu<0x111, 0xf>(v));
  v = max(v, dppu<0x112, 0xf>(v));
  v = max(v, dppu<0x114, 0xf>(v));
  v = max(v, dppu<0x118, 0xf>(v));
  const unsigned a = (unsigned)__builtin_amdgcn_readlane((int)v, 15), b = (unsigned)__builtin_amdgcn_readlane((int)v, 31);
  const unsigned c = (unsigned)__builtin_amdgcn_readlane((int)v, 47), d = (unsigned)__builtin_amdgcn_readlane((int)v, 63);
  return max(max(a, b), max(c, d));
}

__device__ __forceinline__ unsigned row_max_u(unsigned v) {
  v = max(v, dppu<0x111, 0xf>(v));
  v = max(v, dppu<0x112, 0xf>(v));
  v = max(v, dppu<0x114, 0xf>(v));
  v = max(v, dppu<0x118, 0xf>(v));
  return (unsigned)__builtin_amdgcn_readlane((int)v, 15);
}
__device__ __forceinline__ float rdlane(float v, int l) { return __int_as_float(__builtin_amdgcn_readlane(__float_as_int(v), l)); }

__device__ __forceinline__ v4u ld4(rsrc_t rs, int off) { return __builtin_amdgcn_raw_buffer_load_b128(rs, off, 0, SC1); }
__device__ __forceinline__ v2u ld2(rsrc_t rs, int off) { return __builtin_amdgcn_raw_buffer_load_b64(rs, off, 0, SC1); }
__device__ __forceinline__ unsigned ld1(rsrc_t rs, int off) { return __builtin_amdgcn_raw_buffer_load_b32(rs, off, 0, SC1); }
__device__ __forceinline__ v2u ldp2(rsrc_t rs, int off, int soff) { return __builtin_amdgcn_raw_buffer_load_b64(rs, off, soff, 0); }
__device__ __forceinline__ void st4(v4u v, rsrc_t rs, int off) { __builtin_amdgcn_raw_buffer_store_b128(v, rs, off, 0, SC1); }
__device__ __forceinline__ void st2(v2u v, rsrc_t rs, int off) { __builtin_amdgcn_raw_buffer_store_b64(v, rs, off, 0, SC1); }
__device__ __forceinline__ void st1(unsigned v, rsrc_t rs, int off) { __builtin_amdgcn_raw_buffer_store_b32(v, rs, off, 0, SC1); }

__device__ __forceinline__ f32x4 mfma_bf(v4u a, v4u b, f32x4 c) {
  return __builtin_amdgcn_mfma_f32_16x16x32_bf16(__builtin_bit_cast(bf16x8, a), __builtin_bit_cast(bf16x8, b), c, 0, 0, 0);
}
__device__ __forceinline__ f32x4 mfma_h(v4u a, v4u b, f32x4 c) {
  return __builtin_amdgcn_mfma_f32_16x16x32_f16(__builtin_bit_cast(f16x8, a), __builtin_bit_cast(f16x8, b), c, 0, 0, 0);
}

__device__ __forceinline__ float diag(f32x4 q, int lane) {
  const float qa = (lane & 1) ? q[1] : q[0], qb = (lane & 1) ? q[3] : q[2];
  return (lane & 2) ? qb : qa;
}
__device__ __forceinline__ f32x4 sum9(const LAS char* red) {
  f32x4 t[9];
#pragma unroll
  for (int w = 0; w < 9; w++) t[w] = *(const LAS f32x4*)(red + w * 256);
  SCHED();
  return (((t[0] + t[1]) + (t[2] + t[3])) + ((t[4] + t[5]) + (t[6] + t[7]))) + t[8];
}
__device__ __forceinline__ void ln_round(float sum, float sq, float& m, float& s) {
  const float mean = sum * (1.0f / DM);
  const float var = fmaxf(fmaf(-mean, mean, sq * (1.0f / DM)), 0.f);
  const unsigned mv = pkbf(mean, var);
  m = blo(mv);
  s = rbf(__builtin_amdgcn_sqrtf(rbf(bhi(mv) + 1e-5f)));
}
__device__ __forceinline__ unsigned ln_apply(unsigned xp, float m, float rinv) {
  const unsigned d = pkbf(blo(xp) - m, bhi(xp) - m);
  return pkbf(blo(d) * rinv, bhi(d) * rinv);
}
__device__ __forceinline__ unsigned addbf(unsigned a, unsigned b) { return pkbf(blo(a) + blo(b), bhi(a) + bhi(b)); }

__device__ __forceinline__ unsigned now() { return (unsigned)__builtin_amdgcn_s_memrealtime(); }
__device__ __forceinline__ void wait_until(unsigned t0, unsigned ticks) {
#pragma unroll 1
  for (int sp = 0; sp < 512 && now() - t0 < ticks; sp++) __builtin_amdgcn_s_sleep(1);
}

typedef short s16x4 __attribute__((ext_vector_type(4)));
__device__ __forceinline__ v2u ldtr(const LAS char* a) {
  return __builtin_bit_cast(v2u, __builtin_amdgcn_ds_read_tr16_b64_v4i16((LAS s16x4*)a));
}
__device__ __forceinline__ v4u tag_bits(unsigned tag) {
  v4u t;
#pragma unroll
  for (int k = 0; k < 4; k++) t[k] = ((tag >> (6 * k)) & 7u) | (((tag >> (6 * k + 3)) & 7u) << 16);
  return t;
}

template <int N>
__device__ __forceinline__ void fetch_u(rsrc_t rs, int src, char* dst, const int (&chunk)[N], unsigned tag) {
  v4u v[N];
#pragma unroll
  for (int i = 0; i < N; i++) v[i] = __builtin_amdgcn_raw_buffer_load_b128(rs, src, chunk[i] * 1024, SC1);
  SCHED();
  const v4u tb = tag_bits(tag);
#pragma unroll 1
  for (int sp = 0; sp < SPIN_CAP; sp++) {
    v4u o = {0u, 0u, 0u, 0u};
#pragma unroll
    for (int i = 0; i < N; i++) o |= v[i] ^ tb;
    if (__all(((o.x | o.y | o.z | o.w) & 0x70007u) == 0u)) break;
    __builtin_amdgcn_s_sleep(2);
    MEMBAR();
#pragma unroll
    for (int i = 0; i < N; i++) v[i] = __builtin_amdgcn_raw_buffer_load_b128(rs, src, chunk[i] * 1024, SC1);
  }
#pragma unroll
  for (int i = 0; i < N; i++) {
    const v4u w = v[i] ^ (tb ^ 0x40004u);
    v2u* const up = (v2u*)(dst + chunk[i] * 256);
    up[0] = v2u{w.x, w.y};
    up[1] = v2u{w.z, w.w};
  }
}

struct cz { f32x2 v, s; };
__device__ __forceinline__ cz mkz(f32x2 v) {
  cz z;
  z.v = v;
  asm("v_pk_mov_b32 %0, %1, %1 op_sel:[1,0]" : "=v"(z.s) : "v"(v));
  return z;
}
#define CMUL_TXT(r, a, b, bs) "v_pk_mul_f32 " r ", " a ", " bs " op_sel:[1,0] neg_lo:[0,1]\n\tv_pk_fma_f32 " r ", " a ", " b ", " r " op_sel_hi:[0,1,1]\n\t"
__device__ __forceinline__ f32x2 cmul(f32x2 a, cz b) {
  f32x2 r;
  asm(CMUL_TXT("%0", "%1", "%2", "%3") : "=&v"(r) : "v"(a), "v"(b.v), "v"(b.s));
  return r;
}
__device__ __forceinline__ cz csq(cz a) { return mkz(cmul(a.v, a)); }
#define MC_(r, a, bs) "v_pk_mul_f32 " r ", " a ", " bs " op_sel:[1,0]\n\t"
#define FC_(r, a, b) "v_pk_fma_f32 " r ", " a ", " b ", " r " op_sel_hi:[0,1,1] neg_hi:[0,1,0]\n\t"
__device__ __forceinline__ void cmulc3(f32x2 (&r)[3], unsigned (&h)[3], f32x2 a0, cz b0, f32x2 a1, cz b1, f32x2 a2, cz b2) {
  asm(MC_("%0", "%3", "%5") MC_("%1", "%6", "%8") MC_("%2", "%9", "%11") FC_("%0", "%3", "%4") FC_("%1", "%6", "%7") FC_("%2", "%9", "%10")
      : "=&v"(r[0]), "=&v"(r[1]), "=&v"(r[2])
      : "v"(a0), "v"(b0.v), "v"(b0.s), "v"(a1), "v"(b1.v), "v"(b1.s), "v"(a2), "v"(b2.v), "v"(b2.s));
#pragma unroll
  for (int i = 0; i < 3; i++) h[i] = pkh(r[i].x, r[i].y);
}
__device__ __forceinline__ void hmul2(unsigned& r, unsigned& q, unsigned a, unsigned c, unsigned b, unsigned bs) {
  asm("v_pk_mul_f16 %0, %2, %5 op_sel:[1,0] neg_lo:[0,1]\n\tv_pk_mul_f16 %1, %3, %5 op_sel:[1,0] neg_lo:[0,1]\n\t"
      "v_pk_fma_f16 %0, %2, %4, %0 op_sel_hi:[0,1,1]\n\tv_pk_fma_f16 %1, %3, %4, %1 op_sel_hi:[0,1,1]"
      : "=&v"(r), "=&v"(q) : "v"(a), "v"(c), "v"(b), "v"(bs));
}
__device__ __forceinline__ float exp_c(float v) {
  const float t = v * 1.44269502f;
  const float e = fmaf(v, 1.92596303e-8f, fmaf(v, 1.44269502f, -t));
  return __builtin_amdgcn_exp2f(t) * fmaf(e, 0.693147182f, 1.f);
}
__device__ __forceinline__ f32x2 cexp_p2(float p, float ch, float ph, float pl) {
  const float mag = __builtin_amdgcn_exp2f(p * ch);
  const float fr = fmaf(p, pl, __builtin_amdgcn_fractf(p * ph));
  return f32x2{mag * __builtin_amdgcn_cosf(fr), mag * __builtin_amdgcn_sinf(fr)};
}
__device__ __forceinline__ unsigned pkh2(f32x2 v) { return pkh(v.x, v.y); }

#define CONV_ASM_TEXT \
  "s_barrier\n" "ds_read_b128 v[32:35], %8 offset:480\n" \
  "ds_read_b128 v[36:39], %8 offset:992\n" "ds_read_b128 v[40:43], %8 offset:1504\n" \
  "ds_read2_b32 v[44:45], %11 offset0:48 offset1:49\n" "ds_read2_b32 v[46:47], %11 offset0:50 offset1:51\n" \
  "ds_read2_b32 v[48:49], %11 offset0:32 offset1:33\n" "s_waitcnt lgkmcnt(5)\n" \
  "v_mfma_f32_16x16x32_f16 v[132:135], v[32:35], v[16:19], 0\n" "s_nop 0\n" \
  "ds_read2_b32 v[50:51], %11 offset0:34 offset1:35\n" "ds_read2_b32 v[52:53], %11 offset0:16 offset1:17\n" \
  "v_mfma_f32_16x16x32_f16 v[128:131], v[32:35], v[20:23], 0\n" "s_waitcnt lgkmcnt(6)\n" \
  "ds_read2_b32 v[54:55], %11 offset0:18 offset1:19\n" "ds_read2_b32 v[56:57], %11 offset0:0 offset1:1\n" \
  "v_mfma_f32_16x16x32_f16 v[124:127], v[36:39], v[16:19], 0\n" "s_nop 0\n" \
  "ds_read2_b32 v[58:59], %11 offset0:2 offset1:3\n" "ds_read_b128 v[60:63], %8 offset:2016\n" \
  "v_mfma_f32_16x16x32_f16 v[120:123], v[36:39], v[20:23], 0\n" "s_waitcnt lgkmcnt(9)\n" \
  "v_cvt_pk_f16_f32 v116, v132, v128\n" "v_cvt_pk_f16_f32 v117, v133, v129\n" \
  "v_mfma_f32_16x16x32_f16 v[164:167], v[40:43], v[16:19], 0\n" "v_cvt_pk_f16_f32 v118, v134, v130\n" \
  "v_cvt_pk_f16_f32 v119, v135, v131\n" "v_mfma_f32_16x16x32_f16 v[160:163], v[40:43], v[20:23], 0\n" \
  "s_waitcnt lgkmcnt(7)\n" "ds_read_b128 v[64:67], %9 offset:480\n" \
  "v_cvt_pk_f16_f32 v112, v124, v120\n" "v_mfma_f32_16x16x32_f16 v[148:151], v[116:119], v[44:47], 0\n" \
  "s_waitcnt lgkmcnt(6)\n" "ds_read_b128 v[68:71], %9 offset:736\n" \
  "v_cvt_pk_f16_f32 v113, v125, v121\n" "v_mfma_f32_16x16x32_f16 v[144:147], v[116:119], v[48:51], 0\n" \
  "s_waitcnt lgkmcnt(5)\n" "v_cvt_pk_f16_f32 v114, v126, v122\n" \
  "v_cvt_pk_f16_f32 v115, v127, v123\n" "v_mfma_f32_16x16x32_f16 v[140:143], v[116:119], v[52:55], 0\n" \
  "s_waitcnt lgkmcnt(3)\n" "ds_read_b128 v[72:75], %9 offset:992\n" \
  "ds_read_b64 v[76:77], %10 offset:256\n" "v_mfma_f32_16x16x32_f16 v[136:139], v[116:119], v[56:59], 0\n" \
  "s_waitcnt lgkmcnt(4)\n" "ds_read_b64 v[78:79], %10 offset:264\n" \
  "v_cvt_pk_f16_f32 v116, v164, v160\n" "v_mfma_f32_16x16x32_f16 v[132:135], v[60:63], v[16:19], 0\n" \
  "v_cvt_pk_f16_f32 v117, v165, v161\n" "v_cvt_pk_f16_f32 v118, v166, v162\n" \
  "v_mfma_f32_16x16x32_f16 v[128:131], v[60:63], v[20:23], 0\n" "ds_read_b64 v[80:81], %10 offset:128\n" \
  "v_cvt_pk_f16_f32 v119, v167, v163\n" "v_mfma_f32_16x16x32_f16 v[144:147], v[112:115], v[44:47], v[144:147]\n" \
  "ds_read_b64 v[82:83], %10 offset:136\n" "v_mfma_f32_16x16x32_f16 v[140:143], v[112:115], v[48:51], v[140:143]\n" \
  "s_nop 0\n" "ds_read_b64 v[84:85], %10 offset:0\n" \
  "ds_read_b64 v[86:87], %10 offset:8\n" "v_mfma_f32_16x16x32_f16 v[136:139], v[112:115], v[52:55], v[136:139]\n" \
  "s_waitcnt lgkmcnt(7)\n" "ds_read_b128 v[88:91], %9 offset:1248\n" \
  "v_cvt_pk_f16_f32 v112, v132, v128\n" "v_mfma_f32_16x16x32_f16 v[124:127], v[64:67], v[24:27], 0\n" \
  "v_cvt_pk_f16_f32 v113, v133, v129\n" "v_cvt_pk_f16_f32 v114, v134, v130\n" \
  "v_mfma_f32_16x16x32_f16 v[120:123], v[64:67], v[28:31], 0\n" "ds_read_b64 v[92:93], %10 offset:320\n" \
  "v_cvt_pk_f16_f32 v115, v135, v131\n" "v_mfma_f32_16x16x32_f16 v[140:143], v[116:119], v[44:47], v[140:143]\n" \
  "ds_read_b64 v[94:95], %10 offset:328\n" "v_mfma_f32_16x16x32_f16 v[136:139], v[116:119], v[48:51], v[136:139]\n" \
  "s_nop 0\n" "ds_read_b64 v[96:97], %10 offset:192\n" \
  "ds_read_b64 v[98:99], %10 offset:200\n" "v_mfma_f32_16x16x32_f16 v[164:167], v[68:71], v[24:27], 0\n" \
  "s_nop 0\n" "ds_read_b64 v[100:101], %10 offset:64\n" \
  "ds_read_b64 v[102:103], %10 offset:72\n" "v_mfma_f32_16x16x32_f16 v[160:163], v[68:71], v[28:31], 0\n" \
  "v_cvt_pk_f16_f32 v116, v124, v120\n" "v_cvt_pk_f16_f32 v117, v125, v121\n" \
  "v_mfma_f32_16x16x32_f16 v[136:139], v[112:115], v[44:47], v[136:139]\n" "s_waitcnt lgkmcnt(7)\n" \
  "v_cvt_pk_f16_f32 v118, v126, v122\n" "v_cvt_pk_f16_f32 v119, v127, v123\n" \
  "v_mfma_f32_16x16x32_f16 v[132:135], v[72:75], v[24:27], 0\n" "ds_read_b128 v[104:107], %9 offset:1504\n" \
  "v_cvt_pk_f16_f32 v112, v164, v160\n" "v_mfma_f32_16x16x32_f16 v[128:131], v[72:75], v[28:31], 0\n" \
  "v_cvt_pk_f16_f32 v113, v165, v161\n" "v_cvt_pk_f16_f32 v114, v166, v162\n" \
  "v_mfma_f32_16x16x32_f16 v[156:159], v[116:119], v[76:79], 0\n" "ds_read_b128 v[108:111], %9 offset:1760\n" \
  "v_cvt_pk_f16_f32 v115, v167, v163\n" "v_mfma_f32_16x16x32_f16 v[152:155], v[116:119], v[80:83], 0\n" \
  "s_nop 0\n" "ds_read2_b32 v[32:33], %6 offset0:144 offset1:145\n" \
  "ds_read2_b32 v[34:35], %6 offset0:146 offset1:147\n" "v_mfma_f32_16x16x32_f16 v[148:151], v[116:119], v[84:87], v[148:151]\n" \
  "s_waitcnt lgkmcnt(6)\n" "v_cvt_pk_f16_f32 v116, v132, v128\n" \
  "v_cvt_pk_f16_f32 v117, v133, v129\n" "v_mfma_f32_16x16x32_f16 v[124:127], v[88:91], v[24:27], 0\n" \
  "v_cvt_pk_f16_f32 v118, v134, v130\n" "v_cvt_pk_f16_f32 v119, v135, v131\n" \
  "v_mfma_f32_16x16x32_f16 v[120:123], v[88:91], v[28:31], 0\n" "s_nop 0\n" \
  "ds_read_b128 v[36:39], %9 offset:2016\n" "ds_read2_b32 v[40:41], %6 offset0:16 offset1:17\n" \
  "v_mfma_f32_16x16x32_f16 v[156:159], v[112:115], v[92:95], v[156:159]\n" "ds_read2_b32 v[42:43], %6 offset0:18 offset1:19\n" \
  "v_mfma_f32_16x16x32_f16 v[152:155], v[112:115], v[96:99], v[152:155]\n" "s_waitcnt lgkmcnt(6)\n" \
  "ds_read2_b32 v[56:57], %6 offset0:48 offset1:49\n" "ds_read2_b32 v[58:59], %6 offset0:50 offset1:51\n" \
  "v_mfma_f32_16x16x32_f16 v[148:151], v[112:115], v[100:103], v[148:151]\n" "v_cvt_pk_f16_f32 v112, v124, v120\n" \
  "v_cvt_pk_f16_f32 v113, v125, v121\n" "v_mfma_f32_16x16x32_f16 v[164:167], v[104:107], v[24:27], 0\n" \
  "v_cvt_pk_f16_f32 v114, v126, v122\n" "v_cvt_pk_f16_f32 v115, v127, v123\n" \
  "v_mfma_f32_16x16x32_f16 v[160:163], v[104:107], v[28:31], 0\n" "s_nop 0\n" \
  "ds_read_b128 v[16:19], %9 offset:2272\n" "ds_read2_b32 v[60:61], %6 offset0:176 offset1:177\n" \
  "v_mfma_f32_16x16x32_f16 v[152:155], v[116:119], v[76:79], v[152:155]\n" "ds_read2_b32 v[62:63], %6 offset0:178 offset1:179\n" \
  "v_mfma_f32_16x16x32_f16 v[148:151], v[116:119], v[80:83], v[148:151]\n" "s_nop 0\n" \
  "ds_read2_b32 v[20:21], %6 offset0:32 offset1:33\n" "ds_read2_b32 v[22:23], %6 offset0:34 offset1:35\n" \
  "v_mfma_f32_16x16x32_f16 v[144:147], v[116:119], v[84:87], v[144:147]\n" "s_waitcnt lgkmcnt(8)\n" \
  "v_cvt_pk_f16_f32 v116, v164, v160\n" "v_cvt_pk_f16_f32 v117, v165, v161\n" \
  "v_mfma_f32_16x16x32_f16 v[132:135], v[108:111], v[24:27], 0\n" "v_cvt_pk_f16_f32 v118, v166, v162\n" \
  "v_cvt_pk_f16_f32 v119, v167, v163\n" "v_mfma_f32_16x16x32_f16 v[128:131], v[108:111], v[28:31], 0\n" \
  "s_nop 0\n" "ds_read_b128 v[52:55], %9 offset:2528\n" \
  "ds_read2_b32 v[64:65], %6 offset0:160 offset1:161\n" "v_mfma_f32_16x16x32_f16 v[152:155], v[112:115], v[92:95], v[152:155]\n" \
  "ds_read2_b32 v[66:67], %6 offset0:162 offset1:163\n" "v_mfma_f32_16x16x32_f16 v[148:151], v[112:115], v[96:99], v[148:151]\n" \
  "s_nop 0\n" "ds_read2_b32 v[48:49], %6 offset0:80 offset1:81\n" \
  "ds_read2_b32 v[50:51], %6 offset0:82 offset1:83\n" "v_mfma_f32_16x16x32_f16 v[144:147], v[112:115], v[100:103], v[144:147]\n" \
  "v_cvt_pk_f16_f32 v112, v132, v128\n" "v_cvt_pk_f16_f32 v113, v133, v129\n" \
  "v_mfma_f32_16x16x32_f16 v[124:127], v[36:39], v[24:27], 0\n" "v_cvt_pk_f16_f32 v114, v134, v130\n" \
  "v_cvt_pk_f16_f32 v115, v135, v131\n" "v_mfma_f32_16x16x32_f16 v[120:123], v[36:39], v[28:31], 0\n" \
  "s_nop 0\n" "ds_read_b128 v[68:71], %9 offset:2784\n" \
  "ds_read2_b32 v[44:45], %6 offset0:208 offset1:209\n" "v_mfma_f32_16x16x32_f16 v[148:151], v[116:119], v[76:79], v[148:151]\n" \
  "ds_read2_b32 v[46:47], %6 offset0:210 offset1:211\n" "v_mfma_f32_16x16x32_f16 v[144:147], v[116:119], v[80:83], v[144:147]\n" \
  "s_nop 0\n" "ds_read2_b32 v[72:73], %6 offset0:64 offset1:65\n" \
  "ds_read2_b32 v[74:75], %6 offset0:66 offset1:67\n" "v_mfma_f32_16x16x32_f16 v[140:143], v[116:119], v[84:87], v[140:143]\n" \
  "s_waitcnt lgkmcnt(8)\n" "v_cvt_pk_f16_f32 v116, v124, v120\n" \
  "v_cvt_pk_f16_f32 v117, v125, v121\n" "v_mfma_f32_16x16x32_f16 v[132:135], v[16:19], v[24:27], 0\n" \
  "v_cvt_pk_f16_f32 v118, v126, v122\n" "v_cvt_pk_f16_f32 v119, v127, v123\n" \
  "v_mfma_f32_16x16x32_f16 v[128:131], v[16:19], v[28:31], 0\n" "s_nop 0\n" \
  "ds_read_b128 v[88:91], %9 offset:3040\n" "ds_read2_b32 v[104:105], %6 offset0:192 offset1:193\n" \
  "v_mfma_f32_16x16x32_f16 v[148:151], v[112:115], v[92:95], v[148:151]\n" "ds_read2_b32 v[106:107], %6 offset0:194 offset1:195\n" \
  "v_mfma_f32_16x16x32_f16 v[144:147], v[112:115], v[96:99], v[144:147]\n" "s_nop 0\n" \
  "ds_read_b128 v[108:111], %8 offset:0\n" "ds_read2_b32 v[36:37], %6 offset0:224 offset1:225\n" \
  "v_mfma_f32_16x16x32_f16 v[140:143], v[112:115], v[100:103], v[140:143]\n" "v_cvt_pk_f16_f32 v112, v132, v128\n" \
  "v_cvt_pk_f16_f32 v113, v133, v129\n" "v_mfma_f32_16x16x32_f16 v[124:127], v[52:55], v[24:27], 0\n" \
  "v_cvt_pk_f16_f32 v114, v134, v130\n" "v_cvt_pk_f16_f32 v115, v135, v131\n" \
  "v_mfma_f32_16x16x32_f16 v[120:123], v[52:55], v[28:31], 0\n" "s_nop 0\n" \
  "ds_read2_b32 v[38:39], %6 offset0:226 offset1:227\n" "ds_read_b128 v[16:19], %9 offset:3296\n" \
  "v_mfma_f32_16x16x32_f16 v[144:147], v[116:119], v[76:79], v[144:147]\n" "ds_read2_b32 v[52:53], %6 offset0:96 offset1:97\n" \
  "v_mfma_f32_16x16x32_f16 v[140:143], v[116:119], v[80:83], v[140:143]\n" "ds_read2_b32 v[54:55], %6 offset0:98 offset1:99\n" \
  "v_mfma_f32_16x16x32_f16 v[136:139], v[116:119], v[84:87], v[136:139]\n" "s_waitcnt lgkmcnt(7)\n" \
  "v_cvt_pk_f16_f32 v116, v124, v120\n" "v_cvt_pk_f16_f32 v117, v125, v121\n" \
  "v_mfma_f32_16x16x32_f16 v[132:135], v[68:71], v[24:27], 0\n" "v_cvt_pk_f16_f32 v118, v126, v122\n" \
  "v_cvt_pk_f16_f32 v119, v127, v123\n" "v_mfma_f32_16x16x32_f16 v[128:131], v[68:71], v[28:31], 0\n" \
  "s_nop 0\n" "ds_read_b128 v[84:87], %8 offset:128\n" \
  "ds_read2_b32 v[68:69], %6 offset0:240 offset1:241\n" "v_mfma_f32_16x16x32_f16 v[144:147], v[112:115], v[92:95], v[144:147]\n" \
  "ds_read2_b32 v[70:71], %6 offset0:242 offset1:243\n" "v_mfma_f32_16x16x32_f16 v[140:143], v[112:115], v[96:99], v[140:143]\n" \
  "v_mfma_f32_16x16x32_f16 v[136:139], v[112:115], v[100:103], v[136:139]\n" "ds_read_b128 v[100:103], %8 offset:64\n" \
  "v_cvt_pk_f16_f32 v112, v132, v128\n" "v_mfma_f32_16x16x32_f16 v[124:127], v[88:91], v[24:27], 0\n" \
  "v_cvt_pk_f16_f32 v113, v133, v129\n" "v_cvt_pk_f16_f32 v114, v134, v130\n" \
  "v_mfma_f32_16x16x32_f16 v[120:123], v[88:91], v[28:31], 0\n" "s_waitcnt lgkmcnt(4)\n" \
  "ds_read_b128 v[88:91], %8 offset:256\n" "v_cvt_pk_f16_f32 v115, v135, v131\n" \
  "v_mfma_f32_16x16x32_f16 v[164:167], v[32:35], v[108:111], 0\n" "v_mfma_f32_16x16x32_f16 v[140:143], v[116:119], v[76:79], v[140:143]\n" \
  "v_mfma_f32_16x16x32_f16 v[136:139], v[116:119], v[80:83], v[136:139]\n" "s_nop 0\n" \
  "ds_read_b128 v[80:83], %8 offset:192\n" "v_cvt_pk_f16_f32 v116, v124, v120\n" \
  "v_mfma_f32_16x16x32_f16 v[132:135], v[16:19], v[24:27], 0\n" "v_cvt_pk_f16_f32 v117, v125, v121\n" \
  "v_cvt_pk_f16_f32 v118, v126, v122\n" "v_mfma_f32_16x16x32_f16 v[128:131], v[16:19], v[28:31], 0\n" \
  "ds_read_b128 v[24:27], %8 offset:320\n" "v_cvt_pk_f16_f32 v119, v127, v123\n" \
  "v_mfma_f32_16x16x32_f16 v[160:163], v[40:43], v[108:111], 0\n" "s_nop 0\n" \
  "ds_read2_b32 v[16:17], %6 offset0:112 offset1:113\n" "ds_read2_b32 v[18:19], %6 offset0:114 offset1:115\n" \
  "v_mfma_f32_16x16x32_f16 v[140:143], v[112:115], v[92:95], v[140:143]\n" "ds_read_b128 v[28:31], %8 offset:384\n" \
  "v_mfma_f32_16x16x32_f16 v[136:139], v[112:115], v[96:99], v[136:139]\n" "s_waitcnt lgkmcnt(5)\n" \
  "ds_read2_b32 v[124:125], %7 offset0:0 offset1:1\n" "v_cvt_pk_f16_f32 v112, v132, v128\n" \
  "v_mfma_f32_16x16x32_f16 v[160:163], v[56:59], v[84:87], v[160:163]\n" "v_cvt_pk_f16_f32 v113, v133, v129\n" \
  "v_cvt_pk_f16_f32 v114, v134, v130\n" "v_mfma_f32_16x16x32_f16 v[164:167], v[60:63], v[84:87], v[164:167]\n" \
  "ds_read2_b32 v[126:127], %7 offset0:2 offset1:3\n" "v_cvt_pk_f16_f32 v115, v135, v131\n" \
  "v_mfma_f32_16x16x32_f16 v[160:163], v[20:23], v[100:103], v[160:163]\n" "s_nop 0\n" \
  "ds_read_b128 v[108:111], %8 offset:448\n" "ds_read2_b32 v[120:121], %6 offset0:128 offset1:129\n" \
  "v_mfma_f32_16x16x32_f16 v[164:167], v[64:67], v[100:103], v[164:167]\n" "ds_read2_b32 v[122:123], %6 offset0:130 offset1:131\n" \
  "v_mfma_f32_16x16x32_f16 v[136:139], v[116:119], v[76:79], v[136:139]\n" "s_nop 0\n" \
  "ds_read2_b32 v[96:97], %7 offset0:16 offset1:17\n" "ds_read2_b32 v[98:99], %7 offset0:18 offset1:19\n" \
  "v_mfma_f32_16x16x32_f16 v[160:163], v[48:51], v[88:91], v[160:163]\n" "ds_read_b128 v[84:87], %8 offset:512\n" \
  "v_mfma_f32_16x16x32_f16 v[164:167], v[44:47], v[88:91], v[164:167]\n" "s_waitcnt lgkmcnt(8)\n" \
  "v_mfma_f32_16x16x32_f16 v[160:163], v[72:75], v[80:83], v[160:163]\n" "s_nop 0\n" \
  "ds_read2_b32 v[132:133], %6 offset0:0 offset1:1\n" "ds_read2_b32 v[134:135], %6 offset0:2 offset1:3\n" \
  "v_mfma_f32_16x16x32_f16 v[164:167], v[104:107], v[80:83], v[164:167]\n" "ds_read_b128 v[128:131], %8 offset:576\n" \
  "v_mfma_f32_16x16x32_f16 v[136:139], v[112:115], v[92:95], v[136:139]\n" "ds_read_b128 v[100:103], %8 offset:640\n" \
  "v_mfma_f32_16x16x32_f16 v[164:167], v[36:39], v[24:27], v[164:167]\n" "ds_read_b128 v[116:119], %8 offset:704\n" \
  "v_mfma_f32_16x16x32_f16 v[160:163], v[52:55], v[24:27], v[160:163]\n" "v_mfma_f32_16x16x32_f16 v[164:167], v[68:71], v[28:31], v[164:167]\n" \
  "s_nop 0\n" "ds_read_b128 v[76:79], %8 offset:768\n" \
  "ds_read_b128 v[88:91], %8 offset:832\n" "v_mfma_f32_16x16x32_f16 v[160:163], v[16:19], v[28:31], v[160:163]\n" \
  "s_waitcnt lgkmcnt(7)\n" "ds_read_b128 v[80:83], %8 offset:896\n" \
  "v_mfma_f32_16x16x32_f16 v[164:167], v[124:127], v[108:111], v[164:167]\n" "ds_read_b128 v[112:115], %8 offset:960\n" \
  "v_mfma_f32_16x16x32_f16 v[160:163], v[120:123], v[108:111], v[160:163]\n" "ds_read_b128 v[92:95], %8 offset:1024\n" \
  "v_mfma_f32_16x16x32_f16 v[164:167], v[96:99], v[84:87], v[164:167]\n" "v_mfma_f32_16x16x32_f16 v[160:163], v[32:35], v[84:87], v[160:163]\n" \
  "s_nop 0\n" "ds_read_b128 v[24:27], %8 offset:1088\n" \
  "ds_read_b128 v[28:31], %8 offset:1152\n" "v_mfma_f32_16x16x32_f16 v[156:159], v[40:43], v[84:87], v[156:159]\n" \
  "s_waitcnt lgkmcnt(7)\n" "ds_read_b128 v[108:111], %8 offset:1216\n" \
  "v_mfma_f32_16x16x32_f16 v[148:151], v[132:135], v[84:87], v[148:151]\n" "v_mul_f32 v164, %5, v164\n" \
  "v_mul_f32 v165, %5, v165\n" "v_mfma_f32_16x16x32_f16 v[160:163], v[64:67], v[128:131], v[160:163]\n" \
  "v_mul_f32 v166, %5, v166\n" "v_mul_f32 v167, %5, v167\n" \
  "v_mfma_f32_16x16x32_f16 v[156:159], v[20:23], v[128:131], v[156:159]\n" "v_cvt_pk_bf16_f32 v164, v164, v165\n" \
  "v_cvt_pk_bf16_f32 v165, v166, v167\n" "v_mfma_f32_16x16x32_f16 v[160:163], v[60:63], v[100:103], v[160:163]\n" \
  "v_pk_min_u16 v164, v164, %4\n" "v_pk_min_u16 v165, v165, %4\n" \
  "v_mfma_f32_16x16x32_f16 v[156:159], v[56:59], v[100:103], v[156:159]\n" "ds_write_b64 %12, v[164:165] offset:0\n" \
  "v_mfma_f32_16x16x32_f16 v[160:163], v[104:107], v[116:119], v[160:163]\n" "s_nop 0\n" \
  "ds_read_b128 v[84:87], %8 offset:1280\n" "ds_read_b128 v[128:131], %8 offset:1344\n" \
  "v_mfma_f32_16x16x32_f16 v[156:159], v[72:75], v[116:119], v[156:159]\n" "s_waitcnt lgkmcnt(4)\n" \
  "ds_read_b128 v[100:103], %8 offset:1408\n" "ds_read_b128 v[164:167], %8 offset:1472\n" \
  "v_mfma_f32_16x16x32_f16 v[160:163], v[44:47], v[76:79], v[160:163]\n" "v_mfma_f32_16x16x32_f16 v[156:159], v[48:51], v[76:79], v[156:159]\n" \
  "v_mfma_f32_16x16x32_f16 v[160:163], v[36:39], v[88:91], v[160:163]\n" "ds_read_b128 v[116:119], %8 offset:1536\n" \
  "v_mfma_f32_16x16x32_f16 v[156:159], v[52:55], v[88:91], v[156:159]\n" "s_nop 0\n" \
  "ds_read_b128 v[76:79], %8 offset:1600\n" "ds_read_b128 v[88:91], %8 offset:1664\n" \
  "v_mfma_f32_16x16x32_f16 v[160:163], v[68:71], v[80:83], v[160:163]\n" "v_mfma_f32_16x16x32_f16 v[156:159], v[16:19], v[80:83], v[156:159]\n" \
  "ds_read_b128 v[80:83], %8 offset:1728\n" "v_mfma_f32_16x16x32_f16 v[160:163], v[124:127], v[112:115], v[160:163]\n" \
  "v_mfma_f32_16x16x32_f16 v[156:159], v[120:123], v[112:115], v[156:159]\n" "ds_read_b128 v[112:115], %8 offset:1792\n" \
  "v_mfma_f32_16x16x32_f16 v[160:163], v[96:99], v[92:95], v[160:163]\n" "v_mfma_f32_16x16x32_f16 v[156:159], v[32:35], v[92:95], v[156:159]\n" \
  "v_mfma_f32_16x16x32_f16 v[152:155], v[40:43], v[92:95], v[152:155]\n" "v_mfma_f32_16x16x32_f16 v[144:147], v[132:135], v[92:95], v[144:147]\n" \
  "ds_read_b128 v[92:95], %8 offset:1856\n" "v_mfma_f32_16x16x32_f16 v[156:159], v[64:67], v[24:27], v[156:159]\n" \
  "v_mfma_f32_16x16x32_f16 v[152:155], v[20:23], v[24:27], v[152:155]\n" "ds_read_b128 v[24:27], %8 offset:1920\n" \
  "v_mfma_f32_16x16x32_f16 v[156:159], v[60:63], v[28:31], v[156:159]\n" "v_mul_f32 v160, %5, v160\n" \
  "v_mul_f32 v161, %5, v161\n" "v_mfma_f32_16x16x32_f16 v[152:155], v[56:59], v[28:31], v[152:155]\n" \
  "s_waitcnt lgkmcnt(2)\n" "v_mul_f32 v162, %5, v162\n" \
  "v_mul_f32 v163, %5, v163\n" "v_mfma_f32_16x16x32_f16 v[156:159], v[104:107], v[108:111], v[156:159]\n" \
  "v_cvt_pk_bf16_f32 v160, v160, v161\n" "v_cvt_pk_bf16_f32 v161, v162, v163\n" \
  "v_mfma_f32_16x16x32_f16 v[152:155], v[72:75], v[108:111], v[152:155]\n" "v_pk_min_u16 v160, v160, %4\n" \
  "v_pk_min_u16 v161, v161, %4\n" "v_mfma_f32_16x16x32_f16 v[156:159], v[44:47], v[84:87], v[156:159]\n" \
  "ds_write_b64 %12, v[160:161] offset:512\n" "v_mfma_f32_16x16x32_f16 v[152:155], v[48:51], v[84:87], v[152:155]\n" \
  "s_nop 0\n" "ds_read_b128 v[28:31], %8 offset:1984\n" \
  "ds_read_b128 v[108:111], %8 offset:2048\n" "v_mfma_f32_16x16x32_f16 v[156:159], v[36:39], v[128:131], v[156:159]\n" \
  "s_nop 0\n" "ds_read_b128 v[160:163], %8 offset:2112\n" \
  "ds_read_b128 v[84:87], %8 offset:2176\n" "v_mfma_f32_16x16x32_f16 v[152:155], v[52:55], v[128:131], v[152:155]\n" \
  "v_mfma_f32_16x16x32_f16 v[156:159], v[68:71], v[100:103], v[156:159]\n" "v_mfma_f32_16x16x32_f16 v[152:155], v[16:19], v[100:103], v[152:155]\n" \
  "s_waitcnt lgkmcnt(4)\n" "ds_read_b128 v[128:131], %8 offset:2240\n" \
  "v_mfma_f32_16x16x32_f16 v[156:159], v[124:127], v[164:167], v[156:159]\n" "s_barrier\n" \
  "ds_read_b128 v[100:103], %8 offset:2304\n" "v_mfma_f32_16x16x32_f16 v[152:155], v[120:123], v[164:167], v[152:155]\n" \
  "ds_read_b128 v[164:167], %8 offset:2368\n" "v_mfma_f32_16x16x32_f16 v[156:159], v[96:99], v[116:119], v[156:159]\n" \
  "v_mfma_f32_16x16x32_f16 v[152:155], v[32:35], v[116:119], v[152:155]\n" "v_mfma_f32_16x16x32_f16 v[148:151], v[40:43], v[116:119], v[148:151]\n" \
  "v_mfma_f32_16x16x32_f16 v[140:143], v[132:135], v[116:119], v[140:143]\n" "ds_read_b128 v[116:119], %8 offset:2432\n" \
  "v_mfma_f32_16x16x32_f16 v[152:155], v[64:67], v[76:79], v[152:155]\n" "v_mfma_f32_16x16x32_f16 v[148:151], v[20:23], v[76:79], v[148:151]\n" \
  "ds_read_b128 v[76:79], %8 offset:2496\n" "v_mfma_f32_16x16x32_f16 v[152:155], v[60:63], v[88:91], v[152:155]\n" \
  "v_mul_f32 v156, %5, v156\n" "v_mul_f32 v157, %5, v157\n" \
  "v_mfma_f32_16x16x32_f16 v[148:151], v[56:59], v[88:91], v[148:151]\n" "v_mul_f32 v158, %5, v158\n" \
  "v_mul_f32 v159, %5, v159\n" "v_mfma_f32_16x16x32_f16 v[152:155], v[104:107], v[80:83], v[152:155]\n" \
  "v_cvt_pk_bf16_f32 v156, v156, v157\n" "v_cvt_pk_bf16_f32 v157, v158, v159\n" \
  "v_mfma_f32_16x16x32_f16 v[148:151], v[72:75], v[80:83], v[148:151]\n" "v_pk_min_u16 v156, v156, %4\n" \
  "v_pk_min_u16 v157, v157, %4\n" "v_mfma_f32_16x16x32_f16 v[152:155], v[44:47], v[112:115], v[152:155]\n" \
  "ds_write_b64 %12, v[156:157] offset:1024\n" "v_mfma_f32_16x16x32_f16 v[148:151], v[48:51], v[112:115], v[148:151]\n" \
  "s_nop 0\n" "ds_read_b128 v[88:91], %8 offset:2560\n" \
  "ds_read_b128 v[80:83], %8 offset:2624\n" "v_mfma_f32_16x16x32_f16 v[152:155], v[36:39], v[92:95], v[152:155]\n" \
  "s_nop 0\n" "ds_read_b128 v[156:159], %8 offset:2688\n" \
  "ds_read_b128 v[112:115], %8 offset:2752\n" "v_mfma_f32_16x16x32_f16 v[148:151], v[52:55], v[92:95], v[148:151]\n" \
  "v_mfma_f32_16x16x32_f16 v[152:155], v[68:71], v[24:27], v[152:155]\n" "v_mfma_f32_16x16x32_f16 v[148:151], v[16:19], v[24:27], v[148:151]\n" \
  "s_waitcnt lgkmcnt(5)\n" "ds_read_b128 v[92:95], %8 offset:2816\n" \
  "v_mfma_f32_16x16x32_f16 v[152:155], v[124:127], v[28:31], v[152:155]\n" "ds_read_b128 v[24:27], %8 offset:2880\n" \
  "v_mfma_f32_16x16x32_f16 v[148:151], v[120:123], v[28:31], v[148:151]\n" "ds_read_b128 v[28:31], %8 offset:2944\n" \
  "v_mfma_f32_16x16x32_f16 v[152:155], v[96:99], v[108:111], v[152:155]\n" "v_mfma_f32_16x16x32_f16 v[148:151], v[32:35], v[108:111], v[148:151]\n" \
  "v_mfma_f32_16x16x32_f16 v[144:147], v[40:43], v[108:111], v[144:147]\n" "v_mfma_f32_16x16x32_f16 v[136:139], v[132:135], v[108:111], v[136:139]\n" \
  "ds_read_b128 v[132:135], %8 offset:3008\n" "v_mfma_f32_16x16x32_f16 v[148:151], v[64:67], v[160:163], v[148:151]\n" \
  "v_mfma_f32_16x16x32_f16 v[144:147], v[20:23], v[160:163], v[144:147]\n" "v_mfma_f32_16x16x32_f16 v[148:151], v[60:63], v[84:87], v[148:151]\n" \
  "s_nop 0\n" "v_mul_f32 v152, %5, v152\n" \
  "v_mul_f32 v153, %5, v153\n" "v_mfma_f32_16x16x32_f16 v[144:147], v[56:59], v[84:87], v[144:147]\n" \
  "v_mul_f32 v154, %5, v154\n" "v_mul_f32 v155, %5, v155\n" \
  "v_mfma_f32_16x16x32_f16 v[148:151], v[104:107], v[128:131], v[148:151]\n" "v_cvt_pk_bf16_f32 v152, v152, v153\n" \
  "v_cvt_pk_bf16_f32 v153, v154, v155\n" "v_mfma_f32_16x16x32_f16 v[144:147], v[72:75], v[128:131], v[144:147]\n" \
  "v_pk_min_u16 v152, v152, %4\n" "v_pk_min_u16 v153, v153, %4\n" \
  "v_mfma_f32_16x16x32_f16 v[148:151], v[44:47], v[100:103], v[148:151]\n" "ds_write_b64 %12, v[152:153] offset:1536\n" \
  "v_mfma_f32_16x16x32_f16 v[144:147], v[48:51], v[100:103], v[144:147]\n" "v_mfma_f32_16x16x32_f16 v[148:151], v[36:39], v[164:167], v[148:151]\n" \
  "v_mfma_f32_16x16x32_f16 v[144:147], v[52:55], v[164:167], v[144:147]\n" "v_mfma_f32_16x16x32_f16 v[148:151], v[68:71], v[116:119], v[148:151]\n" \
  "v_mfma_f32_16x16x32_f16 v[144:147], v[16:19], v[116:119], v[144:147]\n" "s_waitcnt lgkmcnt(0)\n" \
  "v_mfma_f32_16x16x32_f16 v[148:151], v[124:127], v[76:79], v[148:151]\n" "s_barrier\n" \
  "ds_read_b128 v[108:111], %8 offset:3072\n" "ds_read_b128 v[160:163], %8 offset:3136\n" \
  "v_mfma_f32_16x16x32_f16 v[144:147], v[120:123], v[76:79], v[144:147]\n" "s_nop 0\n" \
  "ds_read_b128 v[84:87], %8 offset:3200\n" "ds_read_b128 v[128:131], %8 offset:3264\n" \
  "v_mfma_f32_16x16x32_f16 v[148:151], v[96:99], v[88:91], v[148:151]\n" "ds_read_b128 v[152:155], %8 offset:3328\n" \
  "v_mfma_f32_16x16x32_f16 v[144:147], v[32:35], v[88:91], v[144:147]\n" "s_nop 2\n" \
  "v_mfma_f32_16x16x32_f16 v[140:143], v[40:43], v[88:91], v[140:143]\n" "s_nop 1\n" \
  "v_mfma_f32_16x16x32_f16 v[144:147], v[64:67], v[80:83], v[144:147]\n" "v_mul_f32 v148, %5, v148\n" \
  "v_mul_f32 v149, %5, v149\n" "v_mfma_f32_16x16x32_f16 v[140:143], v[20:23], v[80:83], v[140:143]\n" \
  "v_mul_f32 v150, %5, v150\n" "v_mul_f32 v151, %5, v151\n" \
  "v_mfma_f32_16x16x32_f16 v[144:147], v[60:63], v[156:159], v[144:147]\n" "v_cvt_pk_bf16_f32 v148, v148, v149\n" \
  "v_cvt_pk_bf16_f32 v149, v150, v151\n" "v_mfma_f32_16x16x32_f16 v[140:143], v[56:59], v[156:159], v[140:143]\n" \
  "v_pk_min_u16 v148, v148, %4\n" "v_pk_min_u16 v149, v149, %4\n" \
  "v_mfma_f32_16x16x32_f16 v[144:147], v[104:107], v[112:115], v[144:147]\n" "ds_write_b64 %12, v[148:149] offset:2048\n" \
  "v_mfma_f32_16x16x32_f16 v[140:143], v[72:75], v[112:115], v[140:143]\n" "s_nop 0\n" \
  "ds_read_b128 v[100:103], %8 offset:3392\n" "ds_read_b128 v[164:167], %8 offset:3456\n" \
  "v_mfma_f32_16x16x32_f16 v[144:147], v[44:47], v[92:95], v[144:147]\n" "s_nop 0\n" \
  "ds_read_b128 v[116:119], %8 offset:3520\n" "ds_read_b128 v[76:79], %8 offset:3584\n" \
  "v_mfma_f32_16x16x32_f16 v[140:143], v[48:51], v[92:95], v[140:143]\n" "v_mfma_f32_16x16x32_f16 v[144:147], v[36:39], v[24:27], v[144:147]\n" \
  "v_mfma_f32_16x16x32_f16 v[140:143], v[52:55], v[24:27], v[140:143]\n" "ds_read_b128 v[88:91], %8 offset:3648\n" \
  "v_mfma_f32_16x16x32_f16 v[144:147], v[68:71], v[28:31], v[144:147]\n" "s_nop 0\n" \
  "ds_read_b128 v[80:83], %8 offset:3712\n" "ds_read_b128 v[156:159], %8 offset:3776\n" \
  "v_mfma_f32_16x16x32_f16 v[140:143], v[16:19], v[28:31], v[140:143]\n" "s_nop 0\n" \
  "ds_read_b128 v[148:151], %8 offset:3840\n" "ds_read_b128 v[112:115], %8 offset:3904\n" \
  "v_mfma_f32_16x16x32_f16 v[144:147], v[124:127], v[132:135], v[144:147]\n" "v_mfma_f32_16x16x32_f16 v[140:143], v[120:123], v[132:135], v[140:143]\n" \
  "s_waitcnt lgkmcnt(7)\n" "v_mfma_f32_16x16x32_f16 v[144:147], v[96:99], v[108:111], v[144:147]\n" \
  "ds_read_b128 v[92:95], %8 offset:3968\n" "v_mfma_f32_16x16x32_f16 v[140:143], v[32:35], v[108:111], v[140:143]\n" \
  "s_nop 0\n" "ds_read_b128 v[24:27], %8 offset:4032\n" \
  "ds_read_b128 v[28:31], %8 offset:4096\n" "v_mfma_f32_16x16x32_f16 v[136:139], v[40:43], v[108:111], v[136:139]\n" \
  "s_nop 1\n" "v_mfma_f32_16x16x32_f16 v[140:143], v[64:67], v[160:163], v[140:143]\n" \
  "v_mul_f32 v144, %5, v144\n" "v_mul_f32 v145, %5, v145\n" \
  "v_mfma_f32_16x16x32_f16 v[140:143], v[60:63], v[84:87], v[140:143]\n" "v_mul_f32 v146, %5, v146\n" \
  "v_mul_f32 v147, %5, v147\n" "v_mfma_f32_16x16x32_f16 v[140:143], v[104:107], v[128:131], v[140:143]\n" \
  "v_cvt_pk_bf16_f32 v144, v144, v145\n" "v_cvt_pk_bf16_f32 v145, v146, v147\n" \
  "v_mfma_f32_16x16x32_f16 v[140:143], v[44:47], v[152:155], v[140:143]\n" "v_pk_min_u16 v144, v144, %4\n" \
  "v_pk_min_u16 v145, v145, %4\n" "v_mfma_f32_16x16x32_f16 v[140:143], v[36:39], v[100:103], v[140:143]\n" \
  "ds_write_b64 %12, v[144:145] offset:2560\n" "v_mfma_f32_16x16x32_f16 v[140:143], v[68:71], v[164:167], v[140:143]\n" \
  "s_waitcnt lgkmcnt(3)\n" "v_mfma_f32_16x16x32_f16 v[140:143], v[124:127], v[116:119], v[140:143]\n" \
  "v_mfma_f32_16x16x32_f16 v[140:143], v[96:99], v[76:79], v[140:143]\n" "s_nop 2\n" \
  "v_mfma_f32_16x16x32_f16 v[136:139], v[20:23], v[160:163], v[136:139]\n" "s_nop 2\n" \
  "v_mfma_f32_16x16x32_f16 v[136:139], v[56:59], v[84:87], v[136:139]\n" "s_waitcnt lgkmcnt(0)\n" \
  "v_mul_f32 v140, %5, v140\n" "v_mul_f32 v141, %5, v141\n" \
  "v_mfma_f32_16x16x32_f16 v[136:139], v[72:75], v[128:131], v[136:139]\n" "s_barrier\n" \
  "v_mul_f32 v142, %5, v142\n" "v_mul_f32 v143, %5, v143\n" \
  "v_mfma_f32_16x16x32_f16 v[136:139], v[48:51], v[152:155], v[136:139]\n" "v_cvt_pk_bf16_f32 v140, v140, v141\n" \
  "v_cvt_pk_bf16_f32 v141, v142, v143\n" "v_mfma_f32_16x16x32_f16 v[136:139], v[52:55], v[100:103], v[136:139]\n" \
  "v_pk_min_u16 v140, v140, %4\n" "v_pk_min_u16 v141, v141, %4\n" \
  "v_mfma_f32_16x16x32_f16 v[136:139], v[16:19], v[164:167], v[136:139]\n" "ds_write_b64 %12, v[140:141] offset:3072\n" \
  "v_mfma_f32_16x16x32_f16 v[136:139], v[120:123], v[116:119], v[136:139]\n" "v_mfma_f32_16x16x32_f16 v[136:139], v[32:35], v[76:79], v[136:139]\n" \
  "v_mfma_f32_16x16x32_f16 v[136:139], v[64:67], v[88:91], v[136:139]\n" "v_mfma_f32_16x16x32_f16 v[136:139], v[60:63], v[80:83], v[136:139]\n" \
  "v_mfma_f32_16x16x32_f16 v[136:139], v[104:107], v[156:159], v[136:139]\n" "v_mfma_f32_16x16x32_f16 v[136:139], v[44:47], v[148:151], v[136:139]\n" \
  "v_mfma_f32_16x16x32_f16 v[136:139], v[36:39], v[112:115], v[136:139]\n" "v_mfma_f32_16x16x32_f16 v[136:139], v[68:71], v[92:95], v[136:139]\n" \
  "v_mfma_f32_16x16x32_f16 v[136:139], v[124:127], v[24:27], v[136:139]\n" "v_mfma_f32_16x16x32_f16 v[136:139], v[96:99], v[28:31], v[136:139]\n" \
  "s_nop 2\n" "s_nop 4\n" \
  "v_mul_f32 v136, %5, v136\n" "v_mul_f32 v137, %5, v137\n" \
  "v_mul_f32 v138, %5, v138\n" "v_mul_f32 v139, %5, v139\n" \
  "v_cvt_pk_bf16_f32 v136, v136, v137\n" "v_cvt_pk_bf16_f32 v137, v138, v139\n" \
  "v_pk_min_u16 v136, v136, %4\n" "v_pk_min_u16 v137, v137, %4\n" \
  "ds_write_b64 %12, v[136:137] offset:3584\n" "s_waitcnt lgkmcnt(0)\n" \
  "s_barrier\n"
#define CONV_ASM_CLOBBERS "v32", "v33", "v34", "v35", "v36", "v37", "v38", "v39", "v40", "v41", "v42", "v43", "v44", "v45", "v46", "v47", "v48", "v49", "v50", "v51", "v52", "v53", "v54", "v55", "v56", "v57", "v58", "v59", "v60", "v61", "v62", "v63", "v64", "v65", "v66", "v67", "v68", "v69", "v70", "v71", "v72", "v73", "v74", "v75", "v76", "v77", "v78", "v79", "v80", "v81", "v82", "v83", "v84", "v85", "v86", "v87", "v88", "v89", "v90", "v91", "v92", "v93", "v94", "v95", "v96", "v97", "v98", "v99", "v100", "v101", "v102", "v103", "v104", "v105", "v106", "v107", "v108", "v109", "v110", "v111", "v112", "v113", "v114", "v115", "v116", "v117", "v118", "v119", "v120", "v121", "v122", "v123", "v124", "v125", "v126", "v127", "v128", "v129", "v130", "v131", "v132", "v133", "v134", "v135", "v136", "v137", "v138", "v139", "v140", "v141", "v142", "v143", "v144", "v145", "v146", "v147", "v148", "v149", "v150", "v151", "v152", "v153", "v154", "v155", "v156", "v157", "v158", "v159", "v160", "v161", "v162", "v163", "v164", "v165", "v166", "v167", "memory"
#define CONV_ASM_PRE(q) "+{v[16:19]}"(q[0]), "+{v[20:23]}"(q[1]), "+{v[24:27]}"(q[2]), "+{v[28:31]}"(q[3])

struct Params {
  const u16 *x, *lar, *aim, *crl, *cim, *ldt, *dskip, *wr, *wg, *wu, *wd;
  u16* out;
  char* ws;
};

typedef unsigned long long u64;
typedef u64 v4q __attribute__((ext_vector_type(4)));
typedef unsigned v8u __attribute__((ext_vector_type(8)));
typedef unsigned v16u __attribute__((ext_vector_type(16)));
#define GAS __attribute__((address_space(1)))

__global__ __launch_bounds__(NT, 3) void k_main(Params) {
  extern __shared__ __attribute__((aligned(16))) char smem[];
  u64 kx, kw, t0_;
  v4q kb_;
  unsigned epoch;
  v4u ga, gb, gc, gd;
  const unsigned bid = blockIdx.x;
  asm volatile(
      ".p2align 12\n\t"
      "s_load_dwordx16 s[8:23], s[0:1], 0x0\n\t"
      "s_getpc_b64 s[4:5]\n\t"
      "s_load_dwordx2 s[36:37], s[0:1], 0x60\n\t"
      "s_load_dwordx8 s[24:31], s[0:1], 0x40\n\t" T0_ASM
      "v_lshrrev_b32 v17, 6, v0\n\t"
      "s_lshl_b32 s3, s2, 4\n\t"
      "v_mov_b32 v1, 0\n\t"
      "v_lshl_add_u32 v17, v17, 12, s3\n\t"
      "s_cmpk_lt_u32 s2, 144\n\t"
      "v_min_u32 v17, %4, v17\n\t"
      "global_load_dword v17, v17, s[4:5]\n\t"
      "s_cbranch_scc1 Lc_%=\n\t"
      "s_add_i32 s3, s2, 0xffffff70\n\t"
      "v_bfe_u32 v18, v0, 1, 3\n\t"
      "v_lshlrev_b32 v19, 1, v0\n\t"
      "s_mulk_i32 s3, 18432\n\t"
      "v_lshlrev_b32 v20, 4, v0\n\t"
      "v_mul_u32_u24 v18, 1152, v18\n\t"
      "v_bfi_b32 v19, 31, v20, v19\n\t"
      "v_add3_u32 v18, v18, v19, s3\n\t"
      "s_cmpk_lt_u32 s2, 240\n\t"
      "v_add_u32 v19, 9216, v18\n\t"
      "v_add_u32 v20, 294912, v18\n\t"
      "v_add_u32 v21, 304128, v18\n\t"
      "s_waitcnt lgkmcnt(0)\n\t"
      "global_load_dword v1, v1, s[36:37] sc1\n\t"
      "global_load_dwordx4 v[2:5], v18, s[8:9]\n\t"
      "global_load_dwordx4 v[6:9], v19, s[8:9]\n\t"
      "s_cbranch_scc1 Le_%=\n\t"
      "global_load_dwordx4 v[10:13], v20, s[8:9]\n\t"
      "global_load_dwordx4 v[14:17], v21, s[8:9]\n\t"
      "s_branch Le_%=\n"
      "Lw_%=:\n\t"
      "v_lshlrev_b32 v7, 2, v0\n\t"
      "s_waitcnt lgkmcnt(0)\n\t"
      "global_load_dword v1, v1, s[36:37] sc1\n\t"
      "global_load_dword v2, v7, s[22:23] offset:-2048\n\t"
      "global_load_dword v3, v7, s[22:23] offset:-1792\n\t"
      "global_load_dword v4, v7, s[22:23] offset:-1536\n\t"
      "global_load_dword v5, v7, s[22:23] offset:-1280\n\t"
      "global_load_dword v6, v7, s[22:23] offset:-1152\n\t"
      "s_branch Le_%=\n"
      "Lc_%=:\n\t"
      "v_readfirstlane_b32 s3, v0\n\t"
      "v_bfe_u32 v6, v0, 6, 2\n\t"
      "s_lshl_b32 s4, s2, 2\n\t"
      "v_bfe_u32 v5, v0, 0, 8\n\t"
      "s_lshl_b32 s5, s2, 10\n\t"
      "s_cmpk_ge_u32 s3, 512\n\t"
      "v_add_lshl_u32 v16, v6, s4, 1\n\t"
      "s_cbranch_scc1 Lw_%=\n\t"
      "v_lshl_add_u32 v5, v5, 2, s5\n\t"
      "s_lshr_b32 s4, s3, 8\n\t"
      "s_bfe_u32 s5, s3, 0x20006\n\t"
      "v_lshl_add_u32 v3, s4, 1, v5\n\t"
      "v_and_b32 v7, 63, v0\n\t"
      "s_mulk_i32 s5, %5\n\t"
      "s_mulk_i32 s4, 3584\n\t"
      "v_mov_b64 v[10:11], 0\n\t"
      "s_add_i32 s5, s5, s4\n\t"
      "v_mov_b64 v[12:13], 0\n\t"
      "s_add_i32 s5, s5, %6\n\t"
      "v_lshlrev_b32 v9, 4, v7\n\t"
      "s_lshl_b32 s3, s3, 8\n\t"
      "v_add_u32 v8, s5, v9\n\t"
      "v_add_u32 v9, %7, v9\n\t"
      "ds_write_b128 v8, v[10:13]\n\t"
      "ds_write_b128 v8, v[10:13] offset:32\n\t"
      "ds_write_b128 v9, v[10:13]\n\t"
      "v_lshl_or_b32 v18, v7, 2, s3\n\t"
      "v_lshrrev_b32 v14, 5, v7\n\t"
      "v_and_b32 v15, 31, v7\n\t"
      "v_bitop3_b32 v14, v14, v7, 15 bitop3:0x28\n\t"
      "v_lshl_or_b32 v15, v15, 8, s3\n\t"
      "s_waitcnt lgkmcnt(0)\n\t"
      "global_load_ushort v6, v16, s[18:19]\n\t"
      "global_load_ushort v2, v3, s[10:11]\n\t"
      "global_load_ushort v3, v3, s[12:13]\n\t"
      "global_load_dword v4, v5, s[14:15]\n\t"
      "global_load_dword v5, v5, s[16:17]\n\t"
      "global_load_ushort v8, v16, s[20:21]\n\t"
      "global_load_dword v1, v1, s[36:37] sc1\n\t"
      "v_xor_b32 v19, 16, v18\n\t"
      "v_xor_b32 v20, 32, v18\n\t"
      "v_xor_b32 v21, 48, v18\n\t"
      "v_xor_b32 v22, 64, v18\n\t"
      "v_xor_b32 v26, 0x80, v18\n\t"
      "v_xor_b32 v23, 16, v22\n\t"
      "v_xor_b32 v24, 32, v22\n\t"
      "v_xor_b32 v25, 48, v22\n\t"
      "v_xor_b32 v30, 64, v26\n\t"
      "v_xor_b32 v27, 16, v26\n\t"
      "v_xor_b32 v28, 32, v26\n\t"
      "v_xor_b32 v29, 48, v26\n\t"
      "v_xor_b32 v31, 16, v30\n\t"
      "v_xor_b32 v32, 32, v30\n\t"
      "v_xor_b32 v33, 48, v30\n\t"
      "v_lshl_or_b32 v34, v14, 4, v15\n\t"
      "v_xor_b32 v35, 32, v34\n\t"
      "v_xor_b32 v36, 64, v34\n\t"
      "v_xor_b32 v38, 0x80, v34\n\t"
      "v_xor_b32 v37, 32, v36\n\t"
      "v_xor_b32 v39, 32, v38\n\t"
      "v_xor_b32 v40, 64, v38\n\t"
      "v_xor_b32 v41, 32, v40\n"
      "Le_%=:"
      : "=&{s[8:9]}"(kx), "=&{s[36:37]}"(kw), "=&{s[24:31]}"(kb_), "=&{s[34:35]}"(t0_)
      : "n"(CODE_BYTES), "n"(USTR), "n"(L_UIMG), "n"(L_MISC), "{s[0:1]}"(__builtin_amdgcn_kernarg_segment_ptr()), "{s2}"(bid), "{v0}"(threadIdx.x)
      : "s3", "s4", "s5", "s10", "s11", "s12", "s13", "s14", "s15", "s16", "s17", "s18", "s19", "s20", "s21", "s22", "s23", "v1", "v2", "v3", "v4",
        "v5", "v6", "v7", "v8", "v9", "v10", "v11", "v12", "v13", "v14", "v15", "v16", "v17", "v18", "v19", "v20", "v21", "v22", "v23", "v24", "v25",
        "v26", "v27", "v28", "v29", "v30", "v31", "v32", "v33", "v34", "v35", "v36", "v37", "v38", "v39", "v40", "v41", "scc", "memory");
  v16u k16;
  v8u k8;
  asm volatile("" : "={v1}"(epoch), "={v[2:5]}"(ga), "={v[6:9]}"(gb), "={v[10:13]}"(gc), "={v[14:17]}"(gd), "={v[18:33]}"(k16), "={v[34:41]}"(k8)::"memory");
  SCHED();
  const int tid = threadIdx.x, lane = tid & 63;
  int wave = __builtin_amdgcn_readfirstlane(tid >> 6);
  const int xcd = bid & 7;
  int loc = bid >> 3;
  STAMP_INIT();
  STAMP(11);
  if (EXPW == 1) { DRAIN(); STAMP(13); SCHED(); }
  const int cl = lane & 15, g = lane >> 4;
  const int cg = bid;
  const int fj = xcd * 9 + (loc - 18);
  const int rb = bid;
  const int side = (bid << 11) & 8192;
  const int ybase = (cg < 128 ? WS_Y : WS_Y2 - 128 * GSTR) + cg * GSTR;
  const int tile = bid - NCONV;
  struct { const GAS char* x; GAS char* ws; } p = {(const GAS char*)kx, (GAS char*)kw};
#define FIRST_WAIT(n) asm volatile("s_waitcnt vmcnt(%5) ; first loads" : "+{v1}"(epoch), "+{v[2:5]}"(ga), "+{v[6:9]}"(gb), "+{v[10:13]}"(gc), "+{v[14:17]}"(gd) : "n"(n) : "memory")
  const rsrc_t rs = __builtin_amdgcn_make_buffer_rsrc((void*)p.ws, 0, WS_END, 0x00020000);
  const v4u sent4 = {SENT, SENT, SENT, SENT};
  const rsrc_t ro = __builtin_amdgcn_make_buffer_rsrc((void*)kb_[3], 0, SEQ * DM * 2, 0x00020000);
  if (loc < 18) {
    if (wave < 4) {
      const unsigned win = (unsigned)(0x0380007cfe0001ffull >> (16 * wave)) & 0xffffu;
      const unsigned bits = (win << cl) >> (8 * g);
      v4u f;
#pragma unroll
      for (int i = 0; i < 4; i++) f[i] = __umul24(__umul24((bits >> (2 * i)) & 3u, 0x8001u) & 0x10001u, 0x3c00u);
      *(LAS v4u*)(L_FILT + wave * 1024 + lane * 16) = f;
    }
    BAR();
    if (wave == 8) asm volatile("s_mov_b64 exec, 0\n\ts_branch Lk_conv\nLk_back:\n\ts_mov_b64 exec, -1");
  }
  asm volatile("" : "+s"(loc), "+s"(wave));

  if (loc < 18) {
    if (wave < 8) {
      const int ch = wave & 3;
      char* const img = smem + ch * WSCR;
      char* const scr = smem + wave * WSCR;
      volatile LAS unsigned* const pf = (volatile LAS unsigned*)(smem + L_MISC);
      if (wave < 4) __builtin_amdgcn_s_setprio(2); else __builtin_amdgcn_s_setprio(1);
      STAMP(12);
      unsigned epoch;
      v4u ga;
      v4u gb;
      v16u wb_;
      v8u rb_;
      asm volatile("s_waitcnt vmcnt(0) ; first loads" : "={v1}"(epoch), "={v[2:5]}"(ga), "={v[6:9]}"(gb), "={v[18:33]}"(wb_), "={v[34:41]}"(rb_)::"memory");
      const int lane = gb.y;
      STAMP(5);
      unsigned tag = __builtin_amdgcn_readfirstlane(epoch) + 1u;
      asm volatile("" : "+s"(tag));
      const float dt = rbf(exp_c(blo(gb.x)));
      const float ar = -rbf(exp_c(blo(ga.x))) * dt * 1.44269504f;
      const float aie = blo(ga.y) * dt;
      const float ph = aie * 0.159154936f;
      const float pl = fmaf(aie, 0.159154936f, -ph) + aie * 7.13048051e-9f;
      auto put2 = [&](int row, unsigned x, unsigned y) {
        *(LAS unsigned*)(wb_[row & 15] + row * 256) = x;
        *(LAS unsigned*)(wb_[row & 15] + (row + 16) * 256) = y;
      };
      auto put1 = [&](int row, unsigned x) { *(LAS unsigned*)(wb_[row & 15] + row * 256) = x; };
      const cz e8 = mkz(cexp_p2(8.f, ar, ph, pl)), e64 = mkz(cexp_p2(64.f, ar, ph, pl));
      const cz e16 = csq(e8), e128 = csq(e64), e32 = csq(e16);
      {
        const f32x2 e24 = cmul(e16.v, e8), e192 = cmul(e128.v, e64);
        const unsigned ha[4] = {0x3c00u, pkh2(e8.v), pkh2(e16.v), pkh2(e24)};
        const unsigned hs[4] = {0u, pkh2(e8.s), pkh2(e16.s), pkh(e24.y, e24.x)};
        const unsigned hb[8] = {0x3c00u, pkh2(e32.v), pkh2(e64.v), pkh2(cmul(e64.v, e32)), pkh2(e128.v), pkh2(cmul(e128.v, e32)), pkh2(e192), pkh2(cmul(e192, e32))};
#pragma unroll
        for (int k = 0; k < 4; k++) put2(4 * k, hb[k], hb[k + 4]);
#pragma unroll
        for (int i = 1; i < 4; i++) {
          unsigned r[8];
          hmul2(r[1], r[5], hb[1], hb[5], ha[i], hs[i]);
          put2(4 + i, r[1], r[5]);
          hmul2(r[2], r[6], hb[2], hb[6], ha[i], hs[i]);
          put2(8 + i, r[2], r[6]);
          hmul2(r[3], r[7], hb[3], hb[7], ha[i], hs[i]);
          put2(12 + i, r[3], r[7]);
        }
        unsigned r1, r2, r3, r4;
        const unsigned s128 = pkh2(e128.s);
        hmul2(r1, r2, ha[1], ha[2], hb[4], s128);
        hmul2(r3, r4, ha[3], ha[3], hb[4], s128);
        put2(1, ha[1], r1);
        put2(2, ha[2], r2);
        put2(3, ha[3], r3);
      }
      MEMBAR();
      f16x8 fa[8], fb[8];
#pragma unroll
      for (int k = 0; k < 8; k++) fb[k] = *(const LAS f16x8*)(rb_[k]);
      SCHED();
      if (wave >= 4) STAMP(11);
      const float cre = (wave < 4 ? blo(ga.z) : bhi(ga.z)) * dt, cim = (wave < 4 ? blo(ga.w) : bhi(ga.w)) * dt;
      const float dsk = blo(gb.z);
      const float cmx = fmaxf(fmaxf(fmaxf(fabsf(blo(ga.z)), fabsf(bhi(ga.z))), fmaxf(fabsf(blo(ga.w)), fabsf(bhi(ga.w)))) * dt, fabsf(dsk) * 0.0078125f);
      const unsigned ce = min(max(wave_max_u(__float_as_uint(cmx)) >> 23, 40u), 250u);
      const float csc = __uint_as_float((261u - ce) << 23);
      f32x16 acc;
      unsigned have;
      auto partial = [&]() {
        MEMBAR();
#pragma unroll
        for (int k = 0; k < 4; k++) {
          const f32x4 o = *(const f32x4*)(img + k * 1024 + lane * 16);
#pragma unroll
          for (int i = 0; i < 4; i++) acc[4 * k + i] = o[i];
        }
      };
#define KSTEP(k) SCHED(); acc = __builtin_amdgcn_mfma_f32_32x32x16_f16(fa[k], fb[k], acc, 0, 0, 0); SCHED()
      {
        const cz e1 = mkz(cexp_p2(1.f, ar, ph, pl)), e512 = mkz(cexp_p2(512.f, ar, ph, pl));
        const cz e2 = csq(e1), e1024 = csq(e512), e256 = csq(e128), e4 = csq(e2);
        const f32x2 w0 = {cre * csc, -cim * csc};
        f32x2 A[3], B[3], C[3], D[3], E[3];
        unsigned a[3], b[3], c[3], d[3], e[3];
        cmulc3(A, a, w0, e8, w0, e1, w0, e256);
        put2(32 + 0, a[0], pkh2(w0));
        put1(32 + 11, a[1]);
        put1(32 + 15, a[2]);
        cmulc3(B, b, w0, e2, A[0], e256, A[0], e512);
        put1(32 + 10, b[0]);
        put2(32 + 4, b[1], a[2]);
        cmulc3(C, c, w0, e4, A[0], e1024, A[1], e2);
        put1(32 + 9, c[2]);
        cmulc3(D, d, B[1], e512, B[1], e1024, B[0], e256);
        put2(32 + 5, b[1], d[0]);
        put2(32 + 8, c[0], d[1]);
        put1(32 + 14, d[2]);
        cmulc3(E, e, C[0], e256, A[1], e4, B[2], e1024);
        put1(32 + 13, e[0]);
        put2(32 + 3, e[1], c[1]);
        put2(32 + 7, e[0], e[2]);
        SCHED();
        if (wave >= 4) {
          have = pf[ch];
          partial();
        }
        SCHED();
        cmulc3(A, a, B[0], e4, C[0], e512, C[2], e4);
        put2(32 + 2, a[0], a[1]);
        put2(32 + 1, a[2], b[2]);
        cmulc3(B, b, D[0], e1024, D[2], e4, E[0], e512);
        put2(32 + 12, e[0], b[0]);
        put2(32 + 6, b[1], b[2]);
      }
      MEMBAR();
#pragma unroll
      for (int k = 0; k < 8; k++) fa[k] = *(const LAS f16x8*)(rb_[k] + 8192);
      SCHED();
      if (wave < 4) {
        acc = f32x16{};
        KSTEP(0);
        *(v4u*)(scr + L_KN + (lane + (lane < 7 ? 0 : 64) + (lane < 19 ? 0 : 64) + (lane < 32 ? 0 : 16) + (lane < 40 ? 0 : 16)) * 16) = v4u{0u, 0u, 0u, 0u};
        KSTEP(1);
        KSTEP(2);
        float d0 = lane == 0 ? dsk * csc * 0.5f : 0.f;
        unsigned pa = wave * WSCR + lane * 16, fl = lane == 0 ? L_MISC + wave * 4 : wave * WSCR + L_DUMMY + lane * 4, one = 1u;
        PIN(d0); PIN(pa); PIN(fl); PIN(one);
#pragma unroll
        for (int k = 3; k < 8; k++) acc = __builtin_amdgcn_mfma_f32_32x32x16_f16(fa[k], fb[k], acc, 0, 0, 0);
        SCHED();
        acc[8] += d0;
        STAMP(2);
#pragma unroll
        for (int k = 0; k < 3; k++) *(LAS f32x4*)(pa + k * 1024) = f32x4{acc[4 * k], acc[4 * k + 1], acc[4 * k + 2], acc[4 * k + 3]};
        *(LAS float*)(pa + 3072) = acc[12];
        MEMBAR();
        *(volatile LAS unsigned*)fl = one;
        __builtin_amdgcn_s_setprio(0);
        STAMP(11);
      }
      asm volatile("" : "+s"(wave));
      if (wave >= 4) {
        if (__builtin_amdgcn_readfirstlane(have) == 0u) {
#pragma unroll 1
          for (int sp = 0; sp < SPIN_CAP * 16 && pf[ch] == 0u; sp++) {}
          partial();
          PIN(acc);
        }
        v4u q[4];
        int ln = lane;
        PIN(ln);
        const int cp = ch * WSCR, c_ = ln & 15, g16 = ln & 48;
        unsigned a_kn, a_k4, a_k8, a_sp, sel_sp, op[7], top = 0xfffefffeu, kinv = (ce - 6u) << 23;
#define KASM(k, pre) asm volatile(pre "\n\tv_mfma_f32_32x32x16_f16 %0, %1, %2, %0" : "+v"(acc) : "v"(fa[k]), "v"(fb[k])); SCHED()
        KASM(0, "s_nop 1");
        a_kn = cp + L_KN + 16 * (70 - ln);
        a_k4 = cp + L_K4 + 4 * (95 - ln);
        a_k8 = cp + L_K8 + 2 * (95 - (ln & 32) - ln);
        PIN(a_kn); PIN(a_k4); PIN(a_k8);
        KASM(1, "");
        op[0] = cp + L_KN + 40 + ((c_ + 1) & 1) * KCOPY + g16 - ((2 * c_ + 2) & ~3);
        op[1] = op[0] + 1024;
        asm volatile("" : "+{v7}"(op[0]), "+{v8}"(op[1]));
        KASM(2, "");
        op[3] = L_UIMG + ch * USTR + g16 + 16 * c_;
        op[2] = op[3] + 16 * c_;
        op[4] = cp + L_K4 + 120 + g16 - 8 * c_;
        asm volatile("" : "+{v9}"(op[2]), "+{v10}"(op[3]), "+{v11}"(op[4]));
        KASM(3, "");
        op[5] = cp + L_K8 + 60 + g16 - 4 * c_;
        op[6] = cp + L_Y + ch * 32 + ln * 8;
        asm volatile("" : "+{v12}"(op[5]), "+{v13}"(op[6]), "+{v14}"(top), "+{v15}"(kinv));
        KASM(4, "");
        const unsigned dummy = cp + L_DUMMY + ln * 4;
        sel_sp = ln == 0 ? 0x0302u : ln < 7 ? 0x0504u : 0x0706u;
        PIN(sel_sp);
        KASM(5, "");
        a_sp = cp + L_KN + (ln == 0 ? 1134 : ln < 7 ? 112 : 48) - ((ln + 1) & ~1);
        PIN(a_sp);
        KASM(6, "");
        a_sp = ln < 11 ? a_sp + ((ln & 1) ? 0 : KCOPY + 2) : dummy;
        PIN(a_sp);
        KASM(7, "");
#pragma unroll
        for (int k = 0; k < 4; k++) q[k] = *(const v4u*)(smem + L_FILT + k * 1024 + lane * 16);
        SCHED();
        asm volatile("s_nop 11" : "+v"(acc));
        STAMP(2);
        const unsigned k8 = pkh(acc[11], acc[12]);
        *(LAS u16*)(a_k8 + 64) = (u16)k8;
        *(LAS u16*)a_k8 = (u16)(k8 >> 16);
        *(LAS unsigned*)a_k4 = pkh(acc[9], acc[10]);
        const v4u c0 = {pkh(acc[1], acc[2]), pkh(acc[3], acc[4]), pkh(acc[5], acc[6]), pkh(acc[7], acc[8])};
        *(LAS v4u*)a_kn = c0;
        *(LAS v4u*)(a_kn + KCOPY) = v4u{pkh(acc[0], acc[1]), pkh(acc[2], acc[3]), pkh(acc[4], acc[5]), pkh(acc[6], acc[7])};
        const unsigned kb = (unsigned)__builtin_amdgcn_readlane((int)pkh(acc[0], acc[9]), 63);
        *(LAS u16*)a_sp = (u16)__builtin_amdgcn_perm(kb, c0.w, sel_sp);
        STAMP(4);
        __builtin_amdgcn_s_setprio(M_PRIO);
        asm volatile("Lk_conv:\n\ts_cbranch_execz Lk_fetch\n" CONV_ASM_TEXT
                     : CONV_ASM_PRE(q)
                     : "{v14}"(top), "{v15}"(kinv), "{v7}"(op[0]), "{v8}"(op[1]), "{v9}"(op[2]), "{v10}"(op[3]), "{v11}"(op[4]), "{v12}"(op[5]),
                       "{v13}"(op[6])
                     : CONV_ASM_CLOBBERS);
        STAMP(6);
        __builtin_amdgcn_s_setprio(0);
      }
      asm volatile("" : "+s"(wave));
      if (wave < 4) {
        asm volatile("s_branch Lk_fetch\n\t.p2align 12\nLk_fetch:\n\ts_cbranch_execz Lk_ln2");
        int lane;
        asm volatile("v_mbcnt_lo_u32_b32 %0, -1, 0\n\tv_mbcnt_hi_u32_b32 %0, -1, %0" : "=v"(lane));
        const int g = lane >> 4;
        int usrc = (cg < 128 ? WS_U : WS_U2 - 128 * GSTR) + cg * GSTR + lane * 16;
        char* const udst = smem + L_UIMG + (lane & 3) * USTR + 520 + (lane >> 2) * 16;
        PIN(usrc);
        __builtin_amdgcn_s_sleep(SLEEP_U);
        __builtin_amdgcn_s_setprio(H_PRIO);
        fetch_u<3>(rs, usrc, udst, {wave, wave + 4, wave + 8}, tag);
        STAMP(4);
        BAR();
        unsigned yb = ((lane >> 2) & 3) * (WSCR + 32) + L_Y + (lane & 1) * 128 + (wave & 1) * 64 + g * 16 + (lane & 2) * 4 + (wave >> 1) * 512;
        int yo = ybase + ((lane & 32) << 8) + wave * 512 + (lane & 31) * 16;
        PIN(yb); PIN(yo);
        __builtin_amdgcn_s_sleep(SLEEP_U7);
        fetch_u<1>(rs, usrc, udst, {12 + wave}, tag);
        STAMP(6);
#pragma unroll
        for (int m = 0; m < 4; m++) {
          BAR();
          if (m == 0) yo ^= *(const LAS int*)(L_MISC + 72);
          const v2u r0 = ldtr((const LAS char*)(yb + m * 1024)), r1 = ldtr((const LAS char*)(yb + m * 1024 + 256));
          __builtin_amdgcn_raw_buffer_store_b128(v4u{r0.x, r0.y, r1.x, r1.y}, rs, yo, m * 2048, SC1);
        }
        __builtin_amdgcn_s_setprio(0);
        if (EXPW == 2) { STAMP(12); DRAIN(); STAMP(13); }
      }
    }
    asm volatile("" : "+s"(wave));
    if (wave == 8) {
      unsigned pw[PF_N], epoch, wb;
      v4u wa;
      __builtin_amdgcn_s_setprio(3);
      asm volatile("s_waitcnt vmcnt(0) ; first loads" : "={v1}"(epoch), "={v[2:5]}"(wa), "={v6}"(wb)::"memory");
      int lane;
      asm volatile("v_mbcnt_lo_u32_b32 %0, -1, 0\n\tv_mbcnt_hi_u32_b32 %0, -1, %0" : "=v"(lane));
      {
        int d = 0;
        unsigned c0 = (unsigned)__builtin_amdgcn_s_memtime();
        int ya = ybase + (bid & 8) * 1024 + lane * 16;
#pragma unroll 1
        for (int h = 0; h < 2; h++) {
#pragma unroll
          for (int i = 0; i < 8; i++) __builtin_amdgcn_raw_buffer_store_b128(sent4, rs, ya + (i & 3) * 1024, (i & 4) * 1024, SC1);
          ya ^= 8192;
          if (loc >= 16) {
            DRAIN();
            const unsigned c1 = (unsigned)__builtin_amdgcn_s_memtime();
            d = (int)min(c1 - c0, 2000u) - d;
            c0 = c1;
          }
        }
        st2(v2u{epoch + 1u, (unsigned)(((loc ^ (bid >> 2)) & 1) ? d : -d)}, rs, loc >= 16 ? WS_VOTE + (cg - 128) * 64 : OOB);
      }
      st4(sent4, rs, (loc < 16 && lane < 8) ? WS_RREC + (lane & 4) * 2048 + rb * 64 + (lane & 3) * 16 : OOB);
      st4(sent4, rs, cg < 64 ? WS_XSEL + cg * 1024 + lane * 16 : OOB);
      SCHED();
      auto warm = [&]() {
        const GAS char* wb = (const GAS char*)(loc < 9 ? kb_[0] : kb_[1]) + xcd * 1152;
        const unsigned s0 = (3u - (unsigned)((u64)wb >> 7)) & 7u, ro_ = ((loc < 9 ? loc : loc - 9) * 64 + lane) * 9216;
        pw[0] = *(const GAS unsigned*)(wb + (ro_ + s0 * 128));
        pw[1] = *(const GAS unsigned*)(wb + (ro_ + (s0 ? s0 : 8u) * 128));
        SCHED();
      };
      if (loc < 16) warm();
      {
#pragma unroll
        for (int k = 0; k < 4; k++) *(unsigned*)(smem + L_WR + (lane + 64 * k) * 4) = wa[k];
        *(unsigned*)(smem + L_WR + (224 + lane) * 4) = wb;
        float ws_ = lane >= 32 ? blo(wb) + bhi(wb) : 0.f;
#pragma unroll
        for (int k = 0; k < 4; k++) ws_ += blo(wa[k]) + bhi(wa[k]);
        ws_ = wave_sum(ws_);
        if (lane == 0) *(float*)(smem + L_MISC + 64) = ws_;
      }
      SCHED();
      asm volatile("s_waitcnt vmcnt(%0)" ::"n"(PF_N) : "memory");
      STAMP(4);
      __builtin_amdgcn_s_setprio(S_PRIO);
      BAR();
      STAMP(5);
      const unsigned t1 = now();
      st2(v2u{epoch + 1u, t1}, rs, lane == 0 ? WS_INIT + cg * 64 : OOB);
      v2u vote;
#pragma unroll 1
      for (int sp = 0; sp < SPIN_CAP; sp++) {
        MEMBAR();
        vote = ld2(rs, WS_VOTE + (lane & 15) * 64);
        if (__all(vote.x == epoch + 1u)) break;
      }
      const unsigned flip = wave_sum((float)(int)vote.y) > 0.f;
      *(LAS unsigned*)(L_MISC + 72) = flip << 13;
      if (loc < 16) {
        const GAS char* xs = p.x + (unsigned)((16 * rb + (lane >> 3)) * 1152 + ((lane & 7) ^ (lane >> 3)) * 16);
#pragma unroll 1
        for (int i = 0; i < 9; i++) {
          __builtin_amdgcn_global_load_lds((const GAS unsigned*)(xs + i * 128), (LAS unsigned*)(L_XROW + i * 2048), 16, 0, 0);
          __builtin_amdgcn_global_load_lds((const GAS unsigned*)(xs + i * 128 + 9216), (LAS unsigned*)(L_XROW + i * 2048 + 1024), 16, 0, 0);
        }
      }
      v2u fl[3];
      unsigned lag;
      auto look = [&]() {
        MEMBAR();
#pragma unroll
        for (int k = 0; k < 3; k++) fl[k] = ld2(rs, WS_INIT + min(lane + 64 * k, NCONV - 1) * 64);
      };
      auto seen = [&]() {
        if (!__all(fl[0].x == epoch + 1u && fl[1].x == epoch + 1u && fl[2].x == epoch + 1u)) return false;
        const unsigned t = t1 - 4096u;
        lag = min(wave_max_u(max(max(fl[0].y - t, fl[1].y - t), fl[2].y - t)), 4096u + 200u);
        *(volatile LAS unsigned*)(L_MISC + 76) = lag;
        return true;
      };
      if (loc >= 16) warm();
      wait_until(t1, T_LOOK);
      look();
      STAMP(6);
      BAR();
      BAR();
      DRAIN();
      bool ok = seen();
      BAR();
      BAR();
      const unsigned t5 = now();
#pragma unroll 1
      for (int sp = 0; sp < SPIN_CAP && !ok; sp++) {
        look();
        ok = seen();
      }
      __builtin_amdgcn_s_waitcnt(0x0f70);
      if (loc == 16) st2(v2u{epoch + 1u, ((t5 + lag - 4096u) & ~1u) | flip}, rs, WS_TIME + xcd * 128);
#pragma unroll
      for (int i = 0; i < PF_N; i++) asm volatile("" ::"v"(pw[i]));
      __builtin_amdgcn_s_setprio(0);
      SCHED();
      asm volatile("s_branch Lk_ln2\n\t.p2align 12\nLk_ln2:\n\ts_cbranch_execz Lk_back");
    }
    STAMP(7);
    if (loc >= 16) { STAMP_FLUSH(); return; }
    int lane;
    asm volatile("v_mbcnt_lo_u32_b32 %0, -1, 0\n\tv_mbcnt_hi_u32_b32 %0, -1, %0" : "=v"(lane));
    const int cl = lane & 15, g = lane >> 4, tid = wave * 64 + lane;
    const unsigned t7 = now() - 4096u;
    v2u fg = *(volatile LAS v2u*)(L_MISC + 72);
    int o0 = (wave < 8 ? WS_Y : WS_Y2 - 128 * GSTR) + (16 * wave + 2 * g) * GSTR + side + (loc * 4 + (rb & 3)) * 128 + (cl & 7) * 16 + (cl >> 3) * 8;
    PIN(o0);
    SCHED();
#pragma unroll 1
    for (int sp = 0; sp < SPIN_CAP * 64 && __builtin_amdgcn_readfirstlane(fg.y) == 0u; sp++) fg = *(volatile LAS v2u*)(L_MISC + 72);
    o0 ^= fg.x;
    const unsigned lag = __builtin_amdgcn_readfirstlane(fg.y), due = lag + T_Y3 - ((0x000f2b46u >> (2 * (loc & 12))) & 255u);
    v2u y[4];
    auto gather = [&]() {
      MEMBAR();
#pragma unroll
      for (int i = 0; i < 4; i++) y[i] = __builtin_amdgcn_raw_buffer_load_b64(rs, o0, ((i & 1) + (i >> 1) * 8) * GSTR, SC1);
    };
    const bool soon = due < 4096u + T_SOON;
    if (soon) {
      if (due > 4096u + 8u) wait_until(t7, due);
      gather();
    }
    SCHED();
    const int near = side ^ __builtin_amdgcn_readfirstlane(fg.x);
    unsigned l_sw = (cl >> 2) == g ? L_RED + wave * 256 + cl * 16 : 7 * WSCR + lane * 16, l_sr = L_RED + cl * 16;
    unsigned krow = SEQ - 1 - (16 * rb + cl);
    int a_key = lane < 32 ? WS_RREC + g * 8192 + (16 * rb + cl) * 4 : OOB;
    PIN(l_sw); PIN(l_sr); PIN(krow); PIN(a_key);
    v4u cw[2];
    f32x2 xf[8];
#pragma unroll
    for (int k = 0; k < 2; k++) {
      const v4u w = *(const v4u*)(smem + L_WR + (64 * wave + 32 * k + 8 * g) * 2);
      const v4u gx = *(const v4u*)(smem + L_XROW + wave * 2048 + cl * 128 + ((4 * k + g) ^ (cl & 7)) * 16);
#pragma unroll
      for (int i = 0; i < 4; i++) {
        cw[k][i] = (lane & 3) != 1 ? 0x3f803f80u : w[i];
        xf[4 * k + i] = f32x2{blo(gx[i]), bhi(gx[i])};
      }
    }
    float wsum = *(const float*)(smem + L_MISC + 64);
#pragma unroll
    for (int i = 0; i < 8; i++) PIN(xf[i]);
    PIN(cw[0]); PIN(cw[1]); PIN(wsum);
    SCHED();
    if (!soon) {
      wait_until(t7, due);
      gather();
    }
    v4u hv[2];
#pragma unroll 1
    for (int sp = 0; sp < SPIN_CAP; sp++) {
#pragma unroll
      for (int i = 0; i < 4; i++) {
        const f32x2 h0 = f32x2{blo(y[i].x), bhi(y[i].x)} + xf[2 * i], h1 = f32x2{blo(y[i].y), bhi(y[i].y)} + xf[2 * i + 1];
        hv[i >> 1][2 * (i & 1)] = pkbf(h0.x, h0.y);
        hv[i >> 1][2 * (i & 1) + 1] = pkbf(h1.x, h1.y);
      }
      PIN(hv[0]); PIN(hv[1]);
      if (__all(valid8(v4u{y[0].x, y[0].y, y[1].x, y[1].y}, v4u{y[2].x, y[2].y, y[3].x, y[3].y}))) break;
      __builtin_amdgcn_s_sleep(2);
      gather();
    }
    STAMP(8);
    {
      f32x4 s = {0.f, 0.f, 0.f, 0.f}, q = {0.f, 0.f, 0.f, 0.f};
      q = mfma_bf(hv[0], hv[0], q);
      q = mfma_bf(hv[1], hv[1], q);
      s = mfma_bf(cw[0], hv[0], s);
      s = mfma_bf(cw[1], hv[1], s);
      s[2] = diag(q, lane);
      *(LAS f32x4*)l_sw = s;
    }
    BAR();
    if (wave == 1) __builtin_amdgcn_s_setprio(3); else __builtin_amdgcn_s_sleep(2);
    float m, s;
    const f32x4 t = sum9((const LAS char*)l_sr);
    ln_round(t[0], t[2], m, s);
    const float rinv = __builtin_amdgcn_rcpf(s);
    unsigned okey;
    {
      const unsigned b = pkbf(0.f, fmaf(-m, wsum, t[1]) * rinv);
      okey = (b ^ (((int)b >> 31 & 0x7fff0000u) | 0x80000000u)) | krow;
      if (wave == 1) st1(okey, rs, a_key);
    }
    STAMP(9);
    __builtin_amdgcn_s_setprio(0);
    v4u key = {0u, 0u, 0u, 0u};
    const int a_all = WS_RREC + near + tid * 16;
    const bool late = wave < 8 && now() - t7 + T_SOON2 > lag + T_KEY;
    if (late) {
      wait_until(t7, lag + T_KEY);
      STAMP(1);
      key = ld4(rs, a_all);
    }
    SCHED();
    char* const tl = smem + wave * 4096;
    *(v4u*)(tl + cl * 144 + g * 16) = hv[0];
    *(v4u*)(tl + cl * 144 + 64 + g * 16) = hv[1];
    MEMBAR();
    unsigned selmask = 0;
#pragma unroll 1
    for (int rd = 0; rd < 4; rd++) {
      const unsigned ka = row_max_u(okey);
      okey = okey == ka ? 0u : okey;
      const unsigned kb = row_max_u(okey);
      okey = okey == kb ? 0u : okey;
      const int ra = 15 - (ka & 15), rb2 = 15 - (kb & 15);
      v4u pub = *(const v4u*)(tl + ((lane & 16) ? rb2 : ra) * 144 + (lane & 7) * 16);
      {
        const float ma = rdlane(m, ra), mb = rdlane(m, rb2), ia = rdlane(rinv, ra), ib = rdlane(rinv, rb2);
        const float pm = (lane & 16) ? mb : ma, pi = (lane & 16) ? ib : ia;
#pragma unroll
        for (int i = 0; i < 4; i++) {
          const unsigned n = ln_apply(pub[i], pm, pi);
          pub[i] = nosent((lane & 8) ? pub[i] : n);
        }
        if (lane >= 32) pub.x = (lane & 16) ? kb : ka;
      }
      PIN(pub);
      if (rd == 0 && wave < 8) {
        if (!late) {
          wait_until(t7, lag + T_KEY);
          STAMP(1);
          key = ld4(rs, a_all);
        }
#pragma unroll 1
        for (int sp = 0; sp < SPIN_CAP && !__all(valid4(key)); sp++) {
          __builtin_amdgcn_s_sleep(2);
          MEMBAR();
          key = ld4(rs, a_all);
        }
        STAMP(10);
      }
      int ca = 0, cb = 0;
#pragma unroll
      for (int i = 0; i < 4; i++) {
        ca += __popcll(__ballot(key[i] > ka));
        cb += __popcll(__ballot(key[i] > kb));
      }
      LAS unsigned* const cnt = (LAS unsigned*)(L_MISC + 32 + rd * 4);
      if (lane == 0) __hip_atomic_fetch_add(cnt, (unsigned)(ca | (cb << 16)), __ATOMIC_RELAXED, __HIP_MEMORY_SCOPE_WORKGROUP);
      BAR();
      const unsigned cs = __builtin_amdgcn_readfirstlane(*cnt);
      const unsigned rka = cs & 0xffffu, rkb = cs >> 16;
      if (rka >= 8u) break;
      const unsigned rank = (lane & 16) ? rkb : rka;
      const int xo = lane < 32 ? (cl >> 3) * 1152 + wave * 128 + (cl & 7) * 16 : 2304;
      const int po = (rank < 8u && (lane < 32 || (cl == 0 && wave == 0))) ? slot_at(rank) + xo : OOB;
      __builtin_amdgcn_raw_buffer_store_b128(pub, rs, po, near ^ 8192, SC1);
      __builtin_amdgcn_raw_buffer_store_b128(pub, rs, po, near, SC1);
      selmask |= 1u << ra;
      if (rkb >= 8u) break;
      selmask |= 1u << rb2;
    }
    STAMP(3);
#pragma unroll
    for (int i = 0; i < 2; i++) {
      const int r = 8 * i + (lane >> 3);
      const v4u hh = *(const v4u*)(tl + r * 144 + (lane & 7) * 16);
      const float pm = __shfl(m, r), pi = __shfl(rinv, r);
      v4u o;
#pragma unroll
      for (int k = 0; k < 4; k++) o[k] = addbf(hh[k], ln_apply(hh[k], pm, pi));
      st4(o, ro, ((selmask >> r) & 1u) ? OOB : (16 * rb + r) * 1152 + wave * 128 + (lane & 7) * 16);
    }
    STAMP_FLUSH();
    return;
  }

  asm volatile("; conv set-up dropped" ::"{v[18:33]}"(k16), "{v[34:41]}"(k8));
  const bool ffn = loc < 27;
  LAS char* xw = (LAS char*)smem + wave * 4608 + (cl >> 1) * 288 + g * 32 + (cl & 1) * 16;
  LAS char* xr_ = (LAS char*)smem + wave * 4608 + ((lane >> 2) & 3) * 288 + (lane & 1) * 128 + g * 16 + (lane & 2) * 4;
  LAS char* rw = (LAS char*)smem + L_RED + wave * 256 + cl * 8;
  int uo = (wave < 8 ? WS_U : WS_U2 - 128 * GSTR) + (16 * wave + (lane >> 3)) * GSTR + tile * 128 + (lane & 7) * 16;
  v4u ones = {0x3f803f80u, 0x3f803f80u, 0x3f803f80u, 0x3f803f80u};
  LAS char* sa = (LAS char*)smem + L_MISC + (lane & 4) * 8;
  unsigned keep = 0xfff8fff8u;
  PIN(xw); PIN(xr_); PIN(rw); PIN(uo); PIN(ones); PIN(sa); PIN(keep);
  SCHED();
  FIRST_WAIT(2);
  const unsigned tag = __builtin_amdgcn_readfirstlane(epoch) + 1u;
  v4u tb = tag_bits(tag);
  asm volatile("" : "+s"(tb));
  const bool fin = wave == 1 || (wave == 2 && loc >= 30);
  SCHED();
  FIRST_WAIT(0);
  {
    const f32x4 z4 = {0.f, 0.f, 0.f, 0.f};
    auto sums = [&](v4u a, v4u b, int tt) {
      const f32x4 s0 = mfma_bf(ones, a, z4), q0 = mfma_bf(a, a, z4), s1 = mfma_bf(ones, b, z4), q1 = mfma_bf(b, b, z4);
      *(LAS v4u*)(xw + tt * 2304) = a;
      *(LAS v4u*)(xw + tt * 2304 + 128) = b;
      if ((cl >> 2) == g) {
        *(LAS f32x2*)(rw + tt * 2304) = f32x2{s0[0], diag(q0, lane)};
        *(LAS f32x2*)(rw + tt * 2304 + 128) = f32x2{s1[0], diag(q1, lane)};
      }
    };
    sums(ga, gb, 0);
    if (loc >= 30) sums(gc, gd, 1);
    STAMP(10);
    BAR();
    STAMP(11);
    if (fin) {
      __builtin_amdgcn_s_setprio(3);
      const f32x4 t = sum9((const LAS char*)smem + L_RED + (wave - 1) * 2304 + cl * 16);
      float m, sd;
      ln_round(t[0] + t[2], t[1] + t[3], m, sd);
      const float rinv = __builtin_amdgcn_rcpf(sd);
      *(float*)(smem + L_MISC + (wave - 1) * 128 + (lane & 31) * 4) = (lane & 16) ? -m * rinv : rinv;
      BAR();
    }
#pragma unroll 1
    for (int tt = 0; tt < 2; tt++) {
      f32x2 xt[2][4];
#pragma unroll
      for (int i = 0; i < 4; i++) {
        const v2u v = ldtr(xr_ + tt * 2304 + (i >> 1) * 64 + (i & 1) * 1152);
        xt[i >> 1][2 * (i & 1)] = f32x2{blo(v.x), bhi(v.x)};
        xt[i >> 1][2 * (i & 1) + 1] = f32x2{blo(v.y), bhi(v.y)};
      }
#pragma unroll
      for (int i = 0; i < 8; i++) PIN(xt[i >> 2][i & 3]);
      if (tt == 0 && !fin) BAR();
      if (tt == 0) STAMP(12);
      const LAS f32x4* st = (const LAS f32x4*)(sa + tt * 128);
      const f32x4 r0 = st[0], r1 = st[1], c0 = st[4], c1 = st[5];
      const f32x2 rr[4] = {r0.xy, r0.zw, r1.xy, r1.zw}, cc[4] = {c0.xy, c0.zw, c1.xy, c1.zw};
#pragma unroll
      for (int i = 0; i < 2; i++) {
        v4u o;
#pragma unroll
        for (int k = 0; k < 4; k++) o[k] = (pkh2(__builtin_elementwise_fma(xt[i][k], rr[k], cc[k])) & keep) | tb[k];
        __builtin_amdgcn_raw_buffer_store_b128(o, rs, uo, i * 8 * GSTR, SC1);
      }
      if (loc < 30) break;
      uo += 16 * 128;
    }
    __builtin_amdgcn_s_setprio(0);
    BAR();
    STAMP(1);
    if (!ffn) {
      const GAS char* dn = (const GAS char*)kb_[2] + (size_t)xcd * 5184 * 128;
      unsigned pf[2];
#pragma unroll
      for (int i = 0; i < 2; i++) pf[i] = *(const GAS unsigned*)(dn + (unsigned)min((i * 5 + loc - 27) * NT + tid, 5183) * 128u);
#pragma unroll
      for (int i = 0; i < 2; i++) asm volatile("" ::"v"(pf[i]));
      STAMP_FLUSH();
      return;
    }
  }
  v4u FW[2][8], FD[2][4];
  v4u Dn[8];
  constexpr int F_GU = 0, F_ZERO = 1280, F_TIME = F_ZERO + 192, F_ALL = 2048, F_RED = 16384;
  {
    v4u W[2][8];
    const GAS char* wp = (const GAS char*)((lane & 8) ? kb_[1] : kb_[0]) + (unsigned)((64 * wave + 8 * g) * (FF * 2) + 128 * fj + 16 * (lane & 7));
    const rsrc_t rd = __builtin_amdgcn_make_buffer_rsrc((void*)kb_[2], 0, DM * FF * 2, 0x00020000);
    const int wdo = (64 * fj + 8 * g + 4 * (lane & 8)) * (DM * 2) + 128 * wave + 16 * (lane & 7);
    st4(sent4, rs, part_at(fj * 9216 + tid * 16));
    st4(sent4, rs, part_at(fj * 9216 + tid * 16) + 8192);
    SCHED();
#pragma unroll
    for (int j = 0; j < 16; j++) W[j >> 3][j & 7] = *(const GAS v4u*)(wp + (32 * (j >> 3) + (j & 7)) * (FF * 2));
    SCHED();
    if (wave == 2) *(unsigned*)(smem + F_ZERO + lane * 4) = 0u;
    asm volatile("s_waitcnt vmcnt(16)" ::: "memory");
    auto down = [&]() {
      BAR();
      STAMP(12);
#pragma unroll
      for (int j = 0; j < 8; j++) Dn[j] = __builtin_amdgcn_raw_buffer_load_b128(rd, wdo, j * (DM * 2), 0);
      SCHED();
    };
    down();
    st1(tag, rs, tid == 0 ? WS_INIT + (NCONV + fj) * 64 : OOB);
    SCHED();
#pragma unroll
    for (int ks = 0; ks < 2; ks++)
#pragma unroll
      for (int c = 0; c < 8; c++) {
#pragma unroll
        for (int i = 0; i < 4; i++)
          FW[ks][c][i] = __builtin_amdgcn_perm(W[ks][2 * i + 1][c >> 1], W[ks][2 * i][c >> 1], (c & 1) ? 0x07060302u : 0x05040100u);
        PIN(FW[ks][c]);
      }
    SCHED();
  }
  STAMP(2);
  const int tk = fj / 9, cb = fj - tk * 9;
  const int fcol = cb * 64 + 8 * (cl & 7) + 4 * (cl >> 3) + (g >> 1) + 2 * (g & 1);
  int a_x = slot_at(lane >> 3) + side + wave * 128 + (lane & 7) * 16, a_f = slot_at(tk) + side + (fcol >> 1) * 4;
  int a_po[2], a_pi[2];
#pragma unroll
  for (int i = 0; i < 2; i++) {
    a_po[i] = part_at(fj * 9216 + ((2 * (g >> 1) + i) * DM + 64 * wave + 4 * cl) * 4) + ((g & 1) << 13);
    a_pi[i] = part_at(((tid >> 4) + 36 * i) * 9216 + ((tk & 3) * DM + cb * 64 + cl * 4) * 4) + side;
    PIN(a_po[i]); PIN(a_pi[i]);
  }
  unsigned l_xw = F_ALL + wave * 1152 + (lane >> 3) * 144 + (lane & 7) * 16, l_xr = F_ALL + wave * 1152 + (lane & 7) * 144 + g * 16;
  unsigned l_lo = (lane & 8) ? F_ZERO : l_xr, l_hi = (lane & 8) ? l_xr : F_ZERO, l_rw = F_RED + wave * 4096 + lane * 16;
  unsigned l_rg = F_RED + (tid >> 5) * 256 + (tid & 31) * 4, l_ru = l_rg + 128;
  unsigned l_gw = F_GU + (((tid >> 5) & 1) * 4 + (tid & 3)) * 144 + (((tid >> 2) & 7) * 8 + (tid >> 7) + ((tid >> 6) & 1) * 4) * 2;
  unsigned l_gr = F_GU + ((cl & 4) + ((cl + (cl >> 2 & 2)) & 3)) * 144 + g * 16, l_s = F_ALL + (wave == 8 ? 0 : wave + 1) * 256 + lane * 4;
  int a_k = slot_at(tk) + side + 2304;
  PIN(a_k); PIN(a_x); PIN(a_f); PIN(l_xw); PIN(l_lo); PIN(l_hi); PIN(l_rw); PIN(l_rg); PIN(l_ru); PIN(l_gw); PIN(l_gr); PIN(l_s);
  if (wave == 0) {
    v2u ts;
#pragma unroll 1
    for (int sp = 0; sp < SPIN_CAP; sp++) {
      MEMBAR();
      ts = ld2(rs, WS_TIME + xcd * 128);
      if (__builtin_amdgcn_readfirstlane(ts.x) == tag) break;
      __builtin_amdgcn_s_sleep(4);
    }
    *(volatile LAS v2u*)F_TIME = v2u{ts.y, 1u};
  }
  {
    v2u te;
#pragma unroll 1
    for (int sp = 0; sp < SPIN_CAP * 16; sp++) {
      te = *(volatile LAS v2u*)F_TIME;
      if (__builtin_amdgcn_readfirstlane(te.y)) break;
      __builtin_amdgcn_s_sleep(2);
    }
    const int flip = (te.x & 1u) << 13;
    a_x ^= flip; a_f ^= flip; a_k ^= flip; a_po[0] ^= flip; a_po[1] ^= flip; a_pi[0] ^= flip; a_pi[1] ^= flip;
    PIN(a_x); PIN(a_f); PIN(a_k); PIN(a_po[0]); PIN(a_po[1]); PIN(a_pi[0]); PIN(a_pi[1]);
    wait_until(__builtin_amdgcn_readfirstlane(te.x) - 4096u, 4096u + T_SEL);
  }
  STAMP(10);
  unsigned w2 = 0u, h2 = 0u, fkey = 0u, f0 = 0u, f1 = 0u;
  auto flook = [&]() {
    MEMBAR();
    f0 = ld1(rs, WS_INIT + (NCONV + lane) * 64);
    f1 = ld1(rs, WS_INIT + (NCONV + 64 + (lane & 7)) * 64);
    w2 = ld1(rs, a_f);
    h2 = ld1(rs, a_f + 1152);
    fkey = ld1(rs, a_k);
  };
  {
    MEMBAR();
    v4u xv = ld4(rs, a_x);
    SCHED();
#pragma unroll
    for (int j = 0; j < 8; j++)
#pragma unroll
      for (int i = 0; i < 2; i++) {
        const int lo = (int)Dn[j][i], hi = (int)Dn[j][2 + i];
        Dn[j][i] = (unsigned)__builtin_amdgcn_update_dpp(lo, hi, 0x128, 0xf, 0xc, false);
        Dn[j][2 + i] = (unsigned)__builtin_amdgcn_update_dpp(hi, lo, 0x128, 0xf, 0x3, false);
      }
#pragma unroll
    for (int ks = 0; ks < 2; ks++)
#pragma unroll
      for (int c = 0; c < 4; c++) {
#pragma unroll
        for (int i = 0; i < 4; i++)
          FD[ks][c][i] = __builtin_amdgcn_perm(Dn[2 * i + 1][2 * ks + (c >> 1)], Dn[2 * i][2 * ks + (c >> 1)], (c & 1) ? 0x07060302u : 0x05040100u);
        PIN(FD[ks][c]);
      }
    SCHED();
    STAMP(3);
#pragma unroll 1
    for (int sp = 0; sp < SPIN_CAP && !__all(valid4(xv)); sp++) {
      __builtin_amdgcn_s_sleep(SLEEP_X);
      MEMBAR();
      xv = ld4(rs, a_x);
    }
    STAMP(6);
    *(LAS v4u*)l_xw = xv;
    MEMBAR();
    const v4u lo0 = *(const LAS v4u*)l_lo, hi0 = *(const LAS v4u*)l_hi, lo1 = *(const LAS v4u*)(l_lo + 64), hi1 = *(const LAS v4u*)(l_hi + 64);
    if (wave == 8) flook();
    f32x4 a[4];
#pragma unroll
    for (int c = 0; c < 5; c++) {
      SCHED();
      if (c < 4) {
        a[c] = mfma_bf(lo0, FW[0][c], f32x4{0.f, 0.f, 0.f, 0.f});
        a[c] = mfma_bf(hi0, FW[0][c + 4], a[c]);
        a[c] = mfma_bf(lo1, FW[1][c], a[c]);
        a[c] = mfma_bf(hi1, FW[1][c + 4], a[c]);
        asm volatile("" ::"v"(FW[1][c + 4]));
      }
      SCHED();
      if (c > 0) *(LAS f32x4*)(l_rw + (c - 1) * 1024) = a[c - 1];
    }
  }
  BAR();
  STAMP(4);
  float fh = 0.f, fw = 0.f, sgm = 0.f;
  int oo = OOB;
  if (wave < 8) {
    f32x2 gv[4], uv[4];
#pragma unroll
    for (int w = 0; w < 4; w++) {
      gv[w] = f32x2{*(const LAS float*)(l_rg + w * 8192), *(const LAS float*)(l_rg + w * 8192 + 4096)};
      uv[w] = f32x2{*(const LAS float*)(l_ru + w * 8192), *(const LAS float*)(l_ru + w * 8192 + 4096)};
    }
    const float g8 = *(const LAS float*)(l_rg + 32768), u8 = *(const LAS float*)(l_ru + 32768);
    SCHED();
    f32x2 g2 = (gv[0] + gv[1]) + (gv[2] + gv[3]), u2 = (uv[0] + uv[1]) + (uv[2] + uv[3]);
    PIN(g2); PIN(u2);
    float gs = g2.x + g2.y;
    PIN(gs);
    gs += g8;
    PIN(gs);
    const float us = (u2.x + u2.y) + u8;
    const unsigned gu = pkbf(gs, us);
    const float gp = blo(gu);
    const float gq = rbf(gp * __builtin_amdgcn_rcpf(1.0f + __expf(-gp)));
    *(LAS u16*)l_gw = (u16)pkbf(gq * bhi(gu), 0.f);
  } else {
#pragma unroll 1
    for (int sp = 0; sp < SPIN_CAP && !__all(valid4(v4u{w2, h2, fkey, 0u}) && f0 == tag && f1 == tag); sp++) flook();
    fw = (fcol & 1) ? bhi(w2) : blo(w2);
    fh = (fcol & 1) ? bhi(h2) : blo(h2);
    const unsigned kb = fkey & 0xffff0000u;
    const float rv = __uint_as_float((kb & 0x80000000u) ? (kb & 0x7fffffffu) : (~kb & 0xffff0000u));
    sgm = rbf(__builtin_amdgcn_rcpf(1.0f + __expf(-rv)));
    oo = (SEQ - 1 - (int)(fkey & 0x7ffu)) * 1152 + fcol * 2;
  }
  PIN(fw); PIN(fh); PIN(sgm); PIN(oo);
  BAR();
  STAMP(5);
  {
    const v4u a0 = *(const LAS v4u*)l_gr, a1 = *(const LAS v4u*)(l_gr + 64);
    f32x4 o[4];
#pragma unroll
    for (int c = 0; c < 4; c++) {
      o[c] = f32x4{0.f, 0.f, 0.f, 0.f};
      o[c] = mfma_bf(a0, FD[0][c], o[c]);
      o[c] = mfma_bf(a1, FD[1][c], o[c]);
    }
#pragma unroll
    for (int i = 0; i < 2; i++) {
      v4u v;
#pragma unroll
      for (int c = 0; c < 4; c++) v[c] = nosent(__float_as_uint(o[c][i]));
      st4(v, rs, a_po[i]);
    }
  }
  STAMP(7);
  wait_until(now(), T_PART);
  {
    v4u a, b;
#pragma unroll 1
    for (int sp = 0; sp < SPIN_CAP; sp++) {
      MEMBAR();
      a = ld4(rs, a_pi[0]);
      b = ld4(rs, a_pi[1]);
      if (__all(valid8(a, b))) break;
      __builtin_amdgcn_s_sleep(2);
    }
    const v4u t = __builtin_bit_cast(v4u, __builtin_bit_cast(f32x4, a) + __builtin_bit_cast(f32x4, b));
    const v2u p0 = __builtin_amdgcn_permlane32_swap(t.x, t.y, false, false), p1 = __builtin_amdgcn_permlane32_swap(t.z, t.w, false, false);
    const v2u p2 = __builtin_amdgcn_permlane16_swap(__float_as_uint(__uint_as_float(p0.x) + __uint_as_float(p0.y)),
                                                    __float_as_uint(__uint_as_float(p1.x) + __uint_as_float(p1.y)), false, false);
    *(LAS float*)l_s = __uint_as_float(p2.x) + __uint_as_float(p2.y);
  }
  STAMP(8);
  BAR();
  if (wave == 8) {
    float t[9];
#pragma unroll
    for (int i = 0; i < 9; i++) t[i] = *(const LAS float*)(l_s + i * 256);
    SCHED();
    const float y = rbf((((t[0] + t[1]) + (t[2] + t[3])) + ((t[4] + t[5]) + (t[6] + t[7]))) + t[8]);
    __builtin_amdgcn_raw_buffer_store_b16((u16)pkbf(fh + rbf(fw + rbf(y * sgm)), 0.f), ro, oo, 0, 0);
    if (fj == 0 && lane == 0) st1(tag, rs, WS_EPOCH);
  }
  STAMP(9);
  STAMP_FLUSH();
  asm volatile("s_endpgm\n\t.p2align 12\n\t.skip (4096 - (Lk_conv - Lk_gen)) & 4095\nLk_gen:" ::: "memory");
}

}

extern "C" void kernel_launch(void* const* d_in, const int* in_sizes, int n_in, void* d_out, int out_size,
                              void* d_ws, size_t ws_size, hipStream_t stream) {
  (void)hipFuncSetAttribute((const void*)k_main, hipFuncAttributeMaxDynamicSharedMemorySize, LDS_BYTES);
  Params p;
  p.x = (const u16*)d_in[0];
  p.lar = (const u16*)d_in[1];
  p.aim = (const u16*)d_in[2];
  p.crl = (const u16*)d_in[3];
  p.cim = (const u16*)d_in[4];
  p.ldt = (const u16*)d_in[5];
  p.dskip = (const u16*)d_in[6];
  p.wr = (const u16*)d_in[8];
  p.wg = (const u16*)d_in[9];
  p.wu = (const u16*)d_in[10];
  p.wd = (const u16*)d_in[11];
  p.out = (u16*)d_out;
  const size_t skip = (size_t)(-(uintptr_t)d_ws) & ((2u << 20) - 1);
  p.ws = (char*)d_ws + (skip + WS_END <= ws_size ? skip : 0);
  k_main<<<256, NT, LDS_BYTES, stream>>>(p);
}
```

```cpp
#include <hip/hip_runtime.h>
#include <stdint.h>
#pragma clang diagnostic ignored "-Wint-to-pointer-cast"

namespace {

constexpr int SEQ = 2048, DM = 576, NM = 128, FF = 4608;
constexpr int NT = 576;
constexpr int NCONV = 144, NFFN = 72;
constexpr unsigned SENT = 0xFFFFFFFFu;
constexpr int SC1 = 16;
constexpr int SPIN_CAP = 1 << 16;
#define T_Y3 -10
#define T_KEY 100
#define T_SEL 180
#define CODE_BYTES 23936
#define H_PRIO 3
#ifndef M_PRIO
#define M_PRIO 1
#endif
#define S_PRIO 3
#define PF_N 2
#define SLEEP_U 5
#define SLEEP_U7 12
#define EXPW 0
#define T_LOOK 90
#define P_SLEEP 0
#define T_PART 28

typedef unsigned short u16;
typedef unsigned v4u __attribute__((ext_vector_type(4)));
typedef unsigned v2u __attribute__((ext_vector_type(2)));
typedef float f32x2 __attribute__((ext_vector_type(2)));
typedef float f32x4 __attribute__((ext_vector_type(4)));
typedef float f32x16 __attribute__((ext_vector_type(16)));
typedef _Float16 f16x2 __attribute__((ext_vector_type(2)));
typedef _Float16 f16x8 __attribute__((ext_vector_type(8)));
typedef __bf16 bf16x2 __attribute__((ext_vector_type(2)));
typedef __bf16 bf16x8 __attribute__((ext_vector_type(8)));
typedef __bf16 bf16x16 __attribute__((ext_vector_type(16)));
typedef unsigned v8u __attribute__((ext_vector_type(8)));
typedef __amdgpu_buffer_rsrc_t rsrc_t;

constexpr int WS_EPOCH = 0;
constexpr int WS_TIME = 2048;
constexpr int WS_INIT = 4096;
constexpr int WS_VOTE = 24576;
constexpr int WS_RREC = 32768;
constexpr int WS_XSEL = 65536;
constexpr int GSTR = 16384;
constexpr int WS_U = 2 << 20;
constexpr int WS_U2 = 196608;
constexpr int WS_Y = 4 << 20;
constexpr int WS_Y2 = WS_U2 + 16 * GSTR;
constexpr int WS_PART = WS_Y2 + 16 * GSTR;
constexpr int WS_END = 6 << 20;
static_assert(WS_PART + 2 * NFFN * 9216 + 16384 <= WS_U, "ws map");
__device__ __forceinline__ int part_at(int v) { return WS_PART + v + (v & ~8191); }
__device__ __forceinline__ int slot_at(int k) { return WS_XSEL + k * 16384; }
constexpr int OOB = 0x7ffffff0;

constexpr int WSCR = 16384;
constexpr int L_KN = 4096;
constexpr int KCOPY = 1216;
constexpr int L_K4 = L_KN + 2 * KCOPY;
constexpr int L_K8 = L_K4 + 512;
constexpr int L_Y = 8192;
constexpr int L_DUMMY = 12800;
constexpr int L_XROW = 4 * WSCR;
constexpr int L_UIMG = 8 * WSCR;
constexpr int USTR = 4640;
constexpr int L_FILT = L_UIMG + 4 * USTR;
constexpr int L_MISC = L_FILT + 4096;
constexpr int L_WR = L_MISC + 256;
constexpr int L_RED = L_WR + 1152;
constexpr int LDS_BYTES = L_RED + 2 * 2304;
static_assert(L_KN + 224 * 16 <= L_Y, "lds map");
static_assert(LDS_BYTES <= 160 * 1024, "lds size");

#define MEMBAR() asm volatile("" ::: "memory")
#define BAR() do { __builtin_amdgcn_sched_barrier(0); asm volatile("s_waitcnt lgkmcnt(0)\n\ts_barrier" ::: "memory"); __builtin_amdgcn_sched_barrier(0); } while (0)
#define LAS __attribute__((address_space(3)))
#define DRAIN() asm volatile("s_waitcnt vmcnt(0)" ::: "memory")
#define LGKM0() asm volatile("s_waitcnt lgkmcnt(0)" ::: "memory")
#define PIN(x) asm volatile("" : "+v"(x))
#define RARE(c) __builtin_expect(!!(c), 0)
#define POLL(k, ok, load) if (!__all(ok)) { RETRY(k); int sp_ = SPIN_CAP; _Pragma("unroll 1") do { if (P_SLEEP) __builtin_amdgcn_s_sleep(P_SLEEP); MEMBAR(); load; sp_ = __all(ok) ? 0 : sp_ - 1; } while (sp_); }
#define SCHED() __builtin_amdgcn_sched_barrier(0)

#define STAMP(i) do {} while (0)
#define STAMP_INIT() do {} while (0)
#define T0_ASM
#define STAMP_FLUSH() do {} while (0)

#define RETRY(k) do {} while (0)

__device__ __forceinline__ float blo(unsigned v) { return __uint_as_float(v << 16); }
__device__ __forceinline__ float bhi(unsigned v) { return __uint_as_float(v & 0xffff0000u); }
__device__ __forceinline__ unsigned pkbf(float a, float b) {
  const f32x2 f = {a, b};
  return __builtin_bit_cast(unsigned, __builtin_convertvector(f, bf16x2));
}
__device__ __forceinline__ float rbf(float f) { return blo(pkbf(f, f)); }
__device__ __forceinline__ unsigned pkh(float a, float b) {
  const f32x2 f = {a, b};
  return __builtin_bit_cast(unsigned, __builtin_convertvector(f, f16x2));
}
__device__ __forceinline__ unsigned nosent(unsigned v) { return min(v, 0xFFFFFFFEu); }
__device__ __forceinline__ bool valid4(v4u v) { return max(max(v.x, v.y), max(v.z, v.w)) != SENT; }

__device__ __forceinline__ bool valid8(v4u a, v4u b) { return max(max(max(max(a.x, a.y), a.z), max(max(a.w, b.x), b.y)), max(b.z, b.w)) != SENT; }

template <int CTRL, int RM> __device__ __forceinline__ float dppf(float v) {
  return __int_as_float(__builtin_amdgcn_update_dpp(0, __float_as_int(v), CTRL, RM, 0xf, false));
}
template <int CTRL, int RM> __device__ __forceinline__ unsigned dppu(unsigned v) {
  return (unsigned)__builtin_amdgcn_update_dpp(0, (int)v, CTRL, RM, 0xf, false);
}
__device__ __forceinline__ float wave_sum(float v) {
  v += dppf<0x111, 0xf>(v);
  v += dppf<0x112, 0xf>(v);
  v += dppf<0x114, 0xf>(v);
  v += dppf<0x118, 0xf>(v);
  v += dppf<0x142, 0xa>(v);
  v += dppf<0x143, 0xc>(v);
  return __int_as_float(__builtin_amdgcn_readlane(__float_as_int(v), 63));
}
__device__ __forceinline__ unsigned wave_max_u(unsigned v) {
  v = max(v, dppu<0x111, 0xf>(v));
  v = max(v, dppu<0x112, 0xf>(v));
  v = max(v, dppu<0x114, 0xf>(v));
  v = max(v, dppu<0x118, 0xf>(v));
  const unsigned a = (unsigned)__builtin_amdgcn_readlane((int)v, 15), b = (unsigned)__builtin_amdgcn_readlane((int)v, 31);
  const unsigned c = (unsigned)__builtin_amdgcn_readlane((int)v, 47), d = (unsigned)__builtin_amdgcn_readlane((int)v, 63);
  return max(max(a, b), max(c, d));
}

__device__ __forceinline__ v4u ld4(rsrc_t rs, int off) { return __builtin_amdgcn_raw_buffer_load_b128(rs, off, 0, SC1); }
__device__ __forceinline__ v2u ld2(rsrc_t rs, int off) { return __builtin_amdgcn_raw_buffer_load_b64(rs, off, 0, SC1); }
__device__ __forceinline__ unsigned ld1(rsrc_t rs, int off) { return __builtin_amdgcn_raw_buffer_load_b32(rs, off, 0, SC1); }
__device__ __forceinline__ v2u ldp2(rsrc_t rs, int off, int soff) { return __builtin_amdgcn_raw_buffer_load_b64(rs, off, soff, 0); }
__device__ __forceinline__ void st4(v4u v, rsrc_t rs, int off) { __builtin_amdgcn_raw_buffer_store_b128(v, rs, off, 0, SC1); }
__device__ __forceinline__ void st2(v2u v, rsrc_t rs, int off) { __builtin_amdgcn_raw_buffer_store_b64(v, rs, off, 0, SC1); }
__device__ __forceinline__ void st1(unsigned v, rsrc_t rs, int off) { __builtin_amdgcn_raw_buffer_store_b32(v, rs, off, 0, SC1); }

__device__ __forceinline__ f32x4 mfma_bf(v4u a, v4u b, f32x4 c) {
  return __builtin_amdgcn_mfma_f32_16x16x32_bf16(__builtin_bit_cast(bf16x8, a), __builtin_bit_cast(bf16x8, b), c, 0, 0, 0);
}
__device__ __forceinline__ f32x4 smfmac_bf(v4u a, v8u b, f32x4 c, int idx) {
  return __builtin_amdgcn_smfmac_f32_16x16x64_bf16(__builtin_bit_cast(bf16x8, a), __builtin_bit_cast(bf16x16, b), c, idx, 0, 0);
}
__device__ __forceinline__ f32x4 mfma_h(v4u a, v4u b, f32x4 c) {
  return __builtin_amdgcn_mfma_f32_16x16x32_f16(__builtin_bit_cast(f16x8, a), __builtin_bit_cast(f16x8, b), c, 0, 0, 0);
}

__device__ __forceinline__ float diag(f32x4 q, int lane) {
  const float qa = (lane & 1) ? q[1] : q[0], qb = (lane & 1) ? q[3] : q[2];
  return (lane & 2) ? qb : qa;
}
__device__ __forceinline__ f32x4 sum9(const LAS char* red) {
  f32x4 t[9];
#pragma unroll
  for (int w = 0; w < 9; w++) t[w] = *(const LAS f32x4*)(red + w * 256);
  SCHED();
  return (((t[0] + t[1]) + (t[2] + t[3])) + ((t[4] + t[5]) + (t[6] + t[7]))) + t[8];
}
__device__ __forceinline__ void ln_round(float sum, float sq, float& m, float& s) {
  const float mean = sum * (1.0f / DM);
  const float var = fmaxf(fmaf(-mean, mean, sq * (1.0f / DM)), 0.f);
  const unsigned mv = pkbf(mean, var);
  m = blo(mv);
  s = rbf(__builtin_amdgcn_sqrtf(rbf(bhi(mv) + 1e-5f)));
}
__device__ __forceinline__ unsigned ln_apply(unsigned xp, float m, float rinv) {
  const unsigned d = pkbf(blo(xp) - m, bhi(xp) - m);
  return pkbf(blo(d) * rinv, bhi(d) * rinv);
}
__device__ __forceinline__ unsigned addbf(unsigned a, unsigned b) { return pkbf(blo(a) + blo(b), bhi(a) + bhi(b)); }

__device__ __forceinline__ unsigned now() { return (unsigned)__builtin_amdgcn_s_memrealtime(); }
__device__ __forceinline__ void wait_until(unsigned t0, unsigned ticks) {
  unsigned n;
  asm volatile(
      "s_movk_i32 %0, 1024\n"
      "1:\n\t"
      "s_memrealtime vcc\n\t"
      "s_waitcnt lgkmcnt(0)\n\t"
      "s_sub_i32 vcc_lo, vcc_lo, %1\n\t"
      "s_cmp_gt_i32 vcc_lo, -16\n\t"
      "s_cbranch_scc1 2f\n\t"
      "s_sleep 2\n\t"
      "s_sub_u32 %0, %0, 4\n\t"
      "s_cbranch_scc0 1b\n\t"
      "s_branch 4f\n"
      "3:\n\t"
      "s_memrealtime vcc\n\t"
      "s_sub_u32 %0, %0, 1\n\t"
      "s_waitcnt lgkmcnt(0)\n\t"
      "s_cselect_b32 vcc_lo, %1, vcc_lo\n\t"
      "s_sub_i32 vcc_lo, vcc_lo, %1\n"
      "2:\n\t"
      "s_cmp_lt_i32 vcc_lo, 0\n\t"
      "s_cbranch_scc1 3b\n"
      "4:"
      : "=&s"(n)
      : "s"(t0 + ticks)
      : "vcc", "scc", "memory");
}

typedef short s16x4 __attribute__((ext_vector_type(4)));
__device__ __forceinline__ v2u ldtr(const LAS char* a) {
  return __builtin_bit_cast(v2u, __builtin_amdgcn_ds_read_tr16_b64_v4i16((LAS s16x4*)a));
}
__device__ __forceinline__ v4u tag_bits(unsigned tag) {
  v4u t;
#pragma unroll
  for (int k = 0; k < 4; k++) t[k] = ((tag >> (6 * k)) & 7u) | (((tag >> (6 * k + 3)) & 7u) << 16);
  return t;
}

template <int N>
__device__ __forceinline__ void fetch_u(rsrc_t rs, int src, char* dst, const int (&chunk)[N], unsigned tag) {
  v4u v[N];
#pragma unroll
  for (int i = 0; i < N; i++) v[i] = __builtin_amdgcn_raw_buffer_load_b128(rs, src, chunk[i] * 1024, SC1);
  SCHED();
  const v4u tb = tag_bits(tag);
#pragma unroll 1
  for (int sp = 0; sp < SPIN_CAP; sp++) {
    v4u o = {0u, 0u, 0u, 0u};
#pragma unroll
    for (int i = 0; i < N; i++) o |= v[i] ^ tb;
    if (__all(((o.x | o.y | o.z | o.w) & 0x70007u) == 0u)) break;
    RETRY(N == 1);
    __builtin_amdgcn_s_sleep(2);
    MEMBAR();
#pragma unroll
    for (int i = 0; i < N; i++) v[i] = __builtin_amdgcn_raw_buffer_load_b128(rs, src, chunk[i] * 1024, SC1);
  }
#pragma unroll
  for (int i = 0; i < N; i++) {
    const v4u w = v[i] ^ (tb ^ 0x40004u);
    v2u* const up = (v2u*)(dst + chunk[i] * 256);
    up[0] = v2u{w.x, w.y};
    up[1] = v2u{w.z, w.w};
  }
}

struct cz { f32x2 v, s; };
__device__ __forceinline__ cz mkz(f32x2 v) {
  cz z;
  z.v = v;
  asm("v_pk_mov_b32 %0, %1, %1 op_sel:[1,0]" : "=v"(z.s) : "v"(v));
  return z;
}
#define CMUL_TXT(r, a, b, bs) "v_pk_mul_f32 " r ", " a ", " bs " op_sel:[1,0] neg_lo:[0,1]\n\tv_pk_fma_f32 " r ", " a ", " b ", " r " op_sel_hi:[0,1,1]\n\t"
__device__ __forceinline__ f32x2 cmul(f32x2 a, cz b) {
  f32x2 r;
  asm(CMUL_TXT("%0", "%1", "%2", "%3") : "=&v"(r) : "v"(a), "v"(b.v), "v"(b.s));
  return r;
}
__device__ __forceinline__ cz csq(cz a) { return mkz(cmul(a.v, a)); }
#define MC_(r, a, bs) "v_pk_mul_f32 " r ", " a ", " bs " op_sel:[1,0]\n\t"
#define FC_(r, a, b) "v_pk_fma_f32 " r ", " a ", " b ", " r " op_sel_hi:[0,1,1] neg_hi:[0,1,0]\n\t"
__device__ __forceinline__ void cmulc3(f32x2 (&r)[3], unsigned (&h)[3], f32x2 a0, cz b0, f32x2 a1, cz b1, f32x2 a2, cz b2) {
  asm(MC_("%0", "%3", "%5") MC_("%1", "%6", "%8") MC_("%2", "%9", "%11") FC_("%0", "%3", "%4") FC_("%1", "%6", "%7") FC_("%2", "%9", "%10")
      : "=&v"(r[0]), "=&v"(r[1]), "=&v"(r[2])
      : "v"(a0), "v"(b0.v), "v"(b0.s), "v"(a1), "v"(b1.v), "v"(b1.s), "v"(a2), "v"(b2.v), "v"(b2.s));
#pragma unroll
  for (int i = 0; i < 3; i++) h[i] = pkh(r[i].x, r[i].y);
}
__device__ __forceinline__ void hmul2(unsigned& r, unsigned& q, unsigned a, unsigned c, unsigned b, unsigned bs) {
  asm("v_pk_mul_f16 %0, %2, %5 op_sel:[1,0] neg_lo:[0,1]\n\tv_pk_mul_f16 %1, %3, %5 op_sel:[1,0] neg_lo:[0,1]\n\t"
      "v_pk_fma_f16 %0, %2, %4, %0 op_sel_hi:[0,1,1]\n\tv_pk_fma_f16 %1, %3, %4, %1 op_sel_hi:[0,1,1]"
      : "=&v"(r), "=&v"(q) : "v"(a), "v"(c), "v"(b), "v"(bs));
}
__device__ __forceinline__ float exp_c(float v) {
  const float t = v * 1.44269502f;
  const float e = fmaf(v, 1.92596303e-8f, fmaf(v, 1.44269502f, -t));
  return __builtin_amdgcn_exp2f(t) * fmaf(e, 0.693147182f, 1.f);
}
__device__ __forceinline__ f32x2 cexp_p2(float p, float ch, float ph, float pl) {
  const float mag = __builtin_amdgcn_exp2f(p * ch);
  const float fr = fmaf(p, pl, __builtin_amdgcn_fractf(p * ph));
  return f32x2{mag * __builtin_amdgcn_cosf(fr), mag * __builtin_amdgcn_sinf(fr)};
}
__device__ __forceinline__ unsigned pkh2(f32x2 v) { return pkh(v.x, v.y); }

#define CONV_ASM_TEXT \
  "s_barrier\n" "ds_read_b128 v[32:35], %8 offset:480\n" \
  "ds_read_b128 v[36:39], %8 offset:992\n" "ds_read_b128 v[40:43], %8 offset:1504\n" \
  "ds_read2_b32 v[44:45], %11 offset0:48 offset1:49\n" "ds_read2_b32 v[46:47], %11 offset0:50 offset1:51\n" \
  "ds_read2_b32 v[48:49], %11 offset0:32 offset1:33\n" "s_waitcnt lgkmcnt(5)\n" \
  "v_mfma_f32_16x16x32_f16 v[132:135], v[32:35], v[16:19], 0\n" "s_nop 0\n" \
  "ds_read2_b32 v[50:51], %11 offset0:34 offset1:35\n" "ds_read2_b32 v[52:53], %11 offset0:16 offset1:17\n" \
  "v_mfma_f32_16x16x32_f16 v[128:131], v[32:35], v[20:23], 0\n" "s_waitcnt lgkmcnt(6)\n" \
  "ds_read2_b32 v[54:55], %11 offset0:18 offset1:19\n" "ds_read2_b32 v[56:57], %11 offset0:0 offset1:1\n" \
  "v_mfma_f32_16x16x32_f16 v[124:127], v[36:39], v[16:19], 0\n" "s_nop 0\n" \
  "ds_read2_b32 v[58:59], %11 offset0:2 offset1:3\n" "ds_read_b128 v[60:63], %8 offset:2016\n" \
  "v_mfma_f32_16x16x32_f16 v[120:123], v[36:39], v[20:23], 0\n" "s_waitcnt lgkmcnt(9)\n" \
  "v_cvt_pk_f16_f32 v116, v132, v128\n" "v_cvt_pk_f16_f32 v117, v133, v129\n" \
  "v_mfma_f32_16x16x32_f16 v[164:167], v[40:43], v[16:19], 0\n" "v_cvt_pk_f16_f32 v118, v134, v130\n" \
  "v_cvt_pk_f16_f32 v119, v135, v131\n" "v_mfma_f32_16x16x32_f16 v[160:163], v[40:43], v[20:23], 0\n" \
  "s_waitcnt lgkmcnt(7)\n" "ds_read_b128 v[64:67], %9 offset:480\n" \
  "v_cvt_pk_f16_f32 v112, v124, v120\n" "v_mfma_f32_16x16x32_f16 v[148:151], v[116:119], v[44:47], 0\n" \
  "s_waitcnt lgkmcnt(6)\n" "ds_read_b128 v[68:71], %9 offset:736\n" \
  "v_cvt_pk_f16_f32 v113, v125, v121\n" "v_mfma_f32_16x16x32_f16 v[144:147], v[116:119], v[48:51], 0\n" \
  "s_waitcnt lgkmcnt(5)\n" "v_cvt_pk_f16_f32 v114, v126, v122\n" \
  "v_cvt_pk_f16_f32 v115, v127, v123\n" "v_mfma_f32_16x16x32_f16 v[140:143], v[116:119], v[52:55], 0\n" \
  "s_waitcnt lgkmcnt(3)\n" "ds_read_b128 v[72:75], %9 offset:992\n" \
  "ds_read_b64 v[76:77], %10 offset:256\n" "v_mfma_f32_16x16x32_f16 v[136:139], v[116:119], v[56:59], 0\n" \
  "s_waitcnt lgkmcnt(4)\n" "ds_read_b64 v[78:79], %10 offset:264\n" \
  "v_cvt_pk_f16_f32 v116, v164, v160\n" "v_mfma_f32_16x16x32_f16 v[132:135], v[60:63], v[16:19], 0\n" \
  "v_cvt_pk_f16_f32 v117, v165, v161\n" "v_cvt_pk_f16_f32 v118, v166, v162\n" \
  "v_mfma_f32_16x16x32_f16 v[128:131], v[60:63], v[20:23], 0\n" "ds_read_b64 v[80:81], %10 offset:128\n" \
  "v_cvt_pk_f16_f32 v119, v167, v163\n" "v_mfma_f32_16x16x32_f16 v[144:147], v[112:115], v[44:47], v[144:147]\n" \
  "ds_read_b64 v[82:83], %10 offset:136\n" "v_mfma_f32_16x16x32_f16 v[140:143], v[112:115], v[48:51], v[140:143]\n" \
  "s_nop 0\n" "ds_read_b64 v[84:85], %10 offset:0\n" \
  "ds_read_b64 v[86:87], %10 offset:8\n" "v_mfma_f32_16x16x32_f16 v[136:139], v[112:115], v[52:55], v[136:139]\n" \
  "s_waitcnt lgkmcnt(7)\n" "ds_read_b128 v[88:91], %9 offset:1248\n" \
  "v_cvt_pk_f16_f32 v112, v132, v128\n" "v_mfma_f32_16x16x32_f16 v[124:127], v[64:67], v[24:27], 0\n" \
  "v_cvt_pk_f16_f32 v113, v133, v129\n" "v_cvt_pk_f16_f32 v114, v134, v130\n" \
  "v_mfma_f32_16x16x32_f16 v[120:123], v[64:67], v[28:31], 0\n" "ds_read_b64 v[92:93], %10 offset:320\n" \
  "v_cvt_pk_f16_f32 v115, v135, v131\n" "v_mfma_f32_16x16x32_f16 v[140:143], v[116:119], v[44:47], v[140:143]\n" \
  "ds_read_b64 v[94:95], %10 offset:328\n" "v_mfma_f32_16x16x32_f16 v[136:139], v[116:119], v[48:51], v[136:139]\n" \
  "s_nop 0\n" "ds_read_b64 v[96:97], %10 offset:192\n" \
  "ds_read_b64 v[98:99], %10 offset:200\n" "v_mfma_f32_16x16x32_f16 v[164:167], v[68:71], v[24:27], 0\n" \
  "s_nop 0\n" "ds_read_b64 v[100:101], %10 offset:64\n" \
  "ds_read_b64 v[102:103], %10 offset:72\n" "v_mfma_f32_16x16x32_f16 v[160:163], v[68:71], v[28:31], 0\n" \
  "v_cvt_pk_f16_f32 v116, v124, v120\n" "v_cvt_pk_f16_f32 v117, v125, v121\n" \
  "v_mfma_f32_16x16x32_f16 v[136:139], v[112:115], v[44:47], v[136:139]\n" "s_waitcnt lgkmcnt(7)\n" \
  "v_cvt_pk_f16_f32 v118, v126, v122\n" "v_cvt_pk_f16_f32 v119, v127, v123\n" \
  "v_mfma_f32_16x16x32_f16 v[132:135], v[72:75], v[24:27], 0\n" "ds_read_b128 v[104:107], %9 offset:1504\n" \
  "v_cvt_pk_f16_f32 v112, v164, v160\n" "v_mfma_f32_16x16x32_f16 v[128:131], v[72:75], v[28:31], 0\n" \
  "v_cvt_pk_f16_f32 v113, v165, v161\n" "v_cvt_pk_f16_f32 v114, v166, v162\n" \
  "v_mfma_f32_16x16x32_f16 v[156:159], v[116:119], v[76:79], 0\n" "ds_read_b128 v[108:111], %9 offset:1760\n" \
  "v_cvt_pk_f16_f32 v115, v167, v163\n" "v_mfma_f32_16x16x32_f16 v[152:155], v[116:119], v[80:83], 0\n" \
  "s_nop 0\n" "ds_read2_b32 v[32:33], %6 offset0:144 offset1:145\n" \
  "ds_read2_b32 v[34:35], %6 offset0:146 offset1:147\n" "v_mfma_f32_16x16x32_f16 v[148:151], v[116:119], v[84:87], v[148:151]\n" \
  "s_waitcnt lgkmcnt(6)\n" "v_cvt_pk_f16_f32 v116, v132, v128\n" \
  "v_cvt_pk_f16_f32 v117, v133, v129\n" "v_mfma_f32_16x16x32_f16 v[124:127], v[88:91], v[24:27], 0\n" \
  "v_cvt_pk_f16_f32 v118, v134, v130\n" "v_cvt_pk_f16_f32 v119, v135, v131\n" \
  "v_mfma_f32_16x16x32_f16 v[120:123], v[88:91], v[28:31], 0\n" "s_nop 0\n" \
  "ds_read_b128 v[36:39], %9 offset:2016\n" "ds_read2_b32 v[40:41], %6 offset0:16 offset1:17\n" \
  "v_mfma_f32_16x16x32_f16 v[156:159], v[112:115], v[92:95], v[156:159]\n" "ds_read2_b32 v[42:43], %6 offset0:18 offset1:19\n" \
  "v_mfma_f32_16x16x32_f16 v[152:155], v[112:115], v[96:99], v[152:155]\n" "s_waitcnt lgkmcnt(6)\n" \
  "ds_read2_b32 v[56:57], %6 offset0:48 offset1:49\n" "ds_read2_b32 v[58:59], %6 offset0:50 offset1:51\n" \
  "v_mfma_f32_16x16x32_f16 v[148:151], v[112:115], v[100:103], v[148:151]\n" "v_cvt_pk_f16_f32 v112, v124, v120\n" \
  "v_cvt_pk_f16_f32 v113, v125, v121\n" "v_mfma_f32_16x16x32_f16 v[164:167], v[104:107], v[24:27], 0\n" \
  "v_cvt_pk_f16_f32 v114, v126, v122\n" "v_cvt_pk_f16_f32 v115, v127, v123\n" \
  "v_mfma_f32_16x16x32_f16 v[160:163], v[104:107], v[28:31], 0\n" "s_nop 0\n" \
  "ds_read_b128 v[16:19], %9 offset:2272\n" "ds_read2_b32 v[60:61], %6 offset0:176 offset1:177\n" \
  "v_mfma_f32_16x16x32_f16 v[152:155], v[116:119], v[76:79], v[152:155]\n" "ds_read2_b32 v[62:63], %6 offset0:178 offset1:179\n" \
  "v_mfma_f32_16x16x32_f16 v[148:151], v[116:119], v[80:83], v[148:151]\n" "s_nop 0\n" \
  "ds_read2_b32 v[20:21], %6 offset0:32 offset1:33\n" "ds_read2_b32 v[22:23], %6 offset0:34 offset1:35\n" \
  "v_mfma_f32_16x16x32_f16 v[144:147], v[116:119], v[84:87], v[144:147]\n" "s_waitcnt lgkmcnt(8)\n" \
  "v_cvt_pk_f16_f32 v116, v164, v160\n" "v_cvt_pk_f16_f32 v117, v165, v161\n" \
  "v_mfma_f32_16x16x32_f16 v[132:135], v[108:111], v[24:27], 0\n" "v_cvt_pk_f16_f32 v118, v166, v162\n" \
  "v_cvt_pk_f16_f32 v119, v167, v163\n" "v_mfma_f32_16x16x32_f16 v[128:131], v[108:111], v[28:31], 0\n" \
  "s_nop 0\n" "ds_read_b128 v[52:55], %9 offset:2528\n" \
  "ds_read2_b32 v[64:65], %6 offset0:160 offset1:161\n" "v_mfma_f32_16x16x32_f16 v[152:155], v[112:115], v[92:95], v[152:155]\n" \
  "ds_read2_b32 v[66:67], %6 offset0:162 offset1:163\n" "v_mfma_f32_16x16x32_f16 v[148:151], v[112:115], v[96:99], v[148:151]\n" \
  "s_nop 0\n" "ds_read2_b32 v[48:49], %6 offset0:80 offset1:81\n" \
  "ds_read2_b32 v[50:51], %6 offset0:82 offset1:83\n" "v_mfma_f32_16x16x32_f16 v[144:147], v[112:115], v[100:103], v[144:147]\n" \
  "v_cvt_pk_f16_f32 v112, v132, v128\n" "v_cvt_pk_f16_f32 v113, v133, v129\n" \
  "v_mfma_f32_16x16x32_f16 v[124:127], v[36:39], v[24:27], 0\n" "v_cvt_pk_f16_f32 v114, v134, v130\n" \
  "v_cvt_pk_f16_f32 v115, v135, v131\n" "v_mfma_f32_16x16x32_f16 v[120:123], v[36:39], v[28:31], 0\n" \
  "s_nop 0\n" "ds_read_b128 v[68:71], %9 offset:2784\n" \
  "ds_read2_b32 v[44:45], %6 offset0:208 offset1:209\n" "v_mfma_f32_16x16x32_f16 v[148:151], v[116:119], v[76:79], v[148:151]\n" \
  "ds_read2_b32 v[46:47], %6 offset0:210 offset1:211\n" "v_mfma_f32_16x16x32_f16 v[144:147], v[116:119], v[80:83], v[144:147]\n" \
  "s_nop 0\n" "ds_read2_b32 v[72:73], %6 offset0:64 offset1:65\n" \
  "ds_read2_b32 v[74:75], %6 offset0:66 offset1:67\n" "v_mfma_f32_16x16x32_f16 v[140:143], v[116:119], v[84:87], v[140:143]\n" \
  "s_waitcnt lgkmcnt(8)\n" "v_cvt_pk_f16_f32 v116, v124, v120\n" \
  "v_cvt_pk_f16_f32 v117, v125, v121\n" "v_mfma_f32_16x16x32_f16 v[132:135], v[16:19], v[24:27], 0\n" \
  "v_cvt_pk_f16_f32 v118, v126, v122\n" "v_cvt_pk_f16_f32 v119, v127, v123\n" \
  "v_mfma_f32_16x16x32_f16 v[128:131], v[16:19], v[28:31], 0\n" "s_nop 0\n" \
  "ds_read_b128 v[88:91], %9 offset:3040\n" "ds_read2_b32 v[104:105], %6 offset0:192 offset1:193\n" \
  "v_mfma_f32_16x16x32_f16 v[148:151], v[112:115], v[92:95], v[148:151]\n" "ds_read2_b32 v[106:107], %6 offset0:194 offset1:195\n" \
  "v_mfma_f32_16x16x32_f16 v[144:147], v[112:115], v[96:99], v[144:147]\n" "s_nop 0\n" \
  "ds_read_b128 v[108:111], %8 offset:0\n" "ds_read2_b32 v[36:37], %6 offset0:224 offset1:225\n" \
  "v_mfma_f32_16x16x32_f16 v[140:143], v[112:115], v[100:103], v[140:143]\n" "v_cvt_pk_f16_f32 v112, v132, v128\n" \
  "v_cvt_pk_f16_f32 v113, v133, v129\n" "v_mfma_f32_16x16x32_f16 v[124:127], v[52:55], v[24:27], 0\n" \
  "v_cvt_pk_f16_f32 v114, v134, v130\n" "v_cvt_pk_f16_f32 v115, v135, v131\n" \
  "v_mfma_f32_16x16x32_f16 v[120:123], v[52:55], v[28:31], 0\n" "s_nop 0\n" \
  "ds_read2_b32 v[38:39], %6 offset0:226 offset1:227\n" "ds_read_b128 v[16:19], %9 offset:3296\n" \
  "v_mfma_f32_16x16x32_f16 v[144:147], v[116:119], v[76:79], v[144:147]\n" "ds_read2_b32 v[52:53], %6 offset0:96 offset1:97\n" \
  "v_mfma_f32_16x16x32_f16 v[140:143], v[116:119], v[80:83], v[140:143]\n" "ds_read2_b32 v[54:55], %6 offset0:98 offset1:99\n" \
  "v_mfma_f32_16x16x32_f16 v[136:139], v[116:119], v[84:87], v[136:139]\n" "s_waitcnt lgkmcnt(7)\n" \
  "v_cvt_pk_f16_f32 v116, v124, v120\n" "v_cvt_pk_f16_f32 v117, v125, v121\n" \
  "v_mfma_f32_16x16x32_f16 v[132:135], v[68:71], v[24:27], 0\n" "v_cvt_pk_f16_f32 v118, v126, v122\n" \
  "v_cvt_pk_f16_f32 v119, v127, v123\n" "v_mfma_f32_16x16x32_f16 v[128:131], v[68:71], v[28:31], 0\n" \
  "s_nop 0\n" "ds_read_b128 v[84:87], %8 offset:128\n" \
  "ds_read2_b32 v[68:69], %6 offset0:240 offset1:241\n" "v_mfma_f32_16x16x32_f16 v[144:147], v[112:115], v[92:95], v[144:147]\n" \
  "ds_read2_b32 v[70:71], %6 offset0:242 offset1:243\n" "v_mfma_f32_16x16x32_f16 v[140:143], v[112:115], v[96:99], v[140:143]\n" \
  "v_mfma_f32_16x16x32_f16 v[136:139], v[112:115], v[100:103], v[136:139]\n" "ds_read_b128 v[100:103], %8 offset:64\n" \
  "v_cvt_pk_f16_f32 v112, v132, v128\n" "v_mfma_f32_16x16x32_f16 v[124:127], v[88:91], v[24:27], 0\n" \
  "v_cvt_pk_f16_f32 v113, v133, v129\n" "v_cvt_pk_f16_f32 v114, v134, v130\n" \
  "v_mfma_f32_16x16x32_f16 v[120:123], v[88:91], v[28:31], 0\n" "s_waitcnt lgkmcnt(4)\n" \
  "ds_read_b128 v[88:91], %8 offset:256\n" "v_cvt_pk_f16_f32 v115, v135, v131\n" \
  "v_mfma_f32_16x16x32_f16 v[164:167], v[32:35], v[108:111], 0\n" "v_mfma_f32_16x16x32_f16 v[140:143], v[116:119], v[76:79], v[140:143]\n" \
  "v_mfma_f32_16x16x32_f16 v[136:139], v[116:119], v[80:83], v[136:139]\n" "s_nop 0\n" \
  "ds_read_b128 v[80:83], %8 offset:192\n" "v_cvt_pk_f16_f32 v116, v124, v120\n" \
  "v_mfma_f32_16x16x32_f16 v[132:135], v[16:19], v[24:27], 0\n" "v_cvt_pk_f16_f32 v117, v125, v121\n" \
  "v_cvt_pk_f16_f32 v118, v126, v122\n" "v_mfma_f32_16x16x32_f16 v[128:131], v[16:19], v[28:31], 0\n" \
  "ds_read_b128 v[24:27], %8 offset:320\n" "v_cvt_pk_f16_f32 v119, v127, v123\n" \
  "v_mfma_f32_16x16x32_f16 v[160:163], v[40:43], v[108:111], 0\n" "s_nop 0\n" \
  "ds_read2_b32 v[16:17], %6 offset0:112 offset1:113\n" "ds_read2_b32 v[18:19], %6 offset0:114 offset1:115\n" \
  "v_mfma_f32_16x16x32_f16 v[140:143], v[112:115], v[92:95], v[140:143]\n" "ds_read_b128 v[28:31], %8 offset:384\n" \
  "v_mfma_f32_16x16x32_f16 v[136:139], v[112:115], v[96:99], v[136:139]\n" "s_waitcnt lgkmcnt(5)\n" \
  "ds_read2_b32 v[124:125], %7 offset0:0 offset1:1\n" "v_cvt_pk_f16_f32 v112, v132, v128\n" \
  "v_mfma_f32_16x16x32_f16 v[160:163], v[56:59], v[84:87], v[160:163]\n" "v_cvt_pk_f16_f32 v113, v133, v129\n" \
  "v_cvt_pk_f16_f32 v114, v134, v130\n" "v_mfma_f32_16x16x32_f16 v[164:167], v[60:63], v[84:87], v[164:167]\n" \
  "ds_read2_b32 v[126:127], %7 offset0:2 offset1:3\n" "v_cvt_pk_f16_f32 v115, v135, v131\n" \
  "v_mfma_f32_16x16x32_f16 v[160:163], v[20:23], v[100:103], v[160:163]\n" "s_nop 0\n" \
  "ds_read_b128 v[108:111], %8 offset:448\n" "ds_read2_b32 v[120:121], %6 offset0:128 offset1:129\n" \
  "v_mfma_f32_16x16x32_f16 v[164:167], v[64:67], v[100:103], v[164:167]\n" "ds_read2_b32 v[122:123], %6 offset0:130 offset1:131\n" \
  "v_mfma_f32_16x16x32_f16 v[136:139], v[116:119], v[76:79], v[136:139]\n" "s_nop 0\n" \
  "ds_read2_b32 v[96:97], %7 offset0:16 offset1:17\n" "ds_read2_b32 v[98:99], %7 offset0:18 offset1:19\n" \
  "v_mfma_f32_16x16x32_f16 v[160:163], v[48:51], v[88:91], v[160:163]\n" "ds_read_b128 v[84:87], %8 offset:512\n" \
  "v_mfma_f32_16x16x32_f16 v[164:167], v[44:47], v[88:91], v[164:167]\n" "s_waitcnt lgkmcnt(8)\n" \
  "v_mfma_f32_16x16x32_f16 v[160:163], v[72:75], v[80:83], v[160:163]\n" "s_nop 0\n" \
  "ds_read2_b32 v[132:133], %6 offset0:0 offset1:1\n" "ds_read2_b32 v[134:135], %6 offset0:2 offset1:3\n" \
  "v_mfma_f32_16x16x32_f16 v[164:167], v[104:107], v[80:83], v[164:167]\n" "ds_read_b128 v[128:131], %8 offset:576\n" \
  "v_mfma_f32_16x16x32_f16 v[136:139], v[112:115], v[92:95], v[136:139]\n" "ds_read_b128 v[100:103], %8 offset:640\n" \
  "v_mfma_f32_16x16x32_f16 v[164:167], v[36:39], v[24:27], v[164:167]\n" "ds_read_b128 v[116:119], %8 offset:704\n" \
  "v_mfma_f32_16x16x32_f16 v[160:163], v[52:55], v[24:27], v[160:163]\n" "v_mfma_f32_16x16x32_f16 v[164:167], v[68:71], v[28:31], v[164:167]\n" \
  "s_nop 0\n" "ds_read_b128 v[76:79], %8 offset:768\n" \
  "ds_read_b128 v[88:91], %8 offset:832\n" "v_mfma_f32_16x16x32_f16 v[160:163], v[16:19], v[28:31], v[160:163]\n" \
  "s_waitcnt lgkmcnt(7)\n" "ds_read_b128 v[80:83], %8 offset:896\n" \
  "v_mfma_f32_16x16x32_f16 v[164:167], v[124:127], v[108:111], v[164:167]\n" "ds_read_b128 v[112:115], %8 offset:960\n" \
  "v_mfma_f32_16x16x32_f16 v[160:163], v[120:123], v[108:111], v[160:163]\n" "ds_read_b128 v[92:95], %8 offset:1024\n" \
  "v_mfma_f32_16x16x32_f16 v[164:167], v[96:99], v[84:87], v[164:167]\n" "v_mfma_f32_16x16x32_f16 v[160:163], v[32:35], v[84:87], v[160:163]\n" \
  "s_nop 0\n" "ds_read_b128 v[24:27], %8 offset:1088\n" \
  "ds_read_b128 v[28:31], %8 offset:1152\n" "v_mfma_f32_16x16x32_f16 v[156:159], v[40:43], v[84:87], v[156:159]\n" \
  "s_waitcnt lgkmcnt(7)\n" "ds_read_b128 v[108:111], %8 offset:1216\n" \
  "v_mfma_f32_16x16x32_f16 v[148:151], v[132:135], v[84:87], v[148:151]\n" "v_mul_f32 v164, %5, v164\n" \
  "v_mul_f32 v165, %5, v165\n" "v_mfma_f32_16x16x32_f16 v[160:163], v[64:67], v[128:131], v[160:163]\n" \
  "v_mul_f32 v166, %5, v166\n" "v_mul_f32 v167, %5, v167\n" \
  "v_mfma_f32_16x16x32_f16 v[156:159], v[20:23], v[128:131], v[156:159]\n" "v_cvt_pk_bf16_f32 v164, v164, v165\n" \
  "v_cvt_pk_bf16_f32 v165, v166, v167\n" "v_mfma_f32_16x16x32_f16 v[160:163], v[60:63], v[100:103], v[160:163]\n" \
  "v_pk_min_u16 v164, v164, %4\n" "v_pk_min_u16 v165, v165, %4\n" \
  "v_mfma_f32_16x16x32_f16 v[156:159], v[56:59], v[100:103], v[156:159]\n" "ds_write_b64 %12, v[164:165] offset:0\n" \
  "v_mfma_f32_16x16x32_f16 v[160:163], v[104:107], v[116:119], v[160:163]\n" "s_nop 0\n" \
  "ds_read_b128 v[84:87], %8 offset:1280\n" "ds_read_b128 v[128:131], %8 offset:1344\n" \
  "v_mfma_f32_16x16x32_f16 v[156:159], v[72:75], v[116:119], v[156:159]\n" "s_waitcnt lgkmcnt(4)\n" \
  "ds_read_b128 v[100:103], %8 offset:1408\n" "ds_read_b128 v[164:167], %8 offset:1472\n" \
  "v_mfma_f32_16x16x32_f16 v[160:163], v[44:47], v[76:79], v[160:163]\n" "v_mfma_f32_16x16x32_f16 v[156:159], v[48:51], v[76:79], v[156:159]\n" \
  "v_mfma_f32_16x16x32_f16 v[160:163], v[36:39], v[88:91], v[160:163]\n" "ds_read_b128 v[116:119], %8 offset:1536\n" \
  "v_mfma_f32_16x16x32_f16 v[156:159], v[52:55], v[88:91], v[156:159]\n" "s_nop 0\n" \
  "ds_read_b128 v[76:79], %8 offset:1600\n" "ds_read_b128 v[88:91], %8 offset:1664\n" \
  "v_mfma_f32_16x16x32_f16 v[160:163], v[68:71], v[80:83], v[160:163]\n" "v_mfma_f32_16x16x32_f16 v[156:159], v[16:19], v[80:83], v[156:159]\n" \
  "ds_read_b128 v[80:83], %8 offset:1728\n" "v_mfma_f32_16x16x32_f16 v[160:163], v[124:127], v[112:115], v[160:163]\n" \
  "v_mfma_f32_16x16x32_f16 v[156:159], v[120:123], v[112:115], v[156:159]\n" "ds_read_b128 v[112:115], %8 offset:1792\n" \
  "v_mfma_f32_16x16x32_f16 v[160:163], v[96:99], v[92:95], v[160:163]\n" "v_mfma_f32_16x16x32_f16 v[156:159], v[32:35], v[92:95], v[156:159]\n" \
  "v_mfma_f32_16x16x32_f16 v[152:155], v[40:43], v[92:95], v[152:155]\n" "v_mfma_f32_16x16x32_f16 v[144:147], v[132:135], v[92:95], v[144:147]\n" \
  "ds_read_b128 v[92:95], %8 offset:1856\n" "v_mfma_f32_16x16x32_f16 v[156:159], v[64:67], v[24:27], v[156:159]\n" \
  "v_mfma_f32_16x16x32_f16 v[152:155], v[20:23], v[24:27], v[152:155]\n" "ds_read_b128 v[24:27], %8 offset:1920\n" \
  "v_mfma_f32_16x16x32_f16 v[156:159], v[60:63], v[28:31], v[156:159]\n" "v_mul_f32 v160, %5, v160\n" \
  "v_mul_f32 v161, %5, v161\n" "v_mfma_f32_16x16x32_f16 v[152:155], v[56:59], v[28:31], v[152:155]\n" \
  "s_waitcnt lgkmcnt(2)\n" "v_mul_f32 v162, %5, v162\n" \
  "v_mul_f32 v163, %5, v163\n" "v_mfma_f32_16x16x32_f16 v[156:159], v[104:107], v[108:111], v[156:159]\n" \
  "v_cvt_pk_bf16_f32 v160, v160, v161\n" "v_cvt_pk_bf16_f32 v161, v162, v163\n" \
  "v_mfma_f32_16x16x32_f16 v[152:155], v[72:75], v[108:111], v[152:155]\n" "v_pk_min_u16 v160, v160, %4\n" \
  "v_pk_min_u16 v161, v161, %4\n" "v_mfma_f32_16x16x32_f16 v[156:159], v[44:47], v[84:87], v[156:159]\n" \
  "ds_write_b64 %12, v[160:161] offset:512\n" "v_mfma_f32_16x16x32_f16 v[152:155], v[48:51], v[84:87], v[152:155]\n" \
  "s_nop 0\n" "ds_read_b128 v[28:31], %8 offset:1984\n" \
  "ds_read_b128 v[108:111], %8 offset:2048\n" "v_mfma_f32_16x16x32_f16 v[156:159], v[36:39], v[128:131], v[156:159]\n" \
  "s_nop 0\n" "ds_read_b128 v[160:163], %8 offset:2112\n" \
  "ds_read_b128 v[84:87], %8 offset:2176\n" "v_mfma_f32_16x16x32_f16 v[152:155], v[52:55], v[128:131], v[152:155]\n" \
  "v_mfma_f32_16x16x32_f16 v[156:159], v[68:71], v[100:103], v[156:159]\n" "v_mfma_f32_16x16x32_f16 v[152:155], v[16:19], v[100:103], v[152:155]\n" \
  "s_waitcnt lgkmcnt(4)\n" "ds_read_b128 v[128:131], %8 offset:2240\n" \
  "v_mfma_f32_16x16x32_f16 v[156:159], v[124:127], v[164:167], v[156:159]\n" "s_barrier\n" \
  "ds_read_b128 v[100:103], %8 offset:2304\n" "v_mfma_f32_16x16x32_f16 v[152:155], v[120:123], v[164:167], v[152:155]\n" \
  "ds_read_b128 v[164:167], %8 offset:2368\n" "v_mfma_f32_16x16x32_f16 v[156:159], v[96:99], v[116:119], v[156:159]\n" \
  "v_mfma_f32_16x16x32_f16 v[152:155], v[32:35], v[116:119], v[152:155]\n" "v_mfma_f32_16x16x32_f16 v[148:151], v[40:43], v[116:119], v[148:151]\n" \
  "v_mfma_f32_16x16x32_f16 v[140:143], v[132:135], v[116:119], v[140:143]\n" "ds_read_b128 v[116:119], %8 offset:2432\n" \
  "v_mfma_f32_16x16x32_f16 v[152:155], v[64:67], v[76:79], v[152:155]\n" "v_mfma_f32_16x16x32_f16 v[148:151], v[20:23], v[76:79], v[148:151]\n" \
  "ds_read_b128 v[76:79], %8 offset:2496\n" "v_mfma_f32_16x16x32_f16 v[152:155], v[60:63], v[88:91], v[152:155]\n" \
  "v_mul_f32 v156, %5, v156\n" "v_mul_f32 v157, %5, v157\n" \
  "v_mfma_f32_16x16x32_f16 v[148:151], v[56:59], v[88:91], v[148:151]\n" "v_mul_f32 v158, %5, v158\n" \
  "v_mul_f32 v159, %5, v159\n" "v_mfma_f32_16x16x32_f16 v[152:155], v[104:107], v[80:83], v[152:155]\n" \
  "v_cvt_pk_bf16_f32 v156, v156, v157\n" "v_cvt_pk_bf16_f32 v157, v158, v159\n" \
  "v_mfma_f32_16x16x32_f16 v[148:151], v[72:75], v[80:83], v[148:151]\n" "v_pk_min_u16 v156, v156, %4\n" \
  "v_pk_min_u16 v157, v157, %4\n" "v_mfma_f32_16x16x32_f16 v[152:155], v[44:47], v[112:115], v[152:155]\n" \
  "ds_write_b64 %12, v[156:157] offset:1024\n" "v_mfma_f32_16x16x32_f16 v[148:151], v[48:51], v[112:115], v[148:151]\n" \
  "s_nop 0\n" "ds_read_b128 v[88:91], %8 offset:2560\n" \
  "ds_read_b128 v[80:83], %8 offset:2624\n" "v_mfma_f32_16x16x32_f16 v[152:155], v[36:39], v[92:95], v[152:155]\n" \
  "s_nop 0\n" "ds_read_b128 v[156:159], %8 offset:2688\n" \
  "ds_read_b128 v[112:115], %8 offset:2752\n" "v_mfma_f32_16x16x32_f16 v[148:151], v[52:55], v[92:95], v[148:151]\n" \
  "v_mfma_f32_16x16x32_f16 v[152:155], v[68:71], v[24:27], v[152:155]\n" "v_mfma_f32_16x16x32_f16 v[148:151], v[16:19], v[24:27], v[148:151]\n" \
  "s_waitcnt lgkmcnt(5)\n" "ds_read_b128 v[92:95], %8 offset:2816\n" \
  "v_mfma_f32_16x16x32_f16 v[152:155], v[124:127], v[28:31], v[152:155]\n" "ds_read_b128 v[24:27], %8 offset:2880\n" \
  "v_mfma_f32_16x16x32_f16 v[148:151], v[120:123], v[28:31], v[148:151]\n" "ds_read_b128 v[28:31], %8 offset:2944\n" \
  "v_mfma_f32_16x16x32_f16 v[152:155], v[96:99], v[108:111], v[152:155]\n" "v_mfma_f32_16x16x32_f16 v[148:151], v[32:35], v[108:111], v[148:151]\n" \
  "v_mfma_f32_16x16x32_f16 v[144:147], v[40:43], v[108:111], v[144:147]\n" "v_mfma_f32_16x16x32_f16 v[136:139], v[132:135], v[108:111], v[136:139]\n" \
  "ds_read_b128 v[132:135], %8 offset:3008\n" "v_mfma_f32_16x16x32_f16 v[148:151], v[64:67], v[160:163], v[148:151]\n" \
  "v_mfma_f32_16x16x32_f16 v[144:147], v[20:23], v[160:163], v[144:147]\n" "v_mfma_f32_16x16x32_f16 v[148:151], v[60:63], v[84:87], v[148:151]\n" \
  "s_nop 0\n" "v_mul_f32 v152, %5, v152\n" \
  "v_mul_f32 v153, %5, v153\n" "v_mfma_f32_16x16x32_f16 v[144:147], v[56:59], v[84:87], v[144:147]\n" \
  "v_mul_f32 v154, %5, v154\n" "v_mul_f32 v155, %5, v155\n" \
  "v_mfma_f32_16x16x32_f16 v[148:151], v[104:107], v[128:131], v[148:151]\n" "v_cvt_pk_bf16_f32 v152, v152, v153\n" \
  "v_cvt_pk_bf16_f32 v153, v154, v155\n" "v_mfma_f32_16x16x32_f16 v[144:147], v[72:75], v[128:131], v[144:147]\n" \
  "v_pk_min_u16 v152, v152, %4\n" "v_pk_min_u16 v153, v153, %4\n" \
  "v_mfma_f32_16x16x32_f16 v[148:151], v[44:47], v[100:103], v[148:151]\n" "ds_write_b64 %12, v[152:153] offset:1536\n" \
  "v_mfma_f32_16x16x32_f16 v[144:147], v[48:51], v[100:103], v[144:147]\n" "v_mfma_f32_16x16x32_f16 v[148:151], v[36:39], v[164:167], v[148:151]\n" \
  "v_mfma_f32_16x16x32_f16 v[144:147], v[52:55], v[164:167], v[144:147]\n" "v_mfma_f32_16x16x32_f16 v[148:151], v[68:71], v[116:119], v[148:151]\n" \
  "v_mfma_f32_16x16x32_f16 v[144:147], v[16:19], v[116:119], v[144:147]\n" "s_waitcnt lgkmcnt(0)\n" \
  "v_mfma_f32_16x16x32_f16 v[148:151], v[124:127], v[76:79], v[148:151]\n" "s_barrier\n" \
  "ds_read_b128 v[108:111], %8 offset:3072\n" "ds_read_b128 v[160:163], %8 offset:3136\n" \
  "v_mfma_f32_16x16x32_f16 v[144:147], v[120:123], v[76:79], v[144:147]\n" "s_nop 0\n" \
  "ds_read_b128 v[84:87], %8 offset:3200\n" "ds_read_b128 v[128:131], %8 offset:3264\n" \
  "v_mfma_f32_16x16x32_f16 v[148:151], v[96:99], v[88:91], v[148:151]\n" "ds_read_b128 v[152:155], %8 offset:3328\n" \
  "v_mfma_f32_16x16x32_f16 v[144:147], v[32:35], v[88:91], v[144:147]\n" "s_nop 2\n" \
  "v_mfma_f32_16x16x32_f16 v[140:143], v[40:43], v[88:91], v[140:143]\n" "s_nop 1\n" \
  "v_mfma_f32_16x16x32_f16 v[144:147], v[64:67], v[80:83], v[144:147]\n" "v_mul_f32 v148, %5, v148\n" \
  "v_mul_f32 v149, %5, v149\n" "v_mfma_f32_16x16x32_f16 v[140:143], v[20:23], v[80:83], v[140:143]\n" \
  "v_mul_f32 v150, %5, v150\n" "v_mul_f32 v151, %5, v151\n" \
  "v_mfma_f32_16x16x32_f16 v[144:147], v[60:63], v[156:159], v[144:147]\n" "v_cvt_pk_bf16_f32 v148, v148, v149\n" \
  "v_cvt_pk_bf16_f32 v149, v150, v151\n" "v_mfma_f32_16x16x32_f16 v[140:143], v[56:59], v[156:159], v[140:143]\n" \
  "v_pk_min_u16 v148, v148, %4\n" "v_pk_min_u16 v149, v149, %4\n" \
  "v_mfma_f32_16x16x32_f16 v[144:147], v[104:107], v[112:115], v[144:147]\n" "ds_write_b64 %12, v[148:149] offset:2048\n" \
  "v_mfma_f32_16x16x32_f16 v[140:143], v[72:75], v[112:115], v[140:143]\n" "s_nop 0\n" \
  "ds_read_b128 v[100:103], %8 offset:3392\n" "ds_read_b128 v[164:167], %8 offset:3456\n" \
  "v_mfma_f32_16x16x32_f16 v[144:147], v[44:47], v[92:95], v[144:147]\n" "s_nop 0\n" \
  "ds_read_b128 v[116:119], %8 offset:3520\n" "ds_read_b128 v[76:79], %8 offset:3584\n" \
  "v_mfma_f32_16x16x32_f16 v[140:143], v[48:51], v[92:95], v[140:143]\n" "v_mfma_f32_16x16x32_f16 v[144:147], v[36:39], v[24:27], v[144:147]\n" \
  "v_mfma_f32_16x16x32_f16 v[140:143], v[52:55], v[24:27], v[140:143]\n" "ds_read_b128 v[88:91], %8 offset:3648\n" \
  "v_mfma_f32_16x16x32_f16 v[144:147], v[68:71], v[28:31], v[144:147]\n" "s_nop 0\n" \
  "ds_read_b128 v[80:83], %8 offset:3712\n" "ds_read_b128 v[156:159], %8 offset:3776\n" \
  "v_mfma_f32_16x16x32_f16 v[140:143], v[16:19], v[28:31], v[140:143]\n" "s_nop 0\n" \
  "ds_read_b128 v[148:151], %8 offset:3840\n" "ds_read_b128 v[112:115], %8 offset:3904\n" \
  "v_mfma_f32_16x16x32_f16 v[144:147], v[124:127], v[132:135], v[144:147]\n" "v_mfma_f32_16x16x32_f16 v[140:143], v[120:123], v[132:135], v[140:143]\n" \
  "s_waitcnt lgkmcnt(7)\n" "v_mfma_f32_16x16x32_f16 v[144:147], v[96:99], v[108:111], v[144:147]\n" \
  "ds_read_b128 v[92:95], %8 offset:3968\n" "v_mfma_f32_16x16x32_f16 v[140:143], v[32:35], v[108:111], v[140:143]\n" \
  "s_nop 0\n" "ds_read_b128 v[24:27], %8 offset:4032\n" \
  "ds_read_b128 v[28:31], %8 offset:4096\n" "v_mfma_f32_16x16x32_f16 v[136:139], v[40:43], v[108:111], v[136:139]\n" \
  "s_nop 1\n" "v_mfma_f32_16x16x32_f16 v[140:143], v[64:67], v[160:163], v[140:143]\n" \
  "v_mul_f32 v144, %5, v144\n" "v_mul_f32 v145, %5, v145\n" \
  "v_mfma_f32_16x16x32_f16 v[140:143], v[60:63], v[84:87], v[140:143]\n" "v_mul_f32 v146, %5, v146\n" \
  "v_mul_f32 v147, %5, v147\n" "v_mfma_f32_16x16x32_f16 v[140:143], v[104:107], v[128:131], v[140:143]\n" \
  "v_cvt_pk_bf16_f32 v144, v144, v145\n" "v_cvt_pk_bf16_f32 v145, v146, v147\n" \
  "v_mfma_f32_16x16x32_f16 v[140:143], v[44:47], v[152:155], v[140:143]\n" "v_pk_min_u16 v144, v144, %4\n" \
  "v_pk_min_u16 v145, v145, %4\n" "v_mfma_f32_16x16x32_f16 v[140:143], v[36:39], v[100:103], v[140:143]\n" \
  "ds_write_b64 %12, v[144:145] offset:2560\n" "v_mfma_f32_16x16x32_f16 v[140:143], v[68:71], v[164:167], v[140:143]\n" \
  "s_waitcnt lgkmcnt(3)\n" "v_mfma_f32_16x16x32_f16 v[140:143], v[124:127], v[116:119], v[140:143]\n" \
  "v_mfma_f32_16x16x32_f16 v[140:143], v[96:99], v[76:79], v[140:143]\n" "s_nop 2\n" \
  "v_mfma_f32_16x16x32_f16 v[136:139], v[20:23], v[160:163], v[136:139]\n" "s_nop 2\n" \
  "v_mfma_f32_16x16x32_f16 v[136:139], v[56:59], v[84:87], v[136:139]\n" "s_waitcnt lgkmcnt(0)\n" \
  "v_mul_f32 v140, %5, v140\n" "v_mul_f32 v141, %5, v141\n" \
  "v_mfma_f32_16x16x32_f16 v[136:139], v[72:75], v[128:131], v[136:139]\n" "s_barrier\n" \
  "v_mul_f32 v142, %5, v142\n" "v_mul_f32 v143, %5, v143\n" \
  "v_mfma_f32_16x16x32_f16 v[136:139], v[48:51], v[152:155], v[136:139]\n" "v_cvt_pk_bf16_f32 v140, v140, v141\n" \
  "v_cvt_pk_bf16_f32 v141, v142, v143\n" "v_mfma_f32_16x16x32_f16 v[136:139], v[52:55], v[100:103], v[136:139]\n" \
  "v_pk_min_u16 v140, v140, %4\n" "v_pk_min_u16 v141, v141, %4\n" \
  "v_mfma_f32_16x16x32_f16 v[136:139], v[16:19], v[164:167], v[136:139]\n" "ds_write_b64 %12, v[140:141] offset:3072\n" \
  "v_mfma_f32_16x16x32_f16 v[136:139], v[120:123], v[116:119], v[136:139]\n" "v_mfma_f32_16x16x32_f16 v[136:139], v[32:35], v[76:79], v[136:139]\n" \
  "v_mfma_f32_16x16x32_f16 v[136:139], v[64:67], v[88:91], v[136:139]\n" "v_mfma_f32_16x16x32_f16 v[136:139], v[60:63], v[80:83], v[136:139]\n" \
  "v_mfma_f32_16x16x32_f16 v[136:139], v[104:107], v[156:159], v[136:139]\n" "v_mfma_f32_16x16x32_f16 v[136:139], v[44:47], v[148:151], v[136:139]\n" \
  "v_mfma_f32_16x16x32_f16 v[136:139], v[36:39], v[112:115], v[136:139]\n" "v_mfma_f32_16x16x32_f16 v[136:139], v[68:71], v[92:95], v[136:139]\n" \
  "v_mfma_f32_16x16x32_f16 v[136:139], v[124:127], v[24:27], v[136:139]\n" "v_mfma_f32_16x16x32_f16 v[136:139], v[96:99], v[28:31], v[136:139]\n" \
  "s_nop 2\n" "s_nop 4\n" \
  "v_mul_f32 v136, %5, v136\n" "v_mul_f32 v137, %5, v137\n" \
  "v_mul_f32 v138, %5, v138\n" "v_mul_f32 v139, %5, v139\n" \
  "v_cvt_pk_bf16_f32 v136, v136, v137\n" "v_cvt_pk_bf16_f32 v137, v138, v139\n" \
  "v_pk_min_u16 v136, v136, %4\n" "v_pk_min_u16 v137, v137, %4\n" \
  "ds_write_b64 %12, v[136:137] offset:3584\n" "s_waitcnt lgkmcnt(0)\n" \
  "s_barrier\n"
#define CONV_ASM_CLOBBERS "v32", "v33", "v34", "v35", "v36", "v37", "v38", "v39", "v40", "v41", "v42", "v43", "v44", "v45", "v46", "v47", "v48", "v49", "v50", "v51", "v52", "v53", "v54", "v55", "v56", "v57", "v58", "v59", "v60", "v61", "v62", "v63", "v64", "v65", "v66", "v67", "v68", "v69", "v70", "v71", "v72", "v73", "v74", "v75", "v76", "v77", "v78", "v79", "v80", "v81", "v82", "v83", "v84", "v85", "v86", "v87", "v88", "v89", "v90", "v91", "v92", "v93", "v94", "v95", "v96", "v97", "v98", "v99", "v100", "v101", "v102", "v103", "v104", "v105", "v106", "v107", "v108", "v109", "v110", "v111", "v112", "v113", "v114", "v115", "v116", "v117", "v118", "v119", "v120", "v121", "v122", "v123", "v124", "v125", "v126", "v127", "v128", "v129", "v130", "v131", "v132", "v133", "v134", "v135", "v136", "v137", "v138", "v139", "v140", "v141", "v142", "v143", "v144", "v145", "v146", "v147", "v148", "v149", "v150", "v151", "v152", "v153", "v154", "v155", "v156", "v157", "v158", "v159", "v160", "v161", "v162", "v163", "v164", "v165", "v166", "v167", "memory"
#define CONV_ASM_PRE(q) "+{v[16:19]}"(q[0]), "+{v[20:23]}"(q[1]), "+{v[24:27]}"(q[2]), "+{v[28:31]}"(q[3])

struct Params {
  const u16 *x, *lar, *aim, *crl, *cim, *ldt, *dskip, *wr, *wg, *wu, *wd;
  u16* out;
  char* ws;
};

typedef unsigned long long u64;
typedef u64 v4q __attribute__((ext_vector_type(4)));
typedef unsigned v16u __attribute__((ext_vector_type(16)));
#define GAS __attribute__((address_space(1)))

__global__ __launch_bounds__(NT, 3) void k_main(Params) {
  extern __shared__ __attribute__((aligned(16))) char smem[];
  u64 kx, kw, t0_;
  v4q kb_;
  unsigned epoch;
  v4u ga, gb, gc, gd;
  const unsigned bid = blockIdx.x;
  asm volatile(
      ".p2align 12\n\t"
      "s_load_dwordx16 s[8:23], s[0:1], 0x0\n\t"
      "s_getpc_b64 s[4:5]\n\t"
      "s_load_dwordx2 s[36:37], s[0:1], 0x60\n\t"
      "s_load_dwordx8 s[24:31], s[0:1], 0x40\n\t" T0_ASM
      "v_lshrrev_b32 v17, 6, v0\n\t"
      "s_lshl_b32 s3, s2, 4\n\t"
      "v_mov_b32 v1, 0\n\t"
      "v_lshl_add_u32 v17, v17, 12, s3\n\t"
      "s_cmpk_lt_u32 s2, 144\n\t"
      "v_min_u32 v17, %4, v17\n\t"
      "global_load_dword v17, v17, s[4:5]\n\t"
      "s_cbranch_scc1 Lc_%=\n\t"
      "s_add_i32 s3, s2, 0xffffff70\n\t"
      "v_bfe_u32 v18, v0, 1, 3\n\t"
      "v_lshlrev_b32 v19, 1, v0\n\t"
      "s_mulk_i32 s3, 18432\n\t"
      "v_lshlrev_b32 v20, 4, v0\n\t"
      "v_mul_u32_u24 v18, 1152, v18\n\t"
      "v_bfi_b32 v19, 31, v20, v19\n\t"
      "v_add3_u32 v18, v18, v19, s3\n\t"
      "s_cmpk_lt_u32 s2, 240\n\t"
      "v_add_u32 v19, 9216, v18\n\t"
      "v_add_u32 v20, 294912, v18\n\t"
      "v_add_u32 v21, 304128, v18\n\t"
      "s_waitcnt lgkmcnt(0)\n\t"
      "global_load_dword v1, v1, s[36:37] sc1\n\t"
      "global_load_dwordx4 v[2:5], v18, s[8:9]\n\t"
      "global_load_dwordx4 v[6:9], v19, s[8:9]\n\t"
      "s_cbranch_scc1 Le_%=\n\t"
      "global_load_dwordx4 v[10:13], v20, s[8:9]\n\t"
      "global_load_dwordx4 v[14:17], v21, s[8:9]\n\t"
      "s_branch Le_%=\n"
      "Lw_%=:\n\t"
      "v_lshlrev_b32 v7, 2, v0\n\t"
      "s_waitcnt lgkmcnt(0)\n\t"
      "global_load_dword v1, v1, s[36:37] sc1\n\t"
      "global_load_dword v2, v7, s[22:23] offset:-2048\n\t"
      "global_load_dword v3, v7, s[22:23] offset:-1792\n\t"
      "global_load_dword v4, v7, s[22:23] offset:-1536\n\t"
      "global_load_dword v5, v7, s[22:23] offset:-1280\n\t"
      "global_load_dword v6, v7, s[22:23] offset:-1152\n\t"
      "s_branch Le_%=\n"
      "Lc_%=:\n\t"
      "v_readfirstlane_b32 s3, v0\n\t"
      "v_bfe_u32 v6, v0, 6, 2\n\t"
      "s_lshl_b32 s4, s2, 2\n\t"
      "v_bfe_u32 v5, v0, 0, 8\n\t"
      "s_lshl_b32 s5, s2, 10\n\t"
      "s_cmpk_ge_u32 s3, 512\n\t"
      "v_add_lshl_u32 v16, v6, s4, 1\n\t"
      "s_cbranch_scc1 Lw_%=\n\t"
      "v_lshl_add_u32 v5, v5, 2, s5\n\t"
      "s_lshr_b32 s4, s3, 8\n\t"
      "s_bfe_u32 s5, s3, 0x20006\n\t"
      "v_lshl_add_u32 v3, s4, 1, v5\n\t"
      "v_and_b32 v7, 63, v0\n\t"
      "s_mulk_i32 s5, %5\n\t"
      "s_mulk_i32 s4, 3584\n\t"
      "v_mov_b64 v[10:11], 0\n\t"
      "s_add_i32 s5, s5, s4\n\t"
      "v_mov_b64 v[12:13], 0\n\t"
      "s_add_i32 s5, s5, %6\n\t"
      "v_lshlrev_b32 v9, 4, v7\n\t"
      "s_lshl_b32 s3, s3, 8\n\t"
      "v_add_u32 v8, s5, v9\n\t"
      "v_add_u32 v9, %7, v9\n\t"
      "ds_write_b128 v8, v[10:13]\n\t"
      "ds_write_b128 v8, v[10:13] offset:32\n\t"
      "ds_write_b128 v9, v[10:13]\n\t"
      "v_lshl_or_b32 v18, v7, 2, s3\n\t"
      "v_lshrrev_b32 v14, 5, v7\n\t"
      "v_and_b32 v15, 31, v7\n\t"
      "v_bitop3_b32 v14, v14, v7, 15 bitop3:0x28\n\t"
      "v_lshl_or_b32 v15, v15, 8, s3\n\t"
      "s_waitcnt lgkmcnt(0)\n\t"
      "global_load_ushort v6, v16, s[18:19]\n\t"
      "global_load_ushort v2, v3, s[10:11]\n\t"
      "global_load_ushort v3, v3, s[12:13]\n\t"
      "global_load_dword v4, v5, s[14:15]\n\t"
      "global_load_dword v5, v5, s[16:17]\n\t"
      "global_load_ushort v8, v16, s[20:21]\n\t"
      "global_load_dword v1, v1, s[36:37] sc1\n\t"
      "v_xor_b32 v19, 16, v18\n\t"
      "v_xor_b32 v20, 32, v18\n\t"
      "v_xor_b32 v21, 48, v18\n\t"
      "v_xor_b32 v22, 64, v18\n\t"
      "v_xor_b32 v26, 0x80, v18\n\t"
      "v_xor_b32 v23, 16, v22\n\t"
      "v_xor_b32 v24, 32, v22\n\t"
      "v_xor_b32 v25, 48, v22\n\t"
      "v_xor_b32 v30, 64, v26\n\t"
      "v_xor_b32 v27, 16, v26\n\t"
      "v_xor_b32 v28, 32, v26\n\t"
      "v_xor_b32 v29, 48, v26\n\t"
      "v_xor_b32 v31, 16, v30\n\t"
      "v_xor_b32 v32, 32, v30\n\t"
      "v_xor_b32 v33, 48, v30\n\t"
      "v_lshl_or_b32 v34, v14, 4, v15\n\t"
      "v_xor_b32 v35, 32, v34\n\t"
      "v_xor_b32 v36, 64, v34\n\t"
      "v_xor_b32 v38, 0x80, v34\n\t"
      "v_xor_b32 v37, 32, v36\n\t"
      "v_xor_b32 v39, 32, v38\n\t"
      "v_xor_b32 v40, 64, v38\n\t"
      "v_xor_b32 v41, 32, v40\n"
      "Le_%=:"
      : "=&{s[8:9]}"(kx), "=&{s[36:37]}"(kw), "=&{s[24:31]}"(kb_), "=&{s[34:35]}"(t0_)
      : "n"(CODE_BYTES), "n"(USTR), "n"(L_UIMG), "n"(L_MISC), "{s[0:1]}"(__builtin_amdgcn_kernarg_segment_ptr()), "{s2}"(bid), "{v0}"(threadIdx.x)
      : "s3", "s4", "s5", "s10", "s11", "s12", "s13", "s14", "s15", "s16", "s17", "s18", "s19", "s20", "s21", "s22", "s23", "v1", "v2", "v3", "v4",
        "v5", "v6", "v7", "v8", "v9", "v10", "v11", "v12", "v13", "v14", "v15", "v16", "v17", "v18", "v19", "v20", "v21", "v22", "v23", "v24", "v25",
        "v26", "v27", "v28", "v29", "v30", "v31", "v32", "v33", "v34", "v35", "v36", "v37", "v38", "v39", "v40", "v41", "scc", "memory");
  v16u k16;
  v8u k8;
  asm volatile("" : "={v1}"(epoch), "={v[2:5]}"(ga), "={v[6:9]}"(gb), "={v[10:13]}"(gc), "={v[14:17]}"(gd), "={v[18:33]}"(k16), "={v[34:41]}"(k8)::"memory");
  SCHED();
  const int tid = threadIdx.x, lane = tid & 63;
  int wave = __builtin_amdgcn_readfirstlane(tid >> 6);
  const int xcd = bid & 7;
  int loc = bid >> 3;
  STAMP_INIT();
  STAMP(11);
  if (EXPW == 1) { DRAIN(); STAMP(13); SCHED(); }
  const int cl = lane & 15, g = lane >> 4;
  const int cg = bid;
  const int fj = xcd * 9 + (loc - 18);
  const int rb = bid;
  const int side = (bid << 11) & 8192;
  const int ybase = (cg < 128 ? WS_Y : WS_Y2 - 128 * GSTR) + cg * GSTR;
  const int tile = bid - NCONV;
  struct { const GAS char* x; GAS char* ws; } p = {(const GAS char*)kx, (GAS char*)kw};
#define FIRST_WAIT(n) asm volatile("s_waitcnt vmcnt(%5) ; first loads" : "+{v1}"(epoch), "+{v[2:5]}"(ga), "+{v[6:9]}"(gb), "+{v[10:13]}"(gc), "+{v[14:17]}"(gd) : "n"(n) : "memory")
  const rsrc_t rs = __builtin_amdgcn_make_buffer_rsrc((void*)p.ws, 0, WS_END, 0x00020000);
  const v4u sent4 = {SENT, SENT, SENT, SENT};
  const rsrc_t ro = __builtin_amdgcn_make_buffer_rsrc((void*)kb_[3], 0, SEQ * DM * 2, 0x00020000);
  if (loc < 18) {
    if (wave < 4) {
      const unsigned win = (unsigned)(0x0380007cfe0001ffull >> (16 * wave)) & 0xffffu;
      const unsigned bits = (win << cl) >> (8 * g);
      v4u f;
#pragma unroll
      for (int i = 0; i < 4; i++) f[i] = __umul24(__umul24((bits >> (2 * i)) & 3u, 0x8001u) & 0x10001u, 0x3c00u);
      *(LAS v4u*)(L_FILT + wave * 1024 + lane * 16) = f;
    }
    BAR();
    if (wave == 8) asm volatile("s_mov_b64 exec, 0\n\ts_branch Lk_conv\nLk_back:\n\ts_mov_b64 exec, -1");
  }
  asm volatile("" : "+s"(loc), "+s"(wave));

  if (loc < 18) {
    int ysrc = (wave < 8 ? WS_Y : WS_Y2 - 128 * GSTR) + 16 * wave * GSTR + side + (loc * 4 + (rb & 3)) * 128;
    unsigned ylag = T_Y3 - ((0x000f2b46u >> (2 * (loc & 12))) & 255u);
    asm volatile("" : "+s"(ysrc), "+s"(ylag));
    unsigned l_x, l_x2, l_sw;
    v4u cw[2];
    f32x2 xf[8];
    auto ln2_operands = [&](int lane) {
      const int cl = lane & 15, g = lane >> 4;
      l_x = L_XROW + wave * 2048 + cl * 128 + (g ^ (cl & 7)) * 16;
      l_x2 = l_x ^ 64;
      const v4u gx[2] = {*(const LAS v4u*)l_x, *(const LAS v4u*)l_x2};
      const unsigned l_w = (lane & 3) == 1 ? L_WR + wave * 128 + g * 16 : L_MISC + 128;
      cw[0] = *(const LAS v4u*)l_w;
      cw[1] = *(const LAS v4u*)(l_w + 64);
      l_sw = (cl >> 2) == g ? L_RED + wave * 256 + cl * 16 : 7 * WSCR + lane * 16;
#pragma unroll
      for (int i = 0; i < 8; i++) {
        xf[i] = f32x2{blo(gx[i >> 2][i & 3]), bhi(gx[i >> 2][i & 3])};
        PIN(xf[i]);
      }
      PIN(cw[0]); PIN(cw[1]); PIN(l_x); PIN(l_x2); PIN(l_sw);
      SCHED();
    };
    if (wave < 8) {
      const int ch = wave & 3;
      char* const img = smem + ch * WSCR;
      char* const scr = smem + wave * WSCR;
      volatile LAS unsigned* const pf = (volatile LAS unsigned*)(smem + L_MISC);
      if (wave < 4) __builtin_amdgcn_s_setprio(2); else __builtin_amdgcn_s_setprio(1);
      STAMP(12);
      unsigned epoch;
      v4u ga;
      v4u gb;
      v16u wb_;
      v8u rb_;
      asm volatile("s_waitcnt vmcnt(0) ; first loads" : "={v1}"(epoch), "={v[2:5]}"(ga), "={v[6:9]}"(gb), "={v[18:33]}"(wb_), "={v[34:41]}"(rb_)::"memory");
      const int lane = gb.y;
      STAMP(5);
      unsigned tag = __builtin_amdgcn_readfirstlane(epoch) + 1u;
      asm volatile("" : "+s"(tag));
      const float dt = rbf(exp_c(blo(gb.x)));
      const float ar = -rbf(exp_c(blo(ga.x))) * dt * 1.44269504f;
      const float aie = blo(ga.y) * dt;
      const float ph = aie * 0.159154936f;
      const float pl = fmaf(aie, 0.159154936f, -ph) + aie * 7.13048051e-9f;
      auto put2 = [&](int row, unsigned x, unsigned y) {
        *(LAS unsigned*)(wb_[row & 15] + row * 256) = x;
        *(LAS unsigned*)(wb_[row & 15] + (row + 16) * 256) = y;
      };
      auto put1 = [&](int row, unsigned x) { *(LAS unsigned*)(wb_[row & 15] + row * 256) = x; };
      const cz e8 = mkz(cexp_p2(8.f, ar, ph, pl)), e64 = mkz(cexp_p2(64.f, ar, ph, pl));
      const cz e16 = csq(e8), e128 = csq(e64), e32 = csq(e16);
      {
        const f32x2 e24 = cmul(e16.v, e8), e192 = cmul(e128.v, e64);
        const unsigned ha[4] = {0x3c00u, pkh2(e8.v), pkh2(e16.v), pkh2(e24)};
        const unsigned hs[4] = {0u, pkh2(e8.s), pkh2(e16.s), pkh(e24.y, e24.x)};
        const unsigned hb[8] = {0x3c00u, pkh2(e32.v), pkh2(e64.v), pkh2(cmul(e64.v, e32)), pkh2(e128.v), pkh2(cmul(e128.v, e32)), pkh2(e192), pkh2(cmul(e192, e32))};
#pragma unroll
        for (int k = 0; k < 4; k++) put2(4 * k, hb[k], hb[k + 4]);
#pragma unroll
        for (int i = 1; i < 4; i++) {
          unsigned r[8];
          hmul2(r[1], r[5], hb[1], hb[5], ha[i], hs[i]);
          put2(4 + i, r[1], r[5]);
          hmul2(r[2], r[6], hb[2], hb[6], ha[i], hs[i]);
          put2(8 + i, r[2], r[6]);
          hmul2(r[3], r[7], hb[3], hb[7], ha[i], hs[i]);
          put2(12 + i, r[3], r[7]);
        }
        unsigned r1, r2, r3, r4;
        const unsigned s128 = pkh2(e128.s);
        hmul2(r1, r2, ha[1], ha[2], hb[4], s128);
        hmul2(r3, r4, ha[3], ha[3], hb[4], s128);
        put2(1, ha[1], r1);
        put2(2, ha[2], r2);
        put2(3, ha[3], r3);
      }
      MEMBAR();
      f16x8 fa[8], fb[8];
#pragma unroll
      for (int k = 0; k < 8; k++) fb[k] = *(const LAS f16x8*)(rb_[k]);
      SCHED();
      if (wave >= 4) STAMP(11);
      const float cre = (wave < 4 ? blo(ga.z) : bhi(ga.z)) * dt, cim = (wave < 4 ? blo(ga.w) : bhi(ga.w)) * dt;
      const float dsk = blo(gb.z);
      const float cmx = fmaxf(fmaxf(fmaxf(fabsf(blo(ga.z)), fabsf(bhi(ga.z))), fmaxf(fabsf(blo(ga.w)), fabsf(bhi(ga.w)))) * dt, fabsf(dsk) * 0.0078125f);
      const unsigned ce = min(max(wave_max_u(__float_as_uint(cmx)) >> 23, 40u), 250u);
      const float csc = __uint_as_float((261u - ce) << 23);
      f32x16 acc;
      unsigned have;
      auto partial = [&]() {
        MEMBAR();
#pragma unroll
        for (int k = 0; k < 4; k++) {
          const f32x4 o = *(const f32x4*)(img + k * 1024 + lane * 16);
#pragma unroll
          for (int i = 0; i < 4; i++) acc[4 * k + i] = o[i];
        }
      };
#define KSTEP(k) SCHED(); acc = __builtin_amdgcn_mfma_f32_32x32x16_f16(fa[k], fb[k], acc, 0, 0, 0); SCHED()
      {
        const cz e1 = mkz(cexp_p2(1.f, ar, ph, pl)), e512 = mkz(cexp_p2(512.f, ar, ph, pl));
        const cz e2 = csq(e1), e1024 = csq(e512), e256 = csq(e128), e4 = csq(e2);
        const f32x2 w0 = {cre * csc, -cim * csc};
        f32x2 A[3], B[3], C[3], D[3], E[3];
        unsigned a[3], b[3], c[3], d[3], e[3];
        cmulc3(A, a, w0, e8, w0, e1, w0, e256);
        put2(32 + 0, a[0], pkh2(w0));
        put1(32 + 11, a[1]);
        put1(32 + 15, a[2]);
        cmulc3(B, b, w0, e2, A[0], e256, A[0], e512);
        put1(32 + 10, b[0]);
        put2(32 + 4, b[1], a[2]);
        cmulc3(C, c, w0, e4, A[0], e1024, A[1], e2);
        put1(32 + 9, c[2]);
        cmulc3(D, d, B[1], e512, B[1], e1024, B[0], e256);
        put2(32 + 5, b[1], d[0]);
        put2(32 + 8, c[0], d[1]);
        put1(32 + 14, d[2]);
        cmulc3(E, e, C[0], e256, A[1], e4, B[2], e1024);
        put1(32 + 13, e[0]);
        put2(32 + 3, e[1], c[1]);
        put2(32 + 7, e[0], e[2]);
        SCHED();
        if (wave >= 4) {
          have = pf[ch];
          partial();
        }
        SCHED();
        cmulc3(A, a, B[0], e4, C[0], e512, C[2], e4);
        put2(32 + 2, a[0], a[1]);
        put2(32 + 1, a[2], b[2]);
        cmulc3(B, b, D[0], e1024, D[2], e4, E[0], e512);
        put2(32 + 12, e[0], b[0]);
        put2(32 + 6, b[1], b[2]);
      }
      MEMBAR();
#pragma unroll
      for (int k = 0; k < 8; k++) fa[k] = *(const LAS f16x8*)(rb_[k] + 8192);
      SCHED();
      if (wave < 4) {
        acc = f32x16{};
        KSTEP(0);
        *(v4u*)(scr + L_KN + (lane + (lane < 7 ? 0 : 64) + (lane < 19 ? 0 : 64) + (lane < 32 ? 0 : 16) + (lane < 40 ? 0 : 16)) * 16) = v4u{0u, 0u, 0u, 0u};
        KSTEP(1);
        KSTEP(2);
        float d0 = lane == 0 ? dsk * csc * 0.5f : 0.f;
        unsigned pa = wave * WSCR + lane * 16, fl = lane == 0 ? L_MISC + wave * 4 : wave * WSCR + L_DUMMY + lane * 4, one = 1u;
        PIN(d0); PIN(pa); PIN(fl); PIN(one);
#pragma unroll
        for (int k = 3; k < 8; k++) acc = __builtin_amdgcn_mfma_f32_32x32x16_f16(fa[k], fb[k], acc, 0, 0, 0);
        SCHED();
        acc[8] += d0;
        STAMP(2);
#pragma unroll
        for (int k = 0; k < 3; k++) *(LAS f32x4*)(pa + k * 1024) = f32x4{acc[4 * k], acc[4 * k + 1], acc[4 * k + 2], acc[4 * k + 3]};
        *(LAS float*)(pa + 3072) = acc[12];
        MEMBAR();
        *(volatile LAS unsigned*)fl = one;
        __builtin_amdgcn_s_setprio(0);
        STAMP(11);
      }
      asm volatile("" : "+s"(wave));
      if (wave >= 4) {
        if (__builtin_amdgcn_readfirstlane(have) == 0u) {
#pragma unroll 1
          for (int sp = 0; sp < SPIN_CAP * 16 && pf[ch] == 0u; sp++) {}
          partial();
          PIN(acc);
        }
        v4u q[4];
        int ln = lane;
        PIN(ln);
        const int cp = ch * WSCR, c_ = ln & 15, g16 = ln & 48;
        unsigned a_kn, a_k4, a_k8, a_sp, sel_sp, op[7], top = 0xfffefffeu, kinv = (ce - 6u) << 23;
#define KASM(k, pre) asm volatile(pre "\n\tv_mfma_f32_32x32x16_f16 %0, %1, %2, %0" : "+v"(acc) : "v"(fa[k]), "v"(fb[k])); SCHED()
        KASM(0, "s_nop 1");
        a_kn = cp + L_KN + 16 * (70 - ln);
        a_k4 = cp + L_K4 + 4 * (95 - ln);
        a_k8 = cp + L_K8 + 2 * (95 - (ln & 32) - ln);
        PIN(a_kn); PIN(a_k4); PIN(a_k8);
        KASM(1, "");
        op[0] = cp + L_KN + 40 + ((c_ + 1) & 1) * KCOPY + g16 - ((2 * c_ + 2) & ~3);
        op[1] = op[0] + 1024;
        asm volatile("" : "+{v7}"(op[0]), "+{v8}"(op[1]));
        KASM(2, "");
        op[3] = L_UIMG + ch * USTR + g16 + 16 * c_;
        op[2] = op[3] + 16 * c_;
        op[4] = cp + L_K4 + 120 + g16 - 8 * c_;
        asm volatile("" : "+{v9}"(op[2]), "+{v10}"(op[3]), "+{v11}"(op[4]));
        KASM(3, "");
        op[5] = cp + L_K8 + 60 + g16 - 4 * c_;
        op[6] = cp + L_Y + ch * 32 + ln * 8;
        asm volatile("" : "+{v12}"(op[5]), "+{v13}"(op[6]), "+{v14}"(top), "+{v15}"(kinv));
        KASM(4, "");
        const unsigned dummy = cp + L_DUMMY + ln * 4;
        sel_sp = ln == 0 ? 0x0302u : ln < 7 ? 0x0504u : 0x0706u;
        PIN(sel_sp);
        KASM(5, "");
        a_sp = cp + L_KN + (ln == 0 ? 1134 : ln < 7 ? 112 : 48) - ((ln + 1) & ~1);
        PIN(a_sp);
        KASM(6, "");
        a_sp = ln < 11 ? a_sp + ((ln & 1) ? 0 : KCOPY + 2) : dummy;
        PIN(a_sp);
        KASM(7, "");
#pragma unroll
        for (int k = 0; k < 4; k++) q[k] = *(const v4u*)(smem + L_FILT + k * 1024 + lane * 16);
        SCHED();
        asm volatile("s_nop 11" : "+v"(acc));
        STAMP(2);
        const unsigned k8 = pkh(acc[11], acc[12]);
        *(LAS u16*)(a_k8 + 64) = (u16)k8;
        *(LAS u16*)a_k8 = (u16)(k8 >> 16);
        *(LAS unsigned*)a_k4 = pkh(acc[9], acc[10]);
        const v4u c0 = {pkh(acc[1], acc[2]), pkh(acc[3], acc[4]), pkh(acc[5], acc[6]), pkh(acc[7], acc[8])};
        *(LAS v4u*)a_kn = c0;
        *(LAS v4u*)(a_kn + KCOPY) = v4u{pkh(acc[0], acc[1]), pkh(acc[2], acc[3]), pkh(acc[4], acc[5]), pkh(acc[6], acc[7])};
        const unsigned kb = (unsigned)__builtin_amdgcn_readlane((int)pkh(acc[0], acc[9]), 63);
        *(LAS u16*)a_sp = (u16)__builtin_amdgcn_perm(kb, c0.w, sel_sp);
        STAMP(4);
        __builtin_amdgcn_s_setprio(M_PRIO);
        asm volatile("Lk_conv:\n\ts_cbranch_execz Lk_fetch\n" CONV_ASM_TEXT
                     : CONV_ASM_PRE(q)
                     : "{v14}"(top), "{v15}"(kinv), "{v7}"(op[0]), "{v8}"(op[1]), "{v9}"(op[2]), "{v10}"(op[3]), "{v11}"(op[4]), "{v12}"(op[5]),
                       "{v13}"(op[6])
                     : CONV_ASM_CLOBBERS);
        STAMP(6);
        __builtin_amdgcn_s_setprio(0);
      }
      asm volatile("" : "+s"(wave));
      if (wave < 4) {
        asm volatile("s_branch Lk_fetch\n\t.p2align 12\nLk_fetch:\n\ts_cbranch_execz Lk_ln2");
        int lane;
        asm volatile("v_mbcnt_lo_u32_b32 %0, -1, 0\n\tv_mbcnt_hi_u32_b32 %0, -1, %0" : "=v"(lane));
        const int g = lane >> 4;
        int usrc = (cg < 128 ? WS_U : WS_U2 - 128 * GSTR) + cg * GSTR + wave * 1024 + lane * 16;
        char* const udst = smem + L_UIMG + (lane & 3) * USTR + 520 + (lane >> 2) * 16 + wave * 256;
        PIN(usrc);
        __builtin_amdgcn_s_sleep(SLEEP_U);
        __builtin_amdgcn_s_setprio(H_PRIO);
        fetch_u<3>(rs, usrc, udst, {0, 4, 8}, tag);
        STAMP(4);
        BAR();
        unsigned yb = ((lane >> 2) & 3) * (WSCR + 32) + L_Y + (lane & 1) * 128 + (wave & 1) * 64 + g * 16 + (lane & 2) * 4 + (wave >> 1) * 512;
        int yo = ybase + ((lane & 32) << 8) + wave * 512 + (lane & 31) * 16;
        PIN(yb); PIN(yo);
        __builtin_amdgcn_s_sleep(SLEEP_U7);
        fetch_u<1>(rs, usrc, udst, {12}, tag);
        STAMP(6);
#pragma unroll
        for (int m = 0; m < 4; m++) {
          BAR();
          if (m == 0) yo ^= *(const LAS int*)(L_MISC + 72);
          const v2u r0 = ldtr((const LAS char*)(yb + m * 1024)), r1 = ldtr((const LAS char*)(yb + m * 1024 + 256));
          __builtin_amdgcn_raw_buffer_store_b128(v4u{r0.x, r0.y, r1.x, r1.y}, rs, yo, m * 2048, SC1);
        }
        __builtin_amdgcn_s_setprio(0);
        if (EXPW == 2) { STAMP(12); DRAIN(); STAMP(13); }
      }
    }
    asm volatile("" : "+s"(wave));
    if (wave == 8) {
      unsigned pw[PF_N], epoch, wb;
      v4u wa;
      __builtin_amdgcn_s_setprio(3);
      asm volatile("s_waitcnt vmcnt(0) ; first loads" : "={v1}"(epoch), "={v[2:5]}"(wa), "={v6}"(wb)::"memory");
      int lane;
      asm volatile("v_mbcnt_lo_u32_b32 %0, -1, 0\n\tv_mbcnt_hi_u32_b32 %0, -1, %0" : "=v"(lane));
      {
        int d = 0;
        unsigned c0 = (unsigned)__builtin_amdgcn_s_memtime();
        int ya = ybase + (bid & 8) * 1024 + lane * 16;
#pragma unroll 1
        for (int h = 0; h < 2; h++) {
#pragma unroll
          for (int i = 0; i < 8; i++) __builtin_amdgcn_raw_buffer_store_b128(sent4, rs, ya + (i & 3) * 1024, (i & 4) * 1024, SC1);
          ya ^= 8192;
          if (loc >= 16) {
            DRAIN();
            const unsigned c1 = (unsigned)__builtin_amdgcn_s_memtime();
            d = (int)min(c1 - c0, 2000u) - d;
            c0 = c1;
          }
        }
        st2(v2u{epoch + 1u, (unsigned)(((loc ^ (bid >> 2)) & 1) ? d : -d)}, rs, loc >= 16 ? WS_VOTE + (cg - 128) * 64 : OOB);
      }
      st4(sent4, rs, (loc < 16 && lane < 8) ? WS_RREC + (lane & 4) * 2048 + rb * 64 + (lane & 3) * 16 : OOB);
      st4(sent4, rs, cg < 48 ? WS_XSEL + (cg & 15) * 8192 + (cg >> 4) * 1024 + lane * 16 : OOB);
      SCHED();
      auto warm = [&]() {
        const GAS char* wb = (const GAS char*)(loc < 9 ? kb_[0] : kb_[1]) + xcd * 1152;
        const unsigned s0 = (3u - (unsigned)((u64)wb >> 7)) & 7u, ro_ = ((loc < 9 ? loc : loc - 9) * 64 + lane) * 9216;
        pw[0] = *(const GAS unsigned*)(wb + (ro_ + s0 * 128));
        pw[1] = *(const GAS unsigned*)(wb + (ro_ + (s0 ? s0 : 8u) * 128));
        SCHED();
      };
      if (loc < 16) warm();
      {
#pragma unroll
        for (int k = 0; k < 4; k++) *(unsigned*)(smem + L_WR + (lane + 64 * k) * 4) = wa[k];
        *(unsigned*)(smem + L_WR + (224 + lane) * 4) = wb;
        *(LAS unsigned*)(L_MISC + 128 + (lane & 31) * 4) = 0x3f803f80u;
        float ws_ = lane >= 32 ? blo(wb) + bhi(wb) : 0.f;
#pragma unroll
        for (int k = 0; k < 4; k++) ws_ += blo(wa[k]) + bhi(wa[k]);
        ws_ = wave_sum(ws_);
        if (lane == 0) *(float*)(smem + L_MISC + 64) = ws_;
      }
      SCHED();
      asm volatile("s_waitcnt vmcnt(%0)" ::"n"(PF_N) : "memory");
      STAMP(4);
      __builtin_amdgcn_s_setprio(S_PRIO);
      BAR();
      STAMP(5);
      const unsigned t1 = now();
      st2(v2u{epoch + 1u, t1}, rs, lane == 0 ? WS_INIT + cg * 64 : OOB);
      v2u vote;
#pragma unroll 1
      for (int sp = 0; sp < SPIN_CAP; sp++) {
        MEMBAR();
        vote = ld2(rs, WS_VOTE + (lane & 15) * 64);
        if (__all(vote.x == epoch + 1u)) break;
      }
      const unsigned flip = wave_sum((float)(int)vote.y) > 0.f;
      *(LAS unsigned*)(L_MISC + 72) = flip << 13;
      if (loc < 16) {
        const GAS char* xs = p.x + (unsigned)((16 * rb + (lane >> 3)) * 1152 + ((lane & 7) ^ (lane >> 3)) * 16);
#pragma unroll 1
        for (int i = 0; i < 9; i++) {
          __builtin_amdgcn_global_load_lds((const GAS unsigned*)(xs + i * 128), (LAS unsigned*)(L_XROW + i * 2048), 16, 0, 0);
          __builtin_amdgcn_global_load_lds((const GAS unsigned*)(xs + i * 128 + 9216), (LAS unsigned*)(L_XROW + i * 2048 + 1024), 16, 0, 0);
        }
      }
      v2u fl[3];
      unsigned span;
      auto look = [&]() {
        MEMBAR();
#pragma unroll
        for (int k = 0; k < 3; k++) fl[k] = ld2(rs, WS_INIT + min(lane + 64 * k, NCONV - 1) * 64);
      };
      auto seen = [&]() {
        if (!__all(fl[0].x == epoch + 1u && fl[1].x == epoch + 1u && fl[2].x == epoch + 1u)) return false;
        const unsigned t = t1 - 4096u;
        const unsigned last = t + span + min(wave_max_u(max(max(fl[0].y - t, fl[1].y - t), fl[2].y - t)), 4096u + 300u);
        *(volatile LAS unsigned*)(L_MISC + 76) = last | 1u;
        if (loc == 16) st2(v2u{epoch + 1u, (last & ~1u) | flip}, rs, WS_TIME + xcd * 128);
        return true;
      };
      if (loc >= 16) warm();
      wait_until(t1, T_LOOK);
      look();
      STAMP(6);
      BAR();
      BAR();
      span = (now() - t1) * 85u >> 6;
      DRAIN();
      bool ok = seen();
      BAR();
      BAR();
#pragma unroll 1
      for (int sp = 0; sp < SPIN_CAP && !ok; sp++) {
        RETRY(7);
        look();
        ok = seen();
      }
      __builtin_amdgcn_s_waitcnt(0x0f70);
      if (bid == 136) st1(epoch + 1u, rs, WS_EPOCH);
#pragma unroll
      for (int i = 0; i < PF_N; i++) asm volatile("" ::"v"(pw[i]));
      __builtin_amdgcn_s_setprio(0);
      SCHED();
      asm volatile("s_branch Lk_ln2\n\t.p2align 12\nLk_ln2:\n\ts_cbranch_execz Lk_back");
    }
    asm volatile("; first loads (62)");
    SCHED();
    STAMP(7);
    if (loc >= 16) { STAMP_FLUSH(); return; }
    int lane;
    asm volatile("v_mbcnt_lo_u32_b32 %0, -1, 0\n\tv_mbcnt_hi_u32_b32 %0, -1, %0" : "=v"(lane));
    const int cl = lane & 15, g = lane >> 4, tid = wave * 64 + lane;
    v2u fg = *(volatile LAS v2u*)(L_MISC + 72);
    int o0 = ysrc + g * 2 * GSTR + (cl & 7) * 16 + (cl & 8);
    PIN(o0);
    SCHED();
    if (RARE(__builtin_amdgcn_readfirstlane(fg.y) == 0u)) {
#pragma unroll 1
      for (int sp = 0; sp < SPIN_CAP * 64; sp++) {
        fg = *(volatile LAS v2u*)(L_MISC + 72);
        if (__builtin_amdgcn_readfirstlane(fg.y)) break;
      }
    }
    o0 ^= fg.x;
    PIN(o0);
    const unsigned last = __builtin_amdgcn_readfirstlane(fg.y);
    wait_until(last, ylag);
    v2u y[4];
    auto gather = [&]() {
      MEMBAR();
#pragma unroll
      for (int i = 0; i < 4; i++) y[i] = __builtin_amdgcn_raw_buffer_load_b64(rs, o0, ((i & 1) + (i >> 1) * 8) * GSTR, SC1);
    };
    gather();
    SCHED();
    ln2_operands(lane);
    constexpr int L_TAB = L_RED + 2304;
    const int flip = __builtin_amdgcn_readfirstlane(fg.x);
    int near = side ^ flip, far = near ^ 8192;
    v4u rx = {(unsigned)(kw + WS_XSEL), (unsigned)((kw + WS_XSEL) >> 32) & 0xffffu, 8 * 16384, 0x00020000};
    asm volatile("" : "+s"(rx), "+s"(far), "+s"(near));
    unsigned l_sr, l_h, l_t, krow, hoff, half, a_pub, a_cnt;
    int a_key;
    float wsum;
    auto addresses = [&]() {
      l_h = L_XROW + wave * 2048 + (lane & 31) * 4;
      l_t = L_TAB + (lane >> 5) * 16;
      half = (lane >> 1) & 16;
      a_pub = wave * 128 + (lane & 31) * 4;
      a_cnt = L_MISC + 32;
      PIN(l_h); PIN(l_t); PIN(half); PIN(a_pub); PIN(a_cnt);
    };
    if (wave == 1) {
      addresses();
      l_sr = L_RED + cl * 16;
      krow = SEQ - 1 - (16 * rb + cl);
      hoff = cl * 128 + (cl & 7) * 16;
      a_key = lane < 32 ? WS_RREC + g * 8192 + (16 * rb + cl) * 4 : OOB;
      wsum = *(const float*)(smem + L_MISC + 64);
      PIN(l_sr); PIN(krow); PIN(hoff); PIN(a_key); PIN(wsum);
    }
    asm volatile("" : "+s"(wave));
    const bool odd = lane & 1, upper = lane & 2;
    SCHED();
#pragma unroll 1
    for (int sp = SPIN_CAP;; sp--) {
      v4u hv[2];
      f32x4 s = {0.f, 0.f, 0.f, 0.f}, q = {0.f, 0.f, 0.f, 0.f};
#pragma unroll
      for (int i = 0; i < 4; i++) {
        const f32x2 h0 = f32x2{blo(y[i].x), bhi(y[i].x)} + xf[2 * i], h1 = f32x2{blo(y[i].y), bhi(y[i].y)} + xf[2 * i + 1];
        hv[i >> 1][2 * (i & 1)] = pkbf(h0.x, h0.y);
        hv[i >> 1][2 * (i & 1) + 1] = pkbf(h1.x, h1.y);
        if (i & 1) {
          q = mfma_bf(hv[i >> 1], hv[i >> 1], q);
          s = mfma_bf(cw[i >> 1], hv[i >> 1], s);
          SCHED();
        }
      }
      const int more = __builtin_amdgcn_ballot_w64(!valid8(v4u{y[0].x, y[0].y, y[1].x, y[1].y}, v4u{y[2].x, y[2].y, y[3].x, y[3].y})) ? sp : 0;
      SCHED();
      const float qa = odd ? q[1] : q[0], qb = odd ? q[3] : q[2];
      s[2] = upper ? qb : qa;
      *(LAS f32x4*)l_sw = s;
      SCHED();
      *(LAS v4u*)l_x = hv[0];
      *(LAS v4u*)l_x2 = hv[1];
      if (more == 0) break;
      RETRY(2);
      gather();
    }
    STAMP(8);
    SCHED();
    asm volatile("s_waitcnt lgkmcnt(2)\n\ts_barrier" ::: "memory");
    SCHED();
    if (wave == 1) {
      float m, s;
      const f32x4 t = sum9((const LAS char*)l_sr);
      ln_round(t[0], t[2], m, s);
      const float rinv = __builtin_amdgcn_rcpf(s);
      const unsigned b = pkbf(0.f, fmaf(-m, wsum, t[1]) * rinv);
      const unsigned okey = (b ^ (((int)b >> 31 & 0x7fff0000u) | 0x80000000u)) | krow;
      st1(okey, rs, a_key);
      SCHED();
      STAMP(9);
      int v = __builtin_amdgcn_update_dpp((int)okey, (int)okey, 0x124, 0x2, 0xf, false);
      v = __builtin_amdgcn_update_dpp(v, (int)okey, 0x128, 0x4, 0xf, false);
      v = __builtin_amdgcn_update_dpp(v, (int)okey, 0x12c, 0x8, 0xf, false);
      unsigned c = ((unsigned)v > okey) + (dppu<0x121, 0xf>(v) > okey) + (dppu<0x122, 0xf>(v) > okey) + (dppu<0x123, 0xf>(v) > okey);
      const v2u c1 = __builtin_amdgcn_permlane32_swap(c, c, false, false);
      c = c1.x + c1.y;
      const v2u c2 = __builtin_amdgcn_permlane16_swap(c, c, false, false);
      *(LAS v4u*)(L_TAB + (c2.x + c2.y) * 16) = v4u{__float_as_uint(m), okey, __float_as_uint(rinv), hoff};
    }
    asm volatile("" : "+s"(wave));
    if (wave != 1) {
      addresses();
      a_key = WS_RREC + near + (tid - (wave ? 64 : 0)) * 16;
      PIN(a_key);
      wait_until(last, T_KEY);
    }
    BAR();
    v4u key;
    if (wave != 1) key = ld4(rs, a_key);
    STAMP(1);
    SCHED();
    unsigned nsel = 0;
#pragma unroll 1
    for (int rd = 0; rd < 4; rd++) {
      const v4u e = *(const LAS v4u*)l_t;
      unsigned hd = *(const LAS unsigned*)(l_h ^ e.w);
      const unsigned ka = __builtin_amdgcn_readlane(e.y, 0), kb = __builtin_amdgcn_readlane(e.y, 32);
      unsigned wd = nosent(ln_apply(hd, __uint_as_float(e.x), __uint_as_float(e.z)));
      hd = nosent(hd);
      PIN(wd); PIN(hd);
      SCHED();
      if (wave != 1) {
        POLL(3, valid4(key), key = ld4(rs, a_key));
        STAMP(10);
        u64 b[8];
#pragma unroll
        for (int i = 0; i < 4; i++) {
          b[2 * i] = __ballot(key[i] > ka);
          b[2 * i + 1] = __ballot(key[i] > kb);
        }
        asm volatile("" : "+s"(b[0]), "+s"(b[1]), "+s"(b[2]), "+s"(b[3]), "+s"(b[4]), "+s"(b[5]), "+s"(b[6]), "+s"(b[7]));
        const unsigned ca = (__popcll(b[0]) + __popcll(b[2])) + (__popcll(b[4]) + __popcll(b[6]));
        const unsigned cb = (__popcll(b[1]) + __popcll(b[3])) + (__popcll(b[5]) + __popcll(b[7]));
        asm volatile("s_mov_b64 exec, 1\n\tds_add_u32 %1, %0\n\ts_mov_b64 exec, -1" ::"v"(ca | (cb << 16)), "v"(a_cnt) : "memory");
      }
      unsigned cs, rank, po;
      asm volatile(
          "s_waitcnt lgkmcnt(0)\n\ts_barrier\n\tds_read_b32 %0, %3\n\ts_waitcnt lgkmcnt(0)\n\t"
          "v_bfe_u32 %1, %0, %4, 16\n\tv_lshl_add_u32 %2, %1, 14, %5\n\t"
          "buffer_store_dword %6, %2, %7, %8 offen sc1\n\tbuffer_store_dword %6, %2, %7, %9 offen sc1"
          : "=&v"(cs), "=&v"(rank), "=&v"(po)
          : "v"(a_cnt), "v"(half), "v"(a_pub), "v"(wd), "s"(rx), "s"(far), "s"(near)
          : "memory");
      const int ph = rank < 8u ? WS_XSEL + po + (((rank & 4) << 11) ^ flip) : OOB - 4096;
      st1(hd, rs, ph + 1152);
      st1(e.y, rs, a_pub == 0 ? ph + 2304 : OOB);
      const unsigned cu = __builtin_amdgcn_readfirstlane(cs);
      nsel += ((cu & 0xffffu) < 8u) + ((cu >> 16) < 8u);
      if ((cu >> 16) >= 8u) break;
      l_t += 32;
      a_cnt += 4;
    }
    STAMP(3);
#pragma unroll
    for (int i = 0; i < 2; i++) {
      const unsigned k = 8 * i + (lane >> 3);
      const v4u e = *(const LAS v4u*)(L_TAB + k * 16);
      const v4u hh = *(const LAS v4u*)((L_XROW + wave * 2048 + (lane & 7) * 16) ^ e.w);
      v4u o;
#pragma unroll
      for (int c = 0; c < 4; c++) o[c] = addbf(hh[c], ln_apply(hh[c], __uint_as_float(e.x), __uint_as_float(e.z)));
      st4(o, ro, k < nsel ? OOB : (16 * rb + (e.w >> 7)) * 1152 + wave * 128 + (lane & 7) * 16);
    }
    STAMP_FLUSH();
    return;
  }

  asm volatile("; conv set-up dropped" ::"{v[18:33]}"(k16), "{v[34:41]}"(k8));
  const bool ffn = loc < 27;
  LAS char* xw = (LAS char*)smem + wave * 4608 + (cl >> 1) * 288 + g * 32 + (cl & 1) * 16;
  LAS char* xr_ = (LAS char*)smem + wave * 4608 + ((lane >> 2) & 3) * 288 + (lane & 1) * 128 + g * 16 + (lane & 2) * 4;
  LAS char* rw = (LAS char*)smem + L_RED + wave * 256 + cl * 8;
  int uo = (wave < 8 ? WS_U : WS_U2 - 128 * GSTR) + (16 * wave + (lane >> 3)) * GSTR + tile * 128 + (lane & 7) * 16;
  v4u ones = {0x3f803f80u, 0x3f803f80u, 0x3f803f80u, 0x3f803f80u};
  LAS char* sa = (LAS char*)smem + L_MISC + (lane & 4) * 8;
  unsigned keep = 0xfff8fff8u;
  PIN(xw); PIN(xr_); PIN(rw); PIN(uo); PIN(ones); PIN(sa); PIN(keep);
  SCHED();
  FIRST_WAIT(2);
  const unsigned tag = __builtin_amdgcn_readfirstlane(epoch) + 1u;
  v4u tb = tag_bits(tag);
  asm volatile("" : "+s"(tb));
  const bool fin = wave == 1 || (wave == 2 && loc >= 30);
  SCHED();
  FIRST_WAIT(0);
  {
    const f32x4 z4 = {0.f, 0.f, 0.f, 0.f};
    auto sums = [&](v4u a, v4u b, int tt) {
      const f32x4 s0 = mfma_bf(ones, a, z4), q0 = mfma_bf(a, a, z4), s1 = mfma_bf(ones, b, z4), q1 = mfma_bf(b, b, z4);
      *(LAS v4u*)(xw + tt * 2304) = a;
      *(LAS v4u*)(xw + tt * 2304 + 128) = b;
      if ((cl >> 2) == g) {
        *(LAS f32x2*)(rw + tt * 2304) = f32x2{s0[0], diag(q0, lane)};
        *(LAS f32x2*)(rw + tt * 2304 + 128) = f32x2{s1[0], diag(q1, lane)};
      }
    };
    sums(ga, gb, 0);
    if (loc >= 30) sums(gc, gd, 1);
    STAMP(10);
    BAR();
    STAMP(11);
    if (fin) {
      __builtin_amdgcn_s_setprio(3);
      const f32x4 t = sum9((const LAS char*)smem + L_RED + (wave - 1) * 2304 + cl * 16);
      float m, sd;
      ln_round(t[0] + t[2], t[1] + t[3], m, sd);
      const float rinv = __builtin_amdgcn_rcpf(sd);
      *(float*)(smem + L_MISC + (wave - 1) * 128 + (lane & 31) * 4) = (lane & 16) ? -m * rinv : rinv;
      BAR();
    }
#pragma unroll 1
    for (int tt = 0; tt < 2; tt++) {
      f32x2 xt[2][4];
#pragma unroll
      for (int i = 0; i < 4; i++) {
        const v2u v = ldtr(xr_ + tt * 2304 + (i >> 1) * 64 + (i & 1) * 1152);
        xt[i >> 1][2 * (i & 1)] = f32x2{blo(v.x), bhi(v.x)};
        xt[i >> 1][2 * (i & 1) + 1] = f32x2{blo(v.y), bhi(v.y)};
      }
#pragma unroll
      for (int i = 0; i < 8; i++) PIN(xt[i >> 2][i & 3]);
      if (tt == 0 && !fin) BAR();
      if (tt == 0) STAMP(12);
      const LAS f32x4* st = (const LAS f32x4*)(sa + tt * 128);
      const f32x4 r0 = st[0], r1 = st[1], c0 = st[4], c1 = st[5];
      const f32x2 rr[4] = {r0.xy, r0.zw, r1.xy, r1.zw}, cc[4] = {c0.xy, c0.zw, c1.xy, c1.zw};
#pragma unroll
      for (int i = 0; i < 2; i++) {
        v4u o;
#pragma unroll
        for (int k = 0; k < 4; k++) o[k] = (pkh2(__builtin_elementwise_fma(xt[i][k], rr[k], cc[k])) & keep) | tb[k];
        __builtin_amdgcn_raw_buffer_store_b128(o, rs, uo, i * 8 * GSTR, SC1);
      }
      if (loc < 30) break;
      uo += 16 * 128;
    }
    __builtin_amdgcn_s_setprio(0);
    BAR();
    STAMP(1);
    if (!ffn) {
      const GAS char* dn = (const GAS char*)kb_[2] + (size_t)xcd * 5184 * 128;
      unsigned pf[2];
#pragma unroll
      for (int i = 0; i < 2; i++) pf[i] = *(const GAS unsigned*)(dn + (unsigned)min((i * 5 + loc - 27) * NT + tid, 5183) * 128u);
#pragma unroll
      for (int i = 0; i < 2; i++) asm volatile("" ::"v"(pf[i]));
      STAMP_FLUSH();
      return;
    }
  }
  v8u FW[2][4];
  v4u FD[2][4];
  v4u Dn[8];
  constexpr int F_GU = 0, F_TIME = 1280, F_ALL = 2048, F_RED = 16384;
    v4u W[2][8];
    const GAS char* wp = (const GAS char*)((lane & 8) ? kb_[1] : kb_[0]) + (unsigned)((64 * wave + 4 * g) * (FF * 2) + 128 * fj + 16 * (lane & 7));
    const rsrc_t rd = __builtin_amdgcn_make_buffer_rsrc((void*)kb_[2], 0, DM * FF * 2, 0x00020000);
    const int wdo = (64 * fj + 8 * g + 4 * (lane & 8)) * (DM * 2) + 128 * wave + 16 * (lane & 7);
    st4(sent4, rs, part_at(fj * 9216 + tid * 16));
    st4(sent4, rs, part_at(fj * 9216 + tid * 16) + 8192);
    SCHED();
#pragma unroll
    for (int j = 0; j < 16; j++) W[j >> 3][j & 7] = *(const GAS v4u*)(wp + (16 * (j >> 2) + (j & 3)) * (FF * 2));
    SCHED();
    STAMP(11);
  if (wave == 2) *(unsigned*)(smem + F_TIME + lane * 4) = 0u;
  asm volatile("s_waitcnt vmcnt(16)" ::: "memory");
  if (wave >= 4) BAR();
  asm volatile("" : "+s"(wave));
  const int tk = fj / 9, cb = fj - tk * 9;
  const int fcol = cb * 64 + 8 * (cl & 7) + 4 * (cl >> 3) + (g >> 1) + 2 * (g & 1);
  int a_x = slot_at(lane >> 3) + side + wave * 128 + (lane & 7) * 16, a_f = slot_at(tk) + side + (fcol >> 1) * 4;
  int a_po[2], a_pi[2];
#pragma unroll
  for (int i = 0; i < 2; i++) {
    a_po[i] = part_at(fj * 9216 + ((2 * (g >> 1) + i) * DM + 64 * wave + 4 * cl) * 4) + ((g & 1) << 13);
    a_pi[i] = part_at(((tid >> 4) + 36 * i) * 9216 + ((tk & 3) * DM + cb * 64 + cl * 4) * 4) + side;
    PIN(a_po[i]); PIN(a_pi[i]);
  }
  unsigned l_xw = F_ALL + wave * 1152 + (lane >> 3) * 144 + (lane & 7) * 16, l_xr = F_ALL + wave * 1152 + (lane & 7) * 144 + g * 16;
  unsigned l_rw = F_RED + wave * 4096 + lane * 16;
  int sidx = (lane & 8) ? 0xeeee : 0x4444;
  unsigned l_rg = F_RED + (tid >> 5) * 256 + (tid & 31) * 4, l_ru = l_rg + 128;
  unsigned l_gw = F_GU + (((tid >> 5) & 1) * 4 + (tid & 3)) * 144 + (((tid >> 2) & 7) * 8 + (tid >> 7) + ((tid >> 6) & 1) * 4) * 2;
  unsigned l_gr = F_GU + ((cl & 4) + ((cl + (cl >> 2 & 2)) & 3)) * 144 + g * 16, l_s = F_ALL + (wave == 8 ? 0 : wave + 1) * 256 + lane * 4;
  int a_k = slot_at(tk) + side + 2304;
  PIN(a_k); PIN(a_x); PIN(a_f); PIN(l_xw); PIN(l_xr); PIN(sidx); PIN(l_rw); PIN(l_rg); PIN(l_ru); PIN(l_gw); PIN(l_gr); PIN(l_s);
#pragma unroll
    for (int ks = 0; ks < 2; ks++)
#pragma unroll
      for (int c = 0; c < 4; c++) {
#pragma unroll
        for (int i = 0; i < 8; i++)
          FW[ks][c][i] = __builtin_amdgcn_perm(W[ks][i | 1][(c >> 1) + 2 * (i & 1)], W[ks][i & 6][(c >> 1) + 2 * (i & 1)], (c & 1) ? 0x07060302u : 0x05040100u);
        PIN(FW[ks][c]);
      }
  SCHED();
  if (wave < 4) BAR();
  STAMP(12);
#pragma unroll
  for (int j = 0; j < 8; j++) Dn[j] = __builtin_amdgcn_raw_buffer_load_b128(rd, wdo, j * (DM * 2), 0);
  SCHED();
  st1(tag, rs, tid == 0 ? WS_INIT + (NCONV + fj) * 64 : OOB);
  SCHED();
  STAMP(2);
  if (wave == 0) {
    v2u ts;
#pragma unroll 1
    for (int sp = 0; sp < SPIN_CAP; sp++) {
      MEMBAR();
      ts = ld2(rs, WS_TIME + xcd * 128);
      if (__builtin_amdgcn_readfirstlane(ts.x) == tag) break;
      __builtin_amdgcn_s_sleep(4);
    }
    *(volatile LAS v2u*)F_TIME = v2u{ts.y, 1u};
  }
  {
    v2u te;
#pragma unroll 1
    for (int sp = 0; sp < SPIN_CAP * 16; sp++) {
      te = *(volatile LAS v2u*)F_TIME;
      if (__builtin_amdgcn_readfirstlane(te.y)) break;
      __builtin_amdgcn_s_sleep(2);
    }
    const int flip = (te.x & 1u) << 13;
    a_x ^= flip; a_f ^= flip; a_k ^= flip; a_po[0] ^= flip; a_po[1] ^= flip; a_pi[0] ^= flip; a_pi[1] ^= flip;
    PIN(a_x); PIN(a_f); PIN(a_k); PIN(a_po[0]); PIN(a_po[1]); PIN(a_pi[0]); PIN(a_pi[1]);
    wait_until(__builtin_amdgcn_readfirstlane(te.x), T_SEL);
  }
  STAMP(10);
  unsigned w2 = 0u, h2 = 0u, fkey = 0u, f0 = 0u, f1 = 0u;
  auto flook = [&]() {
    MEMBAR();
    f0 = ld1(rs, WS_INIT + (NCONV + lane) * 64);
    f1 = ld1(rs, WS_INIT + (NCONV + 64 + (lane & 7)) * 64);
    w2 = ld1(rs, a_f);
    h2 = ld1(rs, a_f + 1152);
    fkey = ld1(rs, a_k);
  };
  {
    MEMBAR();
    v4u xv = ld4(rs, a_x);
    SCHED();
#pragma unroll
    for (int j = 0; j < 8; j++)
#pragma unroll
      for (int i = 0; i < 2; i++) {
        const int lo = (int)Dn[j][i], hi = (int)Dn[j][2 + i];
        Dn[j][i] = (unsigned)__builtin_amdgcn_update_dpp(lo, hi, 0x128, 0xf, 0xc, false);
        Dn[j][2 + i] = (unsigned)__builtin_amdgcn_update_dpp(hi, lo, 0x128, 0xf, 0x3, false);
      }
#pragma unroll
    for (int ks = 0; ks < 2; ks++)
#pragma unroll
      for (int c = 0; c < 4; c++) {
#pragma unroll
        for (int i = 0; i < 4; i++)
          FD[ks][c][i] = __builtin_amdgcn_perm(Dn[2 * i + 1][2 * ks + (c >> 1)], Dn[2 * i][2 * ks + (c >> 1)], (c & 1) ? 0x07060302u : 0x05040100u);
        PIN(FD[ks][c]);
      }
    f32x4 a[4];
#pragma unroll
    for (int c = 0; c < 4; c++) { a[c] = f32x4{0.f, 0.f, 0.f, 0.f}; PIN(a[c]); }
    SCHED();
    STAMP(3);
    POLL(4, valid4(xv), xv = ld4(rs, a_x));
    STAMP(6);
    *(LAS v4u*)l_xw = xv;
    MEMBAR();
    const v4u x0 = *(const LAS v4u*)l_xr, x1 = *(const LAS v4u*)(l_xr + 64);
    if (wave == 8) flook();
#pragma unroll
    for (int c = 0; c < 6; c++) {
      SCHED();
      if (c < 4) {
        a[c] = smfmac_bf(x0, FW[0][c], a[c], sidx);
        a[c] = smfmac_bf(x1, FW[1][c], a[c], sidx);
      }
      SCHED();
      if (c > 1) *(LAS f32x4*)(l_rw + (c - 2) * 1024) = a[c - 2];
    }
  }
  BAR();
  STAMP(4);
  float fh = 0.f, fw = 0.f, sgm = 0.f;
  int oo = OOB;
  if (wave < 8) {
    f32x2 gv[4], uv[4];
#pragma unroll
    for (int w = 0; w < 4; w++) {
      gv[w] = f32x2{*(const LAS float*)(l_rg + w * 8192), *(const LAS float*)(l_rg + w * 8192 + 4096)};
      uv[w] = f32x2{*(const LAS float*)(l_ru + w * 8192), *(const LAS float*)(l_ru + w * 8192 + 4096)};
    }
    const float g8 = *(const LAS float*)(l_rg + 32768), u8 = *(const LAS float*)(l_ru + 32768);
    SCHED();
    f32x2 g2 = (gv[0] + gv[1]) + (gv[2] + gv[3]), u2 = (uv[0] + uv[1]) + (uv[2] + uv[3]);
    PIN(g2); PIN(u2);
    float gs = g2.x + g2.y;
    PIN(gs);
    gs += g8;
    PIN(gs);
    const float us = (u2.x + u2.y) + u8;
    const unsigned gu = pkbf(gs, us);
    const float gp = blo(gu);
    const float gq = rbf(gp * __builtin_amdgcn_rcpf(1.0f + __expf(-gp)));
    *(LAS u16*)l_gw = (u16)pkbf(gq * bhi(gu), 0.f);
  } else {
#pragma unroll 1
    for (int sp = 0; sp < SPIN_CAP && !__all(valid4(v4u{w2, h2, fkey, 0u}) && f0 == tag && f1 == tag); sp++) { RETRY(6); flook(); }
    fw = (fcol & 1) ? bhi(w2) : blo(w2);
    fh = (fcol & 1) ? bhi(h2) : blo(h2);
    const unsigned kb = fkey & 0xffff0000u;
    const float rv = __uint_as_float((kb & 0x80000000u) ? (kb & 0x7fffffffu) : (~kb & 0xffff0000u));
    sgm = rbf(__builtin_amdgcn_rcpf(1.0f + __expf(-rv)));
    oo = (SEQ - 1 - (int)(fkey & 0x7ffu)) * 1152 + fcol * 2;
  }
  PIN(fw); PIN(fh); PIN(sgm); PIN(oo);
  BAR();
  STAMP(5);
  {
    const v4u a0 = *(const LAS v4u*)l_gr, a1 = *(const LAS v4u*)(l_gr + 64);
    f32x4 o[4];
#pragma unroll
    for (int c = 0; c < 4; c++) {
      o[c] = f32x4{0.f, 0.f, 0.f, 0.f};
      o[c] = mfma_bf(a0, FD[0][c], o[c]);
      o[c] = mfma_bf(a1, FD[1][c], o[c]);
    }
#pragma unroll
    for (int i = 0; i < 2; i++) {
      v4u v;
#pragma unroll
      for (int c = 0; c < 4; c++) v[c] = nosent(__float_as_uint(o[c][i]));
      st4(v, rs, a_po[i]);
    }
  }
  STAMP(7);
  wait_until(now(), T_PART);
  {
    v4u a = ld4(rs, a_pi[0]), b = ld4(rs, a_pi[1]);
    POLL(5, valid8(a, b), (a = ld4(rs, a_pi[0]), b = ld4(rs, a_pi[1])));
    const v4u t = __builtin_bit_cast(v4u, __builtin_bit_cast(f32x4, a) + __builtin_bit_cast(f32x4, b));
    const v2u p0 = __builtin_amdgcn_permlane32_swap(t.x, t.y, false, false), p1 = __builtin_amdgcn_permlane32_swap(t.z, t.w, false, false);
    const v2u p2 = __builtin_amdgcn_permlane16_swap(__float_as_uint(__uint_as_float(p0.x) + __uint_as_float(p0.y)),
                                                    __float_as_uint(__uint_as_float(p1.x) + __uint_as_float(p1.y)), false, false);
    *(LAS float*)l_s = __uint_as_float(p2.x) + __uint_as_float(p2.y);
  }
  STAMP(8);
  BAR();
  if (wave == 8) {
    float t[9];
#pragma unroll
    for (int i = 0; i < 9; i++) t[i] = *(const LAS float*)(l_s + i * 256);
    SCHED();
    const float y = rbf((((t[0] + t[1]) + (t[2] + t[3])) + ((t[4] + t[5]) + (t[6] + t[7]))) + t[8]);
    __builtin_amdgcn_raw_buffer_store_b16((u16)pkbf(fh + rbf(fw + rbf(y * sgm)), 0.f), ro, oo, 0, SC1);
  }
  STAMP(9);
  STAMP_FLUSH();
  asm volatile("s_endpgm\n\t.p2align 12\n\t.skip (4096 - (Lk_conv - Lk_gen)) & 4095\nLk_gen:" ::: "memory");
}

}

extern "C" void kernel_launch(void* const* d_in, const int* in_sizes, int n_in, void* d_out, int out_size,
                              void* d_ws, size_t ws_size, hipStream_t stream) {
  (void)hipFuncSetAttribute((const void*)k_main, hipFuncAttributeMaxDynamicSharedMemorySize, LDS_BYTES);
  Params p;
  p.x = (const u16*)d_in[0];
  p.lar = (const u16*)d_in[1];
  p.aim = (const u16*)d_in[2];
  p.crl = (const u16*)d_in[3];
  p.cim = (const u16*)d_in[4];
  p.ldt = (const u16*)d_in[5];
  p.dskip = (const u16*)d_in[6];
  p.wr = (const u16*)d_in[8];
  p.wg = (const u16*)d_in[9];
  p.wu = (const u16*)d_in[10];
  p.wd = (const u16*)d_in[11];
  p.out = (u16*)d_out;
  const size_t skip = (size_t)(-(uintptr_t)d_ws) & ((2u << 20) - 1);
  p.ws = (char*)d_ws + (skip + WS_END <= ws_size ? skip : 0);
  k_main<<<256, NT, LDS_BYTES, stream>>>(p);
}
```

```cpp
#include <hip/hip_runtime.h>
#include <stdint.h>
#pragma clang diagnostic ignored "-Wint-to-pointer-cast"

namespace {

constexpr int SEQ = 2048, DM = 576, NM = 128, FF = 4608;
constexpr int NT = 576;
constexpr int NCONV = 144, NFFN = 72;
constexpr unsigned SENT = 0xFFFFFFFFu;
constexpr int SC1 = 16;
constexpr int SPIN_CAP = 1 << 16;
#define T_Y3 -10
#define T_KEY 85
#define T_SEL 165
#define CODE_BYTES 23936
#define H_PRIO 3
#ifndef M_PRIO
#define M_PRIO 1
#endif
#define S_PRIO 3
#define PF_N 2
#define SLEEP_U7 14
#define EXPW 0
#define T_LOOK 90
#define P_SLEEP 0
#define D_DELAY 0
#define W_DELAY 0
#define ADDTID 1
#define X2_DELAY 4
#define HV_LATE 1
#define Y_EARLY 10
#define KP_H 2
#define S0_PRIO 2
#define T_U 210
#define L1_EARLY 5
#define T_LATE 0
#define T_PART 28

typedef unsigned short u16;
typedef unsigned v4u __attribute__((ext_vector_type(4)));
typedef unsigned v2u __attribute__((ext_vector_type(2)));
typedef float f32x2 __attribute__((ext_vector_type(2)));
typedef float f32x4 __attribute__((ext_vector_type(4)));
typedef float f32x16 __attribute__((ext_vector_type(16)));
typedef _Float16 f16x2 __attribute__((ext_vector_type(2)));
typedef _Float16 f16x8 __attribute__((ext_vector_type(8)));
typedef __bf16 bf16x2 __attribute__((ext_vector_type(2)));
typedef __bf16 bf16x8 __attribute__((ext_vector_type(8)));
typedef __bf16 bf16x16 __attribute__((ext_vector_type(16)));
typedef unsigned v8u __attribute__((ext_vector_type(8)));
typedef __amdgpu_buffer_rsrc_t rsrc_t;

constexpr int WS_EPOCH = 0;
constexpr int WS_TIME = 2048;
constexpr int WS_INIT = 4096;
constexpr int WS_VOTE = 24576;
constexpr int WS_RREC = 32768;
constexpr int WS_XSEL = 65536;
constexpr int GSTR = 16384;
constexpr int WS_U = 2 << 20;
constexpr int WS_U2 = 196608;
constexpr int WS_Y = 4 << 20;
constexpr int WS_Y2 = WS_U2 + 16 * GSTR;
constexpr int WS_PART = WS_Y2 + 16 * GSTR;
constexpr int WS_END = 6 << 20;
static_assert(WS_PART + 2 * NFFN * 9216 + 16384 <= WS_U, "ws map");
__device__ __forceinline__ int part_at(int v) { return WS_PART + v + (v & ~8191); }
__device__ __forceinline__ int slot_at(int k) { return WS_XSEL + k * 16384; }
constexpr int OOB = 0x7ffffff0;

constexpr int WSCR = 16384;
constexpr int L_TBL = 3 * WSCR + 7936, TROW = 272, TSTR = 32 * TROW;
constexpr int L_KN = 4096;
constexpr int KCOPY = 1216;
constexpr int L_K4 = L_KN + 2 * KCOPY;
constexpr int L_K8 = L_K4 + 512;
constexpr int L_Y = 8192;
constexpr int L_DUMMY = 7680;
constexpr int L_XROW = 4 * WSCR;
constexpr int L_UIMG = 8 * WSCR;
constexpr int USTR = 4640;
constexpr int L_FILT = L_UIMG + 4 * USTR;
constexpr int L_MISC = L_FILT + 4096;
constexpr int L_WR = L_MISC + 256;
constexpr int L_RED = L_WR + 1152;
constexpr int LDS_BYTES = L_RED + 2 * 2304;
static_assert(L_KN + 224 * 16 <= L_DUMMY && L_TBL + 8 * TSTR <= L_UIMG && L_TBL + 31 * TROW < 65536 && 7 * TSTR < 65536, "lds map");
static_assert(LDS_BYTES <= 160 * 1024, "lds size");

#define MEMBAR() asm volatile("" ::: "memory")
#define BAR() do { __builtin_amdgcn_sched_barrier(0); asm volatile("s_waitcnt lgkmcnt(0)\n\ts_barrier" ::: "memory"); __builtin_amdgcn_sched_barrier(0); } while (0)
#define LAS __attribute__((address_space(3)))
#define DRAIN() asm volatile("s_waitcnt vmcnt(0)" ::: "memory")
#define LGKM0() asm volatile("s_waitcnt lgkmcnt(0)" ::: "memory")
#define PIN(x) asm volatile("" : "+v"(x))
#define RARE(c) __builtin_expect(!!(c), 0)
#define PC_(k)
#define POLL(k, ok, load) if (!__all(ok)) { RETRY(k); int sp_ = SPIN_CAP; _Pragma("unroll 1") do { if (P_SLEEP) __builtin_amdgcn_s_sleep(P_SLEEP); MEMBAR(); load; PC_(k); sp_ = __all(ok) ? 0 : sp_ - 1; } while (sp_); }
#define SCHED() __builtin_amdgcn_sched_barrier(0)

#define STAMP(i) do {} while (0)
#define STAMP_INIT() do {} while (0)
#define T0_ASM "s_memrealtime s[34:35]\n\t"
#define STAMP_FLUSH() do {} while (0)

#define KSTAMP(i) STAMP(i)
#define RETRY(k) do {} while (0)

__device__ __forceinline__ float blo(unsigned v) { return __uint_as_float(v << 16); }
__device__ __forceinline__ float bhi(unsigned v) { return __uint_as_float(v & 0xffff0000u); }
__device__ __forceinline__ unsigned pkbf(float a, float b) {
  const f32x2 f = {a, b};
  return __builtin_bit_cast(unsigned, __builtin_convertvector(f, bf16x2));
}
__device__ __forceinline__ float rbf(float f) { return __uint_as_float(pkbf(0.f, f)); }
__device__ __forceinline__ unsigned pkh(float a, float b) {
  const f32x2 f = {a, b};
  return __builtin_bit_cast(unsigned, __builtin_convertvector(f, f16x2));
}
__device__ __forceinline__ unsigned nosent(unsigned v) { return min(v, 0xFFFFFFFEu); }
__device__ __forceinline__ bool valid4(v4u v) { return max(max(v.x, v.y), max(v.z, v.w)) != SENT; }

__device__ __forceinline__ bool valid8(v4u a, v4u b) { return max(max(max(max(a.x, a.y), a.z), max(max(a.w, b.x), b.y)), max(b.z, b.w)) != SENT; }

template <int CTRL, int RM> __device__ __forceinline__ float dppf(float v) {
  return __int_as_float(__builtin_amdgcn_update_dpp(0, __float_as_int(v), CTRL, RM, 0xf, false));
}
template <int CTRL, int RM> __device__ __forceinline__ unsigned dppu(unsigned v) {
  return (unsigned)__builtin_amdgcn_update_dpp(0, (int)v, CTRL, RM, 0xf, false);
}
__device__ __forceinline__ float wave_sum(float v) {
  v += dppf<0x111, 0xf>(v);
  v += dppf<0x112, 0xf>(v);
  v += dppf<0x114, 0xf>(v);
  v += dppf<0x118, 0xf>(v);
  v += dppf<0x142, 0xa>(v);
  v += dppf<0x143, 0xc>(v);
  return __int_as_float(__builtin_amdgcn_readlane(__float_as_int(v), 63));
}
__device__ __forceinline__ unsigned wave_max_u(unsigned v) {
  v = max(v, dppu<0x111, 0xf>(v));
  v = max(v, dppu<0x112, 0xf>(v));
  v = max(v, dppu<0x114, 0xf>(v));
  v = max(v, dppu<0x118, 0xf>(v));
  const unsigned a = (unsigned)__builtin_amdgcn_readlane((int)v, 15), b = (unsigned)__builtin_amdgcn_readlane((int)v, 31);
  const unsigned c = (unsigned)__builtin_amdgcn_readlane((int)v, 47), d = (unsigned)__builtin_amdgcn_readlane((int)v, 63);
  return max(max(a, b), max(c, d));
}

__device__ __forceinline__ v4u ld4(rsrc_t rs, int off) { return __builtin_amdgcn_raw_buffer_load_b128(rs, off, 0, SC1); }
__device__ __forceinline__ v2u ld2(rsrc_t rs, int off) { return __builtin_amdgcn_raw_buffer_load_b64(rs, off, 0, SC1); }
__device__ __forceinline__ unsigned ld1(rsrc_t rs, int off) { return __builtin_amdgcn_raw_buffer_load_b32(rs, off, 0, SC1); }
__device__ __forceinline__ v2u ldp2(rsrc_t rs, int off, int soff) { return __builtin_amdgcn_raw_buffer_load_b64(rs, off, soff, 0); }
__device__ __forceinline__ void st4(v4u v, rsrc_t rs, int off) { __builtin_amdgcn_raw_buffer_store_b128(v, rs, off, 0, SC1); }
__device__ __forceinline__ void st2(v2u v, rsrc_t rs, int off) { __builtin_amdgcn_raw_buffer_store_b64(v, rs, off, 0, SC1); }
__device__ __forceinline__ void st1(unsigned v, rsrc_t rs, int off) { __builtin_amdgcn_raw_buffer_store_b32(v, rs, off, 0, SC1); }

__device__ __forceinline__ f32x4 mfma_bf(v4u a, v4u b, f32x4 c) {
  return __builtin_amdgcn_mfma_f32_16x16x32_bf16(__builtin_bit_cast(bf16x8, a), __builtin_bit_cast(bf16x8, b), c, 0, 0, 0);
}
__device__ __forceinline__ f32x4 smfmac_bf(v4u a, v8u b, f32x4 c, int idx) {
  return __builtin_amdgcn_smfmac_f32_16x16x64_bf16(__builtin_bit_cast(bf16x8, a), __builtin_bit_cast(bf16x16, b), c, idx, 0, 0);
}
__device__ __forceinline__ f32x4 mfma_h(v4u a, v4u b, f32x4 c) {
  return __builtin_amdgcn_mfma_f32_16x16x32_f16(__builtin_bit_cast(f16x8, a), __builtin_bit_cast(f16x8, b), c, 0, 0, 0);
}

__device__ __forceinline__ float diag(f32x4 q, int lane) {
  const float qa = (lane & 1) ? q[1] : q[0], qb = (lane & 1) ? q[3] : q[2];
  return (lane & 2) ? qb : qa;
}
__device__ __forceinline__ f32x4 sum9(const LAS char* red) {
  f32x4 t[9];
#pragma unroll
  for (int w = 0; w < 9; w++) t[w] = *(const LAS f32x4*)(red + w * 256);
  SCHED();
  return (((t[0] + t[1]) + (t[2] + t[3])) + ((t[4] + t[5]) + (t[6] + t[7]))) + t[8];
}
__device__ __forceinline__ void ln_round(float sum, float sq, float& m, float& s) {
  const float mean = sum * (1.0f / DM);
  const float var = fmaxf(fmaf(-mean, mean, sq * (1.0f / DM)), 0.f);
  m = rbf(mean);
  s = rbf(__builtin_amdgcn_sqrtf(rbf(rbf(var) + 1e-5f)));
}
__device__ __forceinline__ unsigned ln_apply(unsigned xp, float m, float rinv) {
  const unsigned d = pkbf(blo(xp) - m, bhi(xp) - m);
  return pkbf(blo(d) * rinv, bhi(d) * rinv);
}
__device__ __forceinline__ unsigned addbf(unsigned a, unsigned b) { return pkbf(blo(a) + blo(b), bhi(a) + bhi(b)); }

__device__ __forceinline__ unsigned now() { return (unsigned)__builtin_amdgcn_s_memrealtime(); }
__device__ __forceinline__ void wait_until(unsigned t0, unsigned ticks) {
  unsigned n;
  asm volatile(
      "s_movk_i32 %0, 1024\n"
      "1:\n\t"
      "s_memrealtime vcc\n\t"
      "s_waitcnt lgkmcnt(0)\n\t"
      "s_sub_i32 vcc_lo, vcc_lo, %1\n\t"
      "s_cmp_gt_i32 vcc_lo, -16\n\t"
      "s_cbranch_scc1 2f\n\t"
      "s_sleep 2\n\t"
      "s_sub_u32 %0, %0, 4\n\t"
      "s_cbranch_scc0 1b\n\t"
      "s_branch 4f\n"
      "3:\n\t"
      "s_memrealtime vcc\n\t"
      "s_sub_u32 %0, %0, 1\n\t"
      "s_waitcnt lgkmcnt(0)\n\t"
      "s_cselect_b32 vcc_lo, %1, vcc_lo\n\t"
      "s_sub_i32 vcc_lo, vcc_lo, %1\n"
      "2:\n\t"
      "s_cmp_lt_i32 vcc_lo, 0\n\t"
      "s_cbranch_scc1 3b\n"
      "4:"
      : "=&s"(n)
      : "s"(t0 + ticks)
      : "vcc", "scc", "memory");
}

typedef short s16x4 __attribute__((ext_vector_type(4)));
__device__ __forceinline__ v2u ldtr(const LAS char* a) {
  return __builtin_bit_cast(v2u, __builtin_amdgcn_ds_read_tr16_b64_v4i16((LAS s16x4*)a));
}
__device__ __forceinline__ f32x4 widen(v2u unit, v2u v, f32x4 c) {
  return __builtin_amdgcn_mfma_f32_16x16x16bf16_1k(__builtin_bit_cast(s16x4, unit), __builtin_bit_cast(s16x4, v), c, 0, 0, 0);
}
__device__ __forceinline__ v4u tag_bits(unsigned tag) {
  v4u t;
#pragma unroll
  for (int k = 0; k < 4; k++) t[k] = ((tag >> (6 * k)) & 7u) | (((tag >> (6 * k + 3)) & 7u) << 16);
  return t;
}

template <int N, bool EARLY>
__device__ __forceinline__ void fetch_u(rsrc_t rs, int src, const unsigned (&dst)[N], int first, v4u tx) {
  v4u w[N];
  unsigned bad, mid = 0x40004u, pcnt_ = 0;
  asm volatile("" : "+s"(mid));
  auto put = [&](int i) {
    *(LAS v2u*)dst[i] = v2u{w[i].x, w[i].y};
    *(LAS v2u*)(dst[i] + 8) = v2u{w[i].z, w[i].w};
  };
  auto load = [&]() {
#pragma unroll
    for (int i = 0; i < N; i++) w[i] = __builtin_amdgcn_raw_buffer_load_b128(rs, src, (first + 4 * i) * 1024, SC1);
    bad = 0u;
#pragma unroll
    for (int i = 0; i < N; i++) {
      SCHED();
      w[i] ^= tx;
      if (EARLY) put(i);
      SCHED();
      bad |= __builtin_amdgcn_bitop3_b32(w[i].x, w[i].y, mid, 0x7e) | __builtin_amdgcn_bitop3_b32(w[i].z, w[i].w, mid, 0x7e);
    }
  };
  load();
  POLL(N == 1, (bad & 0x70007u) == 0u, load());
  if (!EARLY) put(0);
}

struct cz { f32x2 v, s; };
__device__ __forceinline__ cz mkz(f32x2 v) {
  cz z;
  z.v = v;
  asm("v_pk_mov_b32 %0, %1, %1 op_sel:[1,0]" : "=v"(z.s) : "v"(v));
  return z;
}
#define CMUL_TXT(r, a, b, bs) "v_pk_mul_f32 " r ", " a ", " bs " op_sel:[1,0] neg_lo:[0,1]\n\tv_pk_fma_f32 " r ", " a ", " b ", " r " op_sel_hi:[0,1,1]\n\t"
__device__ __forceinline__ f32x2 cmul(f32x2 a, cz b) {
  f32x2 r;
  asm(CMUL_TXT("%0", "%1", "%2", "%3") : "=&v"(r) : "v"(a), "v"(b.v), "v"(b.s));
  return r;
}
__device__ __forceinline__ cz csq(cz a) { return mkz(cmul(a.v, a)); }
#define MC_(r, a, bs) "v_pk_mul_f32 " r ", " a ", " bs " op_sel:[1,0]\n\t"
#define FC_(r, a, b) "v_pk_fma_f32 " r ", " a ", " b ", " r " op_sel_hi:[0,1,1] neg_hi:[0,1,0]\n\t"
__device__ __forceinline__ void cmulc3(f32x2 (&r)[3], unsigned (&h)[3], f32x2 a0, cz b0, f32x2 a1, cz b1, f32x2 a2, cz b2) {
  asm(MC_("%0", "%3", "%5") MC_("%1", "%6", "%8") MC_("%2", "%9", "%11") FC_("%0", "%3", "%4") FC_("%1", "%6", "%7") FC_("%2", "%9", "%10")
      : "=&v"(r[0]), "=&v"(r[1]), "=&v"(r[2])
      : "v"(a0), "v"(b0.v), "v"(b0.s), "v"(a1), "v"(b1.v), "v"(b1.s), "v"(a2), "v"(b2.v), "v"(b2.s));
#pragma unroll
  for (int i = 0; i < 3; i++) h[i] = pkh(r[i].x, r[i].y);
}
__device__ __forceinline__ void hmul2(unsigned& r, unsigned& q, unsigned a, unsigned c, unsigned b, unsigned bs) {
  asm("v_pk_mul_f16 %0, %2, %5 op_sel:[1,0] neg_lo:[0,1]\n\tv_pk_mul_f16 %1, %3, %5 op_sel:[1,0] neg_lo:[0,1]\n\t"
      "v_pk_fma_f16 %0, %2, %4, %0 op_sel_hi:[0,1,1]\n\tv_pk_fma_f16 %1, %3, %4, %1 op_sel_hi:[0,1,1]"
      : "=&v"(r), "=&v"(q) : "v"(a), "v"(c), "v"(b), "v"(bs));
}
__device__ __forceinline__ float exp_c(float v) {
  const float t = v * 1.44269502f;
  const float e = fmaf(v, 1.92596303e-8f, fmaf(v, 1.44269502f, -t));
  return __builtin_amdgcn_exp2f(t) * fmaf(e, 0.693147182f, 1.f);
}
__device__ __forceinline__ f32x2 cexp_p2(float p, float ch, float ph, float pl) {
  const float mag = __builtin_amdgcn_exp2f(p * ch);
  const float fr = fmaf(p, pl, __builtin_amdgcn_fractf(p * ph));
  return f32x2{mag * __builtin_amdgcn_cosf(fr), mag * __builtin_amdgcn_sinf(fr)};
}
__device__ __forceinline__ unsigned pkh2(f32x2 v) { return pkh(v.x, v.y); }

#define CONV_ASM_TEXT \
  "s_barrier\n" "ds_read_b128 v[32:35], %8 offset:480\n" \
  "ds_read_b128 v[36:39], %8 offset:992\n" "ds_read_b128 v[40:43], %8 offset:1504\n" \
  "ds_read2_b32 v[44:45], %11 offset0:48 offset1:49\n" "ds_read2_b32 v[46:47], %11 offset0:50 offset1:51\n" \
  "ds_read2_b32 v[48:49], %11 offset0:32 offset1:33\n" "s_waitcnt lgkmcnt(5)\n" \
  "v_mfma_f32_16x16x32_f16 v[132:135], v[32:35], v[16:19], 0\n" "s_nop 0\n" \
  "ds_read2_b32 v[50:51], %11 offset0:34 offset1:35\n" "ds_read2_b32 v[52:53], %11 offset0:16 offset1:17\n" \
  "v_mfma_f32_16x16x32_f16 v[128:131], v[32:35], v[20:23], 0\n" "s_waitcnt lgkmcnt(6)\n" \
  "ds_read2_b32 v[54:55], %11 offset0:18 offset1:19\n" "ds_read2_b32 v[56:57], %11 offset0:0 offset1:1\n" \
  "v_mfma_f32_16x16x32_f16 v[124:127], v[36:39], v[16:19], 0\n" "s_nop 0\n" \
  "ds_read2_b32 v[58:59], %11 offset0:2 offset1:3\n" "ds_read_b128 v[60:63], %8 offset:2016\n" \
  "v_mfma_f32_16x16x32_f16 v[120:123], v[36:39], v[20:23], 0\n" "s_waitcnt lgkmcnt(9)\n" \
  "v_cvt_pk_f16_f32 v116, v132, v128\n" "v_cvt_pk_f16_f32 v117, v133, v129\n" \
  "v_mfma_f32_16x16x32_f16 v[164:167], v[40:43], v[16:19], 0\n" "v_cvt_pk_f16_f32 v118, v134, v130\n" \
  "v_cvt_pk_f16_f32 v119, v135, v131\n" "v_mfma_f32_16x16x32_f16 v[160:163], v[40:43], v[20:23], 0\n" \
  "s_waitcnt lgkmcnt(7)\n" "ds_read_b128 v[64:67], %9 offset:480\n" \
  "v_cvt_pk_f16_f32 v112, v124, v120\n" "v_mfma_f32_16x16x32_f16 v[148:151], v[116:119], v[44:47], 0\n" \
  "s_waitcnt lgkmcnt(6)\n" "ds_read_b128 v[68:71], %9 offset:736\n" \
  "v_cvt_pk_f16_f32 v113, v125, v121\n" "v_mfma_f32_16x16x32_f16 v[144:147], v[116:119], v[48:51], 0\n" \
  "s_waitcnt lgkmcnt(5)\n" "v_cvt_pk_f16_f32 v114, v126, v122\n" \
  "v_cvt_pk_f16_f32 v115, v127, v123\n" "v_mfma_f32_16x16x32_f16 v[140:143], v[116:119], v[52:55], 0\n" \
  "s_waitcnt lgkmcnt(3)\n" "ds_read_b128 v[72:75], %9 offset:992\n" \
  "ds_read_b64 v[76:77], %10 offset:256\n" "v_mfma_f32_16x16x32_f16 v[136:139], v[116:119], v[56:59], 0\n" \
  "s_waitcnt lgkmcnt(4)\n" "ds_read_b64 v[78:79], %10 offset:264\n" \
  "v_cvt_pk_f16_f32 v116, v164, v160\n" "v_mfma_f32_16x16x32_f16 v[132:135], v[60:63], v[16:19], 0\n" \
  "v_cvt_pk_f16_f32 v117, v165, v161\n" "v_cvt_pk_f16_f32 v118, v166, v162\n" \
  "v_mfma_f32_16x16x32_f16 v[128:131], v[60:63], v[20:23], 0\n" "ds_read_b64 v[80:81], %10 offset:128\n" \
  "v_cvt_pk_f16_f32 v119, v167, v163\n" "v_mfma_f32_16x16x32_f16 v[144:147], v[112:115], v[44:47], v[144:147]\n" \
  "ds_read_b64 v[82:83], %10 offset:136\n" "v_mfma_f32_16x16x32_f16 v[140:143], v[112:115], v[48:51], v[140:143]\n" \
  "s_nop 0\n" "ds_read_b64 v[84:85], %10 offset:0\n" \
  "ds_read_b64 v[86:87], %10 offset:8\n" "v_mfma_f32_16x16x32_f16 v[136:139], v[112:115], v[52:55], v[136:139]\n" \
  "s_waitcnt lgkmcnt(7)\n" "ds_read_b128 v[88:91], %9 offset:1248\n" \
  "v_cvt_pk_f16_f32 v112, v132, v128\n" "v_mfma_f32_16x16x32_f16 v[124:127], v[64:67], v[24:27], 0\n" \
  "v_cvt_pk_f16_f32 v113, v133, v129\n" "v_cvt_pk_f16_f32 v114, v134, v130\n" \
  "v_mfma_f32_16x16x32_f16 v[120:123], v[64:67], v[28:31], 0\n" "ds_read_b64 v[92:93], %10 offset:320\n" \
  "v_cvt_pk_f16_f32 v115, v135, v131\n" "v_mfma_f32_16x16x32_f16 v[140:143], v[116:119], v[44:47], v[140:143]\n" \
  "ds_read_b64 v[94:95], %10 offset:328\n" "v_mfma_f32_16x16x32_f16 v[136:139], v[116:119], v[48:51], v[136:139]\n" \
  "s_nop 0\n" "ds_read_b64 v[96:97], %10 offset:192\n" \
  "ds_read_b64 v[98:99], %10 offset:200\n" "v_mfma_f32_16x16x32_f16 v[164:167], v[68:71], v[24:27], 0\n" \
  "s_nop 0\n" "ds_read_b64 v[100:101], %10 offset:64\n" \
  "ds_read_b64 v[102:103], %10 offset:72\n" "v_mfma_f32_16x16x32_f16 v[160:163], v[68:71], v[28:31], 0\n" \
  "v_cvt_pk_f16_f32 v116, v124, v120\n" "v_cvt_pk_f16_f32 v117, v125, v121\n" \
  "v_mfma_f32_16x16x32_f16 v[136:139], v[112:115], v[44:47], v[136:139]\n" "s_waitcnt lgkmcnt(7)\n" \
  "v_cvt_pk_f16_f32 v118, v126, v122\n" "v_cvt_pk_f16_f32 v119, v127, v123\n" \
  "v_mfma_f32_16x16x32_f16 v[132:135], v[72:75], v[24:27], 0\n" "ds_read_b128 v[104:107], %9 offset:1504\n" \
  "v_cvt_pk_f16_f32 v112, v164, v160\n" "v_mfma_f32_16x16x32_f16 v[128:131], v[72:75], v[28:31], 0\n" \
  "v_cvt_pk_f16_f32 v113, v165, v161\n" "v_cvt_pk_f16_f32 v114, v166, v162\n" \
  "v_mfma_f32_16x16x32_f16 v[156:159], v[116:119], v[76:79], 0\n" "ds_read_b128 v[108:111], %9 offset:1760\n" \
  "v_cvt_pk_f16_f32 v115, v167, v163\n" "v_mfma_f32_16x16x32_f16 v[152:155], v[116:119], v[80:83], 0\n" \
  "s_nop 0\n" "ds_read2_b32 v[32:33], %6 offset0:144 offset1:145\n" \
  "ds_read2_b32 v[34:35], %6 offset0:146 offset1:147\n" "v_mfma_f32_16x16x32_f16 v[148:151], v[116:119], v[84:87], v[148:151]\n" \
  "s_waitcnt lgkmcnt(6)\n" "v_cvt_pk_f16_f32 v116, v132, v128\n" \
  "v_cvt_pk_f16_f32 v117, v133, v129\n" "v_mfma_f32_16x16x32_f16 v[124:127], v[88:91], v[24:27], 0\n" \
  "v_cvt_pk_f16_f32 v118, v134, v130\n" "v_cvt_pk_f16_f32 v119, v135, v131\n" \
  "v_mfma_f32_16x16x32_f16 v[120:123], v[88:91], v[28:31], 0\n" "s_nop 0\n" \
  "ds_read_b128 v[36:39], %9 offset:2016\n" "ds_read2_b32 v[40:41], %6 offset0:16 offset1:17\n" \
  "v_mfma_f32_16x16x32_f16 v[156:159], v[112:115], v[92:95], v[156:159]\n" "ds_read2_b32 v[42:43], %6 offset0:18 offset1:19\n" \
  "v_mfma_f32_16x16x32_f16 v[152:155], v[112:115], v[96:99], v[152:155]\n" "s_waitcnt lgkmcnt(6)\n" \
  "ds_read2_b32 v[56:57], %6 offset0:48 offset1:49\n" "ds_read2_b32 v[58:59], %6 offset0:50 offset1:51\n" \
  "v_mfma_f32_16x16x32_f16 v[148:151], v[112:115], v[100:103], v[148:151]\n" "v_cvt_pk_f16_f32 v112, v124, v120\n" \
  "v_cvt_pk_f16_f32 v113, v125, v121\n" "v_mfma_f32_16x16x32_f16 v[164:167], v[104:107], v[24:27], 0\n" \
  "v_cvt_pk_f16_f32 v114, v126, v122\n" "v_cvt_pk_f16_f32 v115, v127, v123\n" \
  "v_mfma_f32_16x16x32_f16 v[160:163], v[104:107], v[28:31], 0\n" "s_nop 0\n" \
  "ds_read_b128 v[16:19], %9 offset:2272\n" "ds_read2_b32 v[60:61], %6 offset0:176 offset1:177\n" \
  "v_mfma_f32_16x16x32_f16 v[152:155], v[116:119], v[76:79], v[152:155]\n" "ds_read2_b32 v[62:63], %6 offset0:178 offset1:179\n" \
  "v_mfma_f32_16x16x32_f16 v[148:151], v[116:119], v[80:83], v[148:151]\n" "s_nop 0\n" \
  "ds_read2_b32 v[20:21], %6 offset0:32 offset1:33\n" "ds_read2_b32 v[22:23], %6 offset0:34 offset1:35\n" \
  "v_mfma_f32_16x16x32_f16 v[144:147], v[116:119], v[84:87], v[144:147]\n" "s_waitcnt lgkmcnt(8)\n" \
  "v_cvt_pk_f16_f32 v116, v164, v160\n" "v_cvt_pk_f16_f32 v117, v165, v161\n" \
  "v_mfma_f32_16x16x32_f16 v[132:135], v[108:111], v[24:27], 0\n" "v_cvt_pk_f16_f32 v118, v166, v162\n" \
  "v_cvt_pk_f16_f32 v119, v167, v163\n" "v_mfma_f32_16x16x32_f16 v[128:131], v[108:111], v[28:31], 0\n" \
  "s_nop 0\n" "ds_read_b128 v[52:55], %9 offset:2528\n" \
  "ds_read2_b32 v[64:65], %6 offset0:160 offset1:161\n" "v_mfma_f32_16x16x32_f16 v[152:155], v[112:115], v[92:95], v[152:155]\n" \
  "ds_read2_b32 v[66:67], %6 offset0:162 offset1:163\n" "v_mfma_f32_16x16x32_f16 v[148:151], v[112:115], v[96:99], v[148:151]\n" \
  "s_nop 0\n" "ds_read2_b32 v[48:49], %6 offset0:80 offset1:81\n" \
  "ds_read2_b32 v[50:51], %6 offset0:82 offset1:83\n" "v_mfma_f32_16x16x32_f16 v[144:147], v[112:115], v[100:103], v[144:147]\n" \
  "v_cvt_pk_f16_f32 v112, v132, v128\n" "v_cvt_pk_f16_f32 v113, v133, v129\n" \
  "v_mfma_f32_16x16x32_f16 v[124:127], v[36:39], v[24:27], 0\n" "v_cvt_pk_f16_f32 v114, v134, v130\n" \
  "v_cvt_pk_f16_f32 v115, v135, v131\n" "v_mfma_f32_16x16x32_f16 v[120:123], v[36:39], v[28:31], 0\n" \
  "s_nop 0\n" "ds_read_b128 v[68:71], %9 offset:2784\n" \
  "ds_read2_b32 v[44:45], %6 offset0:208 offset1:209\n" "v_mfma_f32_16x16x32_f16 v[148:151], v[116:119], v[76:79], v[148:151]\n" \
  "ds_read2_b32 v[46:47], %6 offset0:210 offset1:211\n" "v_mfma_f32_16x16x32_f16 v[144:147], v[116:119], v[80:83], v[144:147]\n" \
  "s_nop 0\n" "ds_read2_b32 v[72:73], %6 offset0:64 offset1:65\n" \
  "ds_read2_b32 v[74:75], %6 offset0:66 offset1:67\n" "v_mfma_f32_16x16x32_f16 v[140:143], v[116:119], v[84:87], v[140:143]\n" \
  "s_waitcnt lgkmcnt(8)\n" "v_cvt_pk_f16_f32 v116, v124, v120\n" \
  "v_cvt_pk_f16_f32 v117, v125, v121\n" "v_mfma_f32_16x16x32_f16 v[132:135], v[16:19], v[24:27], 0\n" \
  "v_cvt_pk_f16_f32 v118, v126, v122\n" "v_cvt_pk_f16_f32 v119, v127, v123\n" \
  "v_mfma_f32_16x16x32_f16 v[128:131], v[16:19], v[28:31], 0\n" "s_nop 0\n" \
  "ds_read_b128 v[88:91], %9 offset:3040\n" "ds_read2_b32 v[104:105], %6 offset0:192 offset1:193\n" \
  "v_mfma_f32_16x16x32_f16 v[148:151], v[112:115], v[92:95], v[148:151]\n" "ds_read2_b32 v[106:107], %6 offset0:194 offset1:195\n" \
  "v_mfma_f32_16x16x32_f16 v[144:147], v[112:115], v[96:99], v[144:147]\n" "s_nop 0\n" \
  "ds_read_b128 v[108:111], %8 offset:0\n" "ds_read2_b32 v[36:37], %6 offset0:224 offset1:225\n" \
  "v_mfma_f32_16x16x32_f16 v[140:143], v[112:115], v[100:103], v[140:143]\n" "v_cvt_pk_f16_f32 v112, v132, v128\n" \
  "v_cvt_pk_f16_f32 v113, v133, v129\n" "v_mfma_f32_16x16x32_f16 v[124:127], v[52:55], v[24:27], 0\n" \
  "v_cvt_pk_f16_f32 v114, v134, v130\n" "v_cvt_pk_f16_f32 v115, v135, v131\n" \
  "v_mfma_f32_16x16x32_f16 v[120:123], v[52:55], v[28:31], 0\n" "s_nop 0\n" \
  "ds_read2_b32 v[38:39], %6 offset0:226 offset1:227\n" "ds_read_b128 v[16:19], %9 offset:3296\n" \
  "v_mfma_f32_16x16x32_f16 v[144:147], v[116:119], v[76:79], v[144:147]\n" "ds_read2_b32 v[52:53], %6 offset0:96 offset1:97\n" \
  "v_mfma_f32_16x16x32_f16 v[140:143], v[116:119], v[80:83], v[140:143]\n" "ds_read2_b32 v[54:55], %6 offset0:98 offset1:99\n" \
  "v_mfma_f32_16x16x32_f16 v[136:139], v[116:119], v[84:87], v[136:139]\n" "s_waitcnt lgkmcnt(7)\n" \
  "v_cvt_pk_f16_f32 v116, v124, v120\n" "v_cvt_pk_f16_f32 v117, v125, v121\n" \
  "v_mfma_f32_16x16x32_f16 v[132:135], v[68:71], v[24:27], 0\n" "v_cvt_pk_f16_f32 v118, v126, v122\n" \
  "v_cvt_pk_f16_f32 v119, v127, v123\n" "v_mfma_f32_16x16x32_f16 v[128:131], v[68:71], v[28:31], 0\n" \
  "s_nop 0\n" "ds_read_b128 v[84:87], %8 offset:128\n" \
  "ds_read2_b32 v[68:69], %6 offset0:240 offset1:241\n" "v_mfma_f32_16x16x32_f16 v[144:147], v[112:115], v[92:95], v[144:147]\n" \
  "ds_read2_b32 v[70:71], %6 offset0:242 offset1:243\n" "v_mfma_f32_16x16x32_f16 v[140:143], v[112:115], v[96:99], v[140:143]\n" \
  "v_mfma_f32_16x16x32_f16 v[136:139], v[112:115], v[100:103], v[136:139]\n" "ds_read_b128 v[100:103], %8 offset:64\n" \
  "v_cvt_pk_f16_f32 v112, v132, v128\n" "v_mfma_f32_16x16x32_f16 v[124:127], v[88:91], v[24:27], 0\n" \
  "v_cvt_pk_f16_f32 v113, v133, v129\n" "v_cvt_pk_f16_f32 v114, v134, v130\n" \
  "v_mfma_f32_16x16x32_f16 v[120:123], v[88:91], v[28:31], 0\n" "s_waitcnt lgkmcnt(4)\n" \
  "ds_read_b128 v[88:91], %8 offset:256\n" "v_cvt_pk_f16_f32 v115, v135, v131\n" \
  "v_mfma_f32_16x16x32_f16 v[164:167], v[32:35], v[108:111], 0\n" "v_mfma_f32_16x16x32_f16 v[140:143], v[116:119], v[76:79], v[140:143]\n" \
  "v_mfma_f32_16x16x32_f16 v[136:139], v[116:119], v[80:83], v[136:139]\n" "s_nop 0\n" \
  "ds_read_b128 v[80:83], %8 offset:192\n" "v_cvt_pk_f16_f32 v116, v124, v120\n" \
  "v_mfma_f32_16x16x32_f16 v[132:135], v[16:19], v[24:27], 0\n" "v_cvt_pk_f16_f32 v117, v125, v121\n" \
  "v_cvt_pk_f16_f32 v118, v126, v122\n" "v_mfma_f32_16x16x32_f16 v[128:131], v[16:19], v[28:31], 0\n" \
  "ds_read_b128 v[24:27], %8 offset:320\n" "v_cvt_pk_f16_f32 v119, v127, v123\n" \
  "v_mfma_f32_16x16x32_f16 v[160:163], v[40:43], v[108:111], 0\n" "s_nop 0\n" \
  "ds_read2_b32 v[16:17], %6 offset0:112 offset1:113\n" "ds_read2_b32 v[18:19], %6 offset0:114 offset1:115\n" \
  "v_mfma_f32_16x16x32_f16 v[140:143], v[112:115], v[92:95], v[140:143]\n" "ds_read_b128 v[28:31], %8 offset:384\n" \
  "v_mfma_f32_16x16x32_f16 v[136:139], v[112:115], v[96:99], v[136:139]\n" "s_waitcnt lgkmcnt(5)\n" \
  "ds_read2_b32 v[124:125], %7 offset0:0 offset1:1\n" "v_cvt_pk_f16_f32 v112, v132, v128\n" \
  "v_mfma_f32_16x16x32_f16 v[160:163], v[56:59], v[84:87], v[160:163]\n" "v_cvt_pk_f16_f32 v113, v133, v129\n" \
  "v_cvt_pk_f16_f32 v114, v134, v130\n" "v_mfma_f32_16x16x32_f16 v[164:167], v[60:63], v[84:87], v[164:167]\n" \
  "ds_read2_b32 v[126:127], %7 offset0:2 offset1:3\n" "v_cvt_pk_f16_f32 v115, v135, v131\n" \
  "v_mfma_f32_16x16x32_f16 v[160:163], v[20:23], v[100:103], v[160:163]\n" "s_nop 0\n" \
  "ds_read_b128 v[108:111], %8 offset:448\n" "ds_read2_b32 v[120:121], %6 offset0:128 offset1:129\n" \
  "v_mfma_f32_16x16x32_f16 v[164:167], v[64:67], v[100:103], v[164:167]\n" "ds_read2_b32 v[122:123], %6 offset0:130 offset1:131\n" \
  "v_mfma_f32_16x16x32_f16 v[136:139], v[116:119], v[76:79], v[136:139]\n" "s_nop 0\n" \
  "ds_read2_b32 v[96:97], %7 offset0:16 offset1:17\n" "ds_read2_b32 v[98:99], %7 offset0:18 offset1:19\n" \
  "v_mfma_f32_16x16x32_f16 v[160:163], v[48:51], v[88:91], v[160:163]\n" "ds_read_b128 v[84:87], %8 offset:512\n" \
  "v_mfma_f32_16x16x32_f16 v[164:167], v[44:47], v[88:91], v[164:167]\n" "s_waitcnt lgkmcnt(8)\n" \
  "v_mfma_f32_16x16x32_f16 v[160:163], v[72:75], v[80:83], v[160:163]\n" "s_nop 0\n" \
  "ds_read2_b32 v[132:133], %6 offset0:0 offset1:1\n" "ds_read2_b32 v[134:135], %6 offset0:2 offset1:3\n" \
  "v_mfma_f32_16x16x32_f16 v[164:167], v[104:107], v[80:83], v[164:167]\n" "ds_read_b128 v[128:131], %8 offset:576\n" \
  "v_mfma_f32_16x16x32_f16 v[136:139], v[112:115], v[92:95], v[136:139]\n" "ds_read_b128 v[100:103], %8 offset:640\n" \
  "v_mfma_f32_16x16x32_f16 v[164:167], v[36:39], v[24:27], v[164:167]\n" "ds_read_b128 v[116:119], %8 offset:704\n" \
  "v_mfma_f32_16x16x32_f16 v[160:163], v[52:55], v[24:27], v[160:163]\n" "v_mfma_f32_16x16x32_f16 v[164:167], v[68:71], v[28:31], v[164:167]\n" \
  "s_nop 0\n" "ds_read_b128 v[76:79], %8 offset:768\n" \
  "ds_read_b128 v[88:91], %8 offset:832\n" "v_mfma_f32_16x16x32_f16 v[160:163], v[16:19], v[28:31], v[160:163]\n" \
  "s_waitcnt lgkmcnt(7)\n" "ds_read_b128 v[80:83], %8 offset:896\n" \
  "v_mfma_f32_16x16x32_f16 v[164:167], v[124:127], v[108:111], v[164:167]\n" "ds_read_b128 v[112:115], %8 offset:960\n" \
  "v_mfma_f32_16x16x32_f16 v[160:163], v[120:123], v[108:111], v[160:163]\n" "ds_read_b128 v[92:95], %8 offset:1024\n" \
  "v_mfma_f32_16x16x32_f16 v[164:167], v[96:99], v[84:87], v[164:167]\n" "v_mfma_f32_16x16x32_f16 v[160:163], v[32:35], v[84:87], v[160:163]\n" \
  "s_nop 0\n" "ds_read_b128 v[24:27], %8 offset:1088\n" \
  "ds_read_b128 v[28:31], %8 offset:1152\n" "v_mfma_f32_16x16x32_f16 v[156:159], v[40:43], v[84:87], v[156:159]\n" \
  "s_waitcnt lgkmcnt(7)\n" "ds_read_b128 v[108:111], %8 offset:1216\n" \
  "v_mfma_f32_16x16x32_f16 v[148:151], v[132:135], v[84:87], v[148:151]\n" "v_mul_f32 v164, %5, v164\n" \
  "v_mul_f32 v165, %5, v165\n" "v_mfma_f32_16x16x32_f16 v[160:163], v[64:67], v[128:131], v[160:163]\n" \
  "v_mul_f32 v166, %5, v166\n" "v_mul_f32 v167, %5, v167\n" \
  "v_mfma_f32_16x16x32_f16 v[156:159], v[20:23], v[128:131], v[156:159]\n" "v_cvt_pk_bf16_f32 v164, v164, v165\n" \
  "v_cvt_pk_bf16_f32 v165, v166, v167\n" "v_mfma_f32_16x16x32_f16 v[160:163], v[60:63], v[100:103], v[160:163]\n" \
  "v_pk_min_u16 v164, v164, %4\n" "v_pk_min_u16 v165, v165, %4\n" \
  "v_mfma_f32_16x16x32_f16 v[156:159], v[56:59], v[100:103], v[156:159]\n" "ds_write_b64 %12, v[164:165] offset:0\n" \
  "v_mfma_f32_16x16x32_f16 v[160:163], v[104:107], v[116:119], v[160:163]\n" "s_nop 0\n" \
  "ds_read_b128 v[84:87], %8 offset:1280\n" "ds_read_b128 v[128:131], %8 offset:1344\n" \
  "v_mfma_f32_16x16x32_f16 v[156:159], v[72:75], v[116:119], v[156:159]\n" "s_waitcnt lgkmcnt(4)\n" \
  "ds_read_b128 v[100:103], %8 offset:1408\n" "ds_read_b128 v[164:167], %8 offset:1472\n" \
  "v_mfma_f32_16x16x32_f16 v[160:163], v[44:47], v[76:79], v[160:163]\n" "v_mfma_f32_16x16x32_f16 v[156:159], v[48:51], v[76:79], v[156:159]\n" \
  "v_mfma_f32_16x16x32_f16 v[160:163], v[36:39], v[88:91], v[160:163]\n" "ds_read_b128 v[116:119], %8 offset:1536\n" \
  "v_mfma_f32_16x16x32_f16 v[156:159], v[52:55], v[88:91], v[156:159]\n" "s_nop 0\n" \
  "ds_read_b128 v[76:79], %8 offset:1600\n" "ds_read_b128 v[88:91], %8 offset:1664\n" \
  "v_mfma_f32_16x16x32_f16 v[160:163], v[68:71], v[80:83], v[160:163]\n" "v_mfma_f32_16x16x32_f16 v[156:159], v[16:19], v[80:83], v[156:159]\n" \
  "ds_read_b128 v[80:83], %8 offset:1728\n" "v_mfma_f32_16x16x32_f16 v[160:163], v[124:127], v[112:115], v[160:163]\n" \
  "v_mfma_f32_16x16x32_f16 v[156:159], v[120:123], v[112:115], v[156:159]\n" "ds_read_b128 v[112:115], %8 offset:1792\n" \
  "v_mfma_f32_16x16x32_f16 v[160:163], v[96:99], v[92:95], v[160:163]\n" "v_mfma_f32_16x16x32_f16 v[156:159], v[32:35], v[92:95], v[156:159]\n" \
  "v_mfma_f32_16x16x32_f16 v[152:155], v[40:43], v[92:95], v[152:155]\n" "v_mfma_f32_16x16x32_f16 v[144:147], v[132:135], v[92:95], v[144:147]\n" \
  "ds_read_b128 v[92:95], %8 offset:1856\n" "v_mfma_f32_16x16x32_f16 v[156:159], v[64:67], v[24:27], v[156:159]\n" \
  "v_mfma_f32_16x16x32_f16 v[152:155], v[20:23], v[24:27], v[152:155]\n" "ds_read_b128 v[24:27], %8 offset:1920\n" \
  "v_mfma_f32_16x16x32_f16 v[156:159], v[60:63], v[28:31], v[156:159]\n" "v_mul_f32 v160, %5, v160\n" \
  "v_mul_f32 v161, %5, v161\n" "v_mfma_f32_16x16x32_f16 v[152:155], v[56:59], v[28:31], v[152:155]\n" \
  "s_waitcnt lgkmcnt(2)\n" "v_mul_f32 v162, %5, v162\n" \
  "v_mul_f32 v163, %5, v163\n" "v_mfma_f32_16x16x32_f16 v[156:159], v[104:107], v[108:111], v[156:159]\n" \
  "v_cvt_pk_bf16_f32 v160, v160, v161\n" "v_cvt_pk_bf16_f32 v161, v162, v163\n" \
  "v_mfma_f32_16x16x32_f16 v[152:155], v[72:75], v[108:111], v[152:155]\n" "v_pk_min_u16 v160, v160, %4\n" \
  "v_pk_min_u16 v161, v161, %4\n" "v_mfma_f32_16x16x32_f16 v[156:159], v[44:47], v[84:87], v[156:159]\n" \
  "ds_write_b64 %12, v[160:161] offset:512\n" "v_mfma_f32_16x16x32_f16 v[152:155], v[48:51], v[84:87], v[152:155]\n" \
  "s_nop 0\n" "ds_read_b128 v[28:31], %8 offset:1984\n" \
  "ds_read_b128 v[108:111], %8 offset:2048\n" "v_mfma_f32_16x16x32_f16 v[156:159], v[36:39], v[128:131], v[156:159]\n" \
  "s_nop 0\n" "ds_read_b128 v[160:163], %8 offset:2112\n" \
  "ds_read_b128 v[84:87], %8 offset:2176\n" "v_mfma_f32_16x16x32_f16 v[152:155], v[52:55], v[128:131], v[152:155]\n" \
  "v_mfma_f32_16x16x32_f16 v[156:159], v[68:71], v[100:103], v[156:159]\n" "v_mfma_f32_16x16x32_f16 v[152:155], v[16:19], v[100:103], v[152:155]\n" \
  "s_waitcnt lgkmcnt(4)\n" "ds_read_b128 v[128:131], %8 offset:2240\n" \
  "v_mfma_f32_16x16x32_f16 v[156:159], v[124:127], v[164:167], v[156:159]\n" "s_barrier\n" \
  "ds_read_b128 v[100:103], %8 offset:2304\n" "v_mfma_f32_16x16x32_f16 v[152:155], v[120:123], v[164:167], v[152:155]\n" \
  "ds_read_b128 v[164:167], %8 offset:2368\n" "v_mfma_f32_16x16x32_f16 v[156:159], v[96:99], v[116:119], v[156:159]\n" \
  "v_mfma_f32_16x16x32_f16 v[152:155], v[32:35], v[116:119], v[152:155]\n" "v_mfma_f32_16x16x32_f16 v[148:151], v[40:43], v[116:119], v[148:151]\n" \
  "v_mfma_f32_16x16x32_f16 v[140:143], v[132:135], v[116:119], v[140:143]\n" "ds_read_b128 v[116:119], %8 offset:2432\n" \
  "v_mfma_f32_16x16x32_f16 v[152:155], v[64:67], v[76:79], v[152:155]\n" "v_mfma_f32_16x16x32_f16 v[148:151], v[20:23], v[76:79], v[148:151]\n" \
  "ds_read_b128 v[76:79], %8 offset:2496\n" "v_mfma_f32_16x16x32_f16 v[152:155], v[60:63], v[88:91], v[152:155]\n" \
  "v_mul_f32 v156, %5, v156\n" "v_mul_f32 v157, %5, v157\n" \
  "v_mfma_f32_16x16x32_f16 v[148:151], v[56:59], v[88:91], v[148:151]\n" "v_mul_f32 v158, %5, v158\n" \
  "v_mul_f32 v159, %5, v159\n" "v_mfma_f32_16x16x32_f16 v[152:155], v[104:107], v[80:83], v[152:155]\n" \
  "v_cvt_pk_bf16_f32 v156, v156, v157\n" "v_cvt_pk_bf16_f32 v157, v158, v159\n" \
  "v_mfma_f32_16x16x32_f16 v[148:151], v[72:75], v[80:83], v[148:151]\n" "v_pk_min_u16 v156, v156, %4\n" \
  "v_pk_min_u16 v157, v157, %4\n" "v_mfma_f32_16x16x32_f16 v[152:155], v[44:47], v[112:115], v[152:155]\n" \
  "ds_write_b64 %12, v[156:157] offset:1024\n" "v_mfma_f32_16x16x32_f16 v[148:151], v[48:51], v[112:115], v[148:151]\n" \
  "s_nop 0\n" "ds_read_b128 v[88:91], %8 offset:2560\n" \
  "ds_read_b128 v[80:83], %8 offset:2624\n" "v_mfma_f32_16x16x32_f16 v[152:155], v[36:39], v[92:95], v[152:155]\n" \
  "s_nop 0\n" "ds_read_b128 v[156:159], %8 offset:2688\n" \
  "ds_read_b128 v[112:115], %8 offset:2752\n" "v_mfma_f32_16x16x32_f16 v[148:151], v[52:55], v[92:95], v[148:151]\n" \
  "v_mfma_f32_16x16x32_f16 v[152:155], v[68:71], v[24:27], v[152:155]\n" "v_mfma_f32_16x16x32_f16 v[148:151], v[16:19], v[24:27], v[148:151]\n" \
  "s_waitcnt lgkmcnt(5)\n" "ds_read_b128 v[92:95], %8 offset:2816\n" \
  "v_mfma_f32_16x16x32_f16 v[152:155], v[124:127], v[28:31], v[152:155]\n" "ds_read_b128 v[24:27], %8 offset:2880\n" \
  "v_mfma_f32_16x16x32_f16 v[148:151], v[120:123], v[28:31], v[148:151]\n" "ds_read_b128 v[28:31], %8 offset:2944\n" \
  "v_mfma_f32_16x16x32_f16 v[152:155], v[96:99], v[108:111], v[152:155]\n" "v_mfma_f32_16x16x32_f16 v[148:151], v[32:35], v[108:111], v[148:151]\n" \
  "v_mfma_f32_16x16x32_f16 v[144:147], v[40:43], v[108:111], v[144:147]\n" "v_mfma_f32_16x16x32_f16 v[136:139], v[132:135], v[108:111], v[136:139]\n" \
  "ds_read_b128 v[132:135], %8 offset:3008\n" "v_mfma_f32_16x16x32_f16 v[148:151], v[64:67], v[160:163], v[148:151]\n" \
  "v_mfma_f32_16x16x32_f16 v[144:147], v[20:23], v[160:163], v[144:147]\n" "v_mfma_f32_16x16x32_f16 v[148:151], v[60:63], v[84:87], v[148:151]\n" \
  "s_nop 0\n" "v_mul_f32 v152, %5, v152\n" \
  "v_mul_f32 v153, %5, v153\n" "v_mfma_f32_16x16x32_f16 v[144:147], v[56:59], v[84:87], v[144:147]\n" \
  "v_mul_f32 v154, %5, v154\n" "v_mul_f32 v155, %5, v155\n" \
  "v_mfma_f32_16x16x32_f16 v[148:151], v[104:107], v[128:131], v[148:151]\n" "v_cvt_pk_bf16_f32 v152, v152, v153\n" \
  "v_cvt_pk_bf16_f32 v153, v154, v155\n" "v_mfma_f32_16x16x32_f16 v[144:147], v[72:75], v[128:131], v[144:147]\n" \
  "v_pk_min_u16 v152, v152, %4\n" "v_pk_min_u16 v153, v153, %4\n" \
  "v_mfma_f32_16x16x32_f16 v[148:151], v[44:47], v[100:103], v[148:151]\n" "ds_write_b64 %12, v[152:153] offset:1536\n" \
  "v_mfma_f32_16x16x32_f16 v[144:147], v[48:51], v[100:103], v[144:147]\n" "v_mfma_f32_16x16x32_f16 v[148:151], v[36:39], v[164:167], v[148:151]\n" \
  "v_mfma_f32_16x16x32_f16 v[144:147], v[52:55], v[164:167], v[144:147]\n" "v_mfma_f32_16x16x32_f16 v[148:151], v[68:71], v[116:119], v[148:151]\n" \
  "v_mfma_f32_16x16x32_f16 v[144:147], v[16:19], v[116:119], v[144:147]\n" "s_waitcnt lgkmcnt(0)\n" \
  "v_mfma_f32_16x16x32_f16 v[148:151], v[124:127], v[76:79], v[148:151]\n" "s_barrier\n" \
  "ds_read_b128 v[108:111], %8 offset:3072\n" "ds_read_b128 v[160:163], %8 offset:3136\n" \
  "v_mfma_f32_16x16x32_f16 v[144:147], v[120:123], v[76:79], v[144:147]\n" "s_nop 0\n" \
  "ds_read_b128 v[84:87], %8 offset:3200\n" "ds_read_b128 v[128:131], %8 offset:3264\n" \
  "v_mfma_f32_16x16x32_f16 v[148:151], v[96:99], v[88:91], v[148:151]\n" "ds_read_b128 v[152:155], %8 offset:3328\n" \
  "v_mfma_f32_16x16x32_f16 v[144:147], v[32:35], v[88:91], v[144:147]\n" "s_nop 2\n" \
  "v_mfma_f32_16x16x32_f16 v[140:143], v[40:43], v[88:91], v[140:143]\n" "s_nop 1\n" \
  "v_mfma_f32_16x16x32_f16 v[144:147], v[64:67], v[80:83], v[144:147]\n" "v_mul_f32 v148, %5, v148\n" \
  "v_mul_f32 v149, %5, v149\n" "v_mfma_f32_16x16x32_f16 v[140:143], v[20:23], v[80:83], v[140:143]\n" \
  "v_mul_f32 v150, %5, v150\n" "v_mul_f32 v151, %5, v151\n" \
  "v_mfma_f32_16x16x32_f16 v[144:147], v[60:63], v[156:159], v[144:147]\n" "v_cvt_pk_bf16_f32 v148, v148, v149\n" \
  "v_cvt_pk_bf16_f32 v149, v150, v151\n" "v_mfma_f32_16x16x32_f16 v[140:143], v[56:59], v[156:159], v[140:143]\n" \
  "v_pk_min_u16 v148, v148, %4\n" "v_pk_min_u16 v149, v149, %4\n" \
  "v_mfma_f32_16x16x32_f16 v[144:147], v[104:107], v[112:115], v[144:147]\n" "ds_write_b64 %12, v[148:149] offset:2048\n" \
  "v_mfma_f32_16x16x32_f16 v[140:143], v[72:75], v[112:115], v[140:143]\n" "s_nop 0\n" \
  "ds_read_b128 v[100:103], %8 offset:3392\n" "ds_read_b128 v[164:167], %8 offset:3456\n" \
  "v_mfma_f32_16x16x32_f16 v[144:147], v[44:47], v[92:95], v[144:147]\n" "s_nop 0\n" \
  "ds_read_b128 v[116:119], %8 offset:3520\n" "ds_read_b128 v[76:79], %8 offset:3584\n" \
  "v_mfma_f32_16x16x32_f16 v[140:143], v[48:51], v[92:95], v[140:143]\n" "v_mfma_f32_16x16x32_f16 v[144:147], v[36:39], v[24:27], v[144:147]\n" \
  "v_mfma_f32_16x16x32_f16 v[140:143], v[52:55], v[24:27], v[140:143]\n" "ds_read_b128 v[88:91], %8 offset:3648\n" \
  "v_mfma_f32_16x16x32_f16 v[144:147], v[68:71], v[28:31], v[144:147]\n" "s_nop 0\n" \
  "ds_read_b128 v[80:83], %8 offset:3712\n" "ds_read_b128 v[156:159], %8 offset:3776\n" \
  "v_mfma_f32_16x16x32_f16 v[140:143], v[16:19], v[28:31], v[140:143]\n" "s_nop 0\n" \
  "ds_read_b128 v[148:151], %8 offset:3840\n" "ds_read_b128 v[112:115], %8 offset:3904\n" \
  "v_mfma_f32_16x16x32_f16 v[144:147], v[124:127], v[132:135], v[144:147]\n" "v_mfma_f32_16x16x32_f16 v[140:143], v[120:123], v[132:135], v[140:143]\n" \
  "s_waitcnt lgkmcnt(7)\n" "v_mfma_f32_16x16x32_f16 v[144:147], v[96:99], v[108:111], v[144:147]\n" \
  "ds_read_b128 v[92:95], %8 offset:3968\n" "v_mfma_f32_16x16x32_f16 v[140:143], v[32:35], v[108:111], v[140:143]\n" \
  "s_nop 0\n" "ds_read_b128 v[24:27], %8 offset:4032\n" \
  "ds_read_b128 v[28:31], %8 offset:4096\n" "v_mfma_f32_16x16x32_f16 v[136:139], v[40:43], v[108:111], v[136:139]\n" \
  "s_nop 1\n" "v_mfma_f32_16x16x32_f16 v[140:143], v[64:67], v[160:163], v[140:143]\n" \
  "v_mul_f32 v144, %5, v144\n" "v_mul_f32 v145, %5, v145\n" \
  "v_mfma_f32_16x16x32_f16 v[140:143], v[60:63], v[84:87], v[140:143]\n" "v_mul_f32 v146, %5, v146\n" \
  "v_mul_f32 v147, %5, v147\n" "v_mfma_f32_16x16x32_f16 v[140:143], v[104:107], v[128:131], v[140:143]\n" \
  "v_cvt_pk_bf16_f32 v144, v144, v145\n" "v_cvt_pk_bf16_f32 v145, v146, v147\n" \
  "v_mfma_f32_16x16x32_f16 v[140:143], v[44:47], v[152:155], v[140:143]\n" "v_pk_min_u16 v144, v144, %4\n" \
  "v_pk_min_u16 v145, v145, %4\n" "v_mfma_f32_16x16x32_f16 v[140:143], v[36:39], v[100:103], v[140:143]\n" \
  "ds_write_b64 %12, v[144:145] offset:2560\n" "v_mfma_f32_16x16x32_f16 v[140:143], v[68:71], v[164:167], v[140:143]\n" \
  "s_waitcnt lgkmcnt(3)\n" "v_mfma_f32_16x16x32_f16 v[140:143], v[124:127], v[116:119], v[140:143]\n" \
  "v_mfma_f32_16x16x32_f16 v[140:143], v[96:99], v[76:79], v[140:143]\n" "s_nop 2\n" \
  "v_mfma_f32_16x16x32_f16 v[136:139], v[20:23], v[160:163], v[136:139]\n" "s_nop 2\n" \
  "v_mfma_f32_16x16x32_f16 v[136:139], v[56:59], v[84:87], v[136:139]\n" "s_waitcnt lgkmcnt(0)\n" \
  "v_mul_f32 v140, %5, v140\n" "v_mul_f32 v141, %5, v141\n" \
  "v_mfma_f32_16x16x32_f16 v[136:139], v[72:75], v[128:131], v[136:139]\n" "s_barrier\n" \
  "v_mul_f32 v142, %5, v142\n" "v_mul_f32 v143, %5, v143\n" \
  "v_mfma_f32_16x16x32_f16 v[136:139], v[48:51], v[152:155], v[136:139]\n" "v_cvt_pk_bf16_f32 v140, v140, v141\n" \
  "v_cvt_pk_bf16_f32 v141, v142, v143\n" "v_mfma_f32_16x16x32_f16 v[136:139], v[52:55], v[100:103], v[136:139]\n" \
  "v_pk_min_u16 v140, v140, %4\n" "v_pk_min_u16 v141, v141, %4\n" \
  "v_mfma_f32_16x16x32_f16 v[136:139], v[16:19], v[164:167], v[136:139]\n" "ds_write_b64 %12, v[140:141] offset:3072\n" \
  "v_mfma_f32_16x16x32_f16 v[136:139], v[120:123], v[116:119], v[136:139]\n" "v_mfma_f32_16x16x32_f16 v[136:139], v[32:35], v[76:79], v[136:139]\n" \
  "v_mfma_f32_16x16x32_f16 v[136:139], v[64:67], v[88:91], v[136:139]\n" "v_mfma_f32_16x16x32_f16 v[136:139], v[60:63], v[80:83], v[136:139]\n" \
  "v_mfma_f32_16x16x32_f16 v[136:139], v[104:107], v[156:159], v[136:139]\n" "v_mfma_f32_16x16x32_f16 v[136:139], v[44:47], v[148:151], v[136:139]\n" \
  "v_mfma_f32_16x16x32_f16 v[136:139], v[36:39], v[112:115], v[136:139]\n" "v_mfma_f32_16x16x32_f16 v[136:139], v[68:71], v[92:95], v[136:139]\n" \
  "v_mfma_f32_16x16x32_f16 v[136:139], v[124:127], v[24:27], v[136:139]\n" "v_mfma_f32_16x16x32_f16 v[136:139], v[96:99], v[28:31], v[136:139]\n" \
  "s_nop 2\n" "s_nop 4\n" \
  "v_mul_f32 v136, %5, v136\n" "v_mul_f32 v137, %5, v137\n" \
  "v_mul_f32 v138, %5, v138\n" "v_mul_f32 v139, %5, v139\n" \
  "v_cvt_pk_bf16_f32 v136, v136, v137\n" "v_cvt_pk_bf16_f32 v137, v138, v139\n" \
  "v_pk_min_u16 v136, v136, %4\n" "v_pk_min_u16 v137, v137, %4\n" \
  "ds_write_b64 %12, v[136:137] offset:3584\n" "s_waitcnt lgkmcnt(0)\n" \
  "s_barrier\n"
#define CONV_ASM_CLOBBERS "v32", "v33", "v34", "v35", "v36", "v37", "v38", "v39", "v40", "v41", "v42", "v43", "v44", "v45", "v46", "v47", "v48", "v49", "v50", "v51", "v52", "v53", "v54", "v55", "v56", "v57", "v58", "v59", "v60", "v61", "v62", "v63", "v64", "v65", "v66", "v67", "v68", "v69", "v70", "v71", "v72", "v73", "v74", "v75", "v76", "v77", "v78", "v79", "v80", "v81", "v82", "v83", "v84", "v85", "v86", "v87", "v88", "v89", "v90", "v91", "v92", "v93", "v94", "v95", "v96", "v97", "v98", "v99", "v100", "v101", "v102", "v103", "v104", "v105", "v106", "v107", "v108", "v109", "v110", "v111", "v112", "v113", "v114", "v115", "v116", "v117", "v118", "v119", "v120", "v121", "v122", "v123", "v124", "v125", "v126", "v127", "v128", "v129", "v130", "v131", "v132", "v133", "v134", "v135", "v136", "v137", "v138", "v139", "v140", "v141", "v142", "v143", "v144", "v145", "v146", "v147", "v148", "v149", "v150", "v151", "v152", "v153", "v154", "v155", "v156", "v157", "v158", "v159", "v160", "v161", "v162", "v163", "v164", "v165", "v166", "v167", "memory"
#define CONV_ASM_PRE(q) "+{v[16:19]}"(q[0]), "+{v[20:23]}"(q[1]), "+{v[24:27]}"(q[2]), "+{v[28:31]}"(q[3])

struct Params {
  const u16 *x, *lar, *aim, *crl, *cim, *ldt, *dskip, *wr, *wg, *wu, *wd;
  u16* out;
  char* ws;
};

typedef unsigned long long u64;
typedef u64 v4q __attribute__((ext_vector_type(4)));
typedef unsigned v16u __attribute__((ext_vector_type(16)));
#define GAS __attribute__((address_space(1)))

#define EP_SC ""
#define EARLY_N 4
#define T3_ASM
#define T3_OUT
#define T1_ASM
#define T2_ASM
#define LD_B \
  "global_load_dword v4, v5, s[14:15]\n\t" \
  "global_load_dword v5, v5, s[16:17]\n\t" \
  "global_load_ushort v16, v16, s[20:21]\n\t" \
  "global_load_dword v1, v1, s[36:37]" EP_SC "\n\t"
#define LDS_ 1
#define LD_B0
#define LD_B1 LD_B
#define SVC_SL "s_sleep 3\n\t"
#define LD_TXT \
  T2_ASM "s_waitcnt lgkmcnt(0)\n\t" T1_ASM \
  "global_load_ushort v6, v16, s[18:19]\n\t" \
  "global_load_ushort v2, v3, s[10:11]\n\t" \
  "global_load_ushort v3, v3, s[12:13]\n\t" LD_B0
#define LDP 3
#define LDP0
#define LDP1
#define LDP2
#define LDP3 LD_TXT

__global__ __launch_bounds__(NT, 3) void k_main(Params) {
  extern __shared__ __attribute__((aligned(16))) char smem[];
  u64 kx, kw, t0_, t1_, t2_;
  v4q kb_;
  unsigned epoch, sep;
  v4u ga, gb, gc, gd;
  const unsigned bid = blockIdx.x;
  asm volatile(
      ".p2align 12\n\t"
      "s_load_dwordx16 s[8:23], s[0:1], 0x0\n\t"
      "s_getpc_b64 s[4:5]\n\t"
      "s_load_dwordx2 s[36:37], s[0:1], 0x60\n\t"
      "s_load_dwordx8 s[24:31], s[0:1], 0x40\n\t" T0_ASM
      "v_lshrrev_b32 v17, 6, v0\n\t"
      "s_lshl_b32 s3, s2, 4\n\t"
      "v_mov_b32 v1, 0\n\t"
      "v_lshl_add_u32 v17, v17, 12, s3\n\t"
      "s_cmpk_lt_u32 s2, 144\n\t"
      "v_min_u32 v17, %[code], v17\n\t"
      "global_load_dword v17, v17, s[4:5]\n\t"
      "s_cbranch_scc1 Lc_%=\n\t"
      "s_add_i32 s3, s2, 0xffffff70\n\t"
      "v_bfe_u32 v18, v0, 3, 3\n\t"
      "v_lshlrev_b32 v19, 1, v0\n\t"
      "s_mulk_i32 s3, 18432\n\t"
      "v_lshlrev_b32 v20, 4, v0\n\t"
      "s_movk_i32 s6, 127\n\t"
      "v_mul_u32_u24 v18, 1152, v18\n\t"
      "v_bfi_b32 v19, s6, v20, v19\n\t"
      "v_add3_u32 v18, v18, v19, s3\n\t"
      "s_cmpk_lt_u32 s2, 240\n\t"
      "v_add_u32 v19, 9216, v18\n\t"
      "v_add_u32 v10, 294912, v18\n\t"
      "v_add_u32 v14, 304128, v18\n\t"
      "s_waitcnt lgkmcnt(0)\n\t"
      "s_cbranch_scc1 Lx_%=\n\t"
      "s_sleep %[x2d]\n"
      "Lx_%=:\n\t"
      "global_load_dwordx4 v[2:5], v18, s[8:9]\n\t"
      "global_load_dwordx4 v[6:9], v19, s[8:9]\n\t"
      "s_load_dword s33, s[36:37], 0x0\n\t"
      "s_branch Le_%=\n"
      "Lw_%=:\n\t"
      "v_lshlrev_b32 v7, 2, v0\n\t"
      "s_waitcnt lgkmcnt(0)\n\t" SVC_SL
      "global_load_dword v1, v1, s[36:37]" EP_SC "\n\t"
      "global_load_dword v2, v7, s[22:23] offset:-2048\n\t"
      "global_load_dword v3, v7, s[22:23] offset:-1792\n\t"
      "global_load_dword v4, v7, s[22:23] offset:-1536\n\t"
      "global_load_dword v5, v7, s[22:23] offset:-1280\n\t"
      "global_load_dword v6, v7, s[22:23] offset:-1152\n\t"
      "s_branch Le_%=\n"
      "Lc_%=:\n\t"
      "v_readfirstlane_b32 s3, v0\n\t"
      "v_bfe_u32 v6, v0, 6, 2\n\t"
      "s_lshl_b32 s4, s2, 2\n\t"
      "v_bfe_u32 v5, v0, 0, 8\n\t"
      "s_lshl_b32 s5, s2, 10\n\t"
      "s_cmpk_ge_u32 s3, 512\n\t"
      "v_add_lshl_u32 v16, v6, s4, 1\n\t"
      "s_cbranch_scc1 Lw_%=\n\t"
      "v_lshl_add_u32 v5, v5, 2, s5\n\t"
      "s_lshr_b32 s4, s3, 8\n\t"
      "s_bfe_u32 s5, s3, 0x20006\n\t"
      "v_lshl_add_u32 v3, s4, 1, v5\n\t"
      LDP0
      "v_and_b32 v7, 63, v0\n\t"
      "s_mulk_i32 s5, %[ustr]\n\t"
      "s_mulk_i32 s4, 3584\n\t"
      "v_mov_b64 v[10:11], 0\n\t"
      "s_add_i32 s5, s5, s4\n\t"
      "v_mov_b64 v[12:13], 0\n\t"
      "s_add_i32 s5, s5, %[uimg]\n\t"
      "v_lshlrev_b32 v9, 4, v7\n\t"
      "s_lshl_b32 s3, s3, 8\n\t"
      "v_add_u32 v8, s5, v9\n\t"
      "v_add_u32 v9, %[filt], v9\n\t"
      "ds_write_b128 v8, v[10:13]\n\t"
      "ds_write_b128 v8, v[10:13] offset:32\n\t"
      "ds_write_b128 v9, v[10:13] offset:4096\n\t"
      "v_and_b32 v15, 31, v7\n\t"
      "s_lshr_b32 s4, s3, 14\n\t"
      "v_lshrrev_b32 v14, 5, v7\n\t"
      "s_mulk_i32 s4, %[tstr]\n\t"
      "v_mul_u32_u24 v15, %[trow], v15\n\t"
      "s_mov_b32 m0, s4\n\t"
      "v_lshlrev_b32 v14, 4, v14\n\t"
      "v_add3_u32 v41, v14, v15, s4\n\t"
      "s_mov_b32 s4, 0xfe0001ff\n\t"
      "s_mov_b32 s5, 0x0380007c\n\t"
      "s_lshr_b32 s6, s3, 10\n\t"
      "v_and_b32 v8, 15, v7\n\t"
      "s_lshr_b64 s[4:5], s[4:5], s6\n\t"
      "v_lshrrev_b32 v14, 1, v7\n\t"
      "s_and_b32 s4, s4, 0xffff\n\t"
      "v_and_b32 v14, 24, v14\n\t"
      "v_lshlrev_b32 v8, v8, s4\n\t"
      "s_mov_b32 s4, 0x8001\n\t"
      "v_lshrrev_b32 v8, v14, v8\n\t"
      "s_mov_b32 s5, 0x10001\n\t"
      "v_bfe_u32 v10, v8, 0, 2\n\t"
      "v_bfe_u32 v11, v8, 2, 2\n\t"
      "v_bfe_u32 v12, v8, 4, 2\n\t"
      "v_bfe_u32 v13, v8, 6, 2\n\t"
      "s_bfe_u32 s6, s3, 0x2000e\n\t"
      "v_mul_u32_u24 v10, s4, v10\n\t"
      "v_mul_u32_u24 v11, s4, v11\n\t"
      "v_mul_u32_u24 v12, s4, v12\n\t"
      "v_mul_u32_u24 v13, s4, v13\n\t"
      "s_movk_i32 s4, 0x3c00\n\t"
      "v_and_b32 v10, s5, v10\n\t"
      "v_and_b32 v11, s5, v11\n\t"
      "v_and_b32 v12, s5, v12\n\t"
      "v_and_b32 v13, s5, v13\n\t"
      "v_lshl_add_u32 v9, s6, 10, v9\n\t"
      "v_mul_u32_u24 v10, s4, v10\n\t"
      "v_mul_u32_u24 v11, s4, v11\n\t"
      "v_mul_u32_u24 v12, s4, v12\n\t"
      "v_mul_u32_u24 v13, s4, v13\n\t"
      "ds_write_b128 v9, v[10:13]\n\t"
      LDP3
      "s_and_b32 s7, s3, 0xc000\n\t"
      "v_lshlrev_b32 v47, 4, v7\n\t"
      "s_add_i32 s4, s7, %[dummy]\n\t"
      "v_add_u32 v48, s7, v47\n\t"
      "s_bitcmp1_b32 s3, 16\n\t"
      "v_lshl_add_u32 v46, v7, 2, s4\n\t"
      "s_cbranch_scc1 Lm_%=\n\t"
      "s_cmpk_lt_u32 s2, 128\n\t"
      "v_and_b32 v8, 3, v7\n\t"
      "s_movk_i32 s4, %[u2]\n\t"
      "v_bfe_u32 v10, v7, 2, 2\n\t"
      "s_cmovk_i32 s4, %[u1]\n\t"
      "v_lshrrev_b32 v11, 5, v7\n\t"
      "s_movk_i32 s5, %[y2]\n\t"
      "v_and_b32 v12, 0x83, v0\n\t"
      "s_cmovk_i32 s5, %[y1]\n\t"
      "v_and_b32 v13, 0x70, v0\n\t"
      "s_add_i32 s4, s4, s2\n\t"
      "v_mul_u32_u24 v8, %[ustr] - 4, v8\n\t"
      "s_add_i32 s5, s5, s2\n\t"
      "v_mul_u32_u24 v10, 0x4020, v10\n\t"
      "s_lshl_b32 s4, s4, 14\n\t"
      "v_mul_u32_u24 v11, 7680, v11\n\t"
      "s_lshl_b32 s5, s5, 5\n\t"
      "v_mul_u32_u24 v12, 132, v12\n\t"
      "s_or_b32 s5, s5, s6\n\t"
      "v_lshl_add_u32 v7, v0, 4, s4\n\t"
      "s_lshl_b32 s5, s5, 9\n\t"
      "v_lshl_add_u32 v8, v0, 2, v8\n\t"
      "s_lshl2_add_u32 s4, s6, %[misc]\n\t"
      "v_and_b32 v12, 0x288, v12\n\t"
      "v_add3_u32 v11, v11, v47, s5\n\t"
      "v_add_u32 v9, %[uimg] + 520 + 2048, v8\n\t"
      "v_add_u32 v8, %[uimg] + 520, v8\n\t"
      "v_add3_u32 v10, v10, v12, v13\n\t"
      "v_mov_b32 v42, v48\n\t"
      "s_movk_i32 exec_lo, 0xff80\n\t"
      "v_add_u32 v42, 1024, v48\n\t"
      "s_mov_b32 exec_lo, 0xfff80000\n\t"
      "v_add_u32 v42, 2048, v48\n\t"
      "s_mov_b32 exec_lo, 0\n\t"
      "v_add_u32 v42, 2304, v48\n\t"
      "s_movk_i32 exec_hi, 0xff00\n\t"
      "v_add_u32 v42, 2560, v48\n\t"
      "s_mov_b64 exec, 1\n\t"
      "v_mov_b32 v46, s4\n\t"
      "s_branch Ll_%=\n"
      "Lm_%=:\n\t"
      "s_mul_i32 s4, s6, %[ustr] - 16384\n\t"
      "v_and_b32 v8, 15, v7\n\t"
      "s_add_i32 s4, s4, %[uimg]\n\t"
      "v_and_or_b32 v12, v7, 48, s7\n\t"
      "s_mul_i32 s5, s6, 0x4020\n\t"
      "v_and_b32 v14, 1, v7\n\t"
      "s_addk_i32 s5, %[ly]\n\t"
      "v_lshlrev_b32 v9, 4, v8\n\t"
      "v_and_b32 v44, 32, v7\n\t"
      "v_mad_i32_i24 v11, v8, -8, v12\n\t"
      "v_add3_u32 v10, v12, v9, s4\n\t"
      "s_add_i32 s4, s7, %[kn] + 1120\n\t"
      "v_mul_u32_u24 v14, %[kc] + 2, v14\n\t"
      "v_lshl_add_u32 v13, v7, 3, s5\n\t"
      "s_add_i32 s5, s7, %[kn] + 2 * %[kc] + 380\n\t"
      "v_sub_u32 v42, s4, v47\n\t"
      "s_add_i32 s4, s7, %[kn] + 2 * %[kc] + 512 + 190\n\t"
      "v_mad_i32_i24 v15, v8, -2, v12\n\t"
      "v_add_u32 v44, v44, v7\n\t"
      "v_mad_i32_i24 v12, v8, -4, v12\n\t"
      "v_mad_i32_i24 v43, v7, -4, s5\n\t"
      "v_add_u32 v9, v10, v9\n\t"
      "v_mad_i32_i24 v44, v44, -2, s4\n\t"
      "v_add_u32 v45, 1, v7\n\t"
      "v_sub_u32 v8, v15, v14\n\t"
      "v_and_b32 v45, -2, v45\n\t"
      "v_add_u32 v11, %[kn] + 2 * %[kc] + 120, v11\n\t"
      "v_add_u32 v15, v45, v14\n\t"
      "v_add_u32 v12, %[kn] + 2 * %[kc] + 512 + 60, v12\n\t"
      "v_add_u32 v7, %[kn] + %[kc] + 40, v8\n\t"
      "v_add_u32 v8, %[kn] + %[kc] + 40 + 1024, v8\n\t"
      "v_add_u32 v47, %[filt], v47\n\t"
      "v_mov_b32 v14, 0xfffefffe\n\t"
      "s_add_i32 s4, s7, %[kn] + %[kc] + 2 + 48\n\t"
      "v_mov_b32 v45, 0x0706\n\t"
      "s_mov_b64 exec, 0x7ff\n\t"
      "v_sub_u32 v46, s4, v15\n\t"
      "s_movk_i32 exec_lo, 0x7f\n\t"
      "v_add_u32 v46, 64, v46\n\t"
      "v_mov_b32 v45, 0x0504\n\t"
      "s_mov_b32 exec_lo, 1\n\t"
      "v_add_u32 v46, 1022, v46\n\t"
      "v_mov_b32 v45, 0x0302\n"
      "Ll_%=:\n\t"
      "s_mov_b64 exec, -1\n\t"
      LD_B1
      "\nLe_%=:"
      : "=&{s[8:9]}"(kx), "=&{s[36:37]}"(kw), "=&{s[24:31]}"(kb_), "=&{s[34:35]}"(t0_), "=&{s33}"(sep)
      : [code] "n"(CODE_BYTES), [ustr] "n"(USTR), [uimg] "n"(L_UIMG), [filt] "n"(L_FILT), "{s[0:1]}"(__builtin_amdgcn_kernarg_segment_ptr()), "{s2}"(bid),
        "{v0}"(threadIdx.x), [dummy] "n"(L_DUMMY), [misc] "n"(L_MISC), [kn] "n"(L_KN), [kc] "n"(KCOPY), [ly] "n"(L_Y), [u1] "n"(WS_U / GSTR),
        [u2] "n"(WS_U2 / GSTR - 128), [y1] "n"(WS_Y / GSTR), [y2] "n"(WS_Y2 / GSTR - 128), [x2d] "n"(X2_DELAY), [tstr] "n"(TSTR), [trow] "n"(TROW)
      : "s3", "s4", "s5", "s6", "s7", "s10", "s11", "s12", "s13", "s14", "s15", "s16", "s17", "s18", "s19", "s20", "s21", "s22", "s23", "v1", "v2", "v3", "v4",
        "v5", "v6", "v7", "v8", "v9", "v10", "v11", "v12", "v13", "v14", "v15", "v16", "v17", "v18", "v19", "v20", "v21", "v22", "v23", "v24", "v25",
        "v26", "v27", "v28", "v29", "v30", "v31", "v32", "v33", "v34", "v35", "v36", "v37", "v38", "v39", "v40", "v41", "v42", "v43", "v44", "v45",
        "v46", "v47", "v48", "scc", "memory");
  static_assert(WSCR == 16384 && GSTR == 16384 && L_K4 == L_KN + 2 * KCOPY && L_K8 == L_K4 + 512, "constants of the block above");
  v16u k8;
  asm volatile("" : "={v1}"(epoch), "={v[2:5]}"(ga), "={v[6:9]}"(gb), "={v[10:13]}"(gc), "={v[14:17]}"(gd), "={v[34:49]}"(k8)::"memory");
  SCHED();
  const int tid = threadIdx.x, lane = tid & 63;
  int wave = __builtin_amdgcn_readfirstlane(tid >> 6);
  const int xcd = bid & 7;
  int loc = bid >> 3;
  STAMP_INIT();
  unsigned pcnt_ = 0;
  STAMP(11);
  if (EXPW == 1) { DRAIN(); STAMP(13); SCHED(); }
  const int cl = lane & 15, g = lane >> 4;
  const int cg = bid;
  const int fj = xcd * 9 + (loc - 18);
  const int rb = bid;
  const int side = (bid << 11) & 8192;
  const int ybase = (cg < 128 ? WS_Y : WS_Y2 - 128 * GSTR) + cg * GSTR;
  const int tile = bid - NCONV;
  struct { const GAS char* x; GAS char* ws; } p = {(const GAS char*)kx, (GAS char*)kw};
  const rsrc_t rs = __builtin_amdgcn_make_buffer_rsrc((void*)p.ws, 0, WS_END, 0x00020000);
  const v4u sent4 = {SENT, SENT, SENT, SENT};
  const rsrc_t ro = __builtin_amdgcn_make_buffer_rsrc((void*)kb_[3], 0, SEQ * DM * 2, 0x00020000);
  if (loc < 18) {
    BAR();
    if (wave == 8) asm volatile("s_mov_b64 exec, 0\n\ts_branch Lk_conv\nLk_back:\n\ts_mov_b64 exec, -1");
  }
  asm volatile("" : "+s"(loc), "+s"(wave));

  if (loc < 18) {
    int ysrc = (wave < 8 ? WS_Y : WS_Y2 - 128 * GSTR) + 16 * wave * GSTR + side + (loc * 4 + (rb & 3)) * 128;
    unsigned ylag = T_Y3 - ((0x000f2b46u >> (2 * (loc & 12))) & 255u);
    asm volatile("" : "+s"(ysrc), "+s"(ylag));
    unsigned l_x, l_x2, l_sw;
    v4u cw[2];
    f32x4 xf[4];
    v2u unit;
    auto ln2_operands = [&](int lane) {
      const int cl = lane & 15, g = lane >> 4;
      l_x = L_XROW + wave * 2048 + cl * 128 + (g ^ (cl & 7)) * 16;
      l_x2 = l_x ^ 64;
      const v4u gx[2] = {*(const LAS v4u*)l_x, *(const LAS v4u*)l_x2};
      const unsigned l_w = (lane & 3) == 1 ? L_WR + wave * 128 + g * 16 : L_MISC + 128;
      cw[0] = *(const LAS v4u*)l_w;
      cw[1] = *(const LAS v4u*)(l_w + 64);
      l_sw = (cl >> 2) == g ? L_RED + wave * 256 + cl * 16 : 7 * WSCR + lane * 16;
      const u64 e = (u64)((cl >> 2) == g ? 0x3f80u : 0u) << (16 * (cl & 3));
      unit = v2u{(unsigned)e, (unsigned)(e >> 32)};
      PIN(unit);
#pragma unroll
      for (int i = 0; i < 4; i++) {
        xf[i] = widen(unit, v2u{gx[i >> 1][2 * (i & 1)], gx[i >> 1][2 * (i & 1) + 1]}, f32x4{0.f, 0.f, 0.f, 0.f});
        PIN(xf[i]);
      }
      PIN(cw[0]); PIN(cw[1]); PIN(l_x); PIN(l_x2); PIN(l_sw);
      SCHED();
    };
    if (wave < 8) {
      const int ch = wave & 3;
      volatile LAS unsigned* const pf = (volatile LAS unsigned*)(smem + L_MISC);
      if (wave < 4) __builtin_amdgcn_s_setprio(KP_H); else __builtin_amdgcn_s_setprio(1);
      STAMP(12);
      unsigned epoch, gdt, gre, gim, gdd;
      v2u gc_;
      unsigned rb_;
      unsigned op[7], top, a_kn, a_k4, a_k8, sel_sp, a_sp, a_q, pa;
      asm volatile("s_waitcnt vmcnt(%22) ; early loads"
                   : "={v1}"(epoch), "={v2}"(gre), "={v3}"(gim), "={v[4:5]}"(gc_), "={v6}"(gdt), "={v7}"(op[0]), "={v8}"(op[1]), "={v9}"(op[2]),
                     "={v10}"(op[3]), "={v11}"(op[4]), "={v12}"(op[5]), "={v13}"(op[6]), "={v14}"(top), "={v16}"(gdd),
                     "={v41}"(rb_), "={v42}"(a_kn), "={v43}"(a_k4), "={v44}"(a_k8), "={v45}"(sel_sp), "={v46}"(a_sp), "={v47}"(a_q), "={v48}"(pa)
                   : "n"(EARLY_N) : "memory");
      SCHED();
      STAMP(5);
      SCHED();
      v4u tx;
      const float dt = rbf(exp_c(blo(gdt)));
      const float ar = -rbf(exp_c(blo(gre))) * dt * 1.44269504f;
      const float aie = blo(gim) * dt;
      const float ph = aie * 0.159154936f;
      const float pl = fmaf(aie, 0.159154936f, -ph) + aie * 7.13048051e-9f;
#define put1(row, x) asm volatile("ds_write_addtid_b32 %0 offset:%1" ::"v"(x), "n"(L_TBL + ((row) & 31) * TROW) : "memory")
#define put2(row, x, y) do { put1(row, x); put1((row) + 16, y); } while (0)
      const cz e8 = mkz(cexp_p2(8.f, ar, ph, pl)), e64 = mkz(cexp_p2(64.f, ar, ph, pl));
      const cz e16 = csq(e8), e128 = csq(e64), e32 = csq(e16);
      {
        const f32x2 e24 = cmul(e16.v, e8), e192 = cmul(e128.v, e64);
        const unsigned ha[4] = {0x3c00u, pkh2(e8.v), pkh2(e16.v), pkh2(e24)};
        const unsigned hs[4] = {0u, pkh2(e8.s), pkh2(e16.s), pkh(e24.y, e24.x)};
        const unsigned hb[8] = {0x3c00u, pkh2(e32.v), pkh2(e64.v), pkh2(cmul(e64.v, e32)), pkh2(e128.v), pkh2(cmul(e128.v, e32)), pkh2(e192), pkh2(cmul(e192, e32))};
#pragma unroll
        for (int k = 0; k < 4; k++) put2(4 * k, hb[k], hb[k + 4]);
#pragma unroll
        for (int i = 1; i < 4; i++) {
          unsigned r[8];
          hmul2(r[1], r[5], hb[1], hb[5], ha[i], hs[i]);
          put2(4 + i, r[1], r[5]);
          hmul2(r[2], r[6], hb[2], hb[6], ha[i], hs[i]);
          put2(8 + i, r[2], r[6]);
          hmul2(r[3], r[7], hb[3], hb[7], ha[i], hs[i]);
          put2(12 + i, r[3], r[7]);
        }
        unsigned r1, r2, r3, r4;
        const unsigned s128 = pkh2(e128.s);
        hmul2(r1, r2, ha[1], ha[2], hb[4], s128);
        hmul2(r3, r4, ha[3], ha[3], hb[4], s128);
        put2(1, ha[1], r1);
        put2(2, ha[2], r2);
        put2(3, ha[3], r3);
      }
      MEMBAR();
      f16x8 fa[8], fb[8];
#pragma unroll
      for (int k = 0; k < 8; k++) fb[k] = *(const LAS f16x8*)(rb_ + L_TBL + 32 * k);
      SCHED();
      if (wave >= 4) STAMP(11);
      asm volatile("s_waitcnt vmcnt(0) ; first loads" : "+{v1}"(epoch), "+{v[4:5]}"(gc_), "+{v16}"(gdd)::"memory");
      const float cre = (wave < 4 ? blo(gc_.x) : bhi(gc_.x)) * dt, cim = (wave < 4 ? blo(gc_.y) : bhi(gc_.y)) * dt;
      const float dsk = blo(gdd);
      const float cmx = fmaxf(fmaxf(fmaxf(fabsf(blo(gc_.x)), fabsf(bhi(gc_.x))), fmaxf(fabsf(blo(gc_.y)), fabsf(bhi(gc_.y)))) * dt, fabsf(dsk) * 0.0078125f);
      const unsigned ce = min(max(wave_max_u(__float_as_uint(cmx)) >> 23, 40u), 250u);
      const float csc = __uint_as_float((261u - ce) << 23);
      f32x16 acc;
      unsigned have;
      auto partial = [&]() {
        MEMBAR();
#pragma unroll
        for (int k = 0; k < 4; k++) {
          const f32x4 o = *(const LAS f32x4*)(pa + k * 1024);
#pragma unroll
          for (int i = 0; i < 4; i++) acc[4 * k + i] = o[i];
        }
      };
#define KSTEP(k) SCHED(); acc = __builtin_amdgcn_mfma_f32_32x32x16_f16(fa[k], fb[k], acc, 0, 0, 0); SCHED()
      {
        const cz e1 = mkz(cexp_p2(1.f, ar, ph, pl)), e512 = mkz(cexp_p2(512.f, ar, ph, pl));
        const cz e2 = csq(e1), e1024 = csq(e512), e256 = csq(e128), e4 = csq(e2);
        const f32x2 w0 = {cre * csc, -cim * csc};
        f32x2 A[3], B[3], C[3], D[3], E[3];
        unsigned a[3], b[3], c[3], d[3], e[3];
        cmulc3(A, a, w0, e8, w0, e1, w0, e256);
        put2(32 + 0, a[0], pkh2(w0));
        put1(32 + 11, a[1]);
        put1(32 + 15, a[2]);
        cmulc3(B, b, w0, e2, A[0], e256, A[0], e512);
        put1(32 + 10, b[0]);
        put2(32 + 4, b[1], a[2]);
        cmulc3(C, c, w0, e4, A[0], e1024, A[1], e2);
        put1(32 + 9, c[2]);
        cmulc3(D, d, B[1], e512, B[1], e1024, B[0], e256);
        put2(32 + 5, b[1], d[0]);
        put2(32 + 8, c[0], d[1]);
        put1(32 + 14, d[2]);
        cmulc3(E, e, C[0], e256, A[1], e4, B[2], e1024);
        put1(32 + 13, e[0]);
        put2(32 + 3, e[1], c[1]);
        put2(32 + 7, e[0], e[2]);
        SCHED();
        if (wave >= 4) {
          have = pf[ch];
          partial();
        }
        SCHED();
        cmulc3(A, a, B[0], e4, C[0], e512, C[2], e4);
        put2(32 + 2, a[0], a[1]);
        put2(32 + 1, a[2], b[2]);
        cmulc3(B, b, D[0], e1024, D[2], e4, E[0], e512);
        put2(32 + 12, e[0], b[0]);
        put2(32 + 6, b[1], b[2]);
      }
      MEMBAR();
#pragma unroll
      for (int k = 0; k < 8; k++) fa[k] = *(const LAS f16x8*)(rb_ + L_TBL + 32 * k);
      SCHED();
      if (wave < 4) {
        acc = f32x16{};
        KSTEP(0);
        *(LAS v4u*)(a_kn + L_KN) = v4u{0u, 0u, 0u, 0u};
        KSTEP(1);
        tx = tag_bits(__builtin_amdgcn_readfirstlane(epoch) + 1u) ^ 0x40004u;
        asm volatile("" : "+s"(tx));
        KSTEP(2);
        asm volatile("" : "+{v7}"(op[0]), "+{v8}"(op[1]), "+{v9}"(op[2]), "+{v10}"(op[3]), "+{v11}"(op[4]));
        float d0 = __builtin_amdgcn_inverse_ballot_w64(1ull) ? dsk * csc * 0.5f : 0.f;
        unsigned one = 1u;
        PIN(d0); PIN(one);
#pragma unroll
        for (int k = 3; k < 8; k++) acc = __builtin_amdgcn_mfma_f32_32x32x16_f16(fa[k], fb[k], acc, 0, 0, 0);
        SCHED();
        acc[8] += d0;
        STAMP(2);
#pragma unroll
        for (int k = 0; k < 3; k++) *(LAS f32x4*)(pa + k * 1024) = f32x4{acc[4 * k], acc[4 * k + 1], acc[4 * k + 2], acc[4 * k + 3]};
        *(LAS float*)(pa + 3072) = acc[12];
        MEMBAR();
        *(volatile LAS unsigned*)a_sp = one;
        STAMP(11);
        __builtin_amdgcn_s_setprio(0);
      }
      asm volatile("" : "+s"(wave));
      if (wave >= 4) {
        if (RARE(__builtin_amdgcn_readfirstlane(have) == 0u)) {
#pragma unroll 1
          for (int sp = 0; sp < SPIN_CAP * 16 && pf[ch] == 0u; sp++) {}
          partial();
          PIN(acc);
        }
        v4u q[4];
        unsigned kinv = (ce - 6u) << 23;
#define KASM(k, pre) asm volatile(pre "\n\tv_mfma_f32_32x32x16_f16 %0, %1, %2, %0" : "+v"(acc) : "v"(fa[k]), "v"(fb[k])); SCHED()
        KASM(0, "s_nop 1");
        asm volatile("" : "+{v7}"(op[0]), "+{v8}"(op[1]), "+{v9}"(op[2]));
        KASM(1, "");
        asm volatile("" : "+{v10}"(op[3]), "+{v11}"(op[4]), "+{v12}"(op[5]));
        KASM(2, "");
        asm volatile("" : "+{v13}"(op[6]), "+{v14}"(top), "+{v15}"(kinv));
        KASM(3, "");
        KASM(4, "");
        KASM(5, "");
        KASM(6, "");
        KASM(7, "");
#pragma unroll
        for (int k = 0; k < 4; k++) q[k] = *(const LAS v4u*)(a_q + k * 1024);
        SCHED();
        asm volatile("s_nop 11" : "+v"(acc));
        STAMP(2);
        const unsigned k8 = pkh(acc[11], acc[12]);
        *(LAS u16*)(a_k8 + 64) = (u16)k8;
        *(LAS u16*)a_k8 = (u16)(k8 >> 16);
        *(LAS unsigned*)a_k4 = pkh(acc[9], acc[10]);
        const v4u c0 = {pkh(acc[1], acc[2]), pkh(acc[3], acc[4]), pkh(acc[5], acc[6]), pkh(acc[7], acc[8])};
        *(LAS v4u*)a_kn = c0;
        *(LAS v4u*)(a_kn + KCOPY) = v4u{pkh(acc[0], acc[1]), pkh(acc[2], acc[3]), pkh(acc[4], acc[5]), pkh(acc[6], acc[7])};
        const unsigned kb = (unsigned)__builtin_amdgcn_readlane((int)pkh(acc[0], acc[9]), 63);
        *(LAS u16*)a_sp = (u16)__builtin_amdgcn_perm(kb, c0.w, sel_sp);
        STAMP(4);
        __builtin_amdgcn_s_setprio(M_PRIO);
        asm volatile("Lk_conv:\n\ts_cbranch_execz Lk_fetch\n" CONV_ASM_TEXT
                     : CONV_ASM_PRE(q)
                     : "{v14}"(top), "{v15}"(kinv), "{v7}"(op[0]), "{v8}"(op[1]), "{v9}"(op[2]), "{v10}"(op[3]), "{v11}"(op[4]), "{v12}"(op[5]),
                       "{v13}"(op[6])
                     : CONV_ASM_CLOBBERS);
        STAMP(6);
        __builtin_amdgcn_s_setprio(0);
      }
      asm volatile("" : "+s"(wave));
      if (wave < 4) {
        asm volatile("s_branch Lk_fetch\n\t.p2align 12\nLk_fetch:\n\ts_cbranch_execz Lk_ln2");
        const unsigned usrc = op[0], udst = op[1], udst2 = op[2], yb = op[3];
        int yo = op[4];
        wait_until((unsigned)t0_, T_U);
        __builtin_amdgcn_s_setprio(H_PRIO);
        fetch_u<3, true>(rs, usrc, {udst, udst + 1024, udst2}, 0, tx);
        STAMP(4);
        BAR();
        __builtin_amdgcn_s_sleep(SLEEP_U7);
        fetch_u<1, false>(rs, usrc, {udst2 + 1024}, 12, tx);
        STAMP(6);
#pragma unroll
        for (int m = 0; m < 4; m++) {
          BAR();
          if (m == 0) yo ^= *(const LAS int*)(L_MISC + 72);
          const v2u r0 = ldtr((const LAS char*)(yb + L_Y + m * 1024)), r1 = ldtr((const LAS char*)(yb + L_Y + m * 1024 + 256));
          __builtin_amdgcn_raw_buffer_store_b128(v4u{r0.x, r0.y, r1.x, r1.y}, rs, yo, m * 2048, SC1);
        }
        __builtin_amdgcn_s_setprio(0);
        if (EXPW == 2) { STAMP(12); DRAIN(); STAMP(13); }
      }
    }
    asm volatile("" : "+s"(wave));
    if (wave == 8) {
      unsigned pw[PF_N], epoch, wb;
      v4u wa;
      if (S0_PRIO || loc >= 16) __builtin_amdgcn_s_setprio(3);
      asm volatile("s_waitcnt vmcnt(0) ; first loads" : "={v1}"(epoch), "={v[2:5]}"(wa), "={v6}"(wb)::"memory");
      int lane;
      asm volatile("v_mbcnt_lo_u32_b32 %0, -1, 0\n\tv_mbcnt_hi_u32_b32 %0, -1, %0" : "=v"(lane));
      {
        int d = 0;
        unsigned c0 = (unsigned)__builtin_amdgcn_s_memtime();
        int ya = ybase + (bid & 8) * 1024 + lane * 16;
#pragma unroll 1
        for (int h = 0; h < 2; h++) {
#pragma unroll
          for (int i = 0; i < 8; i++) __builtin_amdgcn_raw_buffer_store_b128(sent4, rs, ya + (i & 3) * 1024, (i & 4) * 1024, SC1);
          ya ^= 8192;
          if (loc >= 16) {
            DRAIN();
            const unsigned c1 = (unsigned)__builtin_amdgcn_s_memtime();
            d = (int)min(c1 - c0, 2000u) - d;
            c0 = c1;
          }
        }
        st2(v2u{epoch + 1u, (unsigned)(((loc ^ (bid >> 2)) & 1) ? d : -d)}, rs, loc >= 16 ? WS_VOTE + (cg - 128) * 64 : OOB);
      }
      st4(sent4, rs, (loc < 16 && lane < 8) ? WS_RREC + (lane & 4) * 2048 + rb * 64 + (lane & 3) * 16 : OOB);
      st4(sent4, rs, cg < 48 ? WS_XSEL + (cg & 15) * 8192 + (cg >> 4) * 1024 + lane * 16 : OOB);
      SCHED();
      auto warm = [&]() {
        const GAS char* wb = (const GAS char*)(loc < 9 ? kb_[0] : kb_[1]) + xcd * 1152;
        const unsigned s0 = (3u - (unsigned)((u64)wb >> 7)) & 7u, ro_ = ((loc < 9 ? loc : loc - 9) * 64 + lane) * 9216;
        pw[0] = *(const GAS unsigned*)(wb + (ro_ + s0 * 128));
        pw[1] = *(const GAS unsigned*)(wb + (ro_ + (s0 ? s0 : 8u) * 128));
        SCHED();
      };
      if (loc < 16) warm();
      if (S0_PRIO == 2) __builtin_amdgcn_s_setprio(0);
      SCHED();
      {
#pragma unroll
        for (int k = 0; k < 4; k++) *(unsigned*)(smem + L_WR + (lane + 64 * k) * 4) = wa[k];
        *(unsigned*)(smem + L_WR + (224 + lane) * 4) = wb;
        *(LAS unsigned*)(L_MISC + 128 + (lane & 31) * 4) = 0x3f803f80u;
        float ws_ = lane >= 32 ? blo(wb) + bhi(wb) : 0.f;
#pragma unroll
        for (int k = 0; k < 4; k++) ws_ += blo(wa[k]) + bhi(wa[k]);
        ws_ = wave_sum(ws_);
        if (lane == 0) *(float*)(smem + L_MISC + 64) = ws_;
      }
      SCHED();
      __builtin_amdgcn_s_setprio(S_PRIO);
      asm volatile("s_waitcnt vmcnt(%0)" ::"n"(PF_N) : "memory");
      STAMP(4);
      BAR();
      STAMP(5);
      const unsigned t1 = now();
      st2(v2u{epoch + 1u, t1}, rs, lane == 0 ? WS_INIT + cg * 64 : OOB);
      v2u vote;
#pragma unroll 1
      for (int sp = 0; sp < SPIN_CAP; sp++) {
        MEMBAR();
        vote = ld2(rs, WS_VOTE + (lane & 15) * 64);
        if (__all(vote.x == epoch + 1u)) break;
      }
      const unsigned flip = wave_sum((float)(int)vote.y) > 0.f;
      *(LAS unsigned*)(L_MISC + 72) = flip << 13;
      if (loc < 16) {
        const GAS char* xs = p.x + (unsigned)((16 * rb + (lane >> 3)) * 1152 + ((lane & 7) ^ (lane >> 3)) * 16);
#pragma unroll 1
        for (int i = 0; i < 9; i++) {
          __builtin_amdgcn_global_load_lds((const GAS unsigned*)(xs + i * 128), (LAS unsigned*)(L_XROW + i * 2048), 16, 0, 0);
          __builtin_amdgcn_global_load_lds((const GAS unsigned*)(xs + i * 128 + 9216), (LAS unsigned*)(L_XROW + i * 2048 + 1024), 16, 0, 0);
        }
      }
      v2u fl[3];
      unsigned span;
      auto look = [&]() {
        MEMBAR();
#pragma unroll
        for (int k = 0; k < 3; k++) fl[k] = ld2(rs, WS_INIT + min(lane + 64 * k, NCONV - 1) * 64);
      };
      auto seen = [&]() {
        if (!__all(fl[0].x == epoch + 1u && fl[1].x == epoch + 1u && fl[2].x == epoch + 1u)) return false;
        const unsigned t = t1 - 4096u;
        const unsigned last = t + span + min(wave_max_u(max(max(fl[0].y - t, fl[1].y - t), fl[2].y - t)), 4096u + 300u);
        *(volatile LAS unsigned*)(L_MISC + 76) = last | 1u;
        if (loc == 16) st2(v2u{epoch + 1u, (last & ~1u) | flip}, rs, WS_TIME + xcd * 128);
        return true;
      };
      if (loc >= 16) warm();
      wait_until(t1, T_LOOK);
      look();
      STAMP(6);
      BAR();
      BAR();
      span = (now() - t1) * 85u >> 6;
      DRAIN();
      bool ok = seen();
      BAR();
      BAR();
#pragma unroll 1
      for (int sp = 0; sp < SPIN_CAP && !ok; sp++) {
        RETRY(7);
        look();
        ok = seen();
      }
      __builtin_amdgcn_s_waitcnt(0x0f70);
      if (bid == 136) st1(epoch + 1u, rs, WS_EPOCH);
#pragma unroll
      for (int i = 0; i < PF_N; i++) asm volatile("" ::"v"(pw[i]));
      __builtin_amdgcn_s_setprio(0);
      SCHED();
      asm volatile("s_branch Lk_ln2\n\t.p2align 12\nLk_ln2:\n\ts_cbranch_execz Lk_back");
    }
    asm volatile("; first loads (62)");
    SCHED();
    STAMP(7);
    if (loc >= 16) { STAMP_FLUSH(); return; }
    int lane;
    asm volatile("v_mbcnt_lo_u32_b32 %0, -1, 0\n\tv_mbcnt_hi_u32_b32 %0, -1, %0" : "=v"(lane));
    const int cl = lane & 15, g = lane >> 4, tid = wave * 64 + lane;
    v2u fg = *(volatile LAS v2u*)(L_MISC + 72);
    int o0 = ysrc + g * 2 * GSTR + (cl & 7) * 16 + (cl & 8);
    unsigned tn = now() - ylag + Y_EARLY;
    PIN(o0);
    asm volatile("" : "+s"(tn));
    SCHED();
    if (RARE(__builtin_amdgcn_readfirstlane(fg.y) == 0u)) {
#pragma unroll 1
      for (int sp = 0; sp < SPIN_CAP * 64; sp++) {
        fg = *(volatile LAS v2u*)(L_MISC + 72);
        if (__builtin_amdgcn_readfirstlane(fg.y)) break;
      }
    }
    o0 ^= fg.x;
    PIN(o0);
    const unsigned last = __builtin_amdgcn_readfirstlane(fg.y);
    int late = (int)(tn - last);
    v2u y[4];
    auto gather = [&]() {
      MEMBAR();
#pragma unroll
      for (int i = 0; i < 4; i++) y[i] = __builtin_amdgcn_raw_buffer_load_b64(rs, o0, ((i & 1) + (i >> 1) * 8) * GSTR, SC1);
    };
    if (late >= 0) gather();
    SCHED();
    ln2_operands(lane);
    constexpr int L_TAB = L_RED + 2304;
    const int flip = __builtin_amdgcn_readfirstlane(fg.x);
    int near = side ^ flip, far = near ^ 8192;
    v4u rx = {(unsigned)(kw + WS_XSEL), (unsigned)((kw + WS_XSEL) >> 32) & 0xffffu, 8 * 16384, 0x00020000};
    asm volatile("" : "+s"(rx), "+s"(far), "+s"(near));
    unsigned l_sr, l_h, l_t, krow, hoff, half, a_pub, a_cnt;
    int a_key;
    float wsum;
    auto addresses = [&]() {
      l_h = L_XROW + wave * 2048 + (lane & 31) * 4;
      l_t = L_TAB + (lane >> 5) * 16;
      half = (lane >> 1) & 16;
      a_pub = wave * 128 + (lane & 31) * 4;
      a_cnt = L_MISC + 32;
      PIN(l_h); PIN(l_t); PIN(half); PIN(a_pub); PIN(a_cnt);
    };
    if (wave == 1) {
      addresses();
      l_sr = L_RED + cl * 16;
      krow = SEQ - 1 - (16 * rb + cl);
      hoff = cl * 128 + (cl & 7) * 16;
      a_key = lane < 32 ? WS_RREC + g * 8192 + (16 * rb + cl) * 4 : OOB;
      wsum = *(const float*)(smem + L_MISC + 64);
      PIN(l_sr); PIN(krow); PIN(hoff); PIN(a_key); PIN(wsum);
    }
    asm volatile("" : "+s"(wave));
    u64 m_odd = 0xaaaaaaaaaaaaaaaaull, m_up = 0xccccccccccccccccull;
    asm volatile("" : "+s"(m_odd), "+s"(m_up));
    const bool odd = __builtin_amdgcn_inverse_ballot_w64(m_odd), upper = __builtin_amdgcn_inverse_ballot_w64(m_up);
    v4u hv[2];
    asm volatile("" : "+s"(late));
    if (late < 0) {
      wait_until(last, ylag);
      gather();
    }
    SCHED();
    {
      f32x4 s = {0.f, 0.f, 0.f, 0.f}, q = {0.f, 0.f, 0.f, 0.f}, h[4];
      auto add = [&]() {
#pragma unroll
        for (int i = 0; i < 4; i++) {
          h[i] = widen(unit, y[i], xf[i]);
          SCHED();
        }
      };
      add();
      POLL(2, valid8(v4u{y[0].x, y[0].y, y[1].x, y[1].y}, v4u{y[2].x, y[2].y, y[3].x, y[3].y}), (gather(), add()));
      SCHED();
#pragma unroll
      for (int i = 0; i < 2; i++) {
        hv[i] = v4u{pkbf(h[2 * i][0], h[2 * i][1]), pkbf(h[2 * i][2], h[2 * i][3]), pkbf(h[2 * i + 1][0], h[2 * i + 1][1]), pkbf(h[2 * i + 1][2], h[2 * i + 1][3])};
        q = mfma_bf(hv[i], hv[i], q);
        s = mfma_bf(cw[i], hv[i], s);
        SCHED();
      }
      const float qa = odd ? q[1] : q[0], qb = odd ? q[3] : q[2];
      s[2] = upper ? qb : qa;
      *(LAS f32x4*)l_sw = s;
      SCHED();
    }
    STAMP(8);
    SCHED();
    asm volatile("s_waitcnt lgkmcnt(%0)\n\ts_barrier" ::"n"(HV_LATE ? 0 : 2) : "memory");
    SCHED();
    if (wave == 1) {
      float m, s;
      const f32x4 t = sum9((const LAS char*)l_sr);
      ln_round(t[0], t[2], m, s);
      const float rinv = __builtin_amdgcn_rcpf(s);
      const unsigned b = pkbf(0.f, fmaf(-m, wsum, t[1]) * rinv);
      const unsigned okey = (b ^ (((int)b >> 31 & 0x7fff0000u) | 0x80000000u)) | krow;
      st1(okey, rs, a_key);
      SCHED();
      KSTAMP(9);
      *(LAS v4u*)l_x = hv[0];
      *(LAS v4u*)l_x2 = hv[1];
      int v = __builtin_amdgcn_update_dpp((int)okey, (int)okey, 0x124, 0x2, 0xf, false);
      v = __builtin_amdgcn_update_dpp(v, (int)okey, 0x128, 0x4, 0xf, false);
      v = __builtin_amdgcn_update_dpp(v, (int)okey, 0x12c, 0x8, 0xf, false);
      unsigned c = ((unsigned)v > okey) + (dppu<0x121, 0xf>(v) > okey) + (dppu<0x122, 0xf>(v) > okey) + (dppu<0x123, 0xf>(v) > okey);
      const v2u c1 = __builtin_amdgcn_permlane32_swap(c, c, false, false);
      c = c1.x + c1.y;
      const v2u c2 = __builtin_amdgcn_permlane16_swap(c, c, false, false);
      *(LAS v4u*)(L_TAB + (c2.x + c2.y) * 16) = v4u{__float_as_uint(m), okey, __float_as_uint(rinv), hoff};
    }
    asm volatile("" : "+s"(wave));
    if (wave != 1) {
      addresses();
      a_key = WS_RREC + near + (tid - (wave ? 64 : 0)) * 16;
      PIN(a_key);
      SCHED();
      *(LAS v4u*)l_x = hv[0];
      *(LAS v4u*)l_x2 = hv[1];
      wait_until(last, T_KEY);
    }
    BAR();
    v4u key;
    if (wave != 1) key = ld4(rs, a_key);
    STAMP(1);
    SCHED();
    unsigned nsel = 0;
#pragma unroll 1
    for (int rd = 0; rd < 4; rd++) {
      const v4u e = *(const LAS v4u*)l_t;
      unsigned hd = *(const LAS unsigned*)(l_h ^ e.w);
      const unsigned ka = __builtin_amdgcn_readlane(e.y, 0), kb = __builtin_amdgcn_readlane(e.y, 32);
      unsigned wd = nosent(ln_apply(hd, __uint_as_float(e.x), __uint_as_float(e.z)));
      hd = nosent(hd);
      PIN(wd); PIN(hd);
      SCHED();
      if (wave != 1) {
        POLL(3, valid4(key), key = ld4(rs, a_key));
        KSTAMP(10);
        u64 b[8];
#pragma unroll
        for (int i = 0; i < 4; i++) {
          b[2 * i] = __ballot(key[i] > ka);
          b[2 * i + 1] = __ballot(key[i] > kb);
        }
        asm volatile("" : "+s"(b[0]), "+s"(b[1]), "+s"(b[2]), "+s"(b[3]), "+s"(b[4]), "+s"(b[5]), "+s"(b[6]), "+s"(b[7]));
        const unsigned ca = (__popcll(b[0]) + __popcll(b[2])) + (__popcll(b[4]) + __popcll(b[6]));
        const unsigned cb = (__popcll(b[1]) + __popcll(b[3])) + (__popcll(b[5]) + __popcll(b[7]));
        asm volatile("s_mov_b64 exec, 1\n\tds_add_u32 %1, %0\n\ts_mov_b64 exec, -1" ::"v"(ca | (cb << 16)), "v"(a_cnt) : "memory");
      }
      unsigned cs, rank, po;
      asm volatile(
          "s_waitcnt lgkmcnt(0)\n\ts_barrier\n\tds_read_b32 %0, %3\n\ts_waitcnt lgkmcnt(0)\n\t"
          "v_bfe_u32 %1, %0, %4, 16\n\tv_lshl_add_u32 %2, %1, 14, %5\n\t"
          "buffer_store_dword %6, %2, %7, %8 offen sc1\n\tbuffer_store_dword %6, %2, %7, %9 offen sc1"
          : "=&v"(cs), "=&v"(rank), "=&v"(po)
          : "v"(a_cnt), "v"(half), "v"(a_pub), "v"(wd), "s"(rx), "s"(far), "s"(near)
          : "memory");
      const int ph = rank < 8u ? WS_XSEL + po + (((rank & 4) << 11) ^ flip) : OOB - 4096;
      st1(hd, rs, ph + 1152);
      st1(e.y, rs, a_pub == 0 ? ph + 2304 : OOB);
      const unsigned cu = __builtin_amdgcn_readfirstlane(cs);
      nsel += ((cu & 0xffffu) < 8u) + ((cu >> 16) < 8u);
      if ((cu >> 16) >= 8u) break;
      l_t += 32;
      a_cnt += 4;
    }
    STAMP(3);
#pragma unroll
    for (int i = 0; i < 2; i++) {
      const unsigned k = 8 * i + (lane >> 3);
      const v4u e = *(const LAS v4u*)(L_TAB + k * 16);
      const v4u hh = *(const LAS v4u*)((L_XROW + wave * 2048 + (lane & 7) * 16) ^ e.w);
      v4u o;
#pragma unroll
      for (int c = 0; c < 4; c++) o[c] = addbf(hh[c], ln_apply(hh[c], __uint_as_float(e.x), __uint_as_float(e.z)));
      st4(o, ro, k < nsel ? OOB : (16 * rb + (e.w >> 7)) * 1152 + wave * 128 + (lane & 7) * 16);
    }
    STAMP_FLUSH();
    return;
  }

  asm volatile("; conv set-up dropped" ::"{v[34:49]}"(k8));
  const bool ffn = loc < 27;
  LAS char* xw = (LAS char*)smem + wave * 4608 + (lane >> 3) * 288 + (lane & 7) * 16;
  LAS char* xr_ = (LAS char*)smem + wave * 4608 + ((lane >> 2) & 3) * 288 + (lane & 1) * 128 + g * 16 + (lane & 2) * 4;
  unsigned rw = (lane & 3) == 0 ? L_RED + wave * 128 + lane * 2 : L_UIMG + lane * 8, r8 = L_RED + (lane & 28) * 4 + (lane >> 5) * 8, rr_ = r8 + (lane & 3) * 128;
  int uo = (wave < 8 ? WS_U : WS_U2 - 128 * GSTR) + (16 * wave + (lane >> 3)) * GSTR + tile * 128 + (lane & 7) * 16;
  unsigned sa = L_MISC + (lane & 4) * 8, sw = L_MISC + (lane & 60), keep = 0xfff8fff8u, ones = 0x3f803f80u;
  int mode = loc >= 30 ? 2 : 0, far8 = 8 * GSTR;
  PIN(xw); PIN(xr_); PIN(rw); PIN(rr_); PIN(r8); PIN(uo); PIN(sa); PIN(sw); PIN(keep);
  asm volatile("" : "+s"(ones), "+s"(mode), "+s"(far8));
  if (wave == 1) __builtin_amdgcn_s_setprio(3);
  SCHED();
  asm volatile("s_waitcnt lgkmcnt(0) ; (2) first loads" : "+{s33}"(sep), "+{v[2:5]}"(ga), "+{v[6:9]}"(gb), "+{v[10:13]}"(gc), "+{v[14:17]}"(gd) : "{v1}"(epoch) : "memory");
  const unsigned tag = sep + 1u;
  v4u tb = tag_bits(tag);
  asm volatile("" : "+s"(tb));
  SCHED();
  {
#pragma unroll 1
    do {
#define DOT_(s, q, x, cs, cq) "v_dot2_f32_bf16 " s ", " x ", %[ones], " cs "\n\tv_dot2_f32_bf16 " q ", " x ", " x ", " cq "\n\t"
#define RED_(s, q, how) "v_add_f32_dpp v18, " s ", " s " " how "\n\tv_add_f32_dpp v19, " q ", " q " " how "\n\ts_nop 0\n\t"
#define SUMS_(s, q, a, b, c, d) DOT_(s, q, a, "0", "0") DOT_(s, q, b, s, q) DOT_(s, q, c, s, q) DOT_(s, q, d, s, q)
      asm volatile("s_cmp_eq_u32 %[mode], 1\n\ts_cbranch_scc1 1f\n\ts_waitcnt vmcnt(1) ; first loads\n1:\n\t"
                   "ds_write_b128 %[xw], v[2:5]\n\t" SUMS_("v18", "v19", "v2", "v3", "v4", "v5")
                   "s_cbranch_scc1 2f\n\ts_waitcnt vmcnt(0)\n2:\n\t" T3_ASM
                   "ds_write_b128 %[xw], v[6:9] offset:128\n\t" SUMS_("v20", "v21", "v6", "v7", "v8", "v9")
                   RED_("v18", "v19", "row_half_mirror row_mask:0xf bank_mask:0x5") RED_("v20", "v21", "row_half_mirror row_mask:0xf bank_mask:0xa")
                   RED_("v18", "v19", "quad_perm:[1,0,3,2] row_mask:0xf bank_mask:0xf") RED_("v18", "v19", "quad_perm:[2,3,0,1] row_mask:0xf bank_mask:0xf")
                   "s_cmp_lg_u32 %[mode], 2\n\tds_write_b64 %[rw], v[18:19]\n\ts_cbranch_scc1 3f\n\t"
                   "global_load_dwordx4 v[10:13], v10, %[x] ; own\n\tglobal_load_dwordx4 v[14:17], v14, %[x] ; own\n3:"
                   : "+{v[10:13]}"(gc), "+{v[14:17]}"(gd) T3_OUT
                   : "{v[2:5]}"(ga), "{v[6:9]}"(gb), [mode] "s"(mode), [ones] "s"(ones), [xw] "v"(xw), [rw] "v"(rw), [x] "s"(kx)
                   : "v18", "v19", "v20", "v21", "scc", "memory");
      STAMP(10);
      if (wave >= L1_EARLY) { BAR(); STAMP(11); SCHED(); }
      asm volatile("" : "+s"(wave));
      f32x2 xt[2][4];
#pragma unroll
      for (int i = 0; i < 4; i++) {
        const v2u v = ldtr(xr_ + (i >> 1) * 64 + (i & 1) * 1152);
        xt[i >> 1][2 * (i & 1)] = f32x2{blo(v.x), bhi(v.x)};
        xt[i >> 1][2 * (i & 1) + 1] = f32x2{blo(v.y), bhi(v.y)};
      }
#pragma unroll
      for (int i = 0; i < 8; i++) PIN(xt[i >> 2][i & 3]);
      if (wave < L1_EARLY) { BAR(); STAMP(11); SCHED(); }
      asm volatile("" : "+s"(wave));
      if (wave == 1) {
        const f32x2 a = *(const LAS f32x2*)rr_, b = *(const LAS f32x2*)(rr_ + 512), e = *(const LAS f32x2*)(r8 + 1024);
        SCHED();
        const f32x2 h = a + b;
        float hs = h.x, hq = h.y;
        asm("s_nop 1\n\tv_add_f32_dpp %0, %0, %0 quad_perm:[1,0,3,2] row_mask:0xf bank_mask:0xf\n\tv_add_f32_dpp %1, %1, %1 quad_perm:[1,0,3,2] row_mask:0xf bank_mask:0xf\n\t"
            "s_nop 0\n\tv_add_f32_dpp %0, %0, %0 quad_perm:[2,3,0,1] row_mask:0xf bank_mask:0xf\n\tv_add_f32_dpp %1, %1, %1 quad_perm:[2,3,0,1] row_mask:0xf bank_mask:0xf"
            : "+v"(hs), "+v"(hq));
        float m, sd;
        ln_round(hs + e.x, hq + e.y, m, sd);
        const float rinv = __builtin_amdgcn_rcpf(sd);
        *(LAS float*)sw = rinv;
        *(LAS float*)(sw + 64) = -m * rinv;
      }
      SCHED();
      STAMP(7);
      BAR();
      STAMP(12);
      const LAS f32x4* st = (const LAS f32x4*)sa;
      const f32x4 r0 = st[0], c0 = st[4], r1 = st[1], c1 = st[5];
      const f32x2 rr[4] = {r0.xy, r0.zw, r1.xy, r1.zw}, cc[4] = {c0.xy, c0.zw, c1.xy, c1.zw};
#pragma unroll
      for (int i = 0; i < 2; i++) {
        v4u o;
#pragma unroll
        for (int k = 0; k < 4; k++) o[k] = (pkh2(__builtin_elementwise_fma(xt[i][k], rr[k], cc[k])) & keep) | tb[k];
        __builtin_amdgcn_raw_buffer_store_b128(o, rs, uo, i ? far8 : 0, SC1);
        STAMP(8 + i);
        SCHED();
      }
      if (mode != 2) break;
      mode = 1;
      asm volatile("" : "+s"(mode));
      uo += 16 * 128;
      asm volatile("s_waitcnt vmcnt(2) ; second tile\n\tv_mov_b64 v[2:3], v[10:11]\n\tv_mov_b64 v[4:5], v[12:13]\n\tv_mov_b64 v[6:7], v[14:15]\n\tv_mov_b64 v[8:9], v[16:17]"
                   : "={v[2:5]}"(ga), "={v[6:9]}"(gb) : "{v[10:13]}"(gc), "{v[14:17]}"(gd) : "memory");
      STAMP(5);
      SCHED();
    } while (true);
    __builtin_amdgcn_s_setprio(0);
    BAR();
    STAMP(1);
    if (!ffn) {
      if (D_DELAY && loc < 30) __builtin_amdgcn_s_sleep(D_DELAY);
      const GAS char* dn = (const GAS char*)kb_[2] + (size_t)xcd * 5184 * 128;
      unsigned pf[2];
#pragma unroll
      for (int i = 0; i < 2; i++) pf[i] = *(const GAS unsigned*)(dn + (unsigned)min((i * 5 + loc - 27) * NT + tid, 5183) * 128u);
#pragma unroll
      for (int i = 0; i < 2; i++) asm volatile("" ::"v"(pf[i]));
      STAMP_FLUSH();
      return;
    }
  }
  v8u FW[2][4];
  v4u FD[2][4];
  v4u Dn[8];
  constexpr int F_GU = 0, F_TIME = 1280, F_ALL = 2048, F_RED = 16384, RSTR = ADDTID ? 4608 : 4096;
    v4u W[2][8];
    const GAS char* wp = (const GAS char*)((lane & 8) ? kb_[1] : kb_[0]) + (unsigned)((64 * wave + 4 * g) * (FF * 2) + 128 * fj + 16 * (lane & 7));
    const rsrc_t rd = __builtin_amdgcn_make_buffer_rsrc((void*)kb_[2], 0, DM * FF * 2, 0x00020000);
    const int wdo = (64 * fj + 8 * g + 4 * (lane & 8)) * (DM * 2) + 128 * wave + 16 * (lane & 7);
    if (W_DELAY) __builtin_amdgcn_s_sleep(W_DELAY);
    st4(sent4, rs, part_at(fj * 9216 + tid * 16));
    st4(sent4, rs, part_at(fj * 9216 + tid * 16) + 8192);
    SCHED();
#pragma unroll
    for (int j = 0; j < 16; j++) W[j >> 3][j & 7] = *(const GAS v4u*)(wp + (16 * (j >> 2) + (j & 3)) * (FF * 2));
    SCHED();
    STAMP(11);
  asm volatile("s_waitcnt vmcnt(16)" ::: "memory");
  if (wave >= 4) BAR();
  asm volatile("" : "+s"(wave));
  const int tk = fj / 9, cb = fj - tk * 9;
  const int fcol = cb * 64 + 8 * (cl & 7) + 4 * (cl >> 3) + (g >> 1) + 2 * (g & 1);
  int a_x = slot_at(lane >> 3) + side + wave * 128 + (lane & 7) * 16, a_f = slot_at(tk) + side + (fcol >> 1) * 4;
  int a_po[2], a_pi[2];
#pragma unroll
  for (int i = 0; i < 2; i++) {
    a_po[i] = part_at(fj * 9216 + ((2 * (g >> 1) + i) * DM + 64 * wave + 4 * cl) * 4) + ((g & 1) << 13);
    a_pi[i] = part_at(((tid >> 4) + 36 * i) * 9216 + ((tk & 3) * DM + cb * 64 + cl * 4) * 4) + side;
    PIN(a_po[i]); PIN(a_pi[i]);
  }
  unsigned l_xw = F_ALL + wave * 1152 + (lane >> 3) * 144 + (lane & 7) * 16, l_xr = F_ALL + wave * 1152 + (lane & 7) * 144 + g * 16;
  unsigned l_rw = F_RED + wave * RSTR;
  asm volatile("" : "+s"(l_rw));
  int sidx = (lane & 8) ? 0xeeee : 0x4444;
  unsigned l_rg = F_RED + (2 * wave + (g & 1)) * 288 + (lane & 32) * 4 + (lane & 8) * 8 + (lane & 7) * 4, l_ru = l_rg + 32;
  unsigned l_gw = F_GU + (((lane >> 1) & 4) + (wave & 1) * 2 + (g & 1)) * 144 + ((lane & 7) * 8 + (wave >> 1) + (lane >> 5) * 4) * 2;
  unsigned l_gr = F_GU + ((cl & 4) + ((cl + (cl >> 2 & 2)) & 3)) * 144 + g * 16, l_s = F_ALL + (wave == 8 ? 0 : wave + 1) * 256 + lane * 4;
  int a_k = slot_at(tk) + side + 2304;
  PIN(a_k); PIN(a_x); PIN(a_f); PIN(l_xw); PIN(l_xr); PIN(sidx); PIN(l_rg); PIN(l_ru); PIN(l_gw); PIN(l_gr); PIN(l_s);
#pragma unroll
    for (int ks = 0; ks < 2; ks++)
#pragma unroll
      for (int c = 0; c < 4; c++) {
#pragma unroll
        for (int i = 0; i < 8; i++)
          FW[ks][c][i] = __builtin_amdgcn_perm(W[ks][i | 1][(c >> 1) + 2 * (i & 1)], W[ks][i & 6][(c >> 1) + 2 * (i & 1)], (c & 1) ? 0x07060302u : 0x05040100u);
        PIN(FW[ks][c]);
      }
  SCHED();
  if (wave < 4) BAR();
  STAMP(12);
  st1(tag, rs, tid == 0 ? WS_INIT + (NCONV + fj) * 64 : OOB);
  v2u ts = ld2(rs, WS_TIME + xcd * 128);
  SCHED();
#pragma unroll
  for (int j = 0; j < 8; j++) Dn[j] = __builtin_amdgcn_raw_buffer_load_b128(rd, wdo, j * (DM * 2), 0);
  SCHED();
  STAMP(2);
#pragma unroll 1
  for (int sp = 0; sp < SPIN_CAP && __builtin_amdgcn_readfirstlane(ts.x) != tag; sp++) {
    __builtin_amdgcn_s_sleep(4);
    MEMBAR();
    ts = ld2(rs, WS_TIME + xcd * 128);
  }
  const unsigned due = __builtin_amdgcn_readfirstlane(ts.y);
  {
    const int flip = (due & 1u) << 13;
    a_x ^= flip; a_f ^= flip; a_k ^= flip; a_po[0] ^= flip; a_po[1] ^= flip; a_pi[0] ^= flip; a_pi[1] ^= flip;
    PIN(a_x); PIN(a_f); PIN(a_k); PIN(a_po[0]); PIN(a_po[1]); PIN(a_pi[0]); PIN(a_pi[1]);
  }
  unsigned w2 = 0u, h2 = 0u, fkey = 0u, f0 = 0u, f1 = 0u;
  auto flook = [&]() {
    MEMBAR();
    f0 = ld1(rs, WS_INIT + (NCONV + lane) * 64);
    f1 = ld1(rs, WS_INIT + (NCONV + 64 + (lane & 7)) * 64);
    w2 = ld1(rs, a_f);
    h2 = ld1(rs, a_f + 1152);
    fkey = ld1(rs, a_k);
  };
  {
    DRAIN();
    STAMP(10);
    int late = (int)(now() - due) - (T_SEL - T_LATE);
    v4u xv = sent4;
    if (late >= 0) xv = ld4(rs, a_x);
    SCHED();
#pragma unroll
    for (int j = 0; j < 8; j++)
#pragma unroll
      for (int i = 0; i < 2; i++) {
        const int lo = (int)Dn[j][i], hi = (int)Dn[j][2 + i];
        Dn[j][i] = (unsigned)__builtin_amdgcn_update_dpp(lo, hi, 0x128, 0xf, 0xc, false);
        Dn[j][2 + i] = (unsigned)__builtin_amdgcn_update_dpp(hi, lo, 0x128, 0xf, 0x3, false);
      }
#pragma unroll
    for (int ks = 0; ks < 2; ks++)
#pragma unroll
      for (int c = 0; c < 4; c++) {
#pragma unroll
        for (int i = 0; i < 4; i++)
          FD[ks][c][i] = __builtin_amdgcn_perm(Dn[2 * i + 1][2 * ks + (c >> 1)], Dn[2 * i][2 * ks + (c >> 1)], (c & 1) ? 0x07060302u : 0x05040100u);
        PIN(FD[ks][c]);
      }
    f32x4 a[4];
#pragma unroll
    for (int c = 0; c < 4; c++) { a[c] = f32x4{0.f, 0.f, 0.f, 0.f}; PIN(a[c]); }
    SCHED();
    STAMP(3);
    asm volatile("" : "+s"(late));
    if (late < 0) {
      wait_until(due, T_SEL);
      xv = ld4(rs, a_x);
    }
    POLL(4, valid4(xv), xv = ld4(rs, a_x));
    STAMP(6);
    *(LAS v4u*)l_xw = xv;
    MEMBAR();
    const v4u x0 = *(const LAS v4u*)l_xr, x1 = *(const LAS v4u*)(l_xr + 64);
    if (wave == 8) flook();
#pragma unroll
    for (int c = 0; c < 6; c++) {
      SCHED();
      if (c < 4) {
        a[c] = smfmac_bf(x0, FW[0][c], a[c], sidx);
        a[c] = smfmac_bf(x1, FW[1][c], a[c], sidx);
      }
      SCHED();
      if (c == 2) asm volatile("s_mov_b32 m0, %0\n\ts_nop 2" ::"s"(l_rw));
      if (c > 1)
        asm volatile("ds_write_addtid_b32 %0 offset:%4\n\tds_write_addtid_b32 %1 offset:%4 + 288\n\tds_write_addtid_b32 %2 offset:%4 + 576\n\t"
                     "ds_write_addtid_b32 %3 offset:%4 + 864" ::"v"(a[c - 2][0]), "v"(a[c - 2][1]), "v"(a[c - 2][2]), "v"(a[c - 2][3]), "n"((c - 2) * 1152) : "memory");
    }
  }
  BAR();
  STAMP(4);
  float fh = 0.f, fw = 0.f, sgm = 0.f;
  int oo = OOB;
  if (wave < 8) {
    f32x2 gv[4], uv[4];
#pragma unroll
    for (int w = 0; w < 4; w++) {
      gv[w] = f32x2{*(const LAS float*)(l_rg + 2 * w * RSTR), *(const LAS float*)(l_rg + (2 * w + 1) * RSTR)};
      uv[w] = f32x2{*(const LAS float*)(l_ru + 2 * w * RSTR), *(const LAS float*)(l_ru + (2 * w + 1) * RSTR)};
    }
    const float g8 = *(const LAS float*)(l_rg + 8 * RSTR), u8 = *(const LAS float*)(l_ru + 8 * RSTR);
    SCHED();
    f32x2 g2 = (gv[0] + gv[1]) + (gv[2] + gv[3]), u2 = (uv[0] + uv[1]) + (uv[2] + uv[3]);
    PIN(g2); PIN(u2);
    float gs = g2.x + g2.y;
    PIN(gs);
    gs += g8;
    PIN(gs);
    const float us = (u2.x + u2.y) + u8;
    const float gp = rbf(gs);
    const float gq = rbf(gp * __builtin_amdgcn_rcpf(1.0f + __expf(-gp)));
    *(LAS u16*)l_gw = (u16)pkbf(gq * rbf(us), 0.f);
  } else {
#pragma unroll 1
    for (int sp = 0; sp < SPIN_CAP && !__all(valid4(v4u{w2, h2, fkey, 0u}) && f0 == tag && f1 == tag); sp++) { RETRY(6); flook(); }
    fw = (fcol & 1) ? bhi(w2) : blo(w2);
    fh = (fcol & 1) ? bhi(h2) : blo(h2);
    const unsigned kb = fkey & 0xffff0000u;
    const float rv = __uint_as_float((kb & 0x80000000u) ? (kb & 0x7fffffffu) : (~kb & 0xffff0000u));
    sgm = rbf(__builtin_amdgcn_rcpf(1.0f + __expf(-rv)));
    oo = (SEQ - 1 - (int)(fkey & 0x7ffu)) * 1152 + fcol * 2;
  }
  PIN(fw); PIN(fh); PIN(sgm); PIN(oo);
  BAR();
  STAMP(5);
  {
    const v4u a0 = *(const LAS v4u*)l_gr, a1 = *(const LAS v4u*)(l_gr + 64);
    f32x4 o[4];
#pragma unroll
    for (int c = 0; c < 4; c++) {
      o[c] = f32x4{0.f, 0.f, 0.f, 0.f};
      o[c] = mfma_bf(a0, FD[0][c], o[c]);
      o[c] = mfma_bf(a1, FD[1][c], o[c]);
    }
#pragma unroll
    for (int i = 0; i < 2; i++) {
      v4u v;
#pragma unroll
      for (int c = 0; c < 4; c++) v[c] = nosent(__float_as_uint(o[c][i]));
      st4(v, rs, a_po[i]);
    }
  }
  STAMP(7);
  wait_until(now(), T_PART);
  {
    v4u a = ld4(rs, a_pi[0]), b = ld4(rs, a_pi[1]);
    POLL(5, valid8(a, b), (a = ld4(rs, a_pi[0]), b = ld4(rs, a_pi[1])));
    const v4u t = __builtin_bit_cast(v4u, __builtin_bit_cast(f32x4, a) + __builtin_bit_cast(f32x4, b));
    const v2u p0 = __builtin_amdgcn_permlane32_swap(t.x, t.y, false, false), p1 = __builtin_amdgcn_permlane32_swap(t.z, t.w, false, false);
    const v2u p2 = __builtin_amdgcn_permlane16_swap(__float_as_uint(__uint_as_float(p0.x) + __uint_as_float(p0.y)),
                                                    __float_as_uint(__uint_as_float(p1.x) + __uint_as_float(p1.y)), false, false);
    *(LAS float*)l_s = __uint_as_float(p2.x) + __uint_as_float(p2.y);
  }
  STAMP(8);
  BAR();
  if (wave == 8) {
    f32x2 t[4];
#pragma unroll
    for (int i = 0; i < 4; i++) t[i] = f32x2{*(const LAS float*)(l_s + i * 512), *(const LAS float*)(l_s + i * 512 + 256)};
    const float t8 = *(const LAS float*)(l_s + 2048);
    SCHED();
    f32x2 z = (t[0] + t[1]) + (t[2] + t[3]);
    PIN(z);
    const float y = rbf((z.x + t8) + z.y);
    __builtin_amdgcn_raw_buffer_store_b16((u16)pkbf(fh + rbf(fw + rbf(y * sgm)), 0.f), ro, oo, 0, SC1);
  }
  STAMP(9);
  STAMP_FLUSH();
  asm volatile("s_endpgm\n\t.p2align 12\n\t.skip (4096 - (Lk_conv - Lk_gen)) & 4095\nLk_gen:" ::: "memory");
}

}

extern "C" void kernel_launch(void* const* d_in, const int* in_sizes, int n_in, void* d_out, int out_size,
                              void* d_ws, size_t ws_size, hipStream_t stream) {
  (void)hipFuncSetAttribute((const void*)k_main, hipFuncAttributeMaxDynamicSharedMemorySize, LDS_BYTES);
  Params p;
  p.x = (const u16*)d_in[0];
  p.lar = (const u16*)d_in[1];
  p.aim = (const u16*)d_in[2];
  p.crl = (const u16*)d_in[3];
  p.cim = (const u16*)d_in[4];
  p.ldt = (const u16*)d_in[5];
  p.dskip = (const u16*)d_in[6];
  p.wr = (const u16*)d_in[8];
  p.wg = (const u16*)d_in[9];
  p.wu = (const u16*)d_in[10];
  p.wd = (const u16*)d_in[11];
  p.out = (u16*)d_out;
  const size_t skip = (size_t)(-(uintptr_t)d_ws) & ((2u << 20) - 1);
  p.ws = (char*)d_ws + (skip + WS_END <= ws_size ? skip : 0);
  k_main<<<256, NT, LDS_BYTES, stream>>>(p);
}
```

```cpp
#include <hip/hip_runtime.h>
#include <stdint.h>
#pragma clang diagnostic ignored "-Wint-to-pointer-cast"

namespace {

constexpr int SEQ = 2048, DM = 576, NM = 128, FF = 4608;
constexpr int NT = 576;
constexpr int NCONV = 144, NFFN = 72;
constexpr unsigned SENT = 0xFFFFFFFFu;
constexpr int SC1 = 16;
constexpr int SPIN_CAP = 1 << 16;
#define T_Y3 -10
#define T_KEY 85
#define T_SEL 165
#define CODE_BYTES 23936
#define H_PRIO 3
#ifndef M_PRIO
#define M_PRIO 1
#endif
#define S_PRIO 3
#define PF_N 2
#define SLEEP_U7 14
#define T_LOOK 90
#define X2_DELAY 4
#define Y_EARLY 10
#define T_U 170
#define L1_EARLY 5
#define WROWS 40
#define T_LATE 0
#define T_PART 28

typedef unsigned short u16;
typedef unsigned v4u __attribute__((ext_vector_type(4)));
typedef unsigned v2u __attribute__((ext_vector_type(2)));
typedef float f32x2 __attribute__((ext_vector_type(2)));
typedef float f32x4 __attribute__((ext_vector_type(4)));
typedef float f32x16 __attribute__((ext_vector_type(16)));
typedef _Float16 f16x2 __attribute__((ext_vector_type(2)));
typedef _Float16 f16x8 __attribute__((ext_vector_type(8)));
typedef __bf16 bf16x2 __attribute__((ext_vector_type(2)));
typedef __bf16 bf16x8 __attribute__((ext_vector_type(8)));
typedef __bf16 bf16x16 __attribute__((ext_vector_type(16)));
typedef unsigned v8u __attribute__((ext_vector_type(8)));
typedef __amdgpu_buffer_rsrc_t rsrc_t;

constexpr int WS_EPOCH = 0;
constexpr int WS_TIME = 2048;
constexpr int WS_INIT = 4096;
constexpr int WS_VOTE = 24576;
constexpr int WS_RREC = 32768;
constexpr int WS_XSEL = 65536;
constexpr int GSTR = 16384;
constexpr int WS_U = 2 << 20;
constexpr int WS_U2 = 196608;
constexpr int WS_Y = 4 << 20;
constexpr int WS_Y2 = WS_U2 + 16 * GSTR;
constexpr int WS_PART = WS_Y2 + 16 * GSTR;
constexpr int WS_END = 6 << 20;
static_assert(WS_PART + 2 * NFFN * 9216 + 16384 <= WS_U, "ws map");
__device__ __forceinline__ int part_at(int v) { return WS_PART + v + (v & ~8191); }
__device__ __forceinline__ int slot_at(int k) { return WS_XSEL + k * 16384; }
constexpr int OOB = 0x7ffffff0;

constexpr int WSCR = 16384;
constexpr int L_TBL = 3 * WSCR + 7936, TROW = 272, TSTR = 32 * TROW;
constexpr int L_KN = 4096;
constexpr int KCOPY = 1216;
constexpr int L_K4 = L_KN + 2 * KCOPY;
constexpr int L_K8 = L_K4 + 512;
constexpr int L_Y = 8192;
constexpr int L_DUMMY = 7680;
constexpr int L_XROW = 4 * WSCR;
constexpr int L_UIMG = 8 * WSCR;
constexpr int USTR = 4640;
constexpr int L_FILT = L_UIMG + 4 * USTR;
constexpr int L_MISC = L_FILT + 4096;
constexpr int L_WR = L_MISC + 256;
constexpr int L_RED = L_WR + 1152;
constexpr int LDS_BYTES = L_RED + 2 * 2304;
static_assert(L_KN + 224 * 16 <= L_DUMMY && L_TBL + 8 * TSTR <= L_UIMG && L_TBL + 31 * TROW < 65536 && 7 * TSTR < 65536, "lds map");
static_assert(LDS_BYTES <= 160 * 1024, "lds size");

#define MEMBAR() asm volatile("" ::: "memory")
#define BAR() do { __builtin_amdgcn_sched_barrier(0); asm volatile("s_waitcnt lgkmcnt(0)\n\ts_barrier" ::: "memory"); __builtin_amdgcn_sched_barrier(0); } while (0)
#define LAS __attribute__((address_space(3)))
#define DRAIN() asm volatile("s_waitcnt vmcnt(0)" ::: "memory")
#define LGKM0() asm volatile("s_waitcnt lgkmcnt(0)" ::: "memory")
#define PIN(x) asm volatile("" : "+v"(x))
#define RARE(c) __builtin_expect(!!(c), 0)
#define PC_(k)
#define POLL(k, ok, load) if (!__all(ok)) { RETRY(k); int sp_ = SPIN_CAP; _Pragma("unroll 1") do { MEMBAR(); load; PC_(k); sp_ = __all(ok) ? 0 : sp_ - 1; } while (sp_); }
#define SCHED() __builtin_amdgcn_sched_barrier(0)

#define STAMP(i) do {} while (0)
#define STAMP_INIT() do {} while (0)
#define T0_ASM "s_memrealtime s[34:35]\n\t"
#define STAMP_FLUSH() do {} while (0)

#define KSTAMP(i) STAMP(i)
#define RETRY(k) do {} while (0)

__device__ __forceinline__ float blo(unsigned v) { return __uint_as_float(v << 16); }
__device__ __forceinline__ float bhi(unsigned v) { return __uint_as_float(v & 0xffff0000u); }
__device__ __forceinline__ unsigned pkbf(float a, float b) {
  const f32x2 f = {a, b};
  return __builtin_bit_cast(unsigned, __builtin_convertvector(f, bf16x2));
}
__device__ __forceinline__ float rbf(float f) { return __uint_as_float(pkbf(0.f, f)); }
__device__ __forceinline__ unsigned pkh(float a, float b) {
  const f32x2 f = {a, b};
  return __builtin_bit_cast(unsigned, __builtin_convertvector(f, f16x2));
}
__device__ __forceinline__ unsigned nosent(unsigned v) { return min(v, 0xFFFFFFFEu); }
__device__ __forceinline__ bool valid4(v4u v) { return max(max(v.x, v.y), max(v.z, v.w)) != SENT; }

__device__ __forceinline__ bool valid8(v4u a, v4u b) { return max(max(max(max(a.x, a.y), a.z), max(max(a.w, b.x), b.y)), max(b.z, b.w)) != SENT; }

template <int CTRL, int RM> __device__ __forceinline__ float dppf(float v) {
  return __int_as_float(__builtin_amdgcn_update_dpp(0, __float_as_int(v), CTRL, RM, 0xf, false));
}
template <int CTRL, int RM> __device__ __forceinline__ unsigned dppu(unsigned v) {
  return (unsigned)__builtin_amdgcn_update_dpp(0, (int)v, CTRL, RM, 0xf, false);
}
__device__ __forceinline__ float wave_sum(float v) {
  v += dppf<0x111, 0xf>(v);
  v += dppf<0x112, 0xf>(v);
  v += dppf<0x114, 0xf>(v);
  v += dppf<0x118, 0xf>(v);
  v += dppf<0x142, 0xa>(v);
  v += dppf<0x143, 0xc>(v);
  return __int_as_float(__builtin_amdgcn_readlane(__float_as_int(v), 63));
}
__device__ __forceinline__ unsigned wave_max_u(unsigned v) {
  v = max(v, dppu<0x111, 0xf>(v));
  v = max(v, dppu<0x112, 0xf>(v));
  v = max(v, dppu<0x114, 0xf>(v));
  v = max(v, dppu<0x118, 0xf>(v));
  const unsigned a = (unsigned)__builtin_amdgcn_readlane((int)v, 15), b = (unsigned)__builtin_amdgcn_readlane((int)v, 31);
  const unsigned c = (unsigned)__builtin_amdgcn_readlane((int)v, 47), d = (unsigned)__builtin_amdgcn_readlane((int)v, 63);
  return max(max(a, b), max(c, d));
}

__device__ __forceinline__ v4u ld4(rsrc_t rs, int off) { return __builtin_amdgcn_raw_buffer_load_b128(rs, off, 0, SC1); }
__device__ __forceinline__ v2u ld2(rsrc_t rs, int off) { return __builtin_amdgcn_raw_buffer_load_b64(rs, off, 0, SC1); }
__device__ __forceinline__ unsigned ld1(rsrc_t rs, int off) { return __builtin_amdgcn_raw_buffer_load_b32(rs, off, 0, SC1); }
__device__ __forceinline__ v2u ldp2(rsrc_t rs, int off, int soff) { return __builtin_amdgcn_raw_buffer_load_b64(rs, off, soff, 0); }
__device__ __forceinline__ void st4(v4u v, rsrc_t rs, int off) { __builtin_amdgcn_raw_buffer_store_b128(v, rs, off, 0, SC1); }
__device__ __forceinline__ void st2(v2u v, rsrc_t rs, int off) { __builtin_amdgcn_raw_buffer_store_b64(v, rs, off, 0, SC1); }
__device__ __forceinline__ void st1(unsigned v, rsrc_t rs, int off) { __builtin_amdgcn_raw_buffer_store_b32(v, rs, off, 0, SC1); }

__device__ __forceinline__ f32x4 mfma_bf(v4u a, v4u b, f32x4 c) {
  return __builtin_amdgcn_mfma_f32_16x16x32_bf16(__builtin_bit_cast(bf16x8, a), __builtin_bit_cast(bf16x8, b), c, 0, 0, 0);
}
__device__ __forceinline__ f32x4 smfmac_bf(v4u a, v8u b, f32x4 c, int idx) {
  return __builtin_amdgcn_smfmac_f32_16x16x64_bf16(__builtin_bit_cast(bf16x8, a), __builtin_bit_cast(bf16x16, b), c, idx, 0, 0);
}
__device__ __forceinline__ f32x4 mfma_h(v4u a, v4u b, f32x4 c) {
  return __builtin_amdgcn_mfma_f32_16x16x32_f16(__builtin_bit_cast(f16x8, a), __builtin_bit_cast(f16x8, b), c, 0, 0, 0);
}

__device__ __forceinline__ f32x4 sum9(const LAS char* red) {
  f32x4 t[9];
#pragma unroll
  for (int w = 0; w < 9; w++) t[w] = *(const LAS f32x4*)(red + w * 256);
  SCHED();
  return (((t[0] + t[1]) + (t[2] + t[3])) + ((t[4] + t[5]) + (t[6] + t[7]))) + t[8];
}
__device__ __forceinline__ void ln_round(float sum, float sq, float& m, float& s) {
  const float mean = sum * (1.0f / DM);
  const float var = fmaxf(fmaf(-mean, mean, sq * (1.0f / DM)), 0.f);
  m = rbf(mean);
  s = rbf(__builtin_amdgcn_sqrtf(rbf(rbf(var) + 1e-5f)));
}
__device__ __forceinline__ unsigned ln_apply(unsigned xp, float m, float rinv) {
  const unsigned d = pkbf(blo(xp) - m, bhi(xp) - m);
  return pkbf(blo(d) * rinv, bhi(d) * rinv);
}
__device__ __forceinline__ unsigned addbf(unsigned a, unsigned b) { return pkbf(blo(a) + blo(b), bhi(a) + bhi(b)); }

__device__ __forceinline__ unsigned now() { return (unsigned)__builtin_amdgcn_s_memrealtime(); }
__device__ __forceinline__ void wait_until(unsigned t0, unsigned ticks) {
  unsigned n;
  asm volatile(
      "s_movk_i32 %0, 1024\n"
      "1:\n\t"
      "s_memrealtime vcc\n\t"
      "s_waitcnt lgkmcnt(0)\n\t"
      "s_sub_i32 vcc_lo, vcc_lo, %1\n\t"
      "s_cmp_gt_i32 vcc_lo, -16\n\t"
      "s_cbranch_scc1 2f\n\t"
      "s_sleep 2\n\t"
      "s_sub_u32 %0, %0, 4\n\t"
      "s_cbranch_scc0 1b\n\t"
      "s_branch 4f\n"
      "3:\n\t"
      "s_memrealtime vcc\n\t"
      "s_sub_u32 %0, %0, 1\n\t"
      "s_waitcnt lgkmcnt(0)\n\t"
      "s_cselect_b32 vcc_lo, %1, vcc_lo\n\t"
      "s_sub_i32 vcc_lo, vcc_lo, %1\n"
      "2:\n\t"
      "s_cmp_lt_i32 vcc_lo, 0\n\t"
      "s_cbranch_scc1 3b\n"
      "4:"
      : "=&s"(n)
      : "s"(t0 + ticks)
      : "vcc", "scc", "memory");
}

typedef short s16x4 __attribute__((ext_vector_type(4)));
__device__ __forceinline__ v2u ldtr(const LAS char* a) {
  return __builtin_bit_cast(v2u, __builtin_amdgcn_ds_read_tr16_b64_v4i16((LAS s16x4*)a));
}
__device__ __forceinline__ f32x4 widen(v2u unit, v2u v, f32x4 c) {
  return __builtin_amdgcn_mfma_f32_16x16x16bf16_1k(__builtin_bit_cast(s16x4, unit), __builtin_bit_cast(s16x4, v), c, 0, 0, 0);
}
__device__ __forceinline__ v4u tag_bits(unsigned tag) {
  v4u t;
#pragma unroll
  for (int k = 0; k < 4; k++) t[k] = ((tag >> (6 * k)) & 7u) | (((tag >> (6 * k + 3)) & 7u) << 16);
  return t;
}

template <int N, bool EARLY>
__device__ __forceinline__ void fetch_u(rsrc_t rs, int src, const unsigned (&dst)[N], int first, v4u tx) {
  v4u w[N];
  unsigned bad, mid = 0x40004u, pcnt_ = 0;
  asm volatile("" : "+s"(mid));
  auto put = [&](int i) {
    *(LAS v2u*)dst[i] = v2u{w[i].x, w[i].y};
    *(LAS v2u*)(dst[i] + 8) = v2u{w[i].z, w[i].w};
  };
  auto load = [&]() {
#pragma unroll
    for (int i = 0; i < N; i++) w[i] = __builtin_amdgcn_raw_buffer_load_b128(rs, src, (first + 4 * i) * 1024, SC1);
    bad = 0u;
#pragma unroll
    for (int i = 0; i < N; i++) {
      SCHED();
      w[i] ^= tx;
      if (EARLY) put(i);
      SCHED();
      bad |= __builtin_amdgcn_bitop3_b32(w[i].x, w[i].y, mid, 0x7e) | __builtin_amdgcn_bitop3_b32(w[i].z, w[i].w, mid, 0x7e);
    }
  };
  load();
  POLL(N == 1, (bad & 0x70007u) == 0u, load());
  if (!EARLY) put(0);
}

struct cz { f32x2 v, s; };
#define STEP_(txt, c, r, a, b) do { asm(txt : c(r) : "v"(a), "v"(b)); SCHED(); } while (0)
#define mul_(r, a, b) STEP_("v_pk_mul_f32 %0, %1, %2 op_sel:[1,0] neg_lo:[0,1]", "=&v", r, a, (b).s)
#define fma_(r, a, b) STEP_("v_pk_fma_f32 %0, %1, %2, %0 op_sel_hi:[0,1,1]", "+v", r, a, (b).v)
#define mulc_(r, a, b) STEP_("v_pk_mul_f32 %0, %1, %2 op_sel:[1,0]", "=&v", r, a, (b).s)
#define fmac_(r, a, b) STEP_("v_pk_fma_f32 %0, %1, %2, %0 op_sel_hi:[0,1,1] neg_hi:[0,1,0]", "+v", r, a, (b).v)
#define mulh_(r, a, bs) STEP_("v_pk_mul_f16 %0, %1, %2 op_sel:[1,0] neg_lo:[0,1]", "=&v", r, a, bs)
#define fmah_(r, a, b) STEP_("v_pk_fma_f16 %0, %1, %2, %0 op_sel_hi:[0,1,1]", "+v", r, a, b)
#define swap_(z) do { asm("v_pk_mov_b32 %0, %1, %1 op_sel:[1,0]" : "=v"((z).s) : "v"((z).v)); SCHED(); } while (0)
#define half_(h, v) do { h = pkh((v).x, (v).y); SCHED(); } while (0)
__device__ __forceinline__ float exp_c(float v) {
  const float t = v * 1.44269502f;
  const float e = fmaf(v, 1.92596303e-8f, fmaf(v, 1.44269502f, -t));
  return __builtin_amdgcn_exp2f(t) * fmaf(e, 0.693147182f, 1.f);
}
__device__ __forceinline__ f32x2 cexp_p2(float p, float ch, float ph, float pl) {
  const float mag = __builtin_amdgcn_exp2f(p * ch);
  const float fr = fmaf(p, pl, __builtin_amdgcn_fractf(p * ph));
  return f32x2{mag * __builtin_amdgcn_cosf(fr), mag * __builtin_amdgcn_sinf(fr)};
}
__device__ __forceinline__ unsigned pkh2(f32x2 v) { return pkh(v.x, v.y); }

#define CONV_ASM_TEXT \
  "s_barrier\n" "ds_read_b128 v[32:35], %8 offset:480\n" \
  "ds_read_b128 v[36:39], %8 offset:992\n" "ds_read_b128 v[40:43], %8 offset:1504\n" \
  "ds_read2_b32 v[44:45], %11 offset0:48 offset1:49\n" "ds_read2_b32 v[46:47], %11 offset0:50 offset1:51\n" \
  "ds_read2_b32 v[48:49], %11 offset0:32 offset1:33\n" "s_waitcnt lgkmcnt(5)\n" \
  "v_mfma_f32_16x16x32_f16 v[132:135], v[32:35], v[16:19], 0\n" "s_nop 0\n" \
  "ds_read2_b32 v[50:51], %11 offset0:34 offset1:35\n" "ds_read2_b32 v[52:53], %11 offset0:16 offset1:17\n" \
  "v_mfma_f32_16x16x32_f16 v[128:131], v[32:35], v[20:23], 0\n" "s_waitcnt lgkmcnt(6)\n" \
  "ds_read2_b32 v[54:55], %11 offset0:18 offset1:19\n" "ds_read2_b32 v[56:57], %11 offset0:0 offset1:1\n" \
  "v_mfma_f32_16x16x32_f16 v[124:127], v[36:39], v[16:19], 0\n" "s_nop 0\n" \
  "ds_read2_b32 v[58:59], %11 offset0:2 offset1:3\n" "ds_read_b128 v[60:63], %8 offset:2016\n" \
  "v_mfma_f32_16x16x32_f16 v[120:123], v[36:39], v[20:23], 0\n" "s_waitcnt lgkmcnt(9)\n" \
  "v_cvt_pk_f16_f32 v116, v132, v128\n" "v_cvt_pk_f16_f32 v117, v133, v129\n" \
  "v_mfma_f32_16x16x32_f16 v[164:167], v[40:43], v[16:19], 0\n" "v_cvt_pk_f16_f32 v118, v134, v130\n" \
  "v_cvt_pk_f16_f32 v119, v135, v131\n" "v_mfma_f32_16x16x32_f16 v[160:163], v[40:43], v[20:23], 0\n" \
  "s_waitcnt lgkmcnt(7)\n" "ds_read_b128 v[64:67], %9 offset:480\n" \
  "v_cvt_pk_f16_f32 v112, v124, v120\n" "v_mfma_f32_16x16x32_f16 v[148:151], v[116:119], v[44:47], 0\n" \
  "s_waitcnt lgkmcnt(6)\n" "ds_read_b128 v[68:71], %9 offset:736\n" \
  "v_cvt_pk_f16_f32 v113, v125, v121\n" "v_mfma_f32_16x16x32_f16 v[144:147], v[116:119], v[48:51], 0\n" \
  "s_waitcnt lgkmcnt(5)\n" "v_cvt_pk_f16_f32 v114, v126, v122\n" \
  "v_cvt_pk_f16_f32 v115, v127, v123\n" "v_mfma_f32_16x16x32_f16 v[140:143], v[116:119], v[52:55], 0\n" \
  "s_waitcnt lgkmcnt(3)\n" "ds_read_b128 v[72:75], %9 offset:992\n" \
  "ds_read_b64 v[76:77], %10 offset:256\n" "v_mfma_f32_16x16x32_f16 v[136:139], v[116:119], v[56:59], 0\n" \
  "s_waitcnt lgkmcnt(4)\n" "ds_read_b64 v[78:79], %10 offset:264\n" \
  "v_cvt_pk_f16_f32 v116, v164, v160\n" "v_mfma_f32_16x16x32_f16 v[132:135], v[60:63], v[16:19], 0\n" \
  "v_cvt_pk_f16_f32 v117, v165, v161\n" "v_cvt_pk_f16_f32 v118, v166, v162\n" \
  "v_mfma_f32_16x16x32_f16 v[128:131], v[60:63], v[20:23], 0\n" "ds_read_b64 v[80:81], %10 offset:128\n" \
  "v_cvt_pk_f16_f32 v119, v167, v163\n" "v_mfma_f32_16x16x32_f16 v[144:147], v[112:115], v[44:47], v[144:147]\n" \
  "ds_read_b64 v[82:83], %10 offset:136\n" "v_mfma_f32_16x16x32_f16 v[140:143], v[112:115], v[48:51], v[140:143]\n" \
  "s_nop 0\n" "ds_read_b64 v[84:85], %10 offset:0\n" \
  "ds_read_b64 v[86:87], %10 offset:8\n" "v_mfma_f32_16x16x32_f16 v[136:139], v[112:115], v[52:55], v[136:139]\n" \
  "s_waitcnt lgkmcnt(7)\n" "ds_read_b128 v[88:91], %9 offset:1248\n" \
  "v_cvt_pk_f16_f32 v112, v132, v128\n" "v_mfma_f32_16x16x32_f16 v[124:127], v[64:67], v[24:27], 0\n" \
  "v_cvt_pk_f16_f32 v113, v133, v129\n" "v_cvt_pk_f16_f32 v114, v134, v130\n" \
  "v_mfma_f32_16x16x32_f16 v[120:123], v[64:67], v[28:31], 0\n" "ds_read_b64 v[92:93], %10 offset:320\n" \
  "v_cvt_pk_f16_f32 v115, v135, v131\n" "v_mfma_f32_16x16x32_f16 v[140:143], v[116:119], v[44:47], v[140:143]\n" \
  "ds_read_b64 v[94:95], %10 offset:328\n" "v_mfma_f32_16x16x32_f16 v[136:139], v[116:119], v[48:51], v[136:139]\n" \
  "s_nop 0\n" "ds_read_b64 v[96:97], %10 offset:192\n" \
  "ds_read_b64 v[98:99], %10 offset:200\n" "v_mfma_f32_16x16x32_f16 v[164:167], v[68:71], v[24:27], 0\n" \
  "s_nop 0\n" "ds_read_b64 v[100:101], %10 offset:64\n" \
  "ds_read_b64 v[102:103], %10 offset:72\n" "v_mfma_f32_16x16x32_f16 v[160:163], v[68:71], v[28:31], 0\n" \
  "v_cvt_pk_f16_f32 v116, v124, v120\n" "v_cvt_pk_f16_f32 v117, v125, v121\n" \
  "v_mfma_f32_16x16x32_f16 v[136:139], v[112:115], v[44:47], v[136:139]\n" "s_waitcnt lgkmcnt(7)\n" \
  "v_cvt_pk_f16_f32 v118, v126, v122\n" "v_cvt_pk_f16_f32 v119, v127, v123\n" \
  "v_mfma_f32_16x16x32_f16 v[132:135], v[72:75], v[24:27], 0\n" "ds_read_b128 v[104:107], %9 offset:1504\n" \
  "v_cvt_pk_f16_f32 v112, v164, v160\n" "v_mfma_f32_16x16x32_f16 v[128:131], v[72:75], v[28:31], 0\n" \
  "v_cvt_pk_f16_f32 v113, v165, v161\n" "v_cvt_pk_f16_f32 v114, v166, v162\n" \
  "v_mfma_f32_16x16x32_f16 v[156:159], v[116:119], v[76:79], 0\n" "ds_read_b128 v[108:111], %9 offset:1760\n" \
  "v_cvt_pk_f16_f32 v115, v167, v163\n" "v_mfma_f32_16x16x32_f16 v[152:155], v[116:119], v[80:83], 0\n" \
  "s_nop 0\n" "ds_read2_b32 v[32:33], %6 offset0:144 offset1:145\n" \
  "ds_read2_b32 v[34:35], %6 offset0:146 offset1:147\n" "v_mfma_f32_16x16x32_f16 v[148:151], v[116:119], v[84:87], v[148:151]\n" \
  "s_waitcnt lgkmcnt(6)\n" "v_cvt_pk_f16_f32 v116, v132, v128\n" \
  "v_cvt_pk_f16_f32 v117, v133, v129\n" "v_mfma_f32_16x16x32_f16 v[124:127], v[88:91], v[24:27], 0\n" \
  "v_cvt_pk_f16_f32 v118, v134, v130\n" "v_cvt_pk_f16_f32 v119, v135, v131\n" \
  "v_mfma_f32_16x16x32_f16 v[120:123], v[88:91], v[28:31], 0\n" "s_nop 0\n" \
  "ds_read_b128 v[36:39], %9 offset:2016\n" "ds_read2_b32 v[40:41], %6 offset0:16 offset1:17\n" \
  "v_mfma_f32_16x16x32_f16 v[156:159], v[112:115], v[92:95], v[156:159]\n" "ds_read2_b32 v[42:43], %6 offset0:18 offset1:19\n" \
  "v_mfma_f32_16x16x32_f16 v[152:155], v[112:115], v[96:99], v[152:155]\n" "s_waitcnt lgkmcnt(6)\n" \
  "ds_read2_b32 v[56:57], %6 offset0:48 offset1:49\n" "ds_read2_b32 v[58:59], %6 offset0:50 offset1:51\n" \
  "v_mfma_f32_16x16x32_f16 v[148:151], v[112:115], v[100:103], v[148:151]\n" "v_cvt_pk_f16_f32 v112, v124, v120\n" \
  "v_cvt_pk_f16_f32 v113, v125, v121\n" "v_mfma_f32_16x16x32_f16 v[164:167], v[104:107], v[24:27], 0\n" \
  "v_cvt_pk_f16_f32 v114, v126, v122\n" "v_cvt_pk_f16_f32 v115, v127, v123\n" \
  "v_mfma_f32_16x16x32_f16 v[160:163], v[104:107], v[28:31], 0\n" "s_nop 0\n" \
  "ds_read_b128 v[16:19], %9 offset:2272\n" "ds_read2_b32 v[60:61], %6 offset0:176 offset1:177\n" \
  "v_mfma_f32_16x16x32_f16 v[152:155], v[116:119], v[76:79], v[152:155]\n" "ds_read2_b32 v[62:63], %6 offset0:178 offset1:179\n" \
  "v_mfma_f32_16x16x32_f16 v[148:151], v[116:119], v[80:83], v[148:151]\n" "s_nop 0\n" \
  "ds_read2_b32 v[20:21], %6 offset0:32 offset1:33\n" "ds_read2_b32 v[22:23], %6 offset0:34 offset1:35\n" \
  "v_mfma_f32_16x16x32_f16 v[144:147], v[116:119], v[84:87], v[144:147]\n" "s_waitcnt lgkmcnt(8)\n" \
  "v_cvt_pk_f16_f32 v116, v164, v160\n" "v_cvt_pk_f16_f32 v117, v165, v161\n" \
  "v_mfma_f32_16x16x32_f16 v[132:135], v[108:111], v[24:27], 0\n" "v_cvt_pk_f16_f32 v118, v166, v162\n" \
  "v_cvt_pk_f16_f32 v119, v167, v163\n" "v_mfma_f32_16x16x32_f16 v[128:131], v[108:111], v[28:31], 0\n" \
  "s_nop 0\n" "ds_read_b128 v[52:55], %9 offset:2528\n" \
  "ds_read2_b32 v[64:65], %6 offset0:160 offset1:161\n" "v_mfma_f32_16x16x32_f16 v[152:155], v[112:115], v[92:95], v[152:155]\n" \
  "ds_read2_b32 v[66:67], %6 offset0:162 offset1:163\n" "v_mfma_f32_16x16x32_f16 v[148:151], v[112:115], v[96:99], v[148:151]\n" \
  "s_nop 0\n" "ds_read2_b32 v[48:49], %6 offset0:80 offset1:81\n" \
  "ds_read2_b32 v[50:51], %6 offset0:82 offset1:83\n" "v_mfma_f32_16x16x32_f16 v[144:147], v[112:115], v[100:103], v[144:147]\n" \
  "v_cvt_pk_f16_f32 v112, v132, v128\n" "v_cvt_pk_f16_f32 v113, v133, v129\n" \
  "v_mfma_f32_16x16x32_f16 v[124:127], v[36:39], v[24:27], 0\n" "v_cvt_pk_f16_f32 v114, v134, v130\n" \
  "v_cvt_pk_f16_f32 v115, v135, v131\n" "v_mfma_f32_16x16x32_f16 v[120:123], v[36:39], v[28:31], 0\n" \
  "s_nop 0\n" "ds_read_b128 v[68:71], %9 offset:2784\n" \
  "ds_read2_b32 v[44:45], %6 offset0:208 offset1:209\n" "v_mfma_f32_16x16x32_f16 v[148:151], v[116:119], v[76:79], v[148:151]\n" \
  "ds_read2_b32 v[46:47], %6 offset0:210 offset1:211\n" "v_mfma_f32_16x16x32_f16 v[144:147], v[116:119], v[80:83], v[144:147]\n" \
  "s_nop 0\n" "ds_read2_b32 v[72:73], %6 offset0:64 offset1:65\n" \
  "ds_read2_b32 v[74:75], %6 offset0:66 offset1:67\n" "v_mfma_f32_16x16x32_f16 v[140:143], v[116:119], v[84:87], v[140:143]\n" \
  "s_waitcnt lgkmcnt(8)\n" "v_cvt_pk_f16_f32 v116, v124, v120\n" \
  "v_cvt_pk_f16_f32 v117, v125, v121\n" "v_mfma_f32_16x16x32_f16 v[132:135], v[16:19], v[24:27], 0\n" \
  "v_cvt_pk_f16_f32 v118, v126, v122\n" "v_cvt_pk_f16_f32 v119, v127, v123\n" \
  "v_mfma_f32_16x16x32_f16 v[128:131], v[16:19], v[28:31], 0\n" "s_nop 0\n" \
  "ds_read_b128 v[88:91], %9 offset:3040\n" "ds_read2_b32 v[104:105], %6 offset0:192 offset1:193\n" \
  "v_mfma_f32_16x16x32_f16 v[148:151], v[112:115], v[92:95], v[148:151]\n" "ds_read2_b32 v[106:107], %6 offset0:194 offset1:195\n" \
  "v_mfma_f32_16x16x32_f16 v[144:147], v[112:115], v[96:99], v[144:147]\n" "s_nop 0\n" \
  "ds_read_b128 v[108:111], %8 offset:0\n" "ds_read2_b32 v[36:37], %6 offset0:224 offset1:225\n" \
  "v_mfma_f32_16x16x32_f16 v[140:143], v[112:115], v[100:103], v[140:143]\n" "v_cvt_pk_f16_f32 v112, v132, v128\n" \
  "v_cvt_pk_f16_f32 v113, v133, v129\n" "v_mfma_f32_16x16x32_f16 v[124:127], v[52:55], v[24:27], 0\n" \
  "v_cvt_pk_f16_f32 v114, v134, v130\n" "v_cvt_pk_f16_f32 v115, v135, v131\n" \
  "v_mfma_f32_16x16x32_f16 v[120:123], v[52:55], v[28:31], 0\n" "s_nop 0\n" \
  "ds_read2_b32 v[38:39], %6 offset0:226 offset1:227\n" "ds_read_b128 v[16:19], %9 offset:3296\n" \
  "v_mfma_f32_16x16x32_f16 v[144:147], v[116:119], v[76:79], v[144:147]\n" "ds_read2_b32 v[52:53], %6 offset0:96 offset1:97\n" \
  "v_mfma_f32_16x16x32_f16 v[140:143], v[116:119], v[80:83], v[140:143]\n" "ds_read2_b32 v[54:55], %6 offset0:98 offset1:99\n" \
  "v_mfma_f32_16x16x32_f16 v[136:139], v[116:119], v[84:87], v[136:139]\n" "s_waitcnt lgkmcnt(7)\n" \
  "v_cvt_pk_f16_f32 v116, v124, v120\n" "v_cvt_pk_f16_f32 v117, v125, v121\n" \
  "v_mfma_f32_16x16x32_f16 v[132:135], v[68:71], v[24:27], 0\n" "v_cvt_pk_f16_f32 v118, v126, v122\n" \
  "v_cvt_pk_f16_f32 v119, v127, v123\n" "v_mfma_f32_16x16x32_f16 v[128:131], v[68:71], v[28:31], 0\n" \
  "s_nop 0\n" "ds_read_b128 v[84:87], %8 offset:128\n" \
  "ds_read2_b32 v[68:69], %6 offset0:240 offset1:241\n" "v_mfma_f32_16x16x32_f16 v[144:147], v[112:115], v[92:95], v[144:147]\n" \
  "ds_read2_b32 v[70:71], %6 offset0:242 offset1:243\n" "v_mfma_f32_16x16x32_f16 v[140:143], v[112:115], v[96:99], v[140:143]\n" \
  "v_mfma_f32_16x16x32_f16 v[136:139], v[112:115], v[100:103], v[136:139]\n" "ds_read_b128 v[100:103], %8 offset:64\n" \
  "v_cvt_pk_f16_f32 v112, v132, v128\n" "v_mfma_f32_16x16x32_f16 v[124:127], v[88:91], v[24:27], 0\n" \
  "v_cvt_pk_f16_f32 v113, v133, v129\n" "v_cvt_pk_f16_f32 v114, v134, v130\n" \
  "v_mfma_f32_16x16x32_f16 v[120:123], v[88:91], v[28:31], 0\n" "s_waitcnt lgkmcnt(4)\n" \
  "ds_read_b128 v[88:91], %8 offset:256\n" "v_cvt_pk_f16_f32 v115, v135, v131\n" \
  "v_mfma_f32_16x16x32_f16 v[164:167], v[32:35], v[108:111], 0\n" "v_mfma_f32_16x16x32_f16 v[140:143], v[116:119], v[76:79], v[140:143]\n" \
  "v_mfma_f32_16x16x32_f16 v[136:139], v[116:119], v[80:83], v[136:139]\n" "s_nop 0\n" \
  "ds_read_b128 v[80:83], %8 offset:192\n" "v_cvt_pk_f16_f32 v116, v124, v120\n" \
  "v_mfma_f32_16x16x32_f16 v[132:135], v[16:19], v[24:27], 0\n" "v_cvt_pk_f16_f32 v117, v125, v121\n" \
  "v_cvt_pk_f16_f32 v118, v126, v122\n" "v_mfma_f32_16x16x32_f16 v[128:131], v[16:19], v[28:31], 0\n" \
  "ds_read_b128 v[24:27], %8 offset:320\n" "v_cvt_pk_f16_f32 v119, v127, v123\n" \
  "v_mfma_f32_16x16x32_f16 v[160:163], v[40:43], v[108:111], 0\n" "s_nop 0\n" \
  "ds_read2_b32 v[16:17], %6 offset0:112 offset1:113\n" "ds_read2_b32 v[18:19], %6 offset0:114 offset1:115\n" \
  "v_mfma_f32_16x16x32_f16 v[140:143], v[112:115], v[92:95], v[140:143]\n" "ds_read_b128 v[28:31], %8 offset:384\n" \
  "v_mfma_f32_16x16x32_f16 v[136:139], v[112:115], v[96:99], v[136:139]\n" "s_waitcnt lgkmcnt(5)\n" \
  "ds_read2_b32 v[124:125], %7 offset0:0 offset1:1\n" "v_cvt_pk_f16_f32 v112, v132, v128\n" \
  "v_mfma_f32_16x16x32_f16 v[160:163], v[56:59], v[84:87], v[160:163]\n" "v_cvt_pk_f16_f32 v113, v133, v129\n" \
  "v_cvt_pk_f16_f32 v114, v134, v130\n" "v_mfma_f32_16x16x32_f16 v[164:167], v[60:63], v[84:87], v[164:167]\n" \
  "ds_read2_b32 v[126:127], %7 offset0:2 offset1:3\n" "v_cvt_pk_f16_f32 v115, v135, v131\n" \
  "v_mfma_f32_16x16x32_f16 v[160:163], v[20:23], v[100:103], v[160:163]\n" "s_nop 0\n" \
  "ds_read_b128 v[108:111], %8 offset:448\n" "ds_read2_b32 v[120:121], %6 offset0:128 offset1:129\n" \
  "v_mfma_f32_16x16x32_f16 v[164:167], v[64:67], v[100:103], v[164:167]\n" "ds_read2_b32 v[122:123], %6 offset0:130 offset1:131\n" \
  "v_mfma_f32_16x16x32_f16 v[136:139], v[116:119], v[76:79], v[136:139]\n" "s_nop 0\n" \
  "ds_read2_b32 v[96:97], %7 offset0:16 offset1:17\n" "ds_read2_b32 v[98:99], %7 offset0:18 offset1:19\n" \
  "v_mfma_f32_16x16x32_f16 v[160:163], v[48:51], v[88:91], v[160:163]\n" "ds_read_b128 v[84:87], %8 offset:512\n" \
  "v_mfma_f32_16x16x32_f16 v[164:167], v[44:47], v[88:91], v[164:167]\n" "s_waitcnt lgkmcnt(8)\n" \
  "v_mfma_f32_16x16x32_f16 v[160:163], v[72:75], v[80:83], v[160:163]\n" "s_nop 0\n" \
  "ds_read2_b32 v[132:133], %6 offset0:0 offset1:1\n" "ds_read2_b32 v[134:135], %6 offset0:2 offset1:3\n" \
  "v_mfma_f32_16x16x32_f16 v[164:167], v[104:107], v[80:83], v[164:167]\n" "ds_read_b128 v[128:131], %8 offset:576\n" \
  "v_mfma_f32_16x16x32_f16 v[136:139], v[112:115], v[92:95], v[136:139]\n" "ds_read_b128 v[100:103], %8 offset:640\n" \
  "v_mfma_f32_16x16x32_f16 v[164:167], v[36:39], v[24:27], v[164:167]\n" "ds_read_b128 v[116:119], %8 offset:704\n" \
  "v_mfma_f32_16x16x32_f16 v[160:163], v[52:55], v[24:27], v[160:163]\n" "v_mfma_f32_16x16x32_f16 v[164:167], v[68:71], v[28:31], v[164:167]\n" \
  "s_nop 0\n" "ds_read_b128 v[76:79], %8 offset:768\n" \
  "ds_read_b128 v[88:91], %8 offset:832\n" "v_mfma_f32_16x16x32_f16 v[160:163], v[16:19], v[28:31], v[160:163]\n" \
  "s_waitcnt lgkmcnt(7)\n" "ds_read_b128 v[80:83], %8 offset:896\n" \
  "v_mfma_f32_16x16x32_f16 v[164:167], v[124:127], v[108:111], v[164:167]\n" "ds_read_b128 v[112:115], %8 offset:960\n" \
  "v_mfma_f32_16x16x32_f16 v[160:163], v[120:123], v[108:111], v[160:163]\n" "ds_read_b128 v[92:95], %8 offset:1024\n" \
  "v_mfma_f32_16x16x32_f16 v[164:167], v[96:99], v[84:87], v[164:167]\n" "v_mfma_f32_16x16x32_f16 v[160:163], v[32:35], v[84:87], v[160:163]\n" \
  "s_nop 0\n" "ds_read_b128 v[24:27], %8 offset:1088\n" \
  "ds_read_b128 v[28:31], %8 offset:1152\n" "v_mfma_f32_16x16x32_f16 v[156:159], v[40:43], v[84:87], v[156:159]\n" \
  "s_waitcnt lgkmcnt(7)\n" "ds_read_b128 v[108:111], %8 offset:1216\n" \
  "v_mfma_f32_16x16x32_f16 v[148:151], v[132:135], v[84:87], v[148:151]\n" "v_mul_f32 v164, %5, v164\n" \
  "v_mul_f32 v165, %5, v165\n" "v_mfma_f32_16x16x32_f16 v[160:163], v[64:67], v[128:131], v[160:163]\n" \
  "v_mul_f32 v166, %5, v166\n" "v_mul_f32 v167, %5, v167\n" \
  "v_mfma_f32_16x16x32_f16 v[156:159], v[20:23], v[128:131], v[156:159]\n" "v_cvt_pk_bf16_f32 v164, v164, v165\n" \
  "v_cvt_pk_bf16_f32 v165, v166, v167\n" "v_mfma_f32_16x16x32_f16 v[160:163], v[60:63], v[100:103], v[160:163]\n" \
  "v_pk_min_u16 v164, v164, %4\n" "v_pk_min_u16 v165, v165, %4\n" \
  "v_mfma_f32_16x16x32_f16 v[156:159], v[56:59], v[100:103], v[156:159]\n" "ds_write_b64 %12, v[164:165] offset:0\n" \
  "v_mfma_f32_16x16x32_f16 v[160:163], v[104:107], v[116:119], v[160:163]\n" "s_nop 0\n" \
  "ds_read_b128 v[84:87], %8 offset:1280\n" "ds_read_b128 v[128:131], %8 offset:1344\n" \
  "v_mfma_f32_16x16x32_f16 v[156:159], v[72:75], v[116:119], v[156:159]\n" "s_waitcnt lgkmcnt(4)\n" \
  "ds_read_b128 v[100:103], %8 offset:1408\n" "ds_read_b128 v[164:167], %8 offset:1472\n" \
  "v_mfma_f32_16x16x32_f16 v[160:163], v[44:47], v[76:79], v[160:163]\n" "v_mfma_f32_16x16x32_f16 v[156:159], v[48:51], v[76:79], v[156:159]\n" \
  "v_mfma_f32_16x16x32_f16 v[160:163], v[36:39], v[88:91], v[160:163]\n" "ds_read_b128 v[116:119], %8 offset:1536\n" \
  "v_mfma_f32_16x16x32_f16 v[156:159], v[52:55], v[88:91], v[156:159]\n" "s_nop 0\n" \
  "ds_read_b128 v[76:79], %8 offset:1600\n" "ds_read_b128 v[88:91], %8 offset:1664\n" \
  "v_mfma_f32_16x16x32_f16 v[160:163], v[68:71], v[80:83], v[160:163]\n" "v_mfma_f32_16x16x32_f16 v[156:159], v[16:19], v[80:83], v[156:159]\n" \
  "ds_read_b128 v[80:83], %8 offset:1728\n" "v_mfma_f32_16x16x32_f16 v[160:163], v[124:127], v[112:115], v[160:163]\n" \
  "v_mfma_f32_16x16x32_f16 v[156:159], v[120:123], v[112:115], v[156:159]\n" "ds_read_b128 v[112:115], %8 offset:1792\n" \
  "v_mfma_f32_16x16x32_f16 v[160:163], v[96:99], v[92:95], v[160:163]\n" "v_mfma_f32_16x16x32_f16 v[156:159], v[32:35], v[92:95], v[156:159]\n" \
  "v_mfma_f32_16x16x32_f16 v[152:155], v[40:43], v[92:95], v[152:155]\n" "v_mfma_f32_16x16x32_f16 v[144:147], v[132:135], v[92:95], v[144:147]\n" \
  "ds_read_b128 v[92:95], %8 offset:1856\n" "v_mfma_f32_16x16x32_f16 v[156:159], v[64:67], v[24:27], v[156:159]\n" \
  "v_mfma_f32_16x16x32_f16 v[152:155], v[20:23], v[24:27], v[152:155]\n" "ds_read_b128 v[24:27], %8 offset:1920\n" \
  "v_mfma_f32_16x16x32_f16 v[156:159], v[60:63], v[28:31], v[156:159]\n" "v_mul_f32 v160, %5, v160\n" \
  "v_mul_f32 v161, %5, v161\n" "v_mfma_f32_16x16x32_f16 v[152:155], v[56:59], v[28:31], v[152:155]\n" \
  "s_waitcnt lgkmcnt(2)\n" "v_mul_f32 v162, %5, v162\n" \
  "v_mul_f32 v163, %5, v163\n" "v_mfma_f32_16x16x32_f16 v[156:159], v[104:107], v[108:111], v[156:159]\n" \
  "v_cvt_pk_bf16_f32 v160, v160, v161\n" "v_cvt_pk_bf16_f32 v161, v162, v163\n" \
  "v_mfma_f32_16x16x32_f16 v[152:155], v[72:75], v[108:111], v[152:155]\n" "v_pk_min_u16 v160, v160, %4\n" \
  "v_pk_min_u16 v161, v161, %4\n" "v_mfma_f32_16x16x32_f16 v[156:159], v[44:47], v[84:87], v[156:159]\n" \
  "ds_write_b64 %12, v[160:161] offset:512\n" "v_mfma_f32_16x16x32_f16 v[152:155], v[48:51], v[84:87], v[152:155]\n" \
  "s_nop 0\n" "ds_read_b128 v[28:31], %8 offset:1984\n" \
  "ds_read_b128 v[108:111], %8 offset:2048\n" "v_mfma_f32_16x16x32_f16 v[156:159], v[36:39], v[128:131], v[156:159]\n" \
  "s_nop 0\n" "ds_read_b128 v[160:163], %8 offset:2112\n" \
  "ds_read_b128 v[84:87], %8 offset:2176\n" "v_mfma_f32_16x16x32_f16 v[152:155], v[52:55], v[128:131], v[152:155]\n" \
  "v_mfma_f32_16x16x32_f16 v[156:159], v[68:71], v[100:103], v[156:159]\n" "v_mfma_f32_16x16x32_f16 v[152:155], v[16:19], v[100:103], v[152:155]\n" \
  "s_waitcnt lgkmcnt(4)\n" "ds_read_b128 v[128:131], %8 offset:2240\n" \
  "v_mfma_f32_16x16x32_f16 v[156:159], v[124:127], v[164:167], v[156:159]\n" "s_barrier\n" \
  "ds_read_b128 v[100:103], %8 offset:2304\n" "v_mfma_f32_16x16x32_f16 v[152:155], v[120:123], v[164:167], v[152:155]\n" \
  "ds_read_b128 v[164:167], %8 offset:2368\n" "v_mfma_f32_16x16x32_f16 v[156:159], v[96:99], v[116:119], v[156:159]\n" \
  "v_mfma_f32_16x16x32_f16 v[152:155], v[32:35], v[116:119], v[152:155]\n" "v_mfma_f32_16x16x32_f16 v[148:151], v[40:43], v[116:119], v[148:151]\n" \
  "v_mfma_f32_16x16x32_f16 v[140:143], v[132:135], v[116:119], v[140:143]\n" "ds_read_b128 v[116:119], %8 offset:2432\n" \
  "v_mfma_f32_16x16x32_f16 v[152:155], v[64:67], v[76:79], v[152:155]\n" "v_mfma_f32_16x16x32_f16 v[148:151], v[20:23], v[76:79], v[148:151]\n" \
  "ds_read_b128 v[76:79], %8 offset:2496\n" "v_mfma_f32_16x16x32_f16 v[152:155], v[60:63], v[88:91], v[152:155]\n" \
  "v_mul_f32 v156, %5, v156\n" "v_mul_f32 v157, %5, v157\n" \
  "v_mfma_f32_16x16x32_f16 v[148:151], v[56:59], v[88:91], v[148:151]\n" "v_mul_f32 v158, %5, v158\n" \
  "v_mul_f32 v159, %5, v159\n" "v_mfma_f32_16x16x32_f16 v[152:155], v[104:107], v[80:83], v[152:155]\n" \
  "v_cvt_pk_bf16_f32 v156, v156, v157\n" "v_cvt_pk_bf16_f32 v157, v158, v159\n" \
  "v_mfma_f32_16x16x32_f16 v[148:151], v[72:75], v[80:83], v[148:151]\n" "v_pk_min_u16 v156, v156, %4\n" \
  "v_pk_min_u16 v157, v157, %4\n" "v_mfma_f32_16x16x32_f16 v[152:155], v[44:47], v[112:115], v[152:155]\n" \
  "ds_write_b64 %12, v[156:157] offset:1024\n" "v_mfma_f32_16x16x32_f16 v[148:151], v[48:51], v[112:115], v[148:151]\n" \
  "s_nop 0\n" "ds_read_b128 v[88:91], %8 offset:2560\n" \
  "ds_read_b128 v[80:83], %8 offset:2624\n" "v_mfma_f32_16x16x32_f16 v[152:155], v[36:39], v[92:95], v[152:155]\n" \
  "s_nop 0\n" "ds_read_b128 v[156:159], %8 offset:2688\n" \
  "ds_read_b128 v[112:115], %8 offset:2752\n" "v_mfma_f32_16x16x32_f16 v[148:151], v[52:55], v[92:95], v[148:151]\n" \
  "v_mfma_f32_16x16x32_f16 v[152:155], v[68:71], v[24:27], v[152:155]\n" "v_mfma_f32_16x16x32_f16 v[148:151], v[16:19], v[24:27], v[148:151]\n" \
  "s_waitcnt lgkmcnt(5)\n" "ds_read_b128 v[92:95], %8 offset:2816\n" \
  "v_mfma_f32_16x16x32_f16 v[152:155], v[124:127], v[28:31], v[152:155]\n" "ds_read_b128 v[24:27], %8 offset:2880\n" \
  "v_mfma_f32_16x16x32_f16 v[148:151], v[120:123], v[28:31], v[148:151]\n" "ds_read_b128 v[28:31], %8 offset:2944\n" \
  "v_mfma_f32_16x16x32_f16 v[152:155], v[96:99], v[108:111], v[152:155]\n" "v_mfma_f32_16x16x32_f16 v[148:151], v[32:35], v[108:111], v[148:151]\n" \
  "v_mfma_f32_16x16x32_f16 v[144:147], v[40:43], v[108:111], v[144:147]\n" "v_mfma_f32_16x16x32_f16 v[136:139], v[132:135], v[108:111], v[136:139]\n" \
  "ds_read_b128 v[132:135], %8 offset:3008\n" "v_mfma_f32_16x16x32_f16 v[148:151], v[64:67], v[160:163], v[148:151]\n" \
  "v_mfma_f32_16x16x32_f16 v[144:147], v[20:23], v[160:163], v[144:147]\n" "v_mfma_f32_16x16x32_f16 v[148:151], v[60:63], v[84:87], v[148:151]\n" \
  "s_nop 0\n" "v_mul_f32 v152, %5, v152\n" \
  "v_mul_f32 v153, %5, v153\n" "v_mfma_f32_16x16x32_f16 v[144:147], v[56:59], v[84:87], v[144:147]\n" \
  "v_mul_f32 v154, %5, v154\n" "v_mul_f32 v155, %5, v155\n" \
  "v_mfma_f32_16x16x32_f16 v[148:151], v[104:107], v[128:131], v[148:151]\n" "v_cvt_pk_bf16_f32 v152, v152, v153\n" \
  "v_cvt_pk_bf16_f32 v153, v154, v155\n" "v_mfma_f32_16x16x32_f16 v[144:147], v[72:75], v[128:131], v[144:147]\n" \
  "v_pk_min_u16 v152, v152, %4\n" "v_pk_min_u16 v153, v153, %4\n" \
  "v_mfma_f32_16x16x32_f16 v[148:151], v[44:47], v[100:103], v[148:151]\n" "ds_write_b64 %12, v[152:153] offset:1536\n" \
  "v_mfma_f32_16x16x32_f16 v[144:147], v[48:51], v[100:103], v[144:147]\n" "v_mfma_f32_16x16x32_f16 v[148:151], v[36:39], v[164:167], v[148:151]\n" \
  "v_mfma_f32_16x16x32_f16 v[144:147], v[52:55], v[164:167], v[144:147]\n" "v_mfma_f32_16x16x32_f16 v[148:151], v[68:71], v[116:119], v[148:151]\n" \
  "v_mfma_f32_16x16x32_f16 v[144:147], v[16:19], v[116:119], v[144:147]\n" "s_waitcnt lgkmcnt(0)\n" \
  "v_mfma_f32_16x16x32_f16 v[148:151], v[124:127], v[76:79], v[148:151]\n" "s_barrier\n" \
  "ds_read_b128 v[108:111], %8 offset:3072\n" "ds_read_b128 v[160:163], %8 offset:3136\n" \
  "v_mfma_f32_16x16x32_f16 v[144:147], v[120:123], v[76:79], v[144:147]\n" "s_nop 0\n" \
  "ds_read_b128 v[84:87], %8 offset:3200\n" "ds_read_b128 v[128:131], %8 offset:3264\n" \
  "v_mfma_f32_16x16x32_f16 v[148:151], v[96:99], v[88:91], v[148:151]\n" "ds_read_b128 v[152:155], %8 offset:3328\n" \
  "v_mfma_f32_16x16x32_f16 v[144:147], v[32:35], v[88:91], v[144:147]\n" "s_nop 2\n" \
  "v_mfma_f32_16x16x32_f16 v[140:143], v[40:43], v[88:91], v[140:143]\n" "s_nop 1\n" \
  "v_mfma_f32_16x16x32_f16 v[144:147], v[64:67], v[80:83], v[144:147]\n" "v_mul_f32 v148, %5, v148\n" \
  "v_mul_f32 v149, %5, v149\n" "v_mfma_f32_16x16x32_f16 v[140:143], v[20:23], v[80:83], v[140:143]\n" \
  "v_mul_f32 v150, %5, v150\n" "v_mul_f32 v151, %5, v151\n" \
  "v_mfma_f32_16x16x32_f16 v[144:147], v[60:63], v[156:159], v[144:147]\n" "v_cvt_pk_bf16_f32 v148, v148, v149\n" \
  "v_cvt_pk_bf16_f32 v149, v150, v151\n" "v_mfma_f32_16x16x32_f16 v[140:143], v[56:59], v[156:159], v[140:143]\n" \
  "v_pk_min_u16 v148, v148, %4\n" "v_pk_min_u16 v149, v149, %4\n" \
  "v_mfma_f32_16x16x32_f16 v[144:147], v[104:107], v[112:115], v[144:147]\n" "ds_write_b64 %12, v[148:149] offset:2048\n" \
  "v_mfma_f32_16x16x32_f16 v[140:143], v[72:75], v[112:115], v[140:143]\n" "s_nop 0\n" \
  "ds_read_b128 v[100:103], %8 offset:3392\n" "ds_read_b128 v[164:167], %8 offset:3456\n" \
  "v_mfma_f32_16x16x32_f16 v[144:147], v[44:47], v[92:95], v[144:147]\n" "s_nop 0\n" \
  "ds_read_b128 v[116:119], %8 offset:3520\n" "ds_read_b128 v[76:79], %8 offset:3584\n" \
  "v_mfma_f32_16x16x32_f16 v[140:143], v[48:51], v[92:95], v[140:143]\n" "v_mfma_f32_16x16x32_f16 v[144:147], v[36:39], v[24:27], v[144:147]\n" \
  "v_mfma_f32_16x16x32_f16 v[140:143], v[52:55], v[24:27], v[140:143]\n" "ds_read_b128 v[88:91], %8 offset:3648\n" \
  "v_mfma_f32_16x16x32_f16 v[144:147], v[68:71], v[28:31], v[144:147]\n" "s_nop 0\n" \
  "ds_read_b128 v[80:83], %8 offset:3712\n" "ds_read_b128 v[156:159], %8 offset:3776\n" \
  "v_mfma_f32_16x16x32_f16 v[140:143], v[16:19], v[28:31], v[140:143]\n" "s_nop 0\n" \
  "ds_read_b128 v[148:151], %8 offset:3840\n" "ds_read_b128 v[112:115], %8 offset:3904\n" \
  "v_mfma_f32_16x16x32_f16 v[144:147], v[124:127], v[132:135], v[144:147]\n" "v_mfma_f32_16x16x32_f16 v[140:143], v[120:123], v[132:135], v[140:143]\n" \
  "s_waitcnt lgkmcnt(7)\n" "v_mfma_f32_16x16x32_f16 v[144:147], v[96:99], v[108:111], v[144:147]\n" \
  "ds_read_b128 v[92:95], %8 offset:3968\n" "v_mfma_f32_16x16x32_f16 v[140:143], v[32:35], v[108:111], v[140:143]\n" \
  "s_nop 0\n" "ds_read_b128 v[24:27], %8 offset:4032\n" \
  "ds_read_b128 v[28:31], %8 offset:4096\n" "v_mfma_f32_16x16x32_f16 v[136:139], v[40:43], v[108:111], v[136:139]\n" \
  "s_nop 1\n" "v_mfma_f32_16x16x32_f16 v[140:143], v[64:67], v[160:163], v[140:143]\n" \
  "v_mul_f32 v144, %5, v144\n" "v_mul_f32 v145, %5, v145\n" \
  "v_mfma_f32_16x16x32_f16 v[140:143], v[60:63], v[84:87], v[140:143]\n" "v_mul_f32 v146, %5, v146\n" \
  "v_mul_f32 v147, %5, v147\n" "v_mfma_f32_16x16x32_f16 v[140:143], v[104:107], v[128:131], v[140:143]\n" \
  "v_cvt_pk_bf16_f32 v144, v144, v145\n" "v_cvt_pk_bf16_f32 v145, v146, v147\n" \
  "v_mfma_f32_16x16x32_f16 v[140:143], v[44:47], v[152:155], v[140:143]\n" "v_pk_min_u16 v144, v144, %4\n" \
  "v_pk_min_u16 v145, v145, %4\n" "v_mfma_f32_16x16x32_f16 v[140:143], v[36:39], v[100:103], v[140:143]\n" \
  "ds_write_b64 %12, v[144:145] offset:2560\n" "v_mfma_f32_16x16x32_f16 v[140:143], v[68:71], v[164:167], v[140:143]\n" \
  "s_waitcnt lgkmcnt(3)\n" "v_mfma_f32_16x16x32_f16 v[140:143], v[124:127], v[116:119], v[140:143]\n" \
  "v_mfma_f32_16x16x32_f16 v[140:143], v[96:99], v[76:79], v[140:143]\n" "s_nop 2\n" \
  "v_mfma_f32_16x16x32_f16 v[136:139], v[20:23], v[160:163], v[136:139]\n" "s_nop 2\n" \
  "v_mfma_f32_16x16x32_f16 v[136:139], v[56:59], v[84:87], v[136:139]\n" "s_waitcnt lgkmcnt(0)\n" \
  "v_mul_f32 v140, %5, v140\n" "v_mul_f32 v141, %5, v141\n" \
  "v_mfma_f32_16x16x32_f16 v[136:139], v[72:75], v[128:131], v[136:139]\n" "s_barrier\n" \
  "v_mul_f32 v142, %5, v142\n" "v_mul_f32 v143, %5, v143\n" \
  "v_mfma_f32_16x16x32_f16 v[136:139], v[48:51], v[152:155], v[136:139]\n" "v_cvt_pk_bf16_f32 v140, v140, v141\n" \
  "v_cvt_pk_bf16_f32 v141, v142, v143\n" "v_mfma_f32_16x16x32_f16 v[136:139], v[52:55], v[100:103], v[136:139]\n" \
  "v_pk_min_u16 v140, v140, %4\n" "v_pk_min_u16 v141, v141, %4\n" \
  "v_mfma_f32_16x16x32_f16 v[136:139], v[16:19], v[164:167], v[136:139]\n" "ds_write_b64 %12, v[140:141] offset:3072\n" \
  "v_mfma_f32_16x16x32_f16 v[136:139], v[120:123], v[116:119], v[136:139]\n" "v_mfma_f32_16x16x32_f16 v[136:139], v[32:35], v[76:79], v[136:139]\n" \
  "v_mfma_f32_16x16x32_f16 v[136:139], v[64:67], v[88:91], v[136:139]\n" "v_mfma_f32_16x16x32_f16 v[136:139], v[60:63], v[80:83], v[136:139]\n" \
  "v_mfma_f32_16x16x32_f16 v[136:139], v[104:107], v[156:159], v[136:139]\n" "v_mfma_f32_16x16x32_f16 v[136:139], v[44:47], v[148:151], v[136:139]\n" \
  "v_mfma_f32_16x16x32_f16 v[136:139], v[36:39], v[112:115], v[136:139]\n" "v_mfma_f32_16x16x32_f16 v[136:139], v[68:71], v[92:95], v[136:139]\n" \
  "v_mfma_f32_16x16x32_f16 v[136:139], v[124:127], v[24:27], v[136:139]\n" "v_mfma_f32_16x16x32_f16 v[136:139], v[96:99], v[28:31], v[136:139]\n" \
  "s_nop 2\n" "s_nop 4\n" \
  "v_mul_f32 v136, %5, v136\n" "v_mul_f32 v137, %5, v137\n" \
  "v_mul_f32 v138, %5, v138\n" "v_mul_f32 v139, %5, v139\n" \
  "v_cvt_pk_bf16_f32 v136, v136, v137\n" "v_cvt_pk_bf16_f32 v137, v138, v139\n" \
  "v_pk_min_u16 v136, v136, %4\n" "v_pk_min_u16 v137, v137, %4\n" \
  "ds_write_b64 %12, v[136:137] offset:3584\n" "s_waitcnt lgkmcnt(0)\n" \
  "s_barrier\n"
#define CONV_ASM_CLOBBERS "v32", "v33", "v34", "v35", "v36", "v37", "v38", "v39", "v40", "v41", "v42", "v43", "v44", "v45", "v46", "v47", "v48", "v49", "v50", "v51", "v52", "v53", "v54", "v55", "v56", "v57", "v58", "v59", "v60", "v61", "v62", "v63", "v64", "v65", "v66", "v67", "v68", "v69", "v70", "v71", "v72", "v73", "v74", "v75", "v76", "v77", "v78", "v79", "v80", "v81", "v82", "v83", "v84", "v85", "v86", "v87", "v88", "v89", "v90", "v91", "v92", "v93", "v94", "v95", "v96", "v97", "v98", "v99", "v100", "v101", "v102", "v103", "v104", "v105", "v106", "v107", "v108", "v109", "v110", "v111", "v112", "v113", "v114", "v115", "v116", "v117", "v118", "v119", "v120", "v121", "v122", "v123", "v124", "v125", "v126", "v127", "v128", "v129", "v130", "v131", "v132", "v133", "v134", "v135", "v136", "v137", "v138", "v139", "v140", "v141", "v142", "v143", "v144", "v145", "v146", "v147", "v148", "v149", "v150", "v151", "v152", "v153", "v154", "v155", "v156", "v157", "v158", "v159", "v160", "v161", "v162", "v163", "v164", "v165", "v166", "v167", "memory"
#define CONV_ASM_PRE(q) "+{v[16:19]}"(q[0]), "+{v[20:23]}"(q[1]), "+{v[24:27]}"(q[2]), "+{v[28:31]}"(q[3])

struct Params {
  const u16 *x, *lar, *aim, *crl, *cim, *ldt, *dskip, *wr, *wg, *wu, *wd;
  u16* out;
  char* ws;
};

typedef unsigned long long u64;
typedef u64 v4q __attribute__((ext_vector_type(4)));
typedef unsigned v16u __attribute__((ext_vector_type(16)));
#define GAS __attribute__((address_space(1)))

#define T3_ASM
#define T3_OUT
#define T1_ASM
#define T2_ASM

__global__ __launch_bounds__(NT, 3) void k_main(Params) {
  extern __shared__ __attribute__((aligned(16))) char smem[];
  u64 kx, kw, t0_, t1_, t2_;
  v4q kb_;
  unsigned epoch, sep;
  v4u ga, gb, gc, gd;
  const unsigned bid = blockIdx.x;
  asm volatile(
      ".p2align 12\n\t"
      "s_load_dwordx16 s[8:23], s[0:1], 0x0\n\t"
      "s_getpc_b64 s[4:5]\n\t"
      "s_load_dwordx2 s[36:37], s[0:1], 0x60\n\t"
      "s_load_dwordx8 s[24:31], s[0:1], 0x40\n\t" T0_ASM
      "v_lshrrev_b32 v17, 6, v0\n\t"
      "s_lshl_b32 s3, s2, 4\n\t"
      "v_mov_b32 v1, 0\n\t"
      "v_lshl_add_u32 v17, v17, 12, s3\n\t"
      "s_cmpk_lt_u32 s2, 144\n\t"
      "v_min_u32 v17, %[code], v17\n\t"
      "global_load_dword v17, v17, s[4:5]\n\t"
      "s_cbranch_scc1 Lc_%=\n\t"
      "s_add_i32 s3, s2, 0xffffff70\n\t"
      "v_bfe_u32 v18, v0, 3, 3\n\t"
      "v_lshlrev_b32 v19, 1, v0\n\t"
      "s_mulk_i32 s3, 18432\n\t"
      "v_lshlrev_b32 v20, 4, v0\n\t"
      "s_movk_i32 s6, 127\n\t"
      "v_mul_u32_u24 v18, 1152, v18\n\t"
      "v_bfi_b32 v19, s6, v20, v19\n\t"
      "v_add3_u32 v18, v18, v19, s3\n\t"
      "s_cmpk_lt_u32 s2, 240\n\t"
      "v_add_u32 v19, 9216, v18\n\t"
      "v_add_u32 v10, 294912, v18\n\t"
      "v_add_u32 v14, 304128, v18\n\t"
      "s_waitcnt lgkmcnt(0)\n\t"
      "s_cbranch_scc1 Lx_%=\n\t"
      "s_sleep %[x2d]\n"
      "Lx_%=:\n\t"
      "global_load_dwordx4 v[2:5], v18, s[8:9]\n\t"
      "global_load_dwordx4 v[6:9], v19, s[8:9]\n\t"
      "s_load_dword s33, s[36:37], 0x0\n\t"
      "s_branch Le_%=\n"
      "Lw_%=:\n\t"
      "v_lshlrev_b32 v7, 2, v0\n\t"
      "s_waitcnt lgkmcnt(0)\n\t"
      "s_sleep 3\n\t"
      "global_load_dword v1, v1, s[36:37]\n\t"
      "global_load_dword v2, v7, s[22:23] offset:-2048\n\t"
      "global_load_dword v3, v7, s[22:23] offset:-1792\n\t"
      "global_load_dword v4, v7, s[22:23] offset:-1536\n\t"
      "global_load_dword v5, v7, s[22:23] offset:-1280\n\t"
      "global_load_dword v6, v7, s[22:23] offset:-1152\n\t"
      "s_branch Le_%=\n"
      "Lc_%=:\n\t"
      "v_readfirstlane_b32 s3, v0\n\t"
      "v_bfe_u32 v6, v0, 6, 2\n\t"
      "s_lshl_b32 s4, s2, 2\n\t"
      "v_bfe_u32 v5, v0, 0, 8\n\t"
      "s_lshl_b32 s5, s2, 10\n\t"
      "s_cmpk_ge_u32 s3, 512\n\t"
      "v_add_lshl_u32 v16, v6, s4, 1\n\t"
      "s_cbranch_scc1 Lw_%=\n\t"
      "v_lshl_add_u32 v5, v5, 2, s5\n\t"
      "s_lshr_b32 s4, s3, 8\n\t"
      "s_bfe_u32 s5, s3, 0x20006\n\t"
      "v_lshl_add_u32 v3, s4, 1, v5\n\t"
      "v_and_b32 v7, 63, v0\n\t"
      "s_mulk_i32 s5, %[ustr]\n\t"
      "s_mulk_i32 s4, 3584\n\t"
      "v_mov_b64 v[10:11], 0\n\t"
      "s_add_i32 s5, s5, s4\n\t"
      "v_mov_b64 v[12:13], 0\n\t"
      "s_add_i32 s5, s5, %[uimg]\n\t"
      "v_lshlrev_b32 v9, 4, v7\n\t"
      "s_lshl_b32 s3, s3, 8\n\t"
      "v_add_u32 v8, s5, v9\n\t"
      "v_add_u32 v9, %[filt], v9\n\t"
      "ds_write_b128 v8, v[10:13]\n\t"
      "ds_write_b128 v8, v[10:13] offset:32\n\t"
      "ds_write_b128 v9, v[10:13] offset:4096\n\t"
      "v_and_b32 v15, 31, v7\n\t"
      "s_lshr_b32 s4, s3, 14\n\t"
      "v_lshrrev_b32 v14, 5, v7\n\t"
      "s_mulk_i32 s4, %[tstr]\n\t"
      "v_mul_u32_u24 v15, %[trow], v15\n\t"
      "s_mov_b32 m0, s4\n\t"
      "v_lshlrev_b32 v14, 4, v14\n\t"
      "v_add3_u32 v41, v14, v15, s4\n\t"
      "s_mov_b32 s4, 0xfe0001ff\n\t"
      "s_mov_b32 s5, 0x0380007c\n\t"
      "s_lshr_b32 s6, s3, 10\n\t"
      "v_and_b32 v8, 15, v7\n\t"
      "s_lshr_b64 s[4:5], s[4:5], s6\n\t"
      "v_lshrrev_b32 v14, 1, v7\n\t"
      "s_and_b32 s4, s4, 0xffff\n\t"
      "v_and_b32 v14, 24, v14\n\t"
      "v_lshlrev_b32 v8, v8, s4\n\t"
      "s_mov_b32 s4, 0x8001\n\t"
      "v_lshrrev_b32 v8, v14, v8\n\t"
      "s_mov_b32 s5, 0x10001\n\t"
      "v_bfe_u32 v10, v8, 0, 2\n\t"
      "v_bfe_u32 v11, v8, 2, 2\n\t"
      "v_bfe_u32 v12, v8, 4, 2\n\t"
      "v_bfe_u32 v13, v8, 6, 2\n\t"
      "s_bfe_u32 s6, s3, 0x2000e\n\t"
      "v_mul_u32_u24 v10, s4, v10\n\t"
      "v_mul_u32_u24 v11, s4, v11\n\t"
      "v_mul_u32_u24 v12, s4, v12\n\t"
      "v_mul_u32_u24 v13, s4, v13\n\t"
      "s_movk_i32 s4, 0x3c00\n\t"
      "v_and_b32 v10, s5, v10\n\t"
      "v_and_b32 v11, s5, v11\n\t"
      "v_and_b32 v12, s5, v12\n\t"
      "v_and_b32 v13, s5, v13\n\t"
      "v_lshl_add_u32 v9, s6, 10, v9\n\t"
      "v_mul_u32_u24 v10, s4, v10\n\t"
      "v_mul_u32_u24 v11, s4, v11\n\t"
      "v_mul_u32_u24 v12, s4, v12\n\t"
      "v_mul_u32_u24 v13, s4, v13\n\t"
      "ds_write_b128 v9, v[10:13]\n\t"
      T2_ASM "s_waitcnt lgkmcnt(0)\n\t" T1_ASM
      "global_load_ushort v6, v16, s[18:19]\n\t"
      "global_load_ushort v2, v3, s[10:11]\n\t"
      "global_load_ushort v3, v3, s[12:13]\n\t"
      "s_and_b32 s7, s3, 0xc000\n\t"
      "v_lshlrev_b32 v47, 4, v7\n\t"
      "s_add_i32 s4, s7, %[dummy]\n\t"
      "v_add_u32 v48, s7, v47\n\t"
      "s_bitcmp1_b32 s3, 16\n\t"
      "v_lshl_add_u32 v46, v7, 2, s4\n\t"
      "s_cbranch_scc1 Lm_%=\n\t"
      "s_cmpk_lt_u32 s2, 128\n\t"
      "v_and_b32 v8, 3, v7\n\t"
      "s_movk_i32 s4, %[u2]\n\t"
      "v_bfe_u32 v10, v7, 2, 2\n\t"
      "s_cmovk_i32 s4, %[u1]\n\t"
      "v_lshrrev_b32 v11, 5, v7\n\t"
      "s_movk_i32 s5, %[y2]\n\t"
      "v_and_b32 v12, 0x83, v0\n\t"
      "s_cmovk_i32 s5, %[y1]\n\t"
      "v_and_b32 v13, 0x70, v0\n\t"
      "s_add_i32 s4, s4, s2\n\t"
      "v_mul_u32_u24 v8, %[ustr] - 4, v8\n\t"
      "s_add_i32 s5, s5, s2\n\t"
      "v_mul_u32_u24 v10, 0x4020, v10\n\t"
      "s_lshl_b32 s4, s4, 14\n\t"
      "v_mul_u32_u24 v11, 7680, v11\n\t"
      "s_lshl_b32 s5, s5, 5\n\t"
      "v_mul_u32_u24 v12, 132, v12\n\t"
      "s_or_b32 s5, s5, s6\n\t"
      "v_lshl_add_u32 v7, v0, 4, s4\n\t"
      "s_lshl_b32 s5, s5, 9\n\t"
      "v_lshl_add_u32 v8, v0, 2, v8\n\t"
      "s_lshl2_add_u32 s4, s6, %[misc]\n\t"
      "v_and_b32 v12, 0x288, v12\n\t"
      "v_add3_u32 v11, v11, v47, s5\n\t"
      "v_add_u32 v9, %[uimg] + 520 + 2048, v8\n\t"
      "v_add_u32 v8, %[uimg] + 520, v8\n\t"
      "v_add3_u32 v10, v10, v12, v13\n\t"
      "v_mov_b32 v42, v48\n\t"
      "s_movk_i32 exec_lo, 0xff80\n\t"
      "v_add_u32 v42, 1024, v48\n\t"
      "s_mov_b32 exec_lo, 0xfff80000\n\t"
      "v_add_u32 v42, 2048, v48\n\t"
      "s_mov_b32 exec_lo, 0\n\t"
      "v_add_u32 v42, 2304, v48\n\t"
      "s_movk_i32 exec_hi, 0xff00\n\t"
      "v_add_u32 v42, 2560, v48\n\t"
      "s_mov_b64 exec, 1\n\t"
      "v_mov_b32 v46, s4\n\t"
      "s_branch Ll_%=\n"
      "Lm_%=:\n\t"
      "s_mul_i32 s4, s6, %[ustr] - 16384\n\t"
      "v_and_b32 v8, 15, v7\n\t"
      "s_add_i32 s4, s4, %[uimg]\n\t"
      "v_and_or_b32 v12, v7, 48, s7\n\t"
      "s_mul_i32 s5, s6, 0x4020\n\t"
      "v_and_b32 v14, 1, v7\n\t"
      "s_addk_i32 s5, %[ly]\n\t"
      "v_lshlrev_b32 v9, 4, v8\n\t"
      "v_and_b32 v44, 32, v7\n\t"
      "v_mad_i32_i24 v11, v8, -8, v12\n\t"
      "v_add3_u32 v10, v12, v9, s4\n\t"
      "s_add_i32 s4, s7, %[kn] + 1120\n\t"
      "v_mul_u32_u24 v14, %[kc] + 2, v14\n\t"
      "v_lshl_add_u32 v13, v7, 3, s5\n\t"
      "s_add_i32 s5, s7, %[kn] + 2 * %[kc] + 380\n\t"
      "v_sub_u32 v42, s4, v47\n\t"
      "s_add_i32 s4, s7, %[kn] + 2 * %[kc] + 512 + 190\n\t"
      "v_mad_i32_i24 v15, v8, -2, v12\n\t"
      "v_add_u32 v44, v44, v7\n\t"
      "v_mad_i32_i24 v12, v8, -4, v12\n\t"
      "v_mad_i32_i24 v43, v7, -4, s5\n\t"
      "v_add_u32 v9, v10, v9\n\t"
      "v_mad_i32_i24 v44, v44, -2, s4\n\t"
      "v_add_u32 v45, 1, v7\n\t"
      "v_sub_u32 v8, v15, v14\n\t"
      "v_and_b32 v45, -2, v45\n\t"
      "v_add_u32 v11, %[kn] + 2 * %[kc] + 120, v11\n\t"
      "v_add_u32 v15, v45, v14\n\t"
      "v_add_u32 v12, %[kn] + 2 * %[kc] + 512 + 60, v12\n\t"
      "v_add_u32 v7, %[kn] + %[kc] + 40, v8\n\t"
      "v_add_u32 v8, %[kn] + %[kc] + 40 + 1024, v8\n\t"
      "v_add_u32 v47, %[filt], v47\n\t"
      "v_mov_b32 v14, 0xfffefffe\n\t"
      "s_add_i32 s4, s7, %[kn] + %[kc] + 2 + 48\n\t"
      "v_mov_b32 v45, 0x0706\n\t"
      "s_mov_b64 exec, 0x7ff\n\t"
      "v_sub_u32 v46, s4, v15\n\t"
      "s_movk_i32 exec_lo, 0x7f\n\t"
      "v_add_u32 v46, 64, v46\n\t"
      "v_mov_b32 v45, 0x0504\n\t"
      "s_mov_b32 exec_lo, 1\n\t"
      "v_add_u32 v46, 1022, v46\n\t"
      "v_mov_b32 v45, 0x0302\n"
      "Ll_%=:\n\t"
      "s_mov_b64 exec, -1\n\t"
      "global_load_dword v4, v5, s[14:15]\n\t"
      "global_load_dword v5, v5, s[16:17]\n\t"
      "global_load_ushort v16, v16, s[20:21]\n\t"
      "global_load_dword v1, v1, s[36:37]\n"
      "Le_%=:"
      : "=&{s[8:9]}"(kx), "=&{s[36:37]}"(kw), "=&{s[24:31]}"(kb_), "=&{s[34:35]}"(t0_), "=&{s33}"(sep)
      : [code] "n"(CODE_BYTES), [ustr] "n"(USTR), [uimg] "n"(L_UIMG), [filt] "n"(L_FILT), "{s[0:1]}"(__builtin_amdgcn_kernarg_segment_ptr()), "{s2}"(bid),
        "{v0}"(threadIdx.x), [dummy] "n"(L_DUMMY), [misc] "n"(L_MISC), [kn] "n"(L_KN), [kc] "n"(KCOPY), [ly] "n"(L_Y), [u1] "n"(WS_U / GSTR),
        [u2] "n"(WS_U2 / GSTR - 128), [y1] "n"(WS_Y / GSTR), [y2] "n"(WS_Y2 / GSTR - 128), [x2d] "n"(X2_DELAY), [tstr] "n"(TSTR), [trow] "n"(TROW)
      : "s3", "s4", "s5", "s6", "s7", "s10", "s11", "s12", "s13", "s14", "s15", "s16", "s17", "s18", "s19", "s20", "s21", "s22", "s23", "v1", "v2", "v3", "v4",
        "v5", "v6", "v7", "v8", "v9", "v10", "v11", "v12", "v13", "v14", "v15", "v16", "v17", "v18", "v19", "v20", "v21", "v22", "v23", "v24", "v25",
        "v26", "v27", "v28", "v29", "v30", "v31", "v32", "v33", "v34", "v35", "v36", "v37", "v38", "v39", "v40", "v41", "v42", "v43", "v44", "v45",
        "v46", "v47", "v48", "scc", "memory");
  static_assert(WSCR == 16384 && GSTR == 16384 && L_K4 == L_KN + 2 * KCOPY && L_K8 == L_K4 + 512, "constants of the block above");
  v16u k8;
  asm volatile("" : "={v1}"(epoch), "={v[2:5]}"(ga), "={v[6:9]}"(gb), "={v[10:13]}"(gc), "={v[14:17]}"(gd), "={v[34:49]}"(k8)::"memory");
  SCHED();
  const int tid = threadIdx.x, lane = tid & 63;
  int wave = __builtin_amdgcn_readfirstlane(tid >> 6);
  const int xcd = bid & 7;
  int loc = bid >> 3;
  STAMP_INIT();
  unsigned pcnt_ = 0;
  STAMP(11);
  const int cl = lane & 15, g = lane >> 4;
  const int cg = bid;
  const int fj = xcd * 9 + (loc - 18);
  const int rb = bid;
  const int side = (bid << 11) & 8192;
  const int ybase = (cg < 128 ? WS_Y : WS_Y2 - 128 * GSTR) + cg * GSTR;
  const int tile = bid - NCONV;
  struct { const GAS char* x; GAS char* ws; } p = {(const GAS char*)kx, (GAS char*)kw};
  const rsrc_t rs = __builtin_amdgcn_make_buffer_rsrc((void*)p.ws, 0, WS_END, 0x00020000);
  const v4u sent4 = {SENT, SENT, SENT, SENT};
  const rsrc_t ro = __builtin_amdgcn_make_buffer_rsrc((void*)kb_[3], 0, SEQ * DM * 2, 0x00020000);
  if (loc < 18) {
    BAR();
    if (wave == 8) asm volatile("s_mov_b64 exec, 0\n\ts_branch Lk_conv\nLk_back:\n\ts_mov_b64 exec, -1");
  }
  asm volatile("" : "+s"(loc), "+s"(wave));

  if (loc < 18) {
    int ysrc = (wave < 8 ? WS_Y : WS_Y2 - 128 * GSTR) + 16 * wave * GSTR + side + (loc * 4 + (rb & 3)) * 128;
    unsigned ylag = T_Y3 - ((0x000f2b46u >> (2 * (loc & 12))) & 255u);
    asm volatile("" : "+s"(ysrc), "+s"(ylag));
    unsigned l_x, l_x2, l_sw;
    v4u cw[2];
    f32x4 xf[4];
    v2u unit;
    auto ln2_operands = [&](int lane) {
      const int cl = lane & 15, g = lane >> 4;
      l_x = L_XROW + wave * 2048 + cl * 128 + (g ^ (cl & 7)) * 16;
      l_x2 = l_x ^ 64;
      const v4u gx[2] = {*(const LAS v4u*)l_x, *(const LAS v4u*)l_x2};
      const unsigned l_w = (lane & 3) == 1 ? L_WR + wave * 128 + g * 16 : L_MISC + 128;
      cw[0] = *(const LAS v4u*)l_w;
      cw[1] = *(const LAS v4u*)(l_w + 64);
      l_sw = (cl >> 2) == g ? L_RED + wave * 256 + cl * 16 : 7 * WSCR + lane * 16;
      const u64 e = (u64)((cl >> 2) == g ? 0x3f80u : 0u) << (16 * (cl & 3));
      unit = v2u{(unsigned)e, (unsigned)(e >> 32)};
      PIN(unit);
#pragma unroll
      for (int i = 0; i < 4; i++) {
        xf[i] = widen(unit, v2u{gx[i >> 1][2 * (i & 1)], gx[i >> 1][2 * (i & 1) + 1]}, f32x4{0.f, 0.f, 0.f, 0.f});
        PIN(xf[i]);
      }
      PIN(cw[0]); PIN(cw[1]); PIN(l_x); PIN(l_x2); PIN(l_sw);
      SCHED();
    };
    if (wave < 8) {
      const int ch = wave & 3;
      volatile LAS unsigned* const pf = (volatile LAS unsigned*)(smem + L_MISC);
      if (wave < 4) __builtin_amdgcn_s_setprio(2); else __builtin_amdgcn_s_setprio(1);
      STAMP(12);
      unsigned epoch, gdt, gre, gim, gdd;
      v2u gc_;
      unsigned rb_;
      unsigned op[7], top, a_kn, a_k4, a_k8, sel_sp, a_sp, a_q, pa;
      asm volatile("s_waitcnt vmcnt(%22) ; early loads"
                   : "={v1}"(epoch), "={v2}"(gre), "={v3}"(gim), "={v[4:5]}"(gc_), "={v6}"(gdt), "={v7}"(op[0]), "={v8}"(op[1]), "={v9}"(op[2]),
                     "={v10}"(op[3]), "={v11}"(op[4]), "={v12}"(op[5]), "={v13}"(op[6]), "={v14}"(top), "={v16}"(gdd),
                     "={v41}"(rb_), "={v42}"(a_kn), "={v43}"(a_k4), "={v44}"(a_k8), "={v45}"(sel_sp), "={v46}"(a_sp), "={v47}"(a_q), "={v48}"(pa)
                   : "n"(4) : "memory");
      SCHED();
      STAMP(5);
      SCHED();
      v4u tx;
      const float dt = rbf(exp_c(blo(gdt)));
      const float ar = -rbf(exp_c(blo(gre))) * dt * 1.44269504f;
      const float aie = blo(gim) * dt;
      const float ph = aie * 0.159154936f;
      const float pl = fmaf(aie, 0.159154936f, -ph) + aie * 7.13048051e-9f;
#define put1(row, x) asm volatile("ds_write_addtid_b32 %0 offset:%1" ::"v"(x), "n"(L_TBL + ((row) & 31) * TROW) : "memory")
#define put(row, x) do { put1(row, x); SCHED(); } while (0)
      cz e1, e2, e4, e8, e16, e32, e64, e128, e256, e512, e1024;
      e8.v = cexp_p2(8.f, ar, ph, pl);
      e64.v = cexp_p2(64.f, ar, ph, pl);
      e1.v = cexp_p2(1.f, ar, ph, pl);
      e512.v = cexp_p2(512.f, ar, ph, pl);
      unsigned ha[4], hs[4], hb[8], hr[3][8];
      f32x2 e24, e96, e160, e192, e224;
      hb[0] = 0x3c00u;
      PIN(hb[0]); PIN(e8.v); PIN(e64.v); PIN(e1.v); PIN(e512.v);
      SCHED();
      swap_(e8); swap_(e64); swap_(e1); swap_(e512); half_(ha[1], e8.v);
      mul_(e16.v, e8.v, e8); mul_(e128.v, e64.v, e64); mul_(e2.v, e1.v, e1); mul_(e1024.v, e512.v, e512); half_(hb[2], e64.v);
      fma_(e16.v, e8.v, e8); fma_(e128.v, e64.v, e64); fma_(e2.v, e1.v, e1); fma_(e1024.v, e512.v, e512); half_(hs[1], e8.s);
      swap_(e16); swap_(e128); swap_(e2); swap_(e1024); half_(ha[2], e16.v);
      mul_(e32.v, e16.v, e16); mul_(e192, e128.v, e64); mul_(e24, e16.v, e8); mul_(e256.v, e128.v, e128); mul_(e4.v, e2.v, e2); half_(hb[4], e128.v);
      fma_(e32.v, e16.v, e16); fma_(e192, e128.v, e64); fma_(e24, e16.v, e8); fma_(e256.v, e128.v, e128); fma_(e4.v, e2.v, e2); half_(hs[2], e16.s);
      swap_(e32); swap_(e256); swap_(e4); half_(hb[1], e32.v);
      mul_(e96, e64.v, e32); mul_(e160, e128.v, e32); mul_(e224, e192, e32); half_(hb[6], e192);
      fma_(e96, e64.v, e32); fma_(e160, e128.v, e32); fma_(e224, e192, e32); half_(ha[3], e24);
      half_(hb[3], e96); half_(hb[5], e160); half_(hb[7], e224); half_(hs[3], e24.yx);
      put(0, hb[0]); put(1, ha[1]); put(8, hb[2]); put(2, ha[2]); put(16, hb[4]); put(4, hb[1]); put(24, hb[6]); put(3, ha[3]); put(12, hb[3]); put(20, hb[5]); put(28, hb[7]);
      asm volatile("s_waitcnt vmcnt(0) ; first loads" : "+{v1}"(epoch), "+{v[4:5]}"(gc_), "+{v16}"(gdd)::"memory");
      float cre = (wave < 4 ? blo(gc_.x) : bhi(gc_.x)) * dt, cim = (wave < 4 ? blo(gc_.y) : bhi(gc_.y)) * dt;
      const float dsk = blo(gdd);
      unsigned cm = __float_as_uint(fmaxf(fmaxf(fmaxf(fabsf(blo(gc_.x)), fabsf(bhi(gc_.x))), fmaxf(fabsf(blo(gc_.y)), fabsf(bhi(gc_.y)))) * dt, fabsf(dsk) * 0.0078125f));
      PIN(cm); PIN(cre); PIN(cim);
      SCHED();
      unsigned ce;
      float csc;
      f32x2 w0;
#define wmax_(c) do { cm = max(cm, dppu<c, 0xf>(cm)); PIN(cm); SCHED(); } while (0)
#pragma unroll
      for (int i = 1; i < 4; i++) {
#pragma unroll
        for (int k = 1; k < 8; k++) {
          mulh_(hr[i - 1][k], hb[k], hs[i]);
          if (i == 3 && k == 3) {
#pragma unroll
            for (int q = 2; q < 16; q++) put(4 * (q >> 1) + 1 + (q & 1), hr[q & 1][q >> 1]);
          }
          if (i == 1 && k == 2) wmax_(0x111);
          if (i == 1 && k == 4) wmax_(0x112);
          if (i == 1 && k == 6) wmax_(0x114);
        }
#pragma unroll
        for (int k = 1; k < 8; k++) {
          fmah_(hr[i - 1][k], hb[k], ha[i]);
          if (i == 1 && k == 1) wmax_(0x118);
          if (i == 1 && k == 3) {
            unsigned a = (unsigned)__builtin_amdgcn_readlane((int)cm, 15), b = (unsigned)__builtin_amdgcn_readlane((int)cm, 31);
            unsigned c = (unsigned)__builtin_amdgcn_readlane((int)cm, 47), d = (unsigned)__builtin_amdgcn_readlane((int)cm, 63);
            asm("s_max_u32 %0, %1, %2\n\ts_max_u32 %0, %0, %3\n\ts_max_u32 %0, %0, %4\n\ts_lshr_b32 %0, %0, 23\n\ts_max_u32 %0, %0, 40\n\ts_min_u32 %0, %0, 250"
                : "=&s"(ce) : "s"(a), "s"(b), "s"(c), "s"(d) : "scc");
            csc = __uint_as_float((261u - ce) << 23);
            asm volatile("" : "+s"(csc), "+s"(ce));
            SCHED();
          }
          if (i == 2 && k == 1) {
            w0 = f32x2{cre * csc, -cim * csc};
            PIN(w0);
            SCHED();
          }
        }
      }
      if (wave >= 4) STAMP(11);
      f16x8 fa[8], fb[8];
      f32x16 acc;
      unsigned have;
      auto partial = [&]() {
        MEMBAR();
#pragma unroll
        for (int k = 0; k < 4; k++) {
          const f32x4 o = *(const LAS f32x4*)(pa + k * 1024);
#pragma unroll
          for (int i = 0; i < 4; i++) acc[4 * k + i] = o[i];
        }
      };
#define KSTEP(k) SCHED(); acc = __builtin_amdgcn_mfma_f32_32x32x16_f16(fa[k], fb[k], acc, 0, 0, 0); SCHED()
      {
        f32x2 z1, z2, z3, z4, z5, z6, z7, z8, z256, z258, z260, z262, z264, z516, z520, z772, z776, z1032, z1288, z1544, z1800;
        unsigned h0, h1, h2, h3, h4, h5, h6, h7, h8, h256, h258, h260, h262, h264, h516, h520, h772, h776, h1032, h1288, h1544, h1800;
#define get(k) do { fb[k] = *(const LAS f16x8*)(rb_ + L_TBL + 32 * (k)); MEMBAR(); SCHED(); } while (0)
        mulc_(z8, w0, e8); mulc_(z1, w0, e1); mulc_(z2, w0, e2); mulc_(z4, w0, e4); mulc_(z256, w0, e256); half_(h0, w0);
        fmac_(z8, w0, e8); fmac_(z1, w0, e1); fmac_(z2, w0, e2); fmac_(z4, w0, e4); fmac_(z256, w0, e256);
        mulc_(z264, z8, e256); mulc_(z520, z8, e512); mulc_(z1032, z8, e1024); mulc_(z3, z1, e2); mulc_(z258, z2, e256); mulc_(z260, z4, e256);
        half_(h8, z8); half_(h1, z1); half_(h256, z256); put(7, hr[2][1]); put(11, hr[2][2]); put(15, hr[2][3]); put(19, hr[2][4]); put(23, hr[2][5]); put(27, hr[2][6]); put(31, hr[2][7]); get(0); get(1); get(2); get(3); get(4); get(5); get(6); get(7);
        fmac_(z264, z8, e256); fmac_(z520, z8, e512); fmac_(z1032, z8, e1024); fmac_(z3, z1, e2); fmac_(z258, z2, e256); fmac_(z260, z4, e256);
        half_(h2, z2); half_(h4, z4); half_(h264, z264);
        mulc_(z776, z264, e512); mulc_(z1288, z264, e1024); mulc_(z1544, z520, e1024); mulc_(z5, z1, e4); mulc_(z6, z2, e4);
        half_(h520, z520); half_(h1032, z1032); half_(h3, z3);
        fmac_(z776, z264, e512); fmac_(z1288, z264, e1024); fmac_(z1544, z520, e1024); fmac_(z5, z1, e4); fmac_(z6, z2, e4); put(16, h0); put(0, h8); put(11, h1); put(15, h256); put(20, h256); put(10, h2); put(8, h4); put(4, h264); put(5, h264); put(17, h520); put(19, h1032);
        half_(h258, z258); half_(h260, z260);
        mulc_(z1800, z776, e1024); mulc_(z516, z4, e512); mulc_(z7, z3, e4); mulc_(z262, z258, e4); mulc_(z772, z260, e512);
        half_(h776, z776); half_(h1288, z1288); half_(h1544, z1544); half_(h5, z5); half_(h6, z6);
        fmac_(z1800, z776, e1024); fmac_(z516, z4, e512); fmac_(z7, z3, e4); fmac_(z262, z258, e4); fmac_(z772, z260, e512); put(9, h3); put(14, h258); put(13, h260); put(7, h260); put(12, h260); put(21, h776); put(24, h1288); put(23, h1544); put(3, h5); put(2, h6);
        if (wave >= 4) {
          have = pf[ch];
          partial();
        }
        SCHED();
        half_(h1800, z1800); half_(h516, z516); half_(h7, z7); half_(h262, z262); half_(h772, z772); put(28, h1800); put(18, h516); put(1, h7); put(6, h262); put(22, h772);
      }
      MEMBAR();
#pragma unroll
      for (int k = 0; k < 8; k++) fa[k] = *(const LAS f16x8*)(rb_ + L_TBL + 32 * k);
      SCHED();
      if (wave < 4) {
        acc = f32x16{};
        KSTEP(0);
        *(LAS v4u*)(a_kn + L_KN) = v4u{0u, 0u, 0u, 0u};
        KSTEP(1);
        tx = tag_bits(__builtin_amdgcn_readfirstlane(epoch) + 1u) ^ 0x40004u;
        asm volatile("" : "+s"(tx));
        KSTEP(2);
        asm volatile("" : "+{v7}"(op[0]), "+{v8}"(op[1]), "+{v9}"(op[2]), "+{v10}"(op[3]), "+{v11}"(op[4]));
        float d0 = __builtin_amdgcn_inverse_ballot_w64(1ull) ? dsk * csc * 0.5f : 0.f;
        unsigned one = 1u;
        PIN(d0); PIN(one);
#pragma unroll
        for (int k = 3; k < 8; k++) acc = __builtin_amdgcn_mfma_f32_32x32x16_f16(fa[k], fb[k], acc, 0, 0, 0);
        SCHED();
        acc[8] += d0;
        STAMP(2);
#pragma unroll
        for (int k = 0; k < 3; k++) *(LAS f32x4*)(pa + k * 1024) = f32x4{acc[4 * k], acc[4 * k + 1], acc[4 * k + 2], acc[4 * k + 3]};
        *(LAS float*)(pa + 3072) = acc[12];
        MEMBAR();
        *(volatile LAS unsigned*)a_sp = one;
        STAMP(11);
        __builtin_amdgcn_s_setprio(0);
      }
      asm volatile("" : "+s"(wave));
      if (wave >= 4) {
        if (RARE(__builtin_amdgcn_readfirstlane(have) == 0u)) {
#pragma unroll 1
          for (int sp = 0; sp < SPIN_CAP * 16 && pf[ch] == 0u; sp++) {}
          partial();
          PIN(acc);
        }
        v4u q[4];
        unsigned kinv = (ce - 6u) << 23;
#define KASM(k, pre) asm volatile(pre "\n\tv_mfma_f32_32x32x16_f16 %0, %1, %2, %0" : "+v"(acc) : "v"(fa[k]), "v"(fb[k])); SCHED()
        KASM(0, "s_nop 1");
        asm volatile("" : "+{v7}"(op[0]), "+{v8}"(op[1]), "+{v9}"(op[2]));
        KASM(1, "");
        asm volatile("" : "+{v10}"(op[3]), "+{v11}"(op[4]), "+{v12}"(op[5]));
        KASM(2, "");
        asm volatile("" : "+{v13}"(op[6]), "+{v14}"(top), "+{v15}"(kinv));
        KASM(3, "");
        KASM(4, "");
        KASM(5, "");
        KASM(6, "");
        KASM(7, "");
#pragma unroll
        for (int k = 0; k < 4; k++) q[k] = *(const LAS v4u*)(a_q + k * 1024);
        SCHED();
        asm volatile("s_nop 11" : "+v"(acc));
        STAMP(2);
        const unsigned k8 = pkh(acc[11], acc[12]);
        *(LAS u16*)(a_k8 + 64) = (u16)k8;
        *(LAS u16*)a_k8 = (u16)(k8 >> 16);
        *(LAS unsigned*)a_k4 = pkh(acc[9], acc[10]);
        const v4u c0 = {pkh(acc[1], acc[2]), pkh(acc[3], acc[4]), pkh(acc[5], acc[6]), pkh(acc[7], acc[8])};
        *(LAS v4u*)a_kn = c0;
        *(LAS v4u*)(a_kn + KCOPY) = v4u{pkh(acc[0], acc[1]), pkh(acc[2], acc[3]), pkh(acc[4], acc[5]), pkh(acc[6], acc[7])};
        const unsigned kb = (unsigned)__builtin_amdgcn_readlane((int)pkh(acc[0], acc[9]), 63);
        *(LAS u16*)a_sp = (u16)__builtin_amdgcn_perm(kb, c0.w, sel_sp);
        STAMP(4);
        __builtin_amdgcn_s_setprio(M_PRIO);
        asm volatile("Lk_conv:\n\ts_cbranch_execz Lk_fetch\n" CONV_ASM_TEXT
                     : CONV_ASM_PRE(q)
                     : "{v14}"(top), "{v15}"(kinv), "{v7}"(op[0]), "{v8}"(op[1]), "{v9}"(op[2]), "{v10}"(op[3]), "{v11}"(op[4]), "{v12}"(op[5]),
                       "{v13}"(op[6])
                     : CONV_ASM_CLOBBERS);
        STAMP(6);
        __builtin_amdgcn_s_setprio(0);
      }
      asm volatile("" : "+s"(wave));
      if (wave < 4) {
        asm volatile("s_branch Lk_fetch\n\t.p2align 12\nLk_fetch:\n\ts_cbranch_execz Lk_ln2");
        const unsigned usrc = op[0], udst = op[1], udst2 = op[2], yb = op[3];
        int yo = op[4];
        wait_until((unsigned)t0_, T_U);
        __builtin_amdgcn_s_setprio(H_PRIO);
        fetch_u<3, true>(rs, usrc, {udst, udst + 1024, udst2}, 0, tx);
        STAMP(4);
        BAR();
        __builtin_amdgcn_s_sleep(SLEEP_U7);
        fetch_u<1, false>(rs, usrc, {udst2 + 1024}, 12, tx);
        STAMP(6);
#pragma unroll
        for (int m = 0; m < 4; m++) {
          BAR();
          if (m == 0) yo ^= *(const LAS int*)(L_MISC + 72);
          const v2u r0 = ldtr((const LAS char*)(yb + L_Y + m * 1024)), r1 = ldtr((const LAS char*)(yb + L_Y + m * 1024 + 256));
          __builtin_amdgcn_raw_buffer_store_b128(v4u{r0.x, r0.y, r1.x, r1.y}, rs, yo, m * 2048, SC1);
        }
        __builtin_amdgcn_s_setprio(0);
      }
    }
    asm volatile("" : "+s"(wave));
    if (wave == 8) {
      unsigned pw[PF_N], epoch, wb;
      v4u wa;
      __builtin_amdgcn_s_setprio(3);
      asm volatile("s_waitcnt vmcnt(0) ; first loads" : "={v1}"(epoch), "={v[2:5]}"(wa), "={v6}"(wb)::"memory");
      int lane;
      asm volatile("v_mbcnt_lo_u32_b32 %0, -1, 0\n\tv_mbcnt_hi_u32_b32 %0, -1, %0" : "=v"(lane));
      {
        int d = 0;
        unsigned c0 = (unsigned)__builtin_amdgcn_s_memtime();
        int ya = ybase + (bid & 8) * 1024 + lane * 16;
#pragma unroll 1
        for (int h = 0; h < 2; h++) {
#pragma unroll
          for (int i = 0; i < 8; i++) __builtin_amdgcn_raw_buffer_store_b128(sent4, rs, ya + (i & 3) * 1024, (i & 4) * 1024, SC1);
          ya ^= 8192;
          if (loc >= 16) {
            DRAIN();
            const unsigned c1 = (unsigned)__builtin_amdgcn_s_memtime();
            d = (int)min(c1 - c0, 2000u) - d;
            c0 = c1;
          }
        }
        st2(v2u{epoch + 1u, (unsigned)(((loc ^ (bid >> 2)) & 1) ? d : -d)}, rs, loc >= 16 ? WS_VOTE + (cg - 128) * 64 : OOB);
      }
      st4(sent4, rs, (loc < 16 && lane < 8) ? WS_RREC + (lane & 4) * 2048 + rb * 64 + (lane & 3) * 16 : OOB);
      st4(sent4, rs, cg < 48 ? WS_XSEL + (cg & 15) * 8192 + (cg >> 4) * 1024 + lane * 16 : OOB);
      SCHED();
      auto warm = [&]() {
        const GAS char* wb = (const GAS char*)(loc < 9 ? kb_[0] : kb_[1]) + xcd * 1152;
        const unsigned s0 = (3u - (unsigned)((u64)wb >> 7)) & 7u, ro_ = (576 - 9 * WROWS + (loc < 9 ? loc : loc - 9) * WROWS + min(lane, WROWS - 1)) * 9216;
        pw[0] = *(const GAS unsigned*)(wb + (ro_ + s0 * 128));
        pw[1] = *(const GAS unsigned*)(wb + (ro_ + (s0 ? s0 : 8u) * 128));
        SCHED();
      };
      if (loc < 16) warm();
      __builtin_amdgcn_s_setprio(0);
      SCHED();
      {
#pragma unroll
        for (int k = 0; k < 4; k++) *(unsigned*)(smem + L_WR + (lane + 64 * k) * 4) = wa[k];
        *(unsigned*)(smem + L_WR + (224 + lane) * 4) = wb;
        *(LAS unsigned*)(L_MISC + 128 + (lane & 31) * 4) = 0x3f803f80u;
        float ws_ = lane >= 32 ? blo(wb) + bhi(wb) : 0.f;
#pragma unroll
        for (int k = 0; k < 4; k++) ws_ += blo(wa[k]) + bhi(wa[k]);
        ws_ = wave_sum(ws_);
        if (lane == 0) *(float*)(smem + L_MISC + 64) = ws_;
      }
      SCHED();
      __builtin_amdgcn_s_setprio(S_PRIO);
      asm volatile("s_waitcnt vmcnt(%0)" ::"n"(PF_N) : "memory");
      STAMP(4);
      BAR();
      STAMP(5);
      const unsigned t1 = now();
      st2(v2u{epoch + 1u, t1}, rs, lane == 0 ? WS_INIT + cg * 64 : OOB);
      v2u vote;
#pragma unroll 1
      for (int sp = 0; sp < SPIN_CAP; sp++) {
        MEMBAR();
        vote = ld2(rs, WS_VOTE + (lane & 15) * 64);
        if (__all(vote.x == epoch + 1u)) break;
      }
      const unsigned flip = wave_sum((float)(int)vote.y) > 0.f;
      *(LAS unsigned*)(L_MISC + 72) = flip << 13;
      if (loc < 16) {
        const GAS char* xs = p.x + (unsigned)((16 * rb + (lane >> 3)) * 1152 + ((lane & 7) ^ (lane >> 3)) * 16);
#pragma unroll 1
        for (int i = 0; i < 9; i++) {
          __builtin_amdgcn_global_load_lds((const GAS unsigned*)(xs + i * 128), (LAS unsigned*)(L_XROW + i * 2048), 16, 0, 0);
          __builtin_amdgcn_global_load_lds((const GAS unsigned*)(xs + i * 128 + 9216), (LAS unsigned*)(L_XROW + i * 2048 + 1024), 16, 0, 0);
        }
      }
      v2u fl[3];
      unsigned span;
      auto look = [&]() {
        MEMBAR();
#pragma unroll
        for (int k = 0; k < 3; k++) fl[k] = ld2(rs, WS_INIT + min(lane + 64 * k, NCONV - 1) * 64);
      };
      auto seen = [&]() {
        if (!__all(fl[0].x == epoch + 1u && fl[1].x == epoch + 1u && fl[2].x == epoch + 1u)) return false;
        const unsigned t = t1 - 4096u;
        const unsigned last = t + span + min(wave_max_u(max(max(fl[0].y - t, fl[1].y - t), fl[2].y - t)), 4096u + 300u);
        *(volatile LAS unsigned*)(L_MISC + 76) = last | 1u;
        if (loc == 16) st2(v2u{epoch + 1u, (last & ~1u) | flip}, rs, WS_TIME + xcd * 128);
        return true;
      };
      if (loc >= 16) warm();
      wait_until(t1, T_LOOK);
      look();
      STAMP(6);
      BAR();
      BAR();
      span = (now() - t1) * 85u >> 6;
      DRAIN();
      bool ok = seen();
      BAR();
      BAR();
#pragma unroll 1
      for (int sp = 0; sp < SPIN_CAP && !ok; sp++) {
        RETRY(7);
        look();
        ok = seen();
      }
      __builtin_amdgcn_s_waitcnt(0x0f70);
      if (bid == 136) st1(epoch + 1u, rs, WS_EPOCH);
#pragma unroll
      for (int i = 0; i < PF_N; i++) asm volatile("" ::"v"(pw[i]));
      __builtin_amdgcn_s_setprio(0);
      SCHED();
      asm volatile("s_branch Lk_ln2\n\t.p2align 12\nLk_ln2:\n\ts_cbranch_execz Lk_back");
    }
    asm volatile("; first loads (62)");
    SCHED();
    STAMP(7);
    if (loc >= 16) { STAMP_FLUSH(); return; }
    int lane;
    asm volatile("v_mbcnt_lo_u32_b32 %0, -1, 0\n\tv_mbcnt_hi_u32_b32 %0, -1, %0" : "=v"(lane));
    const int cl = lane & 15, g = lane >> 4, tid = wave * 64 + lane;
    v2u fg = *(volatile LAS v2u*)(L_MISC + 72);
    int o0 = ysrc + g * 2 * GSTR + (cl & 7) * 16 + (cl & 8);
    unsigned tn = now() - ylag + Y_EARLY;
    PIN(o0);
    asm volatile("" : "+s"(tn));
    SCHED();
    if (RARE(__builtin_amdgcn_readfirstlane(fg.y) == 0u)) {
#pragma unroll 1
      for (int sp = 0; sp < SPIN_CAP * 64; sp++) {
        fg = *(volatile LAS v2u*)(L_MISC + 72);
        if (__builtin_amdgcn_readfirstlane(fg.y)) break;
      }
    }
    o0 ^= fg.x;
    PIN(o0);
    const unsigned last = __builtin_amdgcn_readfirstlane(fg.y);
    int late = (int)(tn - last);
    v2u y[4];
    auto gather = [&]() {
      MEMBAR();
#pragma unroll
      for (int i = 0; i < 4; i++) y[i] = __builtin_amdgcn_raw_buffer_load_b64(rs, o0, ((i & 1) + (i >> 1) * 8) * GSTR, SC1);
    };
    if (late >= 0) gather();
    SCHED();
    ln2_operands(lane);
    constexpr int L_TAB = L_RED + 2304;
    const int flip = __builtin_amdgcn_readfirstlane(fg.x);
    int near = side ^ flip, far = near ^ 8192;
    v4u rx = {(unsigned)(kw + WS_XSEL), (unsigned)((kw + WS_XSEL) >> 32) & 0xffffu, 8 * 16384, 0x00020000};
    asm volatile("" : "+s"(rx), "+s"(far), "+s"(near));
    unsigned l_sr, l_h, l_t, krow, hoff, half, a_pub, a_cnt;
    int a_key;
    float wsum;
    auto addresses = [&]() {
      l_h = L_XROW + wave * 2048 + (lane & 31) * 4;
      l_t = L_TAB + (lane >> 5) * 16;
      half = (lane >> 1) & 16;
      a_pub = wave * 128 + (lane & 31) * 4;
      a_cnt = L_MISC + 32;
      PIN(l_h); PIN(l_t); PIN(half); PIN(a_pub); PIN(a_cnt);
    };
    if (wave == 1) {
      addresses();
      l_sr = L_RED + cl * 16;
      krow = SEQ - 1 - (16 * rb + cl);
      hoff = cl * 128 + (cl & 7) * 16;
      a_key = lane < 32 ? WS_RREC + g * 8192 + (16 * rb + cl) * 4 : OOB;
      wsum = *(const float*)(smem + L_MISC + 64);
      PIN(l_sr); PIN(krow); PIN(hoff); PIN(a_key); PIN(wsum);
    }
    asm volatile("" : "+s"(wave));
    u64 m_odd = 0xaaaaaaaaaaaaaaaaull, m_up = 0xccccccccccccccccull;
    asm volatile("" : "+s"(m_odd), "+s"(m_up));
    const bool odd = __builtin_amdgcn_inverse_ballot_w64(m_odd), upper = __builtin_amdgcn_inverse_ballot_w64(m_up);
    v4u hv[2];
    asm volatile("" : "+s"(late));
    if (late < 0) {
      wait_until(last, ylag);
      gather();
    }
    SCHED();
    {
      f32x4 s = {0.f, 0.f, 0.f, 0.f}, q = {0.f, 0.f, 0.f, 0.f}, h[4];
      auto add = [&]() {
#pragma unroll
        for (int i = 0; i < 4; i++) {
          h[i] = widen(unit, y[i], xf[i]);
          SCHED();
        }
      };
      add();
      POLL(2, valid8(v4u{y[0].x, y[0].y, y[1].x, y[1].y}, v4u{y[2].x, y[2].y, y[3].x, y[3].y}), (gather(), add()));
      SCHED();
#pragma unroll
      for (int i = 0; i < 2; i++) {
        hv[i] = v4u{pkbf(h[2 * i][0], h[2 * i][1]), pkbf(h[2 * i][2], h[2 * i][3]), pkbf(h[2 * i + 1][0], h[2 * i + 1][1]), pkbf(h[2 * i + 1][2], h[2 * i + 1][3])};
        q = mfma_bf(hv[i], hv[i], q);
        s = mfma_bf(cw[i], hv[i], s);
        SCHED();
      }
      const float qa = odd ? q[1] : q[0], qb = odd ? q[3] : q[2];
      s[2] = upper ? qb : qa;
      *(LAS f32x4*)l_sw = s;
      SCHED();
    }
    STAMP(8);
    SCHED();
    BAR();
    SCHED();
    if (wave == 1) {
      float m, s;
      const f32x4 t = sum9((const LAS char*)l_sr);
      ln_round(t[0], t[2], m, s);
      const float rinv = __builtin_amdgcn_rcpf(s);
      const unsigned b = pkbf(0.f, fmaf(-m, wsum, t[1]) * rinv);
      const unsigned okey = (b ^ (((int)b >> 31 & 0x7fff0000u) | 0x80000000u)) | krow;
      st1(okey, rs, a_key);
      SCHED();
      KSTAMP(9);
      *(LAS v4u*)l_x = hv[0];
      *(LAS v4u*)l_x2 = hv[1];
      int v = __builtin_amdgcn_update_dpp((int)okey, (int)okey, 0x124, 0x2, 0xf, false);
      v = __builtin_amdgcn_update_dpp(v, (int)okey, 0x128, 0x4, 0xf, false);
      v = __builtin_amdgcn_update_dpp(v, (int)okey, 0x12c, 0x8, 0xf, false);
      unsigned c = ((unsigned)v > okey) + (dppu<0x121, 0xf>(v) > okey) + (dppu<0x122, 0xf>(v) > okey) + (dppu<0x123, 0xf>(v) > okey);
      const v2u c1 = __builtin_amdgcn_permlane32_swap(c, c, false, false);
      c = c1.x + c1.y;
      const v2u c2 = __builtin_amdgcn_permlane16_swap(c, c, false, false);
      *(LAS v4u*)(L_TAB + (c2.x + c2.y) * 16) = v4u{__float_as_uint(m), okey, __float_as_uint(rinv), hoff};
    }
    asm volatile("" : "+s"(wave));
    if (wave != 1) {
      addresses();
      a_key = WS_RREC + near + (tid - (wave ? 64 : 0)) * 16;
      PIN(a_key);
      SCHED();
      *(LAS v4u*)l_x = hv[0];
      *(LAS v4u*)l_x2 = hv[1];
      wait_until(last, T_KEY);
    }
    BAR();
    v4u key;
    if (wave != 1) key = ld4(rs, a_key);
    STAMP(1);
    SCHED();
    unsigned nsel = 0;
#pragma unroll 1
    for (int rd = 0; rd < 4; rd++) {
      const v4u e = *(const LAS v4u*)l_t;
      unsigned hd = *(const LAS unsigned*)(l_h ^ e.w);
      const unsigned ka = __builtin_amdgcn_readlane(e.y, 0), kb = __builtin_amdgcn_readlane(e.y, 32);
      unsigned wd = nosent(ln_apply(hd, __uint_as_float(e.x), __uint_as_float(e.z)));
      hd = nosent(hd);
      PIN(wd); PIN(hd);
      SCHED();
      if (wave != 1) {
        POLL(3, valid4(key), key = ld4(rs, a_key));
        KSTAMP(10);
        u64 b[8];
#pragma unroll
        for (int i = 0; i < 4; i++) {
          b[2 * i] = __ballot(key[i] > ka);
          b[2 * i + 1] = __ballot(key[i] > kb);
        }
        asm volatile("" : "+s"(b[0]), "+s"(b[1]), "+s"(b[2]), "+s"(b[3]), "+s"(b[4]), "+s"(b[5]), "+s"(b[6]), "+s"(b[7]));
        const unsigned ca = (__popcll(b[0]) + __popcll(b[2])) + (__popcll(b[4]) + __popcll(b[6]));
        const unsigned cb = (__popcll(b[1]) + __popcll(b[3])) + (__popcll(b[5]) + __popcll(b[7]));
        asm volatile("s_mov_b64 exec, 1\n\tds_add_u32 %1, %0\n\ts_mov_b64 exec, -1" ::"v"(ca | (cb << 16)), "v"(a_cnt) : "memory");
      }
      unsigned cs, rank, po;
      asm volatile(
          "s_waitcnt lgkmcnt(0)\n\ts_barrier\n\tds_read_b32 %0, %3\n\ts_waitcnt lgkmcnt(0)\n\t"
          "v_bfe_u32 %1, %0, %4, 16\n\tv_lshl_add_u32 %2, %1, 14, %5\n\t"
          "buffer_store_dword %6, %2, %7, %8 offen sc1\n\tbuffer_store_dword %6, %2, %7, %9 offen sc1"
          : "=&v"(cs), "=&v"(rank), "=&v"(po)
          : "v"(a_cnt), "v"(half), "v"(a_pub), "v"(wd), "s"(rx), "s"(far), "s"(near)
          : "memory");
      const int ph = rank < 8u ? WS_XSEL + po + (((rank & 4) << 11) ^ flip) : OOB - 4096;
      st1(hd, rs, ph + 1152);
      st1(e.y, rs, a_pub == 0 ? ph + 2304 : OOB);
      const unsigned cu = __builtin_amdgcn_readfirstlane(cs);
      nsel += ((cu & 0xffffu) < 8u) + ((cu >> 16) < 8u);
      if ((cu >> 16) >= 8u) break;
      l_t += 32;
      a_cnt += 4;
    }
    STAMP(3);
#pragma unroll
    for (int i = 0; i < 2; i++) {
      const unsigned k = 8 * i + (lane >> 3);
      const v4u e = *(const LAS v4u*)(L_TAB + k * 16);
      const v4u hh = *(const LAS v4u*)((L_XROW + wave * 2048 + (lane & 7) * 16) ^ e.w);
      v4u o;
#pragma unroll
      for (int c = 0; c < 4; c++) o[c] = addbf(hh[c], ln_apply(hh[c], __uint_as_float(e.x), __uint_as_float(e.z)));
      st4(o, ro, k < nsel ? OOB : (16 * rb + (e.w >> 7)) * 1152 + wave * 128 + (lane & 7) * 16);
    }
    STAMP_FLUSH();
    return;
  }

  asm volatile("; conv set-up dropped" ::"{v[34:49]}"(k8));
  const bool ffn = loc < 27;
  LAS char* xw = (LAS char*)smem + wave * 4608 + (lane >> 3) * 288 + (lane & 7) * 16;
  LAS char* xr_ = (LAS char*)smem + wave * 4608 + ((lane >> 2) & 3) * 288 + (lane & 1) * 128 + g * 16 + (lane & 2) * 4;
  unsigned rw = (lane & 3) == 0 ? L_RED + wave * 128 + lane * 2 : L_UIMG + lane * 8, r8 = L_RED + (lane & 28) * 4 + (lane >> 5) * 8, rr_ = r8 + (lane & 3) * 128;
  int uo = (wave < 8 ? WS_U : WS_U2 - 128 * GSTR) + (16 * wave + (lane >> 3)) * GSTR + tile * 128 + (lane & 7) * 16;
  unsigned sa = L_MISC + (lane & 4) * 8, sw = L_MISC + (lane & 60), keep = 0xfff8fff8u, ones = 0x3f803f80u;
  int mode = loc >= 30 ? 2 : 0, far8 = 8 * GSTR;
  PIN(xw); PIN(xr_); PIN(rw); PIN(rr_); PIN(r8); PIN(uo); PIN(sa); PIN(sw); PIN(keep);
  asm volatile("" : "+s"(ones), "+s"(mode), "+s"(far8));
  if (wave == 1) __builtin_amdgcn_s_setprio(3);
  SCHED();
  asm volatile("s_waitcnt lgkmcnt(0) ; (2) first loads" : "+{s33}"(sep), "+{v[2:5]}"(ga), "+{v[6:9]}"(gb), "+{v[10:13]}"(gc), "+{v[14:17]}"(gd) : "{v1}"(epoch) : "memory");
  const unsigned tag = sep + 1u;
  v4u tb = tag_bits(tag);
  asm volatile("" : "+s"(tb));
  SCHED();
  {
#pragma unroll 1
    do {
#define DOT_(s, q, x, cs, cq) "v_dot2_f32_bf16 " s ", " x ", %[ones], " cs "\n\tv_dot2_f32_bf16 " q ", " x ", " x ", " cq "\n\t"
#define RED_(s, q, how) "v_add_f32_dpp v18, " s ", " s " " how "\n\tv_add_f32_dpp v19, " q ", " q " " how "\n\ts_nop 0\n\t"
#define SUMS_(s, q, a, b, c, d) DOT_(s, q, a, "0", "0") DOT_(s, q, b, s, q) DOT_(s, q, c, s, q) DOT_(s, q, d, s, q)
      asm volatile("s_cmp_eq_u32 %[mode], 1\n\ts_cbranch_scc1 1f\n\ts_waitcnt vmcnt(1) ; first loads\n1:\n\t"
                   "ds_write_b128 %[xw], v[2:5]\n\t" SUMS_("v18", "v19", "v2", "v3", "v4", "v5")
                   "s_cbranch_scc1 2f\n\ts_waitcnt vmcnt(0)\n2:\n\t" T3_ASM
                   "ds_write_b128 %[xw], v[6:9] offset:128\n\t" SUMS_("v20", "v21", "v6", "v7", "v8", "v9")
                   RED_("v18", "v19", "row_half_mirror row_mask:0xf bank_mask:0x5") RED_("v20", "v21", "row_half_mirror row_mask:0xf bank_mask:0xa")
                   RED_("v18", "v19", "quad_perm:[1,0,3,2] row_mask:0xf bank_mask:0xf") RED_("v18", "v19", "quad_perm:[2,3,0,1] row_mask:0xf bank_mask:0xf")
                   "s_cmp_lg_u32 %[mode], 2\n\tds_write_b64 %[rw], v[18:19]\n\ts_cbranch_scc1 3f\n\t"
                   "global_load_dwordx4 v[10:13], v10, %[x] ; own\n\tglobal_load_dwordx4 v[14:17], v14, %[x] ; own\n3:"
                   : "+{v[10:13]}"(gc), "+{v[14:17]}"(gd) T3_OUT
                   : "{v[2:5]}"(ga), "{v[6:9]}"(gb), [mode] "s"(mode), [ones] "s"(ones), [xw] "v"(xw), [rw] "v"(rw), [x] "s"(kx)
                   : "v18", "v19", "v20", "v21", "scc", "memory");
      STAMP(10);
      if (wave >= L1_EARLY) { BAR(); STAMP(11); SCHED(); }
      asm volatile("" : "+s"(wave));
      f32x2 xt[2][4];
#pragma unroll
      for (int i = 0; i < 4; i++) {
        const v2u v = ldtr(xr_ + (i >> 1) * 64 + (i & 1) * 1152);
        xt[i >> 1][2 * (i & 1)] = f32x2{blo(v.x), bhi(v.x)};
        xt[i >> 1][2 * (i & 1) + 1] = f32x2{blo(v.y), bhi(v.y)};
      }
#pragma unroll
      for (int i = 0; i < 8; i++) PIN(xt[i >> 2][i & 3]);
      if (wave < L1_EARLY) { BAR(); STAMP(11); SCHED(); }
      asm volatile("" : "+s"(wave));
      if (wave == 1) {
        const f32x2 a = *(const LAS f32x2*)rr_, b = *(const LAS f32x2*)(rr_ + 512), e = *(const LAS f32x2*)(r8 + 1024);
        SCHED();
        const f32x2 h = a + b;
        float hs = h.x, hq = h.y;
        asm("s_nop 1\n\tv_add_f32_dpp %0, %0, %0 quad_perm:[1,0,3,2] row_mask:0xf bank_mask:0xf\n\tv_add_f32_dpp %1, %1, %1 quad_perm:[1,0,3,2] row_mask:0xf bank_mask:0xf\n\t"
            "s_nop 0\n\tv_add_f32_dpp %0, %0, %0 quad_perm:[2,3,0,1] row_mask:0xf bank_mask:0xf\n\tv_add_f32_dpp %1, %1, %1 quad_perm:[2,3,0,1] row_mask:0xf bank_mask:0xf"
            : "+v"(hs), "+v"(hq));
        float m, sd;
        ln_round(hs + e.x, hq + e.y, m, sd);
        const float rinv = __builtin_amdgcn_rcpf(sd);
        *(LAS float*)sw = rinv;
        *(LAS float*)(sw + 64) = -m * rinv;
      }
      SCHED();
      STAMP(7);
      BAR();
      STAMP(12);
      const LAS f32x4* st = (const LAS f32x4*)sa;
      const f32x4 r0 = st[0], c0 = st[4], r1 = st[1], c1 = st[5];
      const f32x2 rr[4] = {r0.xy, r0.zw, r1.xy, r1.zw}, cc[4] = {c0.xy, c0.zw, c1.xy, c1.zw};
#pragma unroll
      for (int i = 0; i < 2; i++) {
        v4u o;
#pragma unroll
        for (int k = 0; k < 4; k++) o[k] = (pkh2(__builtin_elementwise_fma(xt[i][k], rr[k], cc[k])) & keep) | tb[k];
        __builtin_amdgcn_raw_buffer_store_b128(o, rs, uo, i ? far8 : 0, SC1);
        STAMP(8 + i);
        SCHED();
      }
      if (mode != 2) break;
      mode = 1;
      asm volatile("" : "+s"(mode));
      uo += 16 * 128;
      asm volatile("s_waitcnt vmcnt(2) ; second tile\n\tv_mov_b64 v[2:3], v[10:11]\n\tv_mov_b64 v[4:5], v[12:13]\n\tv_mov_b64 v[6:7], v[14:15]\n\tv_mov_b64 v[8:9], v[16:17]"
                   : "={v[2:5]}"(ga), "={v[6:9]}"(gb) : "{v[10:13]}"(gc), "{v[14:17]}"(gd) : "memory");
      STAMP(5);
      SCHED();
    } while (true);
    __builtin_amdgcn_s_setprio(0);
    BAR();
    STAMP(1);
    if (!ffn) {
      const GAS char* dn = (const GAS char*)kb_[2] + (size_t)xcd * 5184 * 128;
      unsigned pf[2];
#pragma unroll
      for (int i = 0; i < 2; i++) pf[i] = *(const GAS unsigned*)(dn + (unsigned)min((i * 5 + loc - 27) * NT + tid, 5183) * 128u);
#pragma unroll
      for (int i = 0; i < 2; i++) asm volatile("" ::"v"(pf[i]));
      STAMP_FLUSH();
      return;
    }
  }
  v8u FW[2][4];
  v4u FD[2][4];
  v4u Dn[8];
  constexpr int F_GU = 0, F_TIME = 1280, F_ALL = 2048, F_RED = 16384, RSTR = 4608;
    v4u W[2][8];
    const GAS char* wp = (const GAS char*)((lane & 8) ? kb_[1] : kb_[0]) + (unsigned)((64 * wave + 4 * g) * (FF * 2) + 128 * fj + 16 * (lane & 7));
    const rsrc_t rd = __builtin_amdgcn_make_buffer_rsrc((void*)kb_[2], 0, DM * FF * 2, 0x00020000);
    const int wdo = (64 * fj + 8 * g + 4 * (lane & 8)) * (DM * 2) + 128 * wave + 16 * (lane & 7);
    st4(sent4, rs, tid < 288 ? part_at(fj * 4608 + tid * 16) : part_at(fj * 4608 - 4608 + tid * 16) + 8192);
    SCHED();
#pragma unroll
    for (int j = 0; j < 16; j++) W[j >> 3][j & 7] = *(const GAS v4u*)(wp + (16 * (j >> 2) + (j & 3)) * (FF * 2));
    SCHED();
    STAMP(11);
  asm volatile("s_waitcnt vmcnt(16)" ::: "memory");
  if (wave >= 4) BAR();
  asm volatile("" : "+s"(wave));
  const int tk = xcd, fcol = (loc - 18) * 64 + lane;
  int a_x = slot_at(lane >> 3) + side + wave * 128 + (lane & 7) * 16, a_f = slot_at(tk) + side + (fcol >> 1) * 4;
  int a_po = part_at(fj * 4608 + ((g & 2) + (cl >> 3)) * 1152 + wave * 128 + (cl & 7) * 16) + ((g & 1) << 13);
  int a_pi = part_at((tid >> 3) * 4608 + (xcd & 3) * 1152 + (loc - 18) * 128 + (lane & 7) * 16) + side;
  unsigned cap = 0xFFFFFFFEu;
  PIN(cap);
  v4u pick;
#pragma unroll
  for (int i = 0; i < 4; i++) pick[i] = ((cl >> 1) & 3) == i ? 0x3f80u << ((cl & 1) * 16) : 0u;
  PIN(a_po); PIN(a_pi); PIN(pick);
  unsigned l_xw = F_ALL + wave * 1152 + (lane >> 3) * 144 + (lane & 7) * 16, l_xr = F_ALL + wave * 1152 + (lane & 7) * 144 + g * 16;
  unsigned l_rw = F_RED + wave * RSTR;
  asm volatile("" : "+s"(l_rw));
  int sidx = (lane & 8) ? 0xeeee : 0x4444;
  unsigned l_rg = F_RED + (2 * wave + (g & 1)) * 288 + (lane & 32) * 4 + (lane & 8) * 8 + (lane & 7) * 4, l_ru = l_rg + 32;
  unsigned l_gw = F_GU + (((lane >> 1) & 4) + (wave & 1) * 2 + (g & 1)) * 144 + ((lane & 7) * 8 + (wave >> 1) + (lane >> 5) * 4) * 2;
  unsigned l_gr = F_GU + ((cl & 4) + ((cl + (cl >> 2 & 2)) & 3)) * 144 + g * 16;
  unsigned l_s = (lane & 40) ? F_RED + tid * 16 : F_ALL + wave * 256 + cl * 32 + g * 16, l_f = F_ALL + lane * 4;
  int a_k = slot_at(tk) + side + 2304;
  PIN(a_k); PIN(a_x); PIN(a_f); PIN(l_xw); PIN(l_xr); PIN(sidx); PIN(l_rg); PIN(l_ru); PIN(l_gw); PIN(l_gr); PIN(l_s); PIN(l_f);
#pragma unroll
    for (int ks = 0; ks < 2; ks++)
#pragma unroll
      for (int c = 0; c < 4; c++) {
#pragma unroll
        for (int i = 0; i < 8; i++)
          FW[ks][c][i] = __builtin_amdgcn_perm(W[ks][i | 1][(c >> 1) + 2 * (i & 1)], W[ks][i & 6][(c >> 1) + 2 * (i & 1)], (c & 1) ? 0x07060302u : 0x05040100u);
        PIN(FW[ks][c]);
      }
  SCHED();
  if (wave < 4) BAR();
  STAMP(12);
  st1(tag, rs, tid == 0 ? WS_INIT + (NCONV + fj) * 64 : OOB);
  v2u ts = ld2(rs, WS_TIME + xcd * 128);
  SCHED();
#pragma unroll
  for (int j = 0; j < 8; j++) Dn[j] = __builtin_amdgcn_raw_buffer_load_b128(rd, wdo, j * (DM * 2), 0);
  SCHED();
  STAMP(2);
#pragma unroll 1
  for (int sp = 0; sp < SPIN_CAP && __builtin_amdgcn_readfirstlane(ts.x) != tag; sp++) {
    __builtin_amdgcn_s_sleep(4);
    MEMBAR();
    ts = ld2(rs, WS_TIME + xcd * 128);
  }
  const unsigned due = __builtin_amdgcn_readfirstlane(ts.y);
  {
    const int flip = (due & 1u) << 13;
    a_x ^= flip; a_f ^= flip; a_k ^= flip; a_po ^= flip; a_pi ^= flip;
    PIN(a_x); PIN(a_f); PIN(a_k); PIN(a_po); PIN(a_pi);
  }
  unsigned w2 = 0u, h2 = 0u, fkey = 0u, f0 = 0u, f1 = 0u;
  auto flook = [&]() {
    MEMBAR();
    f0 = ld1(rs, WS_INIT + (NCONV + lane) * 64);
    f1 = ld1(rs, WS_INIT + (NCONV + 64 + (lane & 7)) * 64);
    w2 = ld1(rs, a_f);
    h2 = ld1(rs, a_f + 1152);
    fkey = ld1(rs, a_k);
  };
  {
    DRAIN();
    STAMP(10);
    int late = (int)(now() - due) - (T_SEL - T_LATE);
    v4u xv = sent4;
    if (late >= 0) xv = ld4(rs, a_x);
    SCHED();
#pragma unroll
    for (int j = 0; j < 8; j++)
#pragma unroll
      for (int i = 0; i < 2; i++) {
        const int lo = (int)Dn[j][i], hi = (int)Dn[j][2 + i];
        Dn[j][i] = (unsigned)__builtin_amdgcn_update_dpp(lo, hi, 0x128, 0xf, 0xc, false);
        Dn[j][2 + i] = (unsigned)__builtin_amdgcn_update_dpp(hi, lo, 0x128, 0xf, 0x3, false);
      }
#pragma unroll
    for (int ks = 0; ks < 2; ks++)
#pragma unroll
      for (int c = 0; c < 4; c++) {
#pragma unroll
        for (int i = 0; i < 4; i++)
          FD[ks][c][i] = __builtin_amdgcn_perm(Dn[2 * i + 1][2 * ks + (c >> 1)], Dn[2 * i][2 * ks + (c >> 1)], (c & 1) ? 0x07060302u : 0x05040100u);
        PIN(FD[ks][c]);
      }
    f32x4 a[4];
#pragma unroll
    for (int c = 0; c < 4; c++) { a[c] = f32x4{0.f, 0.f, 0.f, 0.f}; PIN(a[c]); }
    SCHED();
    STAMP(3);
    asm volatile("" : "+s"(late));
    if (late < 0) {
      wait_until(due, T_SEL);
      xv = ld4(rs, a_x);
    }
    POLL(4, valid4(xv), xv = ld4(rs, a_x));
    STAMP(6);
    *(LAS v4u*)l_xw = xv;
    MEMBAR();
    const v4u x0 = *(const LAS v4u*)l_xr, x1 = *(const LAS v4u*)(l_xr + 64);
    if (wave == 8) flook();
#pragma unroll
    for (int c = 0; c < 6; c++) {
      SCHED();
      if (c < 4) {
        a[c] = smfmac_bf(x0, FW[0][c], a[c], sidx);
        a[c] = smfmac_bf(x1, FW[1][c], a[c], sidx);
      }
      SCHED();
      if (c == 2) asm volatile("s_mov_b32 m0, %0\n\ts_nop 2" ::"s"(l_rw));
      if (c > 1)
        asm volatile("ds_write_addtid_b32 %0 offset:%4\n\tds_write_addtid_b32 %1 offset:%4 + 288\n\tds_write_addtid_b32 %2 offset:%4 + 576\n\t"
                     "ds_write_addtid_b32 %3 offset:%4 + 864" ::"v"(a[c - 2][0]), "v"(a[c - 2][1]), "v"(a[c - 2][2]), "v"(a[c - 2][3]), "n"((c - 2) * 1152) : "memory");
    }
  }
  BAR();
  STAMP(4);
  float fh = 0.f, fw = 0.f, sgm = 0.f;
  int oo = OOB;
  if (wave < 8) {
    f32x2 gv[4], uv[4];
#pragma unroll
    for (int w = 0; w < 4; w++) {
      gv[w] = f32x2{*(const LAS float*)(l_rg + 2 * w * RSTR), *(const LAS float*)(l_rg + (2 * w + 1) * RSTR)};
      uv[w] = f32x2{*(const LAS float*)(l_ru + 2 * w * RSTR), *(const LAS float*)(l_ru + (2 * w + 1) * RSTR)};
    }
    const float g8 = *(const LAS float*)(l_rg + 8 * RSTR), u8 = *(const LAS float*)(l_ru + 8 * RSTR);
    SCHED();
    f32x2 g2 = (gv[0] + gv[1]) + (gv[2] + gv[3]), u2 = (uv[0] + uv[1]) + (uv[2] + uv[3]);
    PIN(g2); PIN(u2);
    float gs = g2.x + g2.y;
    PIN(gs);
    gs += g8;
    PIN(gs);
    const float us = (u2.x + u2.y) + u8;
    const float gp = rbf(gs);
    const float gq = rbf(gp * __builtin_amdgcn_rcpf(1.0f + __expf(-gp)));
    *(LAS u16*)l_gw = (u16)pkbf(gq * rbf(us), 0.f);
  } else {
#pragma unroll 1
    for (int sp = 0; sp < SPIN_CAP && !__all(valid4(v4u{w2, h2, fkey, 0u}) && f0 == tag && f1 == tag); sp++) { RETRY(6); flook(); }
    fw = (fcol & 1) ? bhi(w2) : blo(w2);
    fh = (fcol & 1) ? bhi(h2) : blo(h2);
    const unsigned kb = fkey & 0xffff0000u;
    const float rv = __uint_as_float((kb & 0x80000000u) ? (kb & 0x7fffffffu) : (~kb & 0xffff0000u));
    sgm = rbf(__builtin_amdgcn_rcpf(1.0f + __expf(-rv)));
    oo = (SEQ - 1 - (int)(fkey & 0x7ffu)) * 1152 + fcol * 2;
  }
  PIN(fw); PIN(fh); PIN(sgm); PIN(oo);
  BAR();
  STAMP(5);
  {
    const v4u a0 = *(const LAS v4u*)l_gr, a1 = *(const LAS v4u*)(l_gr + 64);
    f32x4 o[4];
#pragma unroll
    for (int c = 0; c < 4; c++) o[c] = mfma_bf(a0, FD[0][c], f32x4{0.f, 0.f, 0.f, 0.f});
    SCHED();
#pragma unroll
    for (int c = 0; c < 4; c++) {
      o[c] = mfma_bf(a1, FD[1][c], o[c]);
      SCHED();
    }
    const unsigned p0 = pkbf(o[0][0], o[1][0]), q0 = pkbf(o[0][1], o[1][1]), p1 = pkbf(o[2][0], o[3][0]), q1 = pkbf(o[2][1], o[3][1]);
    v4u v;
#define MINX(d, s, banks) "v_min_u32_dpp " d ", " s ", %5 row_ror:8 row_mask:0xf bank_mask:" banks "\n\t"
    asm("v_min_u32 v0, %1, %5\n\tv_min_u32 v2, %3, %5\n\tv_min_u32 v1, %2, %5\n\tv_min_u32 v3, %4, %5\n\t"
        MINX("v0", "%3", "0xc") MINX("v2", "%1", "0x3") MINX("v1", "%4", "0xc") MINX("v3", "%2", "0x3")
        : "=&{v[0:3]}"(v) : "v"(p0), "v"(p1), "v"(q0), "v"(q1), "v"(cap));
    st4(v, rs, a_po);
  }
  STAMP(7);
  wait_until(now(), T_PART);
  {
    v4u a;
    f32x4 t;
    auto take = [&]() {
      a = ld4(rs, a_pi);
      t = mfma_bf(pick, a, f32x4{0.f, 0.f, 0.f, 0.f});
      SCHED();
    };
    take();
    POLL(5, valid4(a), take());
    SCHED();
#define ROR8(n) "v_add_f32_dpp v" #n ", v" #n ", v" #n " row_ror:8 row_mask:0xf bank_mask:0xf\n\t"
    asm volatile("s_nop 2\n\t" ROR8(0) ROR8(1) ROR8(2) ROR8(3) "ds_write_b128 %1, v[0:3]" : "+{v[0:3]}"(t) : "v"(l_s) : "memory");
  }
  STAMP(8);
  BAR();
  if (wave == 8) {
    f32x2 t[4];
#pragma unroll
    for (int i = 0; i < 4; i++) t[i] = f32x2{*(const LAS float*)(l_f + i * 512), *(const LAS float*)(l_f + i * 512 + 256)};
    const float t8 = *(const LAS float*)(l_f + 2048);
    SCHED();
    f32x2 z = (t[0] + t[1]) + (t[2] + t[3]);
    PIN(z);
    const float y = rbf((z.x + t8) + z.y);
    __builtin_amdgcn_raw_buffer_store_b16((u16)pkbf(fh + rbf(fw + rbf(y * sgm)), 0.f), ro, oo, 0, SC1);
  }
  STAMP(9);
  STAMP_FLUSH();
  asm volatile("s_endpgm\n\t.p2align 12\n\t.skip (4096 - (Lk_conv - Lk_gen)) & 4095\nLk_gen:" ::: "memory");
}

}

extern "C" void kernel_launch(void* const* d_in, const int* in_sizes, int n_in, void* d_out, int out_size,
                              void* d_ws, size_t ws_size, hipStream_t stream) {
  (void)hipFuncSetAttribute((const void*)k_main, hipFuncAttributeMaxDynamicSharedMemorySize, LDS_BYTES);
  Params p;
  p.x = (const u16*)d_in[0];
  p.lar = (const u16*)d_in[1];
  p.aim = (const u16*)d_in[2];
  p.crl = (const u16*)d_in[3];
  p.cim = (const u16*)d_in[4];
  p.ldt = (const u16*)d_in[5];
  p.dskip = (const u16*)d_in[6];
  p.wr = (const u16*)d_in[8];
  p.wg = (const u16*)d_in[9];
  p.wu = (const u16*)d_in[10];
  p.wd = (const u16*)d_in[11];
  p.out = (u16*)d_out;
  const size_t skip = (size_t)(-(uintptr_t)d_ws) & ((2u << 20) - 1);
  p.ws = (char*)d_ws + (skip + WS_END <= ws_size ? skip : 0);
  k_main<<<256, NT, LDS_BYTES, stream>>>(p);
}
```

```cpp
#include <hip/hip_runtime.h>
#include <stdint.h>
#pragma clang diagnostic ignored "-Wint-to-pointer-cast"

namespace {

constexpr int SEQ = 2048, DM = 576, NM = 128, FF = 4608;
constexpr int NT = 576;
constexpr int NCONV = 144, NFFN = 72;
constexpr unsigned SENT = 0xFFFFFFFFu;
constexpr int SC1 = 16;
constexpr int SPIN_CAP = 1 << 16;
#define T_Y3 -10
#define T_KEY 85
#define T_SEL 165
#define CODE_BYTES 23936
#define H_PRIO 3
#ifndef M_PRIO
#define M_PRIO 1
#endif
#define S_PRIO 3
#define PF_N 2
#define SLEEP_U7 16
#define T_LOOK 90
#define X2_DELAY 2
#define Y_EARLY 10
#define T_U 170
#define L1_EARLY 5
#define WROWS 40
#define T_LATE 25
#define T_PART 25

typedef unsigned short u16;
typedef unsigned v4u __attribute__((ext_vector_type(4)));
typedef unsigned v2u __attribute__((ext_vector_type(2)));
typedef float f32x2 __attribute__((ext_vector_type(2)));
typedef float f32x4 __attribute__((ext_vector_type(4)));
typedef float f32x16 __attribute__((ext_vector_type(16)));
typedef _Float16 f16x2 __attribute__((ext_vector_type(2)));
typedef _Float16 f16x8 __attribute__((ext_vector_type(8)));
typedef __bf16 bf16x2 __attribute__((ext_vector_type(2)));
typedef __bf16 bf16x8 __attribute__((ext_vector_type(8)));
typedef __bf16 bf16x16 __attribute__((ext_vector_type(16)));
typedef unsigned v8u __attribute__((ext_vector_type(8)));
typedef __amdgpu_buffer_rsrc_t rsrc_t;

constexpr int WS_EPOCH = 0;
constexpr int WS_TIME = 2048;
constexpr int WS_INIT = 4096;
constexpr int WS_VOTE = 24576;
constexpr int WS_RREC = 32768;
constexpr int WS_XSEL = 65536;
constexpr int GSTR = 16384;
constexpr int WS_U = 2 << 20;
constexpr int WS_U2 = 196608;
constexpr int WS_Y = 4 << 20;
constexpr int WS_Y2 = WS_U2 + 16 * GSTR;
constexpr int WS_PART = WS_Y2 + 16 * GSTR;
constexpr int WS_END = 6 << 20;
static_assert(WS_PART + 2 * NFFN * 9216 + 16384 <= WS_U, "ws map");
__device__ __forceinline__ int part_at(int v) { return WS_PART + v + (v & ~8191); }
__device__ __forceinline__ int slot_at(int k) { return WS_XSEL + k * 16384; }
constexpr int OOB = 0x7ffffff0;

constexpr int WSCR = 16384;
constexpr int L_TBL = 3 * WSCR + 7936, TROW = 272, TSTR = 32 * TROW;
constexpr int L_KN = 4096;
constexpr int KCOPY = 1216;
constexpr int L_K4 = L_KN + 2 * KCOPY;
constexpr int L_K8 = L_K4 + 512;
constexpr int L_Y = 8192;
constexpr int L_DUMMY = 7680;
constexpr int L_XROW = 4 * WSCR;
constexpr int L_UIMG = 8 * WSCR;
constexpr int USTR = 4640;
constexpr int L_FILT = L_UIMG + 4 * USTR;
constexpr int L_MISC = L_FILT + 4096;
constexpr int L_WR = L_MISC + 256;
constexpr int L_RED = L_WR + 1152;
constexpr int LDS_BYTES = L_RED + 2 * 2304;
static_assert(L_KN + 224 * 16 <= L_DUMMY && L_TBL + 8 * TSTR <= L_UIMG && L_TBL + 31 * TROW < 65536 && 7 * TSTR < 65536, "lds map");
static_assert(LDS_BYTES <= 160 * 1024, "lds size");

#define MEMBAR() asm volatile("" ::: "memory")
#define BAR() do { __builtin_amdgcn_sched_barrier(0); asm volatile("s_waitcnt lgkmcnt(0)\n\ts_barrier" ::: "memory"); __builtin_amdgcn_sched_barrier(0); } while (0)
#define LAS __attribute__((address_space(3)))
#define DRAIN() asm volatile("s_waitcnt vmcnt(0)" ::: "memory")
#define LGKM0() asm volatile("s_waitcnt lgkmcnt(0)" ::: "memory")
#define PIN(x) asm volatile("" : "+v"(x))
#define RARE(c) __builtin_expect(!!(c), 0)
#define PC_(k)
#define POLL(k, ok, load) if (__builtin_amdgcn_ballot_w64(!(ok))) { RETRY(k); int sp_ = SPIN_CAP; unsigned long long on_; _Pragma("unroll 1") do { \
    asm volatile("s_sub_u32 %0, %0, 1\n\ts_cselect_b64 %1, 0, -1" : "+s"(sp_), "=s"(on_)::"scc", "memory"); load; PC_(k); } while (__builtin_amdgcn_ballot_w64(!(ok)) & on_); }
#define SCHED() __builtin_amdgcn_sched_barrier(0)

#define STAMP(i) do {} while (0)
#define STAMP_INIT() do {} while (0)
#define T0_ASM "s_memrealtime s[34:35]\n\t"
#define STAMP_FLUSH() do {} while (0)

#define KSTAMP(i) STAMP(i)
#define RETRY(k) do {} while (0)

__device__ __forceinline__ float blo(unsigned v) { return __uint_as_float(v << 16); }
__device__ __forceinline__ float bhi(unsigned v) { return __uint_as_float(v & 0xffff0000u); }
__device__ __forceinline__ unsigned pkbf(float a, float b) {
  const f32x2 f = {a, b};
  return __builtin_bit_cast(unsigned, __builtin_convertvector(f, bf16x2));
}
__device__ __forceinline__ float rbf(float f) { return __uint_as_float(pkbf(0.f, f)); }
__device__ __forceinline__ unsigned pkh(float a, float b) {
  const f32x2 f = {a, b};
  return __builtin_bit_cast(unsigned, __builtin_convertvector(f, f16x2));
}
__device__ __forceinline__ unsigned nosent(unsigned v) { return min(v, 0xFFFFFFFEu); }
__device__ __forceinline__ bool valid4(v4u v) { return max(max(v.x, v.y), max(v.z, v.w)) != SENT; }

__device__ __forceinline__ bool valid8(v4u a, v4u b) { return max(max(max(max(a.x, a.y), a.z), max(max(a.w, b.x), b.y)), max(b.z, b.w)) != SENT; }

template <int CTRL, int RM> __device__ __forceinline__ float dppf(float v) {
  return __int_as_float(__builtin_amdgcn_update_dpp(0, __float_as_int(v), CTRL, RM, 0xf, false));
}
template <int CTRL, int RM> __device__ __forceinline__ unsigned dppu(unsigned v) {
  return (unsigned)__builtin_amdgcn_update_dpp(0, (int)v, CTRL, RM, 0xf, false);
}
__device__ __forceinline__ float wave_sum(float v) {
  v += dppf<0x111, 0xf>(v);
  v += dppf<0x112, 0xf>(v);
  v += dppf<0x114, 0xf>(v);
  v += dppf<0x118, 0xf>(v);
  v += dppf<0x142, 0xa>(v);
  v += dppf<0x143, 0xc>(v);
  return __int_as_float(__builtin_amdgcn_readlane(__float_as_int(v), 63));
}
__device__ __forceinline__ unsigned wave_max_u(unsigned v) {
  v = max(v, dppu<0x111, 0xf>(v));
  v = max(v, dppu<0x112, 0xf>(v));
  v = max(v, dppu<0x114, 0xf>(v));
  v = max(v, dppu<0x118, 0xf>(v));
  const unsigned a = (unsigned)__builtin_amdgcn_readlane((int)v, 15), b = (unsigned)__builtin_amdgcn_readlane((int)v, 31);
  const unsigned c = (unsigned)__builtin_amdgcn_readlane((int)v, 47), d = (unsigned)__builtin_amdgcn_readlane((int)v, 63);
  return max(max(a, b), max(c, d));
}

__device__ __forceinline__ v4u ld4(rsrc_t rs, int off) { return __builtin_amdgcn_raw_buffer_load_b128(rs, off, 0, SC1); }
__device__ __forceinline__ v2u ld2(rsrc_t rs, int off) { return __builtin_amdgcn_raw_buffer_load_b64(rs, off, 0, SC1); }
__device__ __forceinline__ unsigned ld1(rsrc_t rs, int off) { return __builtin_amdgcn_raw_buffer_load_b32(rs, off, 0, SC1); }
__device__ __forceinline__ v2u ldp2(rsrc_t rs, int off, int soff) { return __builtin_amdgcn_raw_buffer_load_b64(rs, off, soff, 0); }
__device__ __forceinline__ void st4(v4u v, rsrc_t rs, int off) { __builtin_amdgcn_raw_buffer_store_b128(v, rs, off, 0, SC1); }
__device__ __forceinline__ void st2(v2u v, rsrc_t rs, int off) { __builtin_amdgcn_raw_buffer_store_b64(v, rs, off, 0, SC1); }
__device__ __forceinline__ void st1(unsigned v, rsrc_t rs, int off) { __builtin_amdgcn_raw_buffer_store_b32(v, rs, off, 0, SC1); }

__device__ __forceinline__ f32x4 mfma_bf(v4u a, v4u b, f32x4 c) {
  return __builtin_amdgcn_mfma_f32_16x16x32_bf16(__builtin_bit_cast(bf16x8, a), __builtin_bit_cast(bf16x8, b), c, 0, 0, 0);
}
__device__ __forceinline__ f32x4 smfmac_bf(v4u a, v8u b, f32x4 c, int idx) {
  return __builtin_amdgcn_smfmac_f32_16x16x64_bf16(__builtin_bit_cast(bf16x8, a), __builtin_bit_cast(bf16x16, b), c, idx, 0, 0);
}
__device__ __forceinline__ f32x4 mfma_h(v4u a, v4u b, f32x4 c) {
  return __builtin_amdgcn_mfma_f32_16x16x32_f16(__builtin_bit_cast(f16x8, a), __builtin_bit_cast(f16x8, b), c, 0, 0, 0);
}

__device__ __forceinline__ void ln_round(float sum, float sq, float& m, float& s) {
  const float mean = sum * (1.0f / DM);
  const float var = fmaxf(fmaf(-mean, mean, sq * (1.0f / DM)), 0.f);
  m = rbf(mean);
  s = rbf(__builtin_amdgcn_sqrtf(rbf(rbf(var) + 1e-5f)));
}
__device__ __forceinline__ unsigned ln_apply(unsigned xp, float m, float rinv) {
  const unsigned d = pkbf(blo(xp) - m, bhi(xp) - m);
  return pkbf(blo(d) * rinv, bhi(d) * rinv);
}
__device__ __forceinline__ unsigned addbf(unsigned a, unsigned b) { return pkbf(blo(a) + blo(b), bhi(a) + bhi(b)); }

__device__ __forceinline__ unsigned now() { return (unsigned)__builtin_amdgcn_s_memrealtime(); }
__device__ __forceinline__ void wait_until(unsigned t0, unsigned ticks) {
  unsigned n;
  asm volatile(
      "s_movk_i32 %0, 1024\n"
      "1:\n\t"
      "s_memrealtime vcc\n\t"
      "s_waitcnt lgkmcnt(0)\n\t"
      "s_sub_i32 vcc_lo, vcc_lo, %1\n\t"
      "s_cmp_gt_i32 vcc_lo, -16\n\t"
      "s_cbranch_scc1 2f\n\t"
      "s_sleep 2\n\t"
      "s_sub_u32 %0, %0, 4\n\t"
      "s_cbranch_scc0 1b\n\t"
      "s_branch 4f\n"
      "3:\n\t"
      "s_memrealtime vcc\n\t"
      "s_sub_u32 %0, %0, 1\n\t"
      "s_waitcnt lgkmcnt(0)\n\t"
      "s_cselect_b32 vcc_lo, %1, vcc_lo\n\t"
      "s_sub_i32 vcc_lo, vcc_lo, %1\n"
      "2:\n\t"
      "s_cmp_lt_i32 vcc_lo, 0\n\t"
      "s_cbranch_scc1 3b\n"
      "4:"
      : "=&s"(n)
      : "s"(t0 + ticks)
      : "vcc", "scc", "memory");
}

typedef short s16x4 __attribute__((ext_vector_type(4)));
__device__ __forceinline__ v2u ldtr(const LAS char* a) {
  return __builtin_bit_cast(v2u, __builtin_amdgcn_ds_read_tr16_b64_v4i16((LAS s16x4*)a));
}
__device__ __forceinline__ f32x4 widen(v2u unit, v2u v, f32x4 c) {
  return __builtin_amdgcn_mfma_f32_16x16x16bf16_1k(__builtin_bit_cast(s16x4, unit), __builtin_bit_cast(s16x4, v), c, 0, 0, 0);
}
__device__ __forceinline__ v4u tag_bits(unsigned tag) {
  v4u t;
#pragma unroll
  for (int k = 0; k < 4; k++) t[k] = ((tag >> (6 * k)) & 7u) | (((tag >> (6 * k + 3)) & 7u) << 16);
  return t;
}

template <int N, bool EARLY>
__device__ __forceinline__ void fetch_u(rsrc_t rs, int src, const unsigned (&dst)[N], int first, v4u tx) {
  v4u w[N];
  unsigned bad, mid = 0x40004u, pcnt_ = 0;
  asm volatile("" : "+s"(mid));
  auto put = [&](int i) {
    *(LAS v2u*)dst[i] = v2u{w[i].x, w[i].y};
    *(LAS v2u*)(dst[i] + 8) = v2u{w[i].z, w[i].w};
  };
  auto load = [&]() {
#pragma unroll
    for (int i = 0; i < N; i++) w[i] = __builtin_amdgcn_raw_buffer_load_b128(rs, src, (first + 4 * i) * 1024, SC1);
    bad = 0u;
#pragma unroll
    for (int i = 0; i < N; i++) {
      SCHED();
      w[i] ^= tx;
      if (EARLY) put(i);
      SCHED();
      bad |= __builtin_amdgcn_bitop3_b32(w[i].x, w[i].y, mid, 0x7e) | __builtin_amdgcn_bitop3_b32(w[i].z, w[i].w, mid, 0x7e);
    }
  };
  load();
  POLL(N == 1, (bad & 0x70007u) == 0u, load());
  if (!EARLY) put(0);
}

struct cz { f32x2 v, s; };
#define STEP_(txt, c, r, a, b) do { asm(txt : c(r) : "v"(a), "v"(b)); SCHED(); } while (0)
#define mul_(r, a, b) STEP_("v_pk_mul_f32 %0, %1, %2 op_sel:[1,0] neg_lo:[0,1]", "=&v", r, a, (b).s)
#define fma_(r, a, b) STEP_("v_pk_fma_f32 %0, %1, %2, %0 op_sel_hi:[0,1,1]", "+v", r, a, (b).v)
#define mulc_(r, a, b) STEP_("v_pk_mul_f32 %0, %1, %2 op_sel:[1,0]", "=&v", r, a, (b).s)
#define fmac_(r, a, b) STEP_("v_pk_fma_f32 %0, %1, %2, %0 op_sel_hi:[0,1,1] neg_hi:[0,1,0]", "+v", r, a, (b).v)
#define mulh_(r, a, bs) STEP_("v_pk_mul_f16 %0, %1, %2 op_sel:[1,0] neg_lo:[0,1]", "=&v", r, a, bs)
#define fmah_(r, a, b) STEP_("v_pk_fma_f16 %0, %1, %2, %0 op_sel_hi:[0,1,1]", "+v", r, a, b)
#define swap_(z) do { asm("v_pk_mov_b32 %0, %1, %1 op_sel:[1,0]" : "=v"((z).s) : "v"((z).v)); SCHED(); } while (0)
#define half_(h, v) do { h = pkh((v).x, (v).y); SCHED(); } while (0)
__device__ __forceinline__ float exp_c(float v) {
  const float t = v * 1.44269502f;
  const float e = fmaf(v, 1.92596303e-8f, fmaf(v, 1.44269502f, -t));
  return __builtin_amdgcn_exp2f(t) * fmaf(e, 0.693147182f, 1.f);
}
__device__ __forceinline__ f32x2 cexp_p2(float p, float ch, float ph, float pl) {
  const float mag = __builtin_amdgcn_exp2f(p * ch);
  const float fr = fmaf(p, pl, __builtin_amdgcn_fractf(p * ph));
  return f32x2{mag * __builtin_amdgcn_cosf(fr), mag * __builtin_amdgcn_sinf(fr)};
}
__device__ __forceinline__ unsigned pkh2(f32x2 v) { return pkh(v.x, v.y); }

#define CONV_ASM_TEXT \
  "s_barrier\n" "ds_read_b128 v[32:35], %8 offset:480\n" \
  "ds_read_b128 v[36:39], %8 offset:992\n" "ds_read_b128 v[40:43], %8 offset:1504\n" \
  "ds_read2_b32 v[44:45], %11 offset0:48 offset1:49\n" "ds_read2_b32 v[46:47], %11 offset0:50 offset1:51\n" \
  "ds_read2_b32 v[48:49], %11 offset0:32 offset1:33\n" "s_waitcnt lgkmcnt(5)\n" \
  "v_mfma_f32_16x16x32_f16 v[132:135], v[32:35], v[16:19], 0\n" "s_nop 0\n" \
  "ds_read2_b32 v[50:51], %11 offset0:34 offset1:35\n" "ds_read2_b32 v[52:53], %11 offset0:16 offset1:17\n" \
  "v_mfma_f32_16x16x32_f16 v[128:131], v[32:35], v[20:23], 0\n" "s_waitcnt lgkmcnt(6)\n" \
  "ds_read2_b32 v[54:55], %11 offset0:18 offset1:19\n" "ds_read2_b32 v[56:57], %11 offset0:0 offset1:1\n" \
  "v_mfma_f32_16x16x32_f16 v[124:127], v[36:39], v[16:19], 0\n" "s_nop 0\n" \
  "ds_read2_b32 v[58:59], %11 offset0:2 offset1:3\n" "ds_read_b128 v[60:63], %8 offset:2016\n" \
  "v_mfma_f32_16x16x32_f16 v[120:123], v[36:39], v[20:23], 0\n" "s_waitcnt lgkmcnt(9)\n" \
  "v_cvt_pk_f16_f32 v116, v132, v128\n" "v_cvt_pk_f16_f32 v117, v133, v129\n" \
  "v_mfma_f32_16x16x32_f16 v[164:167], v[40:43], v[16:19], 0\n" "v_cvt_pk_f16_f32 v118, v134, v130\n" \
  "v_cvt_pk_f16_f32 v119, v135, v131\n" "v_mfma_f32_16x16x32_f16 v[160:163], v[40:43], v[20:23], 0\n" \
  "s_waitcnt lgkmcnt(7)\n" "ds_read_b128 v[64:67], %9 offset:480\n" \
  "v_cvt_pk_f16_f32 v112, v124, v120\n" "v_mfma_f32_16x16x32_f16 v[148:151], v[116:119], v[44:47], 0\n" \
  "s_waitcnt lgkmcnt(6)\n" "ds_read_b128 v[68:71], %9 offset:736\n" \
  "v_cvt_pk_f16_f32 v113, v125, v121\n" "v_mfma_f32_16x16x32_f16 v[144:147], v[116:119], v[48:51], 0\n" \
  "s_waitcnt lgkmcnt(5)\n" "v_cvt_pk_f16_f32 v114, v126, v122\n" \
  "v_cvt_pk_f16_f32 v115, v127, v123\n" "v_mfma_f32_16x16x32_f16 v[140:143], v[116:119], v[52:55], 0\n" \
  "s_waitcnt lgkmcnt(3)\n" "ds_read_b128 v[72:75], %9 offset:992\n" \
  "ds_read_b64 v[76:77], %10 offset:256\n" "v_mfma_f32_16x16x32_f16 v[136:139], v[116:119], v[56:59], 0\n" \
  "s_waitcnt lgkmcnt(4)\n" "ds_read_b64 v[78:79], %10 offset:264\n" \
  "v_cvt_pk_f16_f32 v116, v164, v160\n" "v_mfma_f32_16x16x32_f16 v[132:135], v[60:63], v[16:19], 0\n" \
  "v_cvt_pk_f16_f32 v117, v165, v161\n" "v_cvt_pk_f16_f32 v118, v166, v162\n" \
  "v_mfma_f32_16x16x32_f16 v[128:131], v[60:63], v[20:23], 0\n" "ds_read_b64 v[80:81], %10 offset:128\n" \
  "v_cvt_pk_f16_f32 v119, v167, v163\n" "v_mfma_f32_16x16x32_f16 v[144:147], v[112:115], v[44:47], v[144:147]\n" \
  "ds_read_b64 v[82:83], %10 offset:136\n" "v_mfma_f32_16x16x32_f16 v[140:143], v[112:115], v[48:51], v[140:143]\n" \
  "s_nop 0\n" "ds_read_b64 v[84:85], %10 offset:0\n" \
  "ds_read_b64 v[86:87], %10 offset:8\n" "v_mfma_f32_16x16x32_f16 v[136:139], v[112:115], v[52:55], v[136:139]\n" \
  "s_waitcnt lgkmcnt(7)\n" "ds_read_b128 v[88:91], %9 offset:1248\n" \
  "v_cvt_pk_f16_f32 v112, v132, v128\n" "v_mfma_f32_16x16x32_f16 v[124:127], v[64:67], v[24:27], 0\n" \
  "v_cvt_pk_f16_f32 v113, v133, v129\n" "v_cvt_pk_f16_f32 v114, v134, v130\n" \
  "v_mfma_f32_16x16x32_f16 v[120:123], v[64:67], v[28:31], 0\n" "ds_read_b64 v[92:93], %10 offset:320\n" \
  "v_cvt_pk_f16_f32 v115, v135, v131\n" "v_mfma_f32_16x16x32_f16 v[140:143], v[116:119], v[44:47], v[140:143]\n" \
  "ds_read_b64 v[94:95], %10 offset:328\n" "v_mfma_f32_16x16x32_f16 v[136:139], v[116:119], v[48:51], v[136:139]\n" \
  "s_nop 0\n" "ds_read_b64 v[96:97], %10 offset:192\n" \
  "ds_read_b64 v[98:99], %10 offset:200\n" "v_mfma_f32_16x16x32_f16 v[164:167], v[68:71], v[24:27], 0\n" \
  "s_nop 0\n" "ds_read_b64 v[100:101], %10 offset:64\n" \
  "ds_read_b64 v[102:103], %10 offset:72\n" "v_mfma_f32_16x16x32_f16 v[160:163], v[68:71], v[28:31], 0\n" \
  "v_cvt_pk_f16_f32 v116, v124, v120\n" "v_cvt_pk_f16_f32 v117, v125, v121\n" \
  "v_mfma_f32_16x16x32_f16 v[136:139], v[112:115], v[44:47], v[136:139]\n" "s_waitcnt lgkmcnt(7)\n" \
  "v_cvt_pk_f16_f32 v118, v126, v122\n" "v_cvt_pk_f16_f32 v119, v127, v123\n" \
  "v_mfma_f32_16x16x32_f16 v[132:135], v[72:75], v[24:27], 0\n" "ds_read_b128 v[104:107], %9 offset:1504\n" \
  "v_cvt_pk_f16_f32 v112, v164, v160\n" "v_mfma_f32_16x16x32_f16 v[128:131], v[72:75], v[28:31], 0\n" \
  "v_cvt_pk_f16_f32 v113, v165, v161\n" "v_cvt_pk_f16_f32 v114, v166, v162\n" \
  "v_mfma_f32_16x16x32_f16 v[156:159], v[116:119], v[76:79], 0\n" "ds_read_b128 v[108:111], %9 offset:1760\n" \
  "v_cvt_pk_f16_f32 v115, v167, v163\n" "v_mfma_f32_16x16x32_f16 v[152:155], v[116:119], v[80:83], 0\n" \
  "s_nop 0\n" "ds_read2_b32 v[32:33], %6 offset0:144 offset1:145\n" \
  "ds_read2_b32 v[34:35], %6 offset0:146 offset1:147\n" "v_mfma_f32_16x16x32_f16 v[148:151], v[116:119], v[84:87], v[148:151]\n" \
  "s_waitcnt lgkmcnt(6)\n" "v_cvt_pk_f16_f32 v116, v132, v128\n" \
  "v_cvt_pk_f16_f32 v117, v133, v129\n" "v_mfma_f32_16x16x32_f16 v[124:127], v[88:91], v[24:27], 0\n" \
  "v_cvt_pk_f16_f32 v118, v134, v130\n" "v_cvt_pk_f16_f32 v119, v135, v131\n" \
  "v_mfma_f32_16x16x32_f16 v[120:123], v[88:91], v[28:31], 0\n" "s_nop 0\n" \
  "ds_read_b128 v[36:39], %9 offset:2016\n" "ds_read2_b32 v[40:41], %6 offset0:16 offset1:17\n" \
  "v_mfma_f32_16x16x32_f16 v[156:159], v[112:115], v[92:95], v[156:159]\n" "ds_read2_b32 v[42:43], %6 offset0:18 offset1:19\n" \
  "v_mfma_f32_16x16x32_f16 v[152:155], v[112:115], v[96:99], v[152:155]\n" "s_waitcnt lgkmcnt(6)\n" \
  "ds_read2_b32 v[56:57], %6 offset0:48 offset1:49\n" "ds_read2_b32 v[58:59], %6 offset0:50 offset1:51\n" \
  "v_mfma_f32_16x16x32_f16 v[148:151], v[112:115], v[100:103], v[148:151]\n" "v_cvt_pk_f16_f32 v112, v124, v120\n" \
  "v_cvt_pk_f16_f32 v113, v125, v121\n" "v_mfma_f32_16x16x32_f16 v[164:167], v[104:107], v[24:27], 0\n" \
  "v_cvt_pk_f16_f32 v114, v126, v122\n" "v_cvt_pk_f16_f32 v115, v127, v123\n" \
  "v_mfma_f32_16x16x32_f16 v[160:163], v[104:107], v[28:31], 0\n" "s_nop 0\n" \
  "ds_read_b128 v[16:19], %9 offset:2272\n" "ds_read2_b32 v[60:61], %6 offset0:176 offset1:177\n" \
  "v_mfma_f32_16x16x32_f16 v[152:155], v[116:119], v[76:79], v[152:155]\n" "ds_read2_b32 v[62:63], %6 offset0:178 offset1:179\n" \
  "v_mfma_f32_16x16x32_f16 v[148:151], v[116:119], v[80:83], v[148:151]\n" "s_nop 0\n" \
  "ds_read2_b32 v[20:21], %6 offset0:32 offset1:33\n" "ds_read2_b32 v[22:23], %6 offset0:34 offset1:35\n" \
  "v_mfma_f32_16x16x32_f16 v[144:147], v[116:119], v[84:87], v[144:147]\n" "s_waitcnt lgkmcnt(8)\n" \
  "v_cvt_pk_f16_f32 v116, v164, v160\n" "v_cvt_pk_f16_f32 v117, v165, v161\n" \
  "v_mfma_f32_16x16x32_f16 v[132:135], v[108:111], v[24:27], 0\n" "v_cvt_pk_f16_f32 v118, v166, v162\n" \
  "v_cvt_pk_f16_f32 v119, v167, v163\n" "v_mfma_f32_16x16x32_f16 v[128:131], v[108:111], v[28:31], 0\n" \
  "s_nop 0\n" "ds_read_b128 v[52:55], %9 offset:2528\n" \
  "ds_read2_b32 v[64:65], %6 offset0:160 offset1:161\n" "v_mfma_f32_16x16x32_f16 v[152:155], v[112:115], v[92:95], v[152:155]\n" \
  "ds_read2_b32 v[66:67], %6 offset0:162 offset1:163\n" "v_mfma_f32_16x16x32_f16 v[148:151], v[112:115], v[96:99], v[148:151]\n" \
  "s_nop 0\n" "ds_read2_b32 v[48:49], %6 offset0:80 offset1:81\n" \
  "ds_read2_b32 v[50:51], %6 offset0:82 offset1:83\n" "v_mfma_f32_16x16x32_f16 v[144:147], v[112:115], v[100:103], v[144:147]\n" \
  "v_cvt_pk_f16_f32 v112, v132, v128\n" "v_cvt_pk_f16_f32 v113, v133, v129\n" \
  "v_mfma_f32_16x16x32_f16 v[124:127], v[36:39], v[24:27], 0\n" "v_cvt_pk_f16_f32 v114, v134, v130\n" \
  "v_cvt_pk_f16_f32 v115, v135, v131\n" "v_mfma_f32_16x16x32_f16 v[120:123], v[36:39], v[28:31], 0\n" \
  "s_nop 0\n" "ds_read_b128 v[68:71], %9 offset:2784\n" \
  "ds_read2_b32 v[44:45], %6 offset0:208 offset1:209\n" "v_mfma_f32_16x16x32_f16 v[148:151], v[116:119], v[76:79], v[148:151]\n" \
  "ds_read2_b32 v[46:47], %6 offset0:210 offset1:211\n" "v_mfma_f32_16x16x32_f16 v[144:147], v[116:119], v[80:83], v[144:147]\n" \
  "s_nop 0\n" "ds_read2_b32 v[72:73], %6 offset0:64 offset1:65\n" \
  "ds_read2_b32 v[74:75], %6 offset0:66 offset1:67\n" "v_mfma_f32_16x16x32_f16 v[140:143], v[116:119], v[84:87], v[140:143]\n" \
  "s_waitcnt lgkmcnt(8)\n" "v_cvt_pk_f16_f32 v116, v124, v120\n" \
  "v_cvt_pk_f16_f32 v117, v125, v121\n" "v_mfma_f32_16x16x32_f16 v[132:135], v[16:19], v[24:27], 0\n" \
  "v_cvt_pk_f16_f32 v118, v126, v122\n" "v_cvt_pk_f16_f32 v119, v127, v123\n" \
  "v_mfma_f32_16x16x32_f16 v[128:131], v[16:19], v[28:31], 0\n" "s_nop 0\n" \
  "ds_read_b128 v[88:91], %9 offset:3040\n" "ds_read2_b32 v[104:105], %6 offset0:192 offset1:193\n" \
  "v_mfma_f32_16x16x32_f16 v[148:151], v[112:115], v[92:95], v[148:151]\n" "ds_read2_b32 v[106:107], %6 offset0:194 offset1:195\n" \
  "v_mfma_f32_16x16x32_f16 v[144:147], v[112:115], v[96:99], v[144:147]\n" "s_nop 0\n" \
  "ds_read_b128 v[108:111], %8 offset:0\n" "ds_read2_b32 v[36:37], %6 offset0:224 offset1:225\n" \
  "v_mfma_f32_16x16x32_f16 v[140:143], v[112:115], v[100:103], v[140:143]\n" "v_cvt_pk_f16_f32 v112, v132, v128\n" \
  "v_cvt_pk_f16_f32 v113, v133, v129\n" "v_mfma_f32_16x16x32_f16 v[124:127], v[52:55], v[24:27], 0\n" \
  "v_cvt_pk_f16_f32 v114, v134, v130\n" "v_cvt_pk_f16_f32 v115, v135, v131\n" \
  "v_mfma_f32_16x16x32_f16 v[120:123], v[52:55], v[28:31], 0\n" "s_nop 0\n" \
  "ds_read2_b32 v[38:39], %6 offset0:226 offset1:227\n" "ds_read_b128 v[16:19], %9 offset:3296\n" \
  "v_mfma_f32_16x16x32_f16 v[144:147], v[116:119], v[76:79], v[144:147]\n" "ds_read2_b32 v[52:53], %6 offset0:96 offset1:97\n" \
  "v_mfma_f32_16x16x32_f16 v[140:143], v[116:119], v[80:83], v[140:143]\n" "ds_read2_b32 v[54:55], %6 offset0:98 offset1:99\n" \
  "v_mfma_f32_16x16x32_f16 v[136:139], v[116:119], v[84:87], v[136:139]\n" "s_waitcnt lgkmcnt(7)\n" \
  "v_cvt_pk_f16_f32 v116, v124, v120\n" "v_cvt_pk_f16_f32 v117, v125, v121\n" \
  "v_mfma_f32_16x16x32_f16 v[132:135], v[68:71], v[24:27], 0\n" "v_cvt_pk_f16_f32 v118, v126, v122\n" \
  "v_cvt_pk_f16_f32 v119, v127, v123\n" "v_mfma_f32_16x16x32_f16 v[128:131], v[68:71], v[28:31], 0\n" \
  "s_nop 0\n" "ds_read_b128 v[84:87], %8 offset:128\n" \
  "ds_read2_b32 v[68:69], %6 offset0:240 offset1:241\n" "v_mfma_f32_16x16x32_f16 v[144:147], v[112:115], v[92:95], v[144:147]\n" \
  "ds_read2_b32 v[70:71], %6 offset0:242 offset1:243\n" "v_mfma_f32_16x16x32_f16 v[140:143], v[112:115], v[96:99], v[140:143]\n" \
  "v_mfma_f32_16x16x32_f16 v[136:139], v[112:115], v[100:103], v[136:139]\n" "ds_read_b128 v[100:103], %8 offset:64\n" \
  "v_cvt_pk_f16_f32 v112, v132, v128\n" "v_mfma_f32_16x16x32_f16 v[124:127], v[88:91], v[24:27], 0\n" \
  "v_cvt_pk_f16_f32 v113, v133, v129\n" "v_cvt_pk_f16_f32 v114, v134, v130\n" \
  "v_mfma_f32_16x16x32_f16 v[120:123], v[88:91], v[28:31], 0\n" "s_waitcnt lgkmcnt(4)\n" \
  "ds_read_b128 v[88:91], %8 offset:256\n" "v_cvt_pk_f16_f32 v115, v135, v131\n" \
  "v_mfma_f32_16x16x32_f16 v[164:167], v[32:35], v[108:111], 0\n" "v_mfma_f32_16x16x32_f16 v[140:143], v[116:119], v[76:79], v[140:143]\n" \
  "v_mfma_f32_16x16x32_f16 v[136:139], v[116:119], v[80:83], v[136:139]\n" "s_nop 0\n" \
  "ds_read_b128 v[80:83], %8 offset:192\n" "v_cvt_pk_f16_f32 v116, v124, v120\n" \
  "v_mfma_f32_16x16x32_f16 v[132:135], v[16:19], v[24:27], 0\n" "v_cvt_pk_f16_f32 v117, v125, v121\n" \
  "v_cvt_pk_f16_f32 v118, v126, v122\n" "v_mfma_f32_16x16x32_f16 v[128:131], v[16:19], v[28:31], 0\n" \
  "ds_read_b128 v[24:27], %8 offset:320\n" "v_cvt_pk_f16_f32 v119, v127, v123\n" \
  "v_mfma_f32_16x16x32_f16 v[160:163], v[40:43], v[108:111], 0\n" "s_nop 0\n" \
  "ds_read2_b32 v[16:17], %6 offset0:112 offset1:113\n" "ds_read2_b32 v[18:19], %6 offset0:114 offset1:115\n" \
  "v_mfma_f32_16x16x32_f16 v[140:143], v[112:115], v[92:95], v[140:143]\n" "ds_read_b128 v[28:31], %8 offset:384\n" \
  "v_mfma_f32_16x16x32_f16 v[136:139], v[112:115], v[96:99], v[136:139]\n" "s_waitcnt lgkmcnt(5)\n" \
  "ds_read2_b32 v[124:125], %7 offset0:0 offset1:1\n" "v_cvt_pk_f16_f32 v112, v132, v128\n" \
  "v_mfma_f32_16x16x32_f16 v[160:163], v[56:59], v[84:87], v[160:163]\n" "v_cvt_pk_f16_f32 v113, v133, v129\n" \
  "v_cvt_pk_f16_f32 v114, v134, v130\n" "v_mfma_f32_16x16x32_f16 v[164:167], v[60:63], v[84:87], v[164:167]\n" \
  "ds_read2_b32 v[126:127], %7 offset0:2 offset1:3\n" "v_cvt_pk_f16_f32 v115, v135, v131\n" \
  "v_mfma_f32_16x16x32_f16 v[160:163], v[20:23], v[100:103], v[160:163]\n" "s_nop 0\n" \
  "ds_read_b128 v[108:111], %8 offset:448\n" "ds_read2_b32 v[120:121], %6 offset0:128 offset1:129\n" \
  "v_mfma_f32_16x16x32_f16 v[164:167], v[64:67], v[100:103], v[164:167]\n" "ds_read2_b32 v[122:123], %6 offset0:130 offset1:131\n" \
  "v_mfma_f32_16x16x32_f16 v[136:139], v[116:119], v[76:79], v[136:139]\n" "s_nop 0\n" \
  "ds_read2_b32 v[96:97], %7 offset0:16 offset1:17\n" "ds_read2_b32 v[98:99], %7 offset0:18 offset1:19\n" \
  "v_mfma_f32_16x16x32_f16 v[160:163], v[48:51], v[88:91], v[160:163]\n" "ds_read_b128 v[84:87], %8 offset:512\n" \
  "v_mfma_f32_16x16x32_f16 v[164:167], v[44:47], v[88:91], v[164:167]\n" "s_waitcnt lgkmcnt(8)\n" \
  "v_mfma_f32_16x16x32_f16 v[160:163], v[72:75], v[80:83], v[160:163]\n" "s_nop 0\n" \
  "ds_read2_b32 v[132:133], %6 offset0:0 offset1:1\n" "ds_read2_b32 v[134:135], %6 offset0:2 offset1:3\n" \
  "v_mfma_f32_16x16x32_f16 v[164:167], v[104:107], v[80:83], v[164:167]\n" "ds_read_b128 v[128:131], %8 offset:576\n" \
  "v_mfma_f32_16x16x32_f16 v[136:139], v[112:115], v[92:95], v[136:139]\n" "ds_read_b128 v[100:103], %8 offset:640\n" \
  "v_mfma_f32_16x16x32_f16 v[164:167], v[36:39], v[24:27], v[164:167]\n" "ds_read_b128 v[116:119], %8 offset:704\n" \
  "v_mfma_f32_16x16x32_f16 v[160:163], v[52:55], v[24:27], v[160:163]\n" "v_mfma_f32_16x16x32_f16 v[164:167], v[68:71], v[28:31], v[164:167]\n" \
  "s_nop 0\n" "ds_read_b128 v[76:79], %8 offset:768\n" \
  "ds_read_b128 v[88:91], %8 offset:832\n" "v_mfma_f32_16x16x32_f16 v[160:163], v[16:19], v[28:31], v[160:163]\n" \
  "s_waitcnt lgkmcnt(7)\n" "ds_read_b128 v[80:83], %8 offset:896\n" \
  "v_mfma_f32_16x16x32_f16 v[164:167], v[124:127], v[108:111], v[164:167]\n" "ds_read_b128 v[112:115], %8 offset:960\n" \
  "v_mfma_f32_16x16x32_f16 v[160:163], v[120:123], v[108:111], v[160:163]\n" "ds_read_b128 v[92:95], %8 offset:1024\n" \
  "v_mfma_f32_16x16x32_f16 v[164:167], v[96:99], v[84:87], v[164:167]\n" "v_mfma_f32_16x16x32_f16 v[160:163], v[32:35], v[84:87], v[160:163]\n" \
  "s_nop 0\n" "ds_read_b128 v[24:27], %8 offset:1088\n" \
  "ds_read_b128 v[28:31], %8 offset:1152\n" "v_mfma_f32_16x16x32_f16 v[156:159], v[40:43], v[84:87], v[156:159]\n" \
  "s_waitcnt lgkmcnt(7)\n" "ds_read_b128 v[108:111], %8 offset:1216\n" \
  "v_mfma_f32_16x16x32_f16 v[148:151], v[132:135], v[84:87], v[148:151]\n" "v_mul_f32 v164, %5, v164\n" \
  "v_mul_f32 v165, %5, v165\n" "v_mfma_f32_16x16x32_f16 v[160:163], v[64:67], v[128:131], v[160:163]\n" \
  "v_mul_f32 v166, %5, v166\n" "v_mul_f32 v167, %5, v167\n" \
  "v_mfma_f32_16x16x32_f16 v[156:159], v[20:23], v[128:131], v[156:159]\n" "v_cvt_pk_bf16_f32 v164, v164, v165\n" \
  "v_cvt_pk_bf16_f32 v165, v166, v167\n" "v_mfma_f32_16x16x32_f16 v[160:163], v[60:63], v[100:103], v[160:163]\n" \
  "v_pk_min_u16 v164, v164, %4\n" "v_pk_min_u16 v165, v165, %4\n" \
  "v_mfma_f32_16x16x32_f16 v[156:159], v[56:59], v[100:103], v[156:159]\n" "ds_write_b64 %12, v[164:165] offset:0\n" \
  "v_mfma_f32_16x16x32_f16 v[160:163], v[104:107], v[116:119], v[160:163]\n" "s_nop 0\n" \
  "ds_read_b128 v[84:87], %8 offset:1280\n" "ds_read_b128 v[128:131], %8 offset:1344\n" \
  "v_mfma_f32_16x16x32_f16 v[156:159], v[72:75], v[116:119], v[156:159]\n" "s_waitcnt lgkmcnt(4)\n" \
  "ds_read_b128 v[100:103], %8 offset:1408\n" "ds_read_b128 v[164:167], %8 offset:1472\n" \
  "v_mfma_f32_16x16x32_f16 v[160:163], v[44:47], v[76:79], v[160:163]\n" "v_mfma_f32_16x16x32_f16 v[156:159], v[48:51], v[76:79], v[156:159]\n" \
  "v_mfma_f32_16x16x32_f16 v[160:163], v[36:39], v[88:91], v[160:163]\n" "ds_read_b128 v[116:119], %8 offset:1536\n" \
  "v_mfma_f32_16x16x32_f16 v[156:159], v[52:55], v[88:91], v[156:159]\n" "s_nop 0\n" \
  "ds_read_b128 v[76:79], %8 offset:1600\n" "ds_read_b128 v[88:91], %8 offset:1664\n" \
  "v_mfma_f32_16x16x32_f16 v[160:163], v[68:71], v[80:83], v[160:163]\n" "v_mfma_f32_16x16x32_f16 v[156:159], v[16:19], v[80:83], v[156:159]\n" \
  "ds_read_b128 v[80:83], %8 offset:1728\n" "v_mfma_f32_16x16x32_f16 v[160:163], v[124:127], v[112:115], v[160:163]\n" \
  "v_mfma_f32_16x16x32_f16 v[156:159], v[120:123], v[112:115], v[156:159]\n" "ds_read_b128 v[112:115], %8 offset:1792\n" \
  "v_mfma_f32_16x16x32_f16 v[160:163], v[96:99], v[92:95], v[160:163]\n" "v_mfma_f32_16x16x32_f16 v[156:159], v[32:35], v[92:95], v[156:159]\n" \
  "v_mfma_f32_16x16x32_f16 v[152:155], v[40:43], v[92:95], v[152:155]\n" "v_mfma_f32_16x16x32_f16 v[144:147], v[132:135], v[92:95], v[144:147]\n" \
  "ds_read_b128 v[92:95], %8 offset:1856\n" "v_mfma_f32_16x16x32_f16 v[156:159], v[64:67], v[24:27], v[156:159]\n" \
  "v_mfma_f32_16x16x32_f16 v[152:155], v[20:23], v[24:27], v[152:155]\n" "ds_read_b128 v[24:27], %8 offset:1920\n" \
  "v_mfma_f32_16x16x32_f16 v[156:159], v[60:63], v[28:31], v[156:159]\n" "v_mul_f32 v160, %5, v160\n" \
  "v_mul_f32 v161, %5, v161\n" "v_mfma_f32_16x16x32_f16 v[152:155], v[56:59], v[28:31], v[152:155]\n" \
  "s_waitcnt lgkmcnt(2)\n" "v_mul_f32 v162, %5, v162\n" \
  "v_mul_f32 v163, %5, v163\n" "v_mfma_f32_16x16x32_f16 v[156:159], v[104:107], v[108:111], v[156:159]\n" \
  "v_cvt_pk_bf16_f32 v160, v160, v161\n" "v_cvt_pk_bf16_f32 v161, v162, v163\n" \
  "v_mfma_f32_16x16x32_f16 v[152:155], v[72:75], v[108:111], v[152:155]\n" "v_pk_min_u16 v160, v160, %4\n" \
  "v_pk_min_u16 v161, v161, %4\n" "v_mfma_f32_16x16x32_f16 v[156:159], v[44:47], v[84:87], v[156:159]\n" \
  "ds_write_b64 %12, v[160:161] offset:512\n" "v_mfma_f32_16x16x32_f16 v[152:155], v[48:51], v[84:87], v[152:155]\n" \
  "s_nop 0\n" "ds_read_b128 v[28:31], %8 offset:1984\n" \
  "ds_read_b128 v[108:111], %8 offset:2048\n" "v_mfma_f32_16x16x32_f16 v[156:159], v[36:39], v[128:131], v[156:159]\n" \
  "s_nop 0\n" "ds_read_b128 v[160:163], %8 offset:2112\n" \
  "ds_read_b128 v[84:87], %8 offset:2176\n" "v_mfma_f32_16x16x32_f16 v[152:155], v[52:55], v[128:131], v[152:155]\n" \
  "v_mfma_f32_16x16x32_f16 v[156:159], v[68:71], v[100:103], v[156:159]\n" "v_mfma_f32_16x16x32_f16 v[152:155], v[16:19], v[100:103], v[152:155]\n" \
  "s_waitcnt lgkmcnt(4)\n" "ds_read_b128 v[128:131], %8 offset:2240\n" \
  "v_mfma_f32_16x16x32_f16 v[156:159], v[124:127], v[164:167], v[156:159]\n" "s_barrier\n" \
  "ds_read_b128 v[100:103], %8 offset:2304\n" "v_mfma_f32_16x16x32_f16 v[152:155], v[120:123], v[164:167], v[152:155]\n" \
  "ds_read_b128 v[164:167], %8 offset:2368\n" "v_mfma_f32_16x16x32_f16 v[156:159], v[96:99], v[116:119], v[156:159]\n" \
  "v_mfma_f32_16x16x32_f16 v[152:155], v[32:35], v[116:119], v[152:155]\n" "v_mfma_f32_16x16x32_f16 v[148:151], v[40:43], v[116:119], v[148:151]\n" \
  "v_mfma_f32_16x16x32_f16 v[140:143], v[132:135], v[116:119], v[140:143]\n" "ds_read_b128 v[116:119], %8 offset:2432\n" \
  "v_mfma_f32_16x16x32_f16 v[152:155], v[64:67], v[76:79], v[152:155]\n" "v_mfma_f32_16x16x32_f16 v[148:151], v[20:23], v[76:79], v[148:151]\n" \
  "ds_read_b128 v[76:79], %8 offset:2496\n" "v_mfma_f32_16x16x32_f16 v[152:155], v[60:63], v[88:91], v[152:155]\n" \
  "v_mul_f32 v156, %5, v156\n" "v_mul_f32 v157, %5, v157\n" \
  "v_mfma_f32_16x16x32_f16 v[148:151], v[56:59], v[88:91], v[148:151]\n" "v_mul_f32 v158, %5, v158\n" \
  "v_mul_f32 v159, %5, v159\n" "v_mfma_f32_16x16x32_f16 v[152:155], v[104:107], v[80:83], v[152:155]\n" \
  "v_cvt_pk_bf16_f32 v156, v156, v157\n" "v_cvt_pk_bf16_f32 v157, v158, v159\n" \
  "v_mfma_f32_16x16x32_f16 v[148:151], v[72:75], v[80:83], v[148:151]\n" "v_pk_min_u16 v156, v156, %4\n" \
  "v_pk_min_u16 v157, v157, %4\n" "v_mfma_f32_16x16x32_f16 v[152:155], v[44:47], v[112:115], v[152:155]\n" \
  "ds_write_b64 %12, v[156:157] offset:1024\n" "v_mfma_f32_16x16x32_f16 v[148:151], v[48:51], v[112:115], v[148:151]\n" \
  "s_nop 0\n" "ds_read_b128 v[88:91], %8 offset:2560\n" \
  "ds_read_b128 v[80:83], %8 offset:2624\n" "v_mfma_f32_16x16x32_f16 v[152:155], v[36:39], v[92:95], v[152:155]\n" \
  "s_nop 0\n" "ds_read_b128 v[156:159], %8 offset:2688\n" \
  "ds_read_b128 v[112:115], %8 offset:2752\n" "v_mfma_f32_16x16x32_f16 v[148:151], v[52:55], v[92:95], v[148:151]\n" \
  "v_mfma_f32_16x16x32_f16 v[152:155], v[68:71], v[24:27], v[152:155]\n" "v_mfma_f32_16x16x32_f16 v[148:151], v[16:19], v[24:27], v[148:151]\n" \
  "s_waitcnt lgkmcnt(5)\n" "ds_read_b128 v[92:95], %8 offset:2816\n" \
  "v_mfma_f32_16x16x32_f16 v[152:155], v[124:127], v[28:31], v[152:155]\n" "ds_read_b128 v[24:27], %8 offset:2880\n" \
  "v_mfma_f32_16x16x32_f16 v[148:151], v[120:123], v[28:31], v[148:151]\n" "ds_read_b128 v[28:31], %8 offset:2944\n" \
  "v_mfma_f32_16x16x32_f16 v[152:155], v[96:99], v[108:111], v[152:155]\n" "v_mfma_f32_16x16x32_f16 v[148:151], v[32:35], v[108:111], v[148:151]\n" \
  "v_mfma_f32_16x16x32_f16 v[144:147], v[40:43], v[108:111], v[144:147]\n" "v_mfma_f32_16x16x32_f16 v[136:139], v[132:135], v[108:111], v[136:139]\n" \
  "ds_read_b128 v[132:135], %8 offset:3008\n" "v_mfma_f32_16x16x32_f16 v[148:151], v[64:67], v[160:163], v[148:151]\n" \
  "v_mfma_f32_16x16x32_f16 v[144:147], v[20:23], v[160:163], v[144:147]\n" "v_mfma_f32_16x16x32_f16 v[148:151], v[60:63], v[84:87], v[148:151]\n" \
  "s_nop 0\n" "v_mul_f32 v152, %5, v152\n" \
  "v_mul_f32 v153, %5, v153\n" "v_mfma_f32_16x16x32_f16 v[144:147], v[56:59], v[84:87], v[144:147]\n" \
  "v_mul_f32 v154, %5, v154\n" "v_mul_f32 v155, %5, v155\n" \
  "v_mfma_f32_16x16x32_f16 v[148:151], v[104:107], v[128:131], v[148:151]\n" "v_cvt_pk_bf16_f32 v152, v152, v153\n" \
  "v_cvt_pk_bf16_f32 v153, v154, v155\n" "v_mfma_f32_16x16x32_f16 v[144:147], v[72:75], v[128:131], v[144:147]\n" \
  "v_pk_min_u16 v152, v152, %4\n" "v_pk_min_u16 v153, v153, %4\n" \
  "v_mfma_f32_16x16x32_f16 v[148:151], v[44:47], v[100:103], v[148:151]\n" "ds_write_b64 %12, v[152:153] offset:1536\n" \
  "v_mfma_f32_16x16x32_f16 v[144:147], v[48:51], v[100:103], v[144:147]\n" "v_mfma_f32_16x16x32_f16 v[148:151], v[36:39], v[164:167], v[148:151]\n" \
  "v_mfma_f32_16x16x32_f16 v[144:147], v[52:55], v[164:167], v[144:147]\n" "v_mfma_f32_16x16x32_f16 v[148:151], v[68:71], v[116:119], v[148:151]\n" \
  "v_mfma_f32_16x16x32_f16 v[144:147], v[16:19], v[116:119], v[144:147]\n" "s_waitcnt lgkmcnt(0)\n" \
  "v_mfma_f32_16x16x32_f16 v[148:151], v[124:127], v[76:79], v[148:151]\n" "s_barrier\n" \
  "ds_read_b128 v[108:111], %8 offset:3072\n" "ds_read_b128 v[160:163], %8 offset:3136\n" \
  "v_mfma_f32_16x16x32_f16 v[144:147], v[120:123], v[76:79], v[144:147]\n" "s_nop 0\n" \
  "ds_read_b128 v[84:87], %8 offset:3200\n" "ds_read_b128 v[128:131], %8 offset:3264\n" \
  "v_mfma_f32_16x16x32_f16 v[148:151], v[96:99], v[88:91], v[148:151]\n" "ds_read_b128 v[152:155], %8 offset:3328\n" \
  "v_mfma_f32_16x16x32_f16 v[144:147], v[32:35], v[88:91], v[144:147]\n" "s_nop 2\n" \
  "v_mfma_f32_16x16x32_f16 v[140:143], v[40:43], v[88:91], v[140:143]\n" "s_nop 1\n" \
  "v_mfma_f32_16x16x32_f16 v[144:147], v[64:67], v[80:83], v[144:147]\n" "v_mul_f32 v148, %5, v148\n" \
  "v_mul_f32 v149, %5, v149\n" "v_mfma_f32_16x16x32_f16 v[140:143], v[20:23], v[80:83], v[140:143]\n" \
  "v_mul_f32 v150, %5, v150\n" "v_mul_f32 v151, %5, v151\n" \
  "v_mfma_f32_16x16x32_f16 v[144:147], v[60:63], v[156:159], v[144:147]\n" "v_cvt_pk_bf16_f32 v148, v148, v149\n" \
  "v_cvt_pk_bf16_f32 v149, v150, v151\n" "v_mfma_f32_16x16x32_f16 v[140:143], v[56:59], v[156:159], v[140:143]\n" \
  "v_pk_min_u16 v148, v148, %4\n" "v_pk_min_u16 v149, v149, %4\n" \
  "v_mfma_f32_16x16x32_f16 v[144:147], v[104:107], v[112:115], v[144:147]\n" "ds_write_b64 %12, v[148:149] offset:2048\n" \
  "v_mfma_f32_16x16x32_f16 v[140:143], v[72:75], v[112:115], v[140:143]\n" "s_nop 0\n" \
  "ds_read_b128 v[100:103], %8 offset:3392\n" "ds_read_b128 v[164:167], %8 offset:3456\n" \
  "v_mfma_f32_16x16x32_f16 v[144:147], v[44:47], v[92:95], v[144:147]\n" "s_nop 0\n" \
  "ds_read_b128 v[116:119], %8 offset:3520\n" "ds_read_b128 v[76:79], %8 offset:3584\n" \
  "v_mfma_f32_16x16x32_f16 v[140:143], v[48:51], v[92:95], v[140:143]\n" "v_mfma_f32_16x16x32_f16 v[144:147], v[36:39], v[24:27], v[144:147]\n" \
  "v_mfma_f32_16x16x32_f16 v[140:143], v[52:55], v[24:27], v[140:143]\n" "ds_read_b128 v[88:91], %8 offset:3648\n" \
  "v_mfma_f32_16x16x32_f16 v[144:147], v[68:71], v[28:31], v[144:147]\n" "s_nop 0\n" \
  "ds_read_b128 v[80:83], %8 offset:3712\n" "ds_read_b128 v[156:159], %8 offset:3776\n" \
  "v_mfma_f32_16x16x32_f16 v[140:143], v[16:19], v[28:31], v[140:143]\n" "s_nop 0\n" \
  "ds_read_b128 v[148:151], %8 offset:3840\n" "ds_read_b128 v[112:115], %8 offset:3904\n" \
  "v_mfma_f32_16x16x32_f16 v[144:147], v[124:127], v[132:135], v[144:147]\n" "v_mfma_f32_16x16x32_f16 v[140:143], v[120:123], v[132:135], v[140:143]\n" \
  "s_waitcnt lgkmcnt(7)\n" "v_mfma_f32_16x16x32_f16 v[144:147], v[96:99], v[108:111], v[144:147]\n" \
  "ds_read_b128 v[92:95], %8 offset:3968\n" "v_mfma_f32_16x16x32_f16 v[140:143], v[32:35], v[108:111], v[140:143]\n" \
  "s_nop 0\n" "ds_read_b128 v[24:27], %8 offset:4032\n" \
  "ds_read_b128 v[28:31], %8 offset:4096\n" "v_mfma_f32_16x16x32_f16 v[136:139], v[40:43], v[108:111], v[136:139]\n" \
  "s_nop 1\n" "v_mfma_f32_16x16x32_f16 v[140:143], v[64:67], v[160:163], v[140:143]\n" \
  "v_mul_f32 v144, %5, v144\n" "v_mul_f32 v145, %5, v145\n" \
  "v_mfma_f32_16x16x32_f16 v[140:143], v[60:63], v[84:87], v[140:143]\n" "v_mul_f32 v146, %5, v146\n" \
  "v_mul_f32 v147, %5, v147\n" "v_mfma_f32_16x16x32_f16 v[140:143], v[104:107], v[128:131], v[140:143]\n" \
  "v_cvt_pk_bf16_f32 v144, v144, v145\n" "v_cvt_pk_bf16_f32 v145, v146, v147\n" \
  "v_mfma_f32_16x16x32_f16 v[140:143], v[44:47], v[152:155], v[140:143]\n" "v_pk_min_u16 v144, v144, %4\n" \
  "v_pk_min_u16 v145, v145, %4\n" "v_mfma_f32_16x16x32_f16 v[140:143], v[36:39], v[100:103], v[140:143]\n" \
  "ds_write_b64 %12, v[144:145] offset:2560\n" "v_mfma_f32_16x16x32_f16 v[140:143], v[68:71], v[164:167], v[140:143]\n" \
  "s_waitcnt lgkmcnt(3)\n" "v_mfma_f32_16x16x32_f16 v[140:143], v[124:127], v[116:119], v[140:143]\n" \
  "v_mfma_f32_16x16x32_f16 v[140:143], v[96:99], v[76:79], v[140:143]\n" "s_nop 2\n" \
  "v_mfma_f32_16x16x32_f16 v[136:139], v[20:23], v[160:163], v[136:139]\n" "s_nop 2\n" \
  "v_mfma_f32_16x16x32_f16 v[136:139], v[56:59], v[84:87], v[136:139]\n" "s_waitcnt lgkmcnt(0)\n" \
  "v_mul_f32 v140, %5, v140\n" "v_mul_f32 v141, %5, v141\n" \
  "v_mfma_f32_16x16x32_f16 v[136:139], v[72:75], v[128:131], v[136:139]\n" "s_barrier\n" \
  "v_mul_f32 v142, %5, v142\n" "v_mul_f32 v143, %5, v143\n" \
  "v_mfma_f32_16x16x32_f16 v[136:139], v[48:51], v[152:155], v[136:139]\n" "v_cvt_pk_bf16_f32 v140, v140, v141\n" \
  "v_cvt_pk_bf16_f32 v141, v142, v143\n" "v_mfma_f32_16x16x32_f16 v[136:139], v[52:55], v[100:103], v[136:139]\n" \
  "v_pk_min_u16 v140, v140, %4\n" "v_pk_min_u16 v141, v141, %4\n" \
  "v_mfma_f32_16x16x32_f16 v[136:139], v[16:19], v[164:167], v[136:139]\n" "ds_write_b64 %12, v[140:141] offset:3072\n" \
  "v_mfma_f32_16x16x32_f16 v[136:139], v[120:123], v[116:119], v[136:139]\n" "v_mfma_f32_16x16x32_f16 v[136:139], v[32:35], v[76:79], v[136:139]\n" \
  "v_mfma_f32_16x16x32_f16 v[136:139], v[64:67], v[88:91], v[136:139]\n" "v_mfma_f32_16x16x32_f16 v[136:139], v[60:63], v[80:83], v[136:139]\n" \
  "v_mfma_f32_16x16x32_f16 v[136:139], v[104:107], v[156:159], v[136:139]\n" "v_mfma_f32_16x16x32_f16 v[136:139], v[44:47], v[148:151], v[136:139]\n" \
  "v_mfma_f32_16x16x32_f16 v[136:139], v[36:39], v[112:115], v[136:139]\n" "v_mfma_f32_16x16x32_f16 v[136:139], v[68:71], v[92:95], v[136:139]\n" \
  "v_mfma_f32_16x16x32_f16 v[136:139], v[124:127], v[24:27], v[136:139]\n" "v_mfma_f32_16x16x32_f16 v[136:139], v[96:99], v[28:31], v[136:139]\n" \
  "s_nop 2\n" "s_nop 4\n" \
  "v_mul_f32 v136, %5, v136\n" "v_mul_f32 v137, %5, v137\n" \
  "v_mul_f32 v138, %5, v138\n" "v_mul_f32 v139, %5, v139\n" \
  "v_cvt_pk_bf16_f32 v136, v136, v137\n" "v_cvt_pk_bf16_f32 v137, v138, v139\n" \
  "v_pk_min_u16 v136, v136, %4\n" "v_pk_min_u16 v137, v137, %4\n" \
  "ds_write_b64 %12, v[136:137] offset:3584\n" "s_waitcnt lgkmcnt(0)\n" \
  "s_barrier\n"
#define CONV_ASM_CLOBBERS "v32", "v33", "v34", "v35", "v36", "v37", "v38", "v39", "v40", "v41", "v42", "v43", "v44", "v45", "v46", "v47", "v48", "v49", "v50", "v51", "v52", "v53", "v54", "v55", "v56", "v57", "v58", "v59", "v60", "v61", "v62", "v63", "v64", "v65", "v66", "v67", "v68", "v69", "v70", "v71", "v72", "v73", "v74", "v75", "v76", "v77", "v78", "v79", "v80", "v81", "v82", "v83", "v84", "v85", "v86", "v87", "v88", "v89", "v90", "v91", "v92", "v93", "v94", "v95", "v96", "v97", "v98", "v99", "v100", "v101", "v102", "v103", "v104", "v105", "v106", "v107", "v108", "v109", "v110", "v111", "v112", "v113", "v114", "v115", "v116", "v117", "v118", "v119", "v120", "v121", "v122", "v123", "v124", "v125", "v126", "v127", "v128", "v129", "v130", "v131", "v132", "v133", "v134", "v135", "v136", "v137", "v138", "v139", "v140", "v141", "v142", "v143", "v144", "v145", "v146", "v147", "v148", "v149", "v150", "v151", "v152", "v153", "v154", "v155", "v156", "v157", "v158", "v159", "v160", "v161", "v162", "v163", "v164", "v165", "v166", "v167", "memory"
#define CONV_ASM_PRE(q) "+{v[16:19]}"(q[0]), "+{v[20:23]}"(q[1]), "+{v[24:27]}"(q[2]), "+{v[28:31]}"(q[3])

struct Params {
  const u16 *x, *lar, *aim, *crl, *cim, *ldt, *dskip, *wr, *wg, *wu, *wd;
  u16* out;
  char* ws;
};

typedef unsigned long long u64;
typedef u64 v4q __attribute__((ext_vector_type(4)));
typedef unsigned v16u __attribute__((ext_vector_type(16)));
#define GAS __attribute__((address_space(1)))

#define T3_ASM
#define T3_OUT
#define T1_ASM
#define T2_ASM

__global__ __launch_bounds__(NT, 3) void k_main(Params) {
  extern __shared__ __attribute__((aligned(16))) char smem[];
  u64 kx, kw, t0_, t1_, t2_;
  v4q kb_;
  unsigned epoch, sep;
  v4u ga, gb, gc, gd;
  const unsigned bid = blockIdx.x;
  asm volatile(
      ".p2align 12\n\t"
      "s_load_dwordx16 s[8:23], s[0:1], 0x0\n\t"
      "s_getpc_b64 s[4:5]\n\t"
      "s_load_dwordx2 s[36:37], s[0:1], 0x60\n\t"
      "s_load_dwordx8 s[24:31], s[0:1], 0x40\n\t" T0_ASM
      "v_lshrrev_b32 v17, 6, v0\n\t"
      "s_lshl_b32 s3, s2, 4\n\t"
      "v_mov_b32 v1, 0\n\t"
      "v_lshl_add_u32 v17, v17, 12, s3\n\t"
      "s_cmpk_lt_u32 s2, 144\n\t"
      "v_min_u32 v17, %[code], v17\n\t"
      "global_load_dword v17, v17, s[4:5]\n\t"
      "s_cbranch_scc1 Lc_%=\n\t"
      "s_add_i32 s3, s2, 0xffffff70\n\t"
      "v_bfe_u32 v18, v0, 3, 3\n\t"
      "v_lshlrev_b32 v19, 1, v0\n\t"
      "s_mulk_i32 s3, 18432\n\t"
      "v_lshlrev_b32 v20, 4, v0\n\t"
      "s_movk_i32 s6, 127\n\t"
      "v_mul_u32_u24 v18, 1152, v18\n\t"
      "v_bfi_b32 v19, s6, v20, v19\n\t"
      "v_add3_u32 v18, v18, v19, s3\n\t"
      "s_cmpk_lt_u32 s2, 240\n\t"
      "v_add_u32 v19, 9216, v18\n\t"
      "v_add_u32 v10, 294912, v18\n\t"
      "v_add_u32 v14, 304128, v18\n\t"
      "s_waitcnt lgkmcnt(0)\n\t"
      "s_cbranch_scc1 Lx_%=\n\t"
      "s_sleep %[x2d]\n"
      "Lx_%=:\n\t"
      "global_load_dwordx4 v[2:5], v18, s[8:9]\n\t"
      "global_load_dwordx4 v[6:9], v19, s[8:9]\n\t"
      "s_load_dword s33, s[36:37], 0x0\n\t"
      "s_branch Le_%=\n"
      "Lw_%=:\n\t"
      "v_lshlrev_b32 v7, 2, v0\n\t"
      "s_waitcnt lgkmcnt(0)\n\t"
      "s_sleep 3\n\t"
      "global_load_dword v1, v1, s[36:37]\n\t"
      "global_load_dword v2, v7, s[22:23] offset:-2048\n\t"
      "global_load_dword v3, v7, s[22:23] offset:-1792\n\t"
      "global_load_dword v4, v7, s[22:23] offset:-1536\n\t"
      "global_load_dword v5, v7, s[22:23] offset:-1280\n\t"
      "global_load_dword v6, v7, s[22:23] offset:-1152\n\t"
      "s_branch Le_%=\n"
      "Lc_%=:\n\t"
      "v_readfirstlane_b32 s3, v0\n\t"
      "v_bfe_u32 v6, v0, 6, 2\n\t"
      "s_lshl_b32 s4, s2, 2\n\t"
      "v_bfe_u32 v5, v0, 0, 8\n\t"
      "s_lshl_b32 s5, s2, 10\n\t"
      "s_cmpk_ge_u32 s3, 512\n\t"
      "v_add_lshl_u32 v16, v6, s4, 1\n\t"
      "s_cbranch_scc1 Lw_%=\n\t"
      "v_lshl_add_u32 v5, v5, 2, s5\n\t"
      "s_lshr_b32 s4, s3, 8\n\t"
      "s_bfe_u32 s5, s3, 0x20006\n\t"
      "v_lshl_add_u32 v3, s4, 1, v5\n\t"
      "v_and_b32 v7, 63, v0\n\t"
      "s_mulk_i32 s5, %[ustr]\n\t"
      "s_mulk_i32 s4, 3584\n\t"
      "v_mov_b64 v[10:11], 0\n\t"
      "s_add_i32 s5, s5, s4\n\t"
      "v_mov_b64 v[12:13], 0\n\t"
      "s_add_i32 s5, s5, %[uimg]\n\t"
      "v_lshlrev_b32 v9, 4, v7\n\t"
      "s_lshl_b32 s3, s3, 8\n\t"
      "v_add_u32 v8, s5, v9\n\t"
      "v_add_u32 v9, %[filt], v9\n\t"
      "ds_write_b128 v8, v[10:13]\n\t"
      "ds_write_b128 v8, v[10:13] offset:32\n\t"
      "ds_write_b128 v9, v[10:13] offset:4096\n\t"
      "v_and_b32 v15, 31, v7\n\t"
      "s_lshr_b32 s4, s3, 14\n\t"
      "v_lshrrev_b32 v14, 5, v7\n\t"
      "s_mulk_i32 s4, %[tstr]\n\t"
      "v_mul_u32_u24 v15, %[trow], v15\n\t"
      "s_mov_b32 m0, s4\n\t"
      "v_lshlrev_b32 v14, 4, v14\n\t"
      "v_add3_u32 v41, v14, v15, s4\n\t"
      "s_mov_b32 s4, 0xfe0001ff\n\t"
      "s_mov_b32 s5, 0x0380007c\n\t"
      "s_lshr_b32 s6, s3, 10\n\t"
      "v_and_b32 v8, 15, v7\n\t"
      "s_lshr_b64 s[4:5], s[4:5], s6\n\t"
      "v_lshrrev_b32 v14, 1, v7\n\t"
      "s_and_b32 s4, s4, 0xffff\n\t"
      "v_and_b32 v14, 24, v14\n\t"
      "v_lshlrev_b32 v8, v8, s4\n\t"
      "s_mov_b32 s4, 0x8001\n\t"
      "v_lshrrev_b32 v8, v14, v8\n\t"
      "s_mov_b32 s5, 0x10001\n\t"
      "v_bfe_u32 v10, v8, 0, 2\n\t"
      "v_bfe_u32 v11, v8, 2, 2\n\t"
      "v_bfe_u32 v12, v8, 4, 2\n\t"
      "v_bfe_u32 v13, v8, 6, 2\n\t"
      "s_bfe_u32 s6, s3, 0x2000e\n\t"
      "v_mul_u32_u24 v10, s4, v10\n\t"
      "v_mul_u32_u24 v11, s4, v11\n\t"
      "v_mul_u32_u24 v12, s4, v12\n\t"
      "v_mul_u32_u24 v13, s4, v13\n\t"
      "s_movk_i32 s4, 0x3c00\n\t"
      "v_and_b32 v10, s5, v10\n\t"
      "v_and_b32 v11, s5, v11\n\t"
      "v_and_b32 v12, s5, v12\n\t"
      "v_and_b32 v13, s5, v13\n\t"
      "v_lshl_add_u32 v9, s6, 10, v9\n\t"
      "v_mul_u32_u24 v10, s4, v10\n\t"
      "v_mul_u32_u24 v11, s4, v11\n\t"
      "v_mul_u32_u24 v12, s4, v12\n\t"
      "v_mul_u32_u24 v13, s4, v13\n\t"
      "ds_write_b128 v9, v[10:13]\n\t"
      T2_ASM "s_waitcnt lgkmcnt(0)\n\t" T1_ASM
      "global_load_ushort v6, v16, s[18:19]\n\t"
      "global_load_ushort v2, v3, s[10:11]\n\t"
      "global_load_ushort v3, v3, s[12:13]\n\t"
      "s_and_b32 s7, s3, 0xc000\n\t"
      "v_lshlrev_b32 v47, 4, v7\n\t"
      "s_add_i32 s4, s7, %[dummy]\n\t"
      "v_add_u32 v48, s7, v47\n\t"
      "s_bitcmp1_b32 s3, 16\n\t"
      "v_lshl_add_u32 v46, v7, 2, s4\n\t"
      "s_cbranch_scc1 Lm_%=\n\t"
      "s_cmpk_lt_u32 s2, 128\n\t"
      "v_and_b32 v8, 3, v7\n\t"
      "s_movk_i32 s4, %[u2]\n\t"
      "v_bfe_u32 v10, v7, 2, 2\n\t"
      "s_cmovk_i32 s4, %[u1]\n\t"
      "v_lshrrev_b32 v11, 5, v7\n\t"
      "s_movk_i32 s5, %[y2]\n\t"
      "v_and_b32 v12, 0x83, v0\n\t"
      "s_cmovk_i32 s5, %[y1]\n\t"
      "v_and_b32 v13, 0x70, v0\n\t"
      "s_add_i32 s4, s4, s2\n\t"
      "v_mul_u32_u24 v8, %[ustr] - 4, v8\n\t"
      "s_add_i32 s5, s5, s2\n\t"
      "v_mul_u32_u24 v10, 0x4020, v10\n\t"
      "s_lshl_b32 s4, s4, 14\n\t"
      "v_mul_u32_u24 v11, 7680, v11\n\t"
      "s_lshl_b32 s5, s5, 5\n\t"
      "v_mul_u32_u24 v12, 132, v12\n\t"
      "s_or_b32 s5, s5, s6\n\t"
      "v_lshl_add_u32 v7, v0, 4, s4\n\t"
      "s_lshl_b32 s5, s5, 9\n\t"
      "v_lshl_add_u32 v8, v0, 2, v8\n\t"
      "s_lshl2_add_u32 s4, s6, %[misc]\n\t"
      "v_and_b32 v12, 0x288, v12\n\t"
      "v_add3_u32 v11, v11, v47, s5\n\t"
      "v_add_u32 v9, %[uimg] + 520 + 2048, v8\n\t"
      "v_add_u32 v8, %[uimg] + 520, v8\n\t"
      "v_add3_u32 v10, v10, v12, v13\n\t"
      "v_mov_b32 v42, v48\n\t"
      "s_movk_i32 exec_lo, 0xff80\n\t"
      "v_add_u32 v42, 1024, v48\n\t"
      "s_mov_b32 exec_lo, 0xfff80000\n\t"
      "v_add_u32 v42, 2048, v48\n\t"
      "s_mov_b32 exec_lo, 0\n\t"
      "v_add_u32 v42, 2304, v48\n\t"
      "s_movk_i32 exec_hi, 0xff00\n\t"
      "v_add_u32 v42, 2560, v48\n\t"
      "s_mov_b64 exec, 1\n\t"
      "v_mov_b32 v46, s4\n\t"
      "s_branch Ll_%=\n"
      "Lm_%=:\n\t"
      "s_mul_i32 s4, s6, %[ustr] - 16384\n\t"
      "v_and_b32 v8, 15, v7\n\t"
      "s_add_i32 s4, s4, %[uimg]\n\t"
      "v_and_or_b32 v12, v7, 48, s7\n\t"
      "s_mul_i32 s5, s6, 0x4020\n\t"
      "v_and_b32 v14, 1, v7\n\t"
      "s_addk_i32 s5, %[ly]\n\t"
      "v_lshlrev_b32 v9, 4, v8\n\t"
      "v_and_b32 v44, 32, v7\n\t"
      "v_mad_i32_i24 v11, v8, -8, v12\n\t"
      "v_add3_u32 v10, v12, v9, s4\n\t"
      "s_add_i32 s4, s7, %[kn] + 1120\n\t"
      "v_mul_u32_u24 v14, %[kc] + 2, v14\n\t"
      "v_lshl_add_u32 v13, v7, 3, s5\n\t"
      "s_add_i32 s5, s7, %[kn] + 2 * %[kc] + 380\n\t"
      "v_sub_u32 v42, s4, v47\n\t"
      "s_add_i32 s4, s7, %[kn] + 2 * %[kc] + 512 + 190\n\t"
      "v_mad_i32_i24 v15, v8, -2, v12\n\t"
      "v_add_u32 v44, v44, v7\n\t"
      "v_mad_i32_i24 v12, v8, -4, v12\n\t"
      "v_mad_i32_i24 v43, v7, -4, s5\n\t"
      "v_add_u32 v9, v10, v9\n\t"
      "v_mad_i32_i24 v44, v44, -2, s4\n\t"
      "v_add_u32 v45, 1, v7\n\t"
      "v_sub_u32 v8, v15, v14\n\t"
      "v_and_b32 v45, -2, v45\n\t"
      "v_add_u32 v11, %[kn] + 2 * %[kc] + 120, v11\n\t"
      "v_add_u32 v15, v45, v14\n\t"
      "v_add_u32 v12, %[kn] + 2 * %[kc] + 512 + 60, v12\n\t"
      "v_add_u32 v7, %[kn] + %[kc] + 40, v8\n\t"
      "v_add_u32 v8, %[kn] + %[kc] + 40 + 1024, v8\n\t"
      "v_add_u32 v47, %[filt], v47\n\t"
      "v_mov_b32 v14, 0xfffefffe\n\t"
      "s_add_i32 s4, s7, %[kn] + %[kc] + 2 + 48\n\t"
      "v_mov_b32 v45, 0x0706\n\t"
      "s_mov_b64 exec, 0x7ff\n\t"
      "v_sub_u32 v46, s4, v15\n\t"
      "s_movk_i32 exec_lo, 0x7f\n\t"
      "v_add_u32 v46, 64, v46\n\t"
      "v_mov_b32 v45, 0x0504\n\t"
      "s_mov_b32 exec_lo, 1\n\t"
      "v_add_u32 v46, 1022, v46\n\t"
      "v_mov_b32 v45, 0x0302\n"
      "Ll_%=:\n\t"
      "s_mov_b64 exec, -1\n\t"
      "global_load_dword v4, v5, s[14:15]\n\t"
      "global_load_dword v5, v5, s[16:17]\n\t"
      "global_load_ushort v16, v16, s[20:21]\n\t"
      "global_load_dword v1, v1, s[36:37]\n"
      "Le_%=:"
      : "=&{s[8:9]}"(kx), "=&{s[36:37]}"(kw), "=&{s[24:31]}"(kb_), "=&{s[34:35]}"(t0_), "=&{s33}"(sep)
      : [code] "n"(CODE_BYTES), [ustr] "n"(USTR), [uimg] "n"(L_UIMG), [filt] "n"(L_FILT), "{s[0:1]}"(__builtin_amdgcn_kernarg_segment_ptr()), "{s2}"(bid),
        "{v0}"(threadIdx.x), [dummy] "n"(L_DUMMY), [misc] "n"(L_MISC), [kn] "n"(L_KN), [kc] "n"(KCOPY), [ly] "n"(L_Y), [u1] "n"(WS_U / GSTR),
        [u2] "n"(WS_U2 / GSTR - 128), [y1] "n"(WS_Y / GSTR), [y2] "n"(WS_Y2 / GSTR - 128), [x2d] "n"(X2_DELAY), [tstr] "n"(TSTR), [trow] "n"(TROW)
      : "s3", "s4", "s5", "s6", "s7", "s10", "s11", "s12", "s13", "s14", "s15", "s16", "s17", "s18", "s19", "s20", "s21", "s22", "s23", "v1", "v2", "v3", "v4",
        "v5", "v6", "v7", "v8", "v9", "v10", "v11", "v12", "v13", "v14", "v15", "v16", "v17", "v18", "v19", "v20", "v21", "v22", "v23", "v24", "v25",
        "v26", "v27", "v28", "v29", "v30", "v31", "v32", "v33", "v34", "v35", "v36", "v37", "v38", "v39", "v40", "v41", "v42", "v43", "v44", "v45",
        "v46", "v47", "v48", "scc", "memory");
  static_assert(WSCR == 16384 && GSTR == 16384 && L_K4 == L_KN + 2 * KCOPY && L_K8 == L_K4 + 512, "constants of the block above");
  v16u k8;
  asm volatile("" : "={v1}"(epoch), "={v[2:5]}"(ga), "={v[6:9]}"(gb), "={v[10:13]}"(gc), "={v[14:17]}"(gd), "={v[34:49]}"(k8)::"memory");
  SCHED();
  const int tid = threadIdx.x, lane = tid & 63;
  int wave = __builtin_amdgcn_readfirstlane(tid >> 6);
  const int xcd = bid & 7;
  int loc = bid >> 3;
  STAMP_INIT();
  unsigned pcnt_ = 0;
  STAMP(11);
  const int cl = lane & 15, g = lane >> 4;
  const int cg = bid;
  const int fj = xcd * 9 + (loc - 18);
  const int rb = bid;
  const int side = (bid << 11) & 8192;
  const int ybase = (cg < 128 ? WS_Y : WS_Y2 - 128 * GSTR) + cg * GSTR;
  const int tile = bid - NCONV;
  struct { const GAS char* x; GAS char* ws; } p = {(const GAS char*)kx, (GAS char*)kw};
  const rsrc_t rs = __builtin_amdgcn_make_buffer_rsrc((void*)p.ws, 0, WS_END, 0x00020000);
  const v4u sent4 = {SENT, SENT, SENT, SENT};
  const rsrc_t ro = __builtin_amdgcn_make_buffer_rsrc((void*)kb_[3], 0, SEQ * DM * 2, 0x00020000);
  if (loc < 18) {
    BAR();
    if (wave == 8) asm volatile("s_mov_b64 exec, 0\n\ts_branch Lk_conv\nLk_back:\n\ts_mov_b64 exec, -1");
  }
  asm volatile("" : "+s"(loc), "+s"(wave));

  if (loc < 18) {
    int ysrc = (wave < 8 ? WS_Y : WS_Y2 - 128 * GSTR) + 16 * wave * GSTR + side + (loc * 4 + (rb & 3)) * 128;
    unsigned ylag = T_Y3 - ((0x000f2b46u >> (2 * (loc & 12))) & 255u);
    asm volatile("" : "+s"(ysrc), "+s"(ylag));
    unsigned l_x, l_x2, l_sw;
    v4u cw[2];
    f32x4 xf[4];
    v2u unit;
    auto ln2_operands = [&](int lane) {
      const int cl = lane & 15, g = lane >> 4;
      l_x = L_XROW + wave * 2048 + cl * 128 + (g ^ (cl & 7)) * 16;
      l_x2 = l_x ^ 64;
      const v4u gx[2] = {*(const LAS v4u*)l_x, *(const LAS v4u*)l_x2};
      const unsigned l_w = (lane & 3) == 1 ? L_WR + wave * 128 + g * 16 : L_MISC + 128;
      cw[0] = *(const LAS v4u*)l_w;
      cw[1] = *(const LAS v4u*)(l_w + 64);
      l_sw = (cl >> 2) == g ? L_RED + wave * 320 + cl * 16 : 7 * WSCR + lane * 16;
      const u64 e = (u64)((cl >> 2) == g ? 0x3f80u : 0u) << (16 * (cl & 3));
      unit = v2u{(unsigned)e, (unsigned)(e >> 32)};
      PIN(unit);
#pragma unroll
      for (int i = 0; i < 4; i++) {
        xf[i] = widen(unit, v2u{gx[i >> 1][2 * (i & 1)], gx[i >> 1][2 * (i & 1) + 1]}, f32x4{0.f, 0.f, 0.f, 0.f});
        PIN(xf[i]);
      }
      PIN(cw[0]); PIN(cw[1]); PIN(l_x); PIN(l_x2); PIN(l_sw);
      SCHED();
    };
    if (wave < 8) {
      const int ch = wave & 3;
      volatile LAS unsigned* const pf = (volatile LAS unsigned*)(smem + L_MISC);
      if (wave < 4) __builtin_amdgcn_s_setprio(2); else __builtin_amdgcn_s_setprio(1);
      STAMP(12);
      unsigned epoch, gdt, gre, gim, gdd;
      v2u gc_;
      unsigned rb_;
      unsigned op[7], top, a_kn, a_k4, a_k8, sel_sp, a_sp, a_q, pa;
      asm volatile("s_waitcnt vmcnt(%22) ; early loads"
                   : "={v1}"(epoch), "={v2}"(gre), "={v3}"(gim), "={v[4:5]}"(gc_), "={v6}"(gdt), "={v7}"(op[0]), "={v8}"(op[1]), "={v9}"(op[2]),
                     "={v10}"(op[3]), "={v11}"(op[4]), "={v12}"(op[5]), "={v13}"(op[6]), "={v14}"(top), "={v16}"(gdd),
                     "={v41}"(rb_), "={v42}"(a_kn), "={v43}"(a_k4), "={v44}"(a_k8), "={v45}"(sel_sp), "={v46}"(a_sp), "={v47}"(a_q), "={v48}"(pa)
                   : "n"(4) : "memory");
      SCHED();
      STAMP(5);
      SCHED();
      v4u tx;
      const float dt = rbf(exp_c(blo(gdt)));
      const float ar = -rbf(exp_c(blo(gre))) * dt * 1.44269504f;
      const float aie = blo(gim) * dt;
      const float ph = aie * 0.159154936f;
      const float pl = fmaf(aie, 0.159154936f, -ph) + aie * 7.13048051e-9f;
#define put1(row, x) asm volatile("ds_write_addtid_b32 %0 offset:%1" ::"v"(x), "n"(L_TBL + ((row) & 31) * TROW) : "memory")
#define put(row, x) do { put1(row, x); SCHED(); } while (0)
      cz e1, e2, e4, e8, e16, e32, e64, e128, e256, e512, e1024;
      e8.v = cexp_p2(8.f, ar, ph, pl);
      e64.v = cexp_p2(64.f, ar, ph, pl);
      e1.v = cexp_p2(1.f, ar, ph, pl);
      e512.v = cexp_p2(512.f, ar, ph, pl);
      unsigned ha[4], hs[4], hb[8], hr[3][8];
      f32x2 e24, e96, e160, e192, e224;
      hb[0] = 0x3c00u;
      PIN(hb[0]); PIN(e8.v); PIN(e64.v); PIN(e1.v); PIN(e512.v);
      SCHED();
      swap_(e8); swap_(e64); swap_(e1); swap_(e512); half_(ha[1], e8.v);
      mul_(e16.v, e8.v, e8); mul_(e128.v, e64.v, e64); mul_(e2.v, e1.v, e1); mul_(e1024.v, e512.v, e512); half_(hb[2], e64.v);
      fma_(e16.v, e8.v, e8); fma_(e128.v, e64.v, e64); fma_(e2.v, e1.v, e1); fma_(e1024.v, e512.v, e512); half_(hs[1], e8.s);
      swap_(e16); swap_(e128); swap_(e2); swap_(e1024); half_(ha[2], e16.v);
      mul_(e32.v, e16.v, e16); mul_(e192, e128.v, e64); mul_(e24, e16.v, e8); mul_(e256.v, e128.v, e128); mul_(e4.v, e2.v, e2); half_(hb[4], e128.v);
      fma_(e32.v, e16.v, e16); fma_(e192, e128.v, e64); fma_(e24, e16.v, e8); fma_(e256.v, e128.v, e128); fma_(e4.v, e2.v, e2); half_(hs[2], e16.s);
      swap_(e32); swap_(e256); swap_(e4); half_(hb[1], e32.v);
      mul_(e96, e64.v, e32); mul_(e160, e128.v, e32); mul_(e224, e192, e32); half_(hb[6], e192);
      fma_(e96, e64.v, e32); fma_(e160, e128.v, e32); fma_(e224, e192, e32); half_(ha[3], e24);
      half_(hb[3], e96); half_(hb[5], e160); half_(hb[7], e224); half_(hs[3], e24.yx);
      put(0, hb[0]); put(1, ha[1]); put(8, hb[2]); put(2, ha[2]); put(16, hb[4]); put(4, hb[1]); put(24, hb[6]); put(3, ha[3]); put(12, hb[3]); put(20, hb[5]); put(28, hb[7]);
      asm volatile("s_waitcnt vmcnt(0) ; first loads" : "+{v1}"(epoch), "+{v[4:5]}"(gc_), "+{v16}"(gdd)::"memory");
      float cre = (wave < 4 ? blo(gc_.x) : bhi(gc_.x)) * dt, cim = (wave < 4 ? blo(gc_.y) : bhi(gc_.y)) * dt;
      const float dsk = blo(gdd);
      unsigned cm = __float_as_uint(fmaxf(fmaxf(fmaxf(fabsf(blo(gc_.x)), fabsf(bhi(gc_.x))), fmaxf(fabsf(blo(gc_.y)), fabsf(bhi(gc_.y)))) * dt, fabsf(dsk) * 0.0078125f));
      PIN(cm); PIN(cre); PIN(cim);
      SCHED();
      unsigned ce;
      float csc;
      f32x2 w0;
#define wmax_(c) do { cm = max(cm, dppu<c, 0xf>(cm)); PIN(cm); SCHED(); } while (0)
#pragma unroll
      for (int i = 1; i < 4; i++) {
#pragma unroll
        for (int k = 1; k < 8; k++) {
          mulh_(hr[i - 1][k], hb[k], hs[i]);
          if (i == 3 && k == 3) {
#pragma unroll
            for (int q = 2; q < 16; q++) put(4 * (q >> 1) + 1 + (q & 1), hr[q & 1][q >> 1]);
          }
          if (i == 1 && k == 2) wmax_(0x111);
          if (i == 1 && k == 4) wmax_(0x112);
          if (i == 1 && k == 6) wmax_(0x114);
        }
#pragma unroll
        for (int k = 1; k < 8; k++) {
          fmah_(hr[i - 1][k], hb[k], ha[i]);
          if (i == 1 && k == 1) wmax_(0x118);
          if (i == 1 && k == 3) {
            unsigned a = (unsigned)__builtin_amdgcn_readlane((int)cm, 15), b = (unsigned)__builtin_amdgcn_readlane((int)cm, 31);
            unsigned c = (unsigned)__builtin_amdgcn_readlane((int)cm, 47), d = (unsigned)__builtin_amdgcn_readlane((int)cm, 63);
            asm("s_max_u32 %0, %1, %2\n\ts_max_u32 %0, %0, %3\n\ts_max_u32 %0, %0, %4\n\ts_lshr_b32 %0, %0, 23\n\ts_max_u32 %0, %0, 40\n\ts_min_u32 %0, %0, 250"
                : "=&s"(ce) : "s"(a), "s"(b), "s"(c), "s"(d) : "scc");
            csc = __uint_as_float((261u - ce) << 23);
            asm volatile("" : "+s"(csc), "+s"(ce));
            SCHED();
          }
          if (i == 2 && k == 1) {
            w0 = f32x2{cre * csc, -cim * csc};
            PIN(w0);
            SCHED();
          }
        }
      }
      if (wave >= 4) STAMP(11);
      f16x8 fa[8], fb[8];
      f32x16 acc;
      unsigned have;
      auto partial = [&]() {
        MEMBAR();
#pragma unroll
        for (int k = 0; k < 4; k++) {
          const f32x4 o = *(const LAS f32x4*)(pa + k * 1024);
#pragma unroll
          for (int i = 0; i < 4; i++) acc[4 * k + i] = o[i];
        }
      };
#define KSTEP(k) SCHED(); acc = __builtin_amdgcn_mfma_f32_32x32x16_f16(fa[k], fb[k], acc, 0, 0, 0); SCHED()
      {
        f32x2 z1, z2, z3, z4, z5, z6, z7, z8, z256, z258, z260, z262, z264, z516, z520, z772, z776, z1032, z1288, z1544, z1800;
        unsigned h0, h1, h2, h3, h4, h5, h6, h7, h8, h256, h258, h260, h262, h264, h516, h520, h772, h776, h1032, h1288, h1544, h1800;
#define get(k) do { fb[k] = *(const LAS f16x8*)(rb_ + L_TBL + 32 * (k)); MEMBAR(); SCHED(); } while (0)
        mulc_(z8, w0, e8); mulc_(z1, w0, e1); mulc_(z2, w0, e2); mulc_(z4, w0, e4); mulc_(z256, w0, e256); half_(h0, w0);
        fmac_(z8, w0, e8); fmac_(z1, w0, e1); fmac_(z2, w0, e2); fmac_(z4, w0, e4); fmac_(z256, w0, e256);
        mulc_(z264, z8, e256); mulc_(z520, z8, e512); mulc_(z1032, z8, e1024); mulc_(z3, z1, e2); mulc_(z258, z2, e256); mulc_(z260, z4, e256);
        half_(h8, z8); half_(h1, z1); half_(h256, z256); put(7, hr[2][1]); put(11, hr[2][2]); put(15, hr[2][3]); put(19, hr[2][4]); put(23, hr[2][5]); put(27, hr[2][6]); put(31, hr[2][7]); get(0); get(1); get(2); get(3); get(4); get(5); get(6); get(7);
        fmac_(z264, z8, e256); fmac_(z520, z8, e512); fmac_(z1032, z8, e1024); fmac_(z3, z1, e2); fmac_(z258, z2, e256); fmac_(z260, z4, e256);
        half_(h2, z2); half_(h4, z4); half_(h264, z264);
        mulc_(z776, z264, e512); mulc_(z1288, z264, e1024); mulc_(z1544, z520, e1024); mulc_(z5, z1, e4); mulc_(z6, z2, e4);
        half_(h520, z520); half_(h1032, z1032); half_(h3, z3);
        fmac_(z776, z264, e512); fmac_(z1288, z264, e1024); fmac_(z1544, z520, e1024); fmac_(z5, z1, e4); fmac_(z6, z2, e4); put(16, h0); put(0, h8); put(11, h1); put(15, h256); put(20, h256); put(10, h2); put(8, h4); put(4, h264); put(5, h264); put(17, h520); put(19, h1032);
        half_(h258, z258); half_(h260, z260);
        mulc_(z1800, z776, e1024); mulc_(z516, z4, e512); mulc_(z7, z3, e4); mulc_(z262, z258, e4); mulc_(z772, z260, e512);
        half_(h776, z776); half_(h1288, z1288); half_(h1544, z1544); half_(h5, z5); half_(h6, z6);
        fmac_(z1800, z776, e1024); fmac_(z516, z4, e512); fmac_(z7, z3, e4); fmac_(z262, z258, e4); fmac_(z772, z260, e512); put(9, h3); put(14, h258); put(13, h260); put(7, h260); put(12, h260); put(21, h776); put(24, h1288); put(23, h1544); put(3, h5); put(2, h6);
        if (wave >= 4) {
          have = pf[ch];
          partial();
        }
        SCHED();
        half_(h1800, z1800); half_(h516, z516); half_(h7, z7); half_(h262, z262); half_(h772, z772); put(28, h1800); put(18, h516); put(1, h7); put(6, h262); put(22, h772);
      }
      MEMBAR();
#pragma unroll
      for (int k = 0; k < 8; k++) fa[k] = *(const LAS f16x8*)(rb_ + L_TBL + 32 * k);
      SCHED();
      if (wave < 4) {
        acc = f32x16{};
        KSTEP(0);
        *(LAS v4u*)(a_kn + L_KN) = v4u{0u, 0u, 0u, 0u};
        KSTEP(1);
        tx = tag_bits(__builtin_amdgcn_readfirstlane(epoch) + 1u) ^ 0x40004u;
        asm volatile("" : "+s"(tx));
        KSTEP(2);
        asm volatile("" : "+{v7}"(op[0]), "+{v8}"(op[1]), "+{v9}"(op[2]), "+{v10}"(op[3]), "+{v11}"(op[4]));
        float d0 = __builtin_amdgcn_inverse_ballot_w64(1ull) ? dsk * csc * 0.5f : 0.f;
        unsigned one = 1u;
        PIN(d0); PIN(one);
#pragma unroll
        for (int k = 3; k < 8; k++) acc = __builtin_amdgcn_mfma_f32_32x32x16_f16(fa[k], fb[k], acc, 0, 0, 0);
        SCHED();
        acc[8] += d0;
        STAMP(2);
#pragma unroll
        for (int k = 0; k < 3; k++) *(LAS f32x4*)(pa + k * 1024) = f32x4{acc[4 * k], acc[4 * k + 1], acc[4 * k + 2], acc[4 * k + 3]};
        *(LAS float*)(pa + 3072) = acc[12];
        MEMBAR();
        *(volatile LAS unsigned*)a_sp = one;
        STAMP(11);
        __builtin_amdgcn_s_setprio(0);
      }
      asm volatile("" : "+s"(wave));
      if (wave >= 4) {
        if (RARE(__builtin_amdgcn_readfirstlane(have) == 0u)) {
#pragma unroll 1
          for (int sp = 0; sp < SPIN_CAP * 16 && pf[ch] == 0u; sp++) {}
          partial();
          PIN(acc);
        }
        v4u q[4];
        unsigned kinv = (ce - 6u) << 23;
#define KASM(k, pre) asm volatile(pre "\n\tv_mfma_f32_32x32x16_f16 %0, %1, %2, %0" : "+v"(acc) : "v"(fa[k]), "v"(fb[k])); SCHED()
        KASM(0, "s_nop 1");
        asm volatile("" : "+{v7}"(op[0]), "+{v8}"(op[1]), "+{v9}"(op[2]));
        KASM(1, "");
        asm volatile("" : "+{v10}"(op[3]), "+{v11}"(op[4]), "+{v12}"(op[5]));
        KASM(2, "");
        asm volatile("" : "+{v13}"(op[6]), "+{v14}"(top), "+{v15}"(kinv));
        KASM(3, "");
        KASM(4, "");
        KASM(5, "");
        KASM(6, "");
        KASM(7, "");
#pragma unroll
        for (int k = 0; k < 4; k++) q[k] = *(const LAS v4u*)(a_q + k * 1024);
        SCHED();
        asm volatile("s_nop 11" : "+v"(acc));
        STAMP(2);
        const unsigned k8 = pkh(acc[11], acc[12]);
        *(LAS u16*)(a_k8 + 64) = (u16)k8;
        *(LAS u16*)a_k8 = (u16)(k8 >> 16);
        *(LAS unsigned*)a_k4 = pkh(acc[9], acc[10]);
        const v4u c0 = {pkh(acc[1], acc[2]), pkh(acc[3], acc[4]), pkh(acc[5], acc[6]), pkh(acc[7], acc[8])};
        *(LAS v4u*)a_kn = c0;
        *(LAS v4u*)(a_kn + KCOPY) = v4u{pkh(acc[0], acc[1]), pkh(acc[2], acc[3]), pkh(acc[4], acc[5]), pkh(acc[6], acc[7])};
        const unsigned kb = (unsigned)__builtin_amdgcn_readlane((int)pkh(acc[0], acc[9]), 63);
        *(LAS u16*)a_sp = (u16)__builtin_amdgcn_perm(kb, c0.w, sel_sp);
        STAMP(4);
        __builtin_amdgcn_s_setprio(M_PRIO);
        asm volatile("Lk_conv:\n\ts_cbranch_execz Lk_fetch\n" CONV_ASM_TEXT
                     : CONV_ASM_PRE(q)
                     : "{v14}"(top), "{v15}"(kinv), "{v7}"(op[0]), "{v8}"(op[1]), "{v9}"(op[2]), "{v10}"(op[3]), "{v11}"(op[4]), "{v12}"(op[5]),
                       "{v13}"(op[6])
                     : CONV_ASM_CLOBBERS);
        STAMP(6);
        __builtin_amdgcn_s_setprio(0);
      }
      asm volatile("" : "+s"(wave));
      if (wave < 4) {
        asm volatile("s_branch Lk_fetch\n\t.p2align 12\nLk_fetch:\n\ts_cbranch_execz Lk_ln2");
        const unsigned usrc = op[0], udst = op[1], udst2 = op[2], yb = op[3];
        int yo = op[4];
        wait_until((unsigned)t0_, T_U);
        __builtin_amdgcn_s_setprio(H_PRIO);
        fetch_u<3, true>(rs, usrc, {udst, udst + 1024, udst2}, 0, tx);
        STAMP(4);
        BAR();
        __builtin_amdgcn_s_sleep(SLEEP_U7);
        fetch_u<1, false>(rs, usrc, {udst2 + 1024}, 12, tx);
        STAMP(6);
#pragma unroll
        for (int m = 0; m < 4; m++) {
          BAR();
          if (m == 0) yo ^= *(const LAS int*)(L_MISC + 72);
          const v2u r0 = ldtr((const LAS char*)(yb + L_Y + m * 1024)), r1 = ldtr((const LAS char*)(yb + L_Y + m * 1024 + 256));
          __builtin_amdgcn_raw_buffer_store_b128(v4u{r0.x, r0.y, r1.x, r1.y}, rs, yo, m * 2048, SC1);
        }
        __builtin_amdgcn_s_setprio(0);
      }
    }
    asm volatile("" : "+s"(wave));
    if (wave == 8) {
      unsigned pw[PF_N], epoch, wb;
      v4u wa;
      __builtin_amdgcn_s_setprio(3);
      asm volatile("s_waitcnt vmcnt(0) ; first loads" : "={v1}"(epoch), "={v[2:5]}"(wa), "={v6}"(wb)::"memory");
      int lane;
      asm volatile("v_mbcnt_lo_u32_b32 %0, -1, 0\n\tv_mbcnt_hi_u32_b32 %0, -1, %0" : "=v"(lane));
      {
        int d = 0;
        unsigned c0 = (unsigned)__builtin_amdgcn_s_memtime();
        int ya = ybase + (bid & 8) * 1024 + lane * 16;
#pragma unroll 1
        for (int h = 0; h < 2; h++) {
#pragma unroll
          for (int i = 0; i < 8; i++) __builtin_amdgcn_raw_buffer_store_b128(sent4, rs, ya + (i & 3) * 1024, (i & 4) * 1024, SC1);
          ya ^= 8192;
          if (loc >= 16) {
            DRAIN();
            const unsigned c1 = (unsigned)__builtin_amdgcn_s_memtime();
            d = (int)min(c1 - c0, 2000u) - d;
            c0 = c1;
          }
        }
        st2(v2u{epoch + 1u, (unsigned)(((loc ^ (bid >> 2)) & 1) ? d : -d)}, rs, loc >= 16 ? WS_VOTE + (cg - 128) * 64 : OOB);
      }
      st4(sent4, rs, (loc < 16 && lane < 8) ? WS_RREC + (lane & 4) * 2048 + rb * 64 + (lane & 3) * 16 : OOB);
      st4(sent4, rs, cg < 48 ? WS_XSEL + (cg & 15) * 8192 + (cg >> 4) * 1024 + lane * 16 : OOB);
      SCHED();
      auto warm = [&]() {
        const GAS char* wb = (const GAS char*)(loc < 9 ? kb_[0] : kb_[1]) + xcd * 1152;
        const unsigned s0 = (3u - (unsigned)((u64)wb >> 7)) & 7u, ro_ = (576 - 9 * WROWS + (loc < 9 ? loc : loc - 9) * WROWS + min(lane, WROWS - 1)) * 9216;
        pw[0] = *(const GAS unsigned*)(wb + (ro_ + s0 * 128));
        pw[1] = *(const GAS unsigned*)(wb + (ro_ + (s0 ? s0 : 8u) * 128));
        SCHED();
      };
      if (loc < 16) warm();
      __builtin_amdgcn_s_setprio(0);
      SCHED();
      {
#pragma unroll
        for (int k = 0; k < 4; k++) *(unsigned*)(smem + L_WR + (lane + 64 * k) * 4) = wa[k];
        *(unsigned*)(smem + L_WR + (224 + lane) * 4) = wb;
        *(LAS unsigned*)(L_MISC + 128 + (lane & 31) * 4) = 0x3f803f80u;
        float ws_ = lane >= 32 ? blo(wb) + bhi(wb) : 0.f;
#pragma unroll
        for (int k = 0; k < 4; k++) ws_ += blo(wa[k]) + bhi(wa[k]);
        ws_ = wave_sum(ws_);
        if (lane == 0) *(float*)(smem + L_MISC + 64) = ws_;
      }
      SCHED();
      __builtin_amdgcn_s_setprio(S_PRIO);
      asm volatile("s_waitcnt vmcnt(%0)" ::"n"(PF_N) : "memory");
      STAMP(4);
      BAR();
      STAMP(5);
      const unsigned t1 = now();
      st2(v2u{epoch + 1u, t1}, rs, lane == 0 ? WS_INIT + cg * 64 : OOB);
      v2u vote;
#pragma unroll 1
      for (int sp = 0; sp < SPIN_CAP; sp++) {
        MEMBAR();
        vote = ld2(rs, WS_VOTE + (lane & 15) * 64);
        if (__all(vote.x == epoch + 1u)) break;
      }
      const unsigned flip = wave_sum((float)(int)vote.y) > 0.f;
      *(LAS unsigned*)(L_MISC + 72) = flip << 13;
      if (loc < 16) {
        const GAS char* xs = p.x + (unsigned)((16 * rb + (lane >> 3)) * 1152 + ((lane & 7) ^ (lane >> 3)) * 16);
#pragma unroll 1
        for (int i = 0; i < 9; i++) {
          __builtin_amdgcn_global_load_lds((const GAS unsigned*)(xs + i * 128), (LAS unsigned*)(L_XROW + i * 2048), 16, 0, 0);
          __builtin_amdgcn_global_load_lds((const GAS unsigned*)(xs + i * 128 + 9216), (LAS unsigned*)(L_XROW + i * 2048 + 1024), 16, 0, 0);
        }
      }
      v2u fl[3];
      unsigned span;
      auto look = [&]() {
        MEMBAR();
#pragma unroll
        for (int k = 0; k < 3; k++) fl[k] = ld2(rs, WS_INIT + min(lane + 64 * k, NCONV - 1) * 64);
      };
      auto seen = [&]() {
        if (!__all(fl[0].x == epoch + 1u && fl[1].x == epoch + 1u && fl[2].x == epoch + 1u)) return false;
        const unsigned t = t1 - 4096u;
        const unsigned last = t + span + min(wave_max_u(max(max(fl[0].y - t, fl[1].y - t), fl[2].y - t)), 4096u + 300u);
        *(volatile LAS unsigned*)(L_MISC + 76) = last | 1u;
        if (loc == 16) st2(v2u{epoch + 1u, (last & ~1u) | flip}, rs, WS_TIME + xcd * 128);
        return true;
      };
      if (loc >= 16) warm();
      wait_until(t1, T_LOOK);
      look();
      STAMP(6);
      BAR();
      BAR();
      span = (now() - t1) * 85u >> 6;
      DRAIN();
      bool ok = seen();
      BAR();
      BAR();
#pragma unroll 1
      for (int sp = 0; sp < SPIN_CAP && !ok; sp++) {
        RETRY(7);
        look();
        ok = seen();
      }
      __builtin_amdgcn_s_waitcnt(0x0f70);
      if (bid == 136) st1(epoch + 1u, rs, WS_EPOCH);
#pragma unroll
      for (int i = 0; i < PF_N; i++) asm volatile("" ::"v"(pw[i]));
      __builtin_amdgcn_s_setprio(0);
      SCHED();
      asm volatile("s_branch Lk_ln2\n\t.p2align 12\nLk_ln2:\n\ts_cbranch_execz Lk_back");
    }
    asm volatile("; first loads (62)");
    SCHED();
    STAMP(7);
    if (loc >= 16) { STAMP_FLUSH(); return; }
    int lane;
    asm volatile("v_mbcnt_lo_u32_b32 %0, -1, 0\n\tv_mbcnt_hi_u32_b32 %0, -1, %0" : "=v"(lane));
    const int cl = lane & 15, g = lane >> 4, tid = wave * 64 + lane;
    v2u fg = *(volatile LAS v2u*)(L_MISC + 72);
    int o0 = ysrc + g * 2 * GSTR + (cl & 7) * 16 + (cl & 8);
    unsigned tn = now() - ylag + Y_EARLY;
    PIN(o0);
    asm volatile("" : "+s"(tn));
    SCHED();
    if (RARE(__builtin_amdgcn_readfirstlane(fg.y) == 0u)) {
#pragma unroll 1
      for (int sp = 0; sp < SPIN_CAP * 64; sp++) {
        fg = *(volatile LAS v2u*)(L_MISC + 72);
        if (__builtin_amdgcn_readfirstlane(fg.y)) break;
      }
    }
    o0 ^= fg.x;
    PIN(o0);
    const unsigned last = __builtin_amdgcn_readfirstlane(fg.y);
    int late = (int)(tn - last);
    v2u y[4];
    auto gather = [&]() {
      MEMBAR();
#pragma unroll
      for (int i = 0; i < 4; i++) y[i] = __builtin_amdgcn_raw_buffer_load_b64(rs, o0, ((i & 1) + (i >> 1) * 8) * GSTR, SC1);
    };
    if (late >= 0) gather();
    SCHED();
    ln2_operands(lane);
    constexpr int L_TAB = L_RED + 2880;
    const int flip = __builtin_amdgcn_readfirstlane(fg.x);
    int near = side ^ flip, far = near ^ 8192;
    v4u rx = {(unsigned)(kw + WS_XSEL), (unsigned)((kw + WS_XSEL) >> 32) & 0xffffu, 8 * 16384, 0x00020000};
    asm volatile("" : "+s"(rx), "+s"(far), "+s"(near));
    unsigned l_sr, l_s8, l_kw, l_kr, l_h, l_t, krow, hoff, half, a_pub, a_cnt;
    int a_key;
    float wsum;
    auto addresses = [&]() {
      l_h = L_XROW + wave * 2048 + (lane & 31) * 4;
      l_t = L_TAB + (lane >> 5) * 16;
      half = (lane >> 1) & 16;
      a_pub = wave * 128 + (lane & 31) * 4;
      a_cnt = L_MISC + 32;
      PIN(l_h); PIN(l_t); PIN(half); PIN(a_pub); PIN(a_cnt);
    };
    if (wave == 1) {
      addresses();
      const int row = lane >> 2;
      l_s8 = L_RED + row * 16;
      l_sr = l_s8 + (lane & 3) * 320;
      krow = 0x80000000u + SEQ - 1 - (16 * rb + row);
      hoff = row * 128 + (row & 7) * 16;
      a_key = (lane & 2) ? OOB : WS_RREC + (lane & 1) * 8192 + (16 * rb + row) * 4;
      l_kw = L_TAB + 256 + row * 4;
      l_kr = L_TAB + 256 + (lane & 3) * 16;
      PIN(l_s8); PIN(l_kw); PIN(l_kr);
      __builtin_amdgcn_s_setprio(3);
      wsum = *(const float*)(smem + L_MISC + 64);
      PIN(l_sr); PIN(krow); PIN(hoff); PIN(a_key); PIN(wsum);
    }
    asm volatile("" : "+s"(wave));
    u64 m_odd = 0xaaaaaaaaaaaaaaaaull, m_up = 0xccccccccccccccccull;
    unsigned kmask = 0x7fff0000u;
    asm volatile("" : "+s"(m_odd), "+s"(m_up), "+s"(kmask));
    const bool odd = __builtin_amdgcn_inverse_ballot_w64(m_odd), upper = __builtin_amdgcn_inverse_ballot_w64(m_up);
    v4u hv[2];
    asm volatile("" : "+s"(late));
    if (late < 0) {
      wait_until(last, ylag);
      gather();
    }
    SCHED();
    {
      f32x4 s = {0.f, 0.f, 0.f, 0.f}, q = {0.f, 0.f, 0.f, 0.f}, h[4];
      auto add = [&]() {
#pragma unroll
        for (int i = 0; i < 4; i++) {
          h[i] = widen(unit, y[i], xf[i]);
          SCHED();
        }
      };
      add();
      POLL(2, valid8(v4u{y[0].x, y[0].y, y[1].x, y[1].y}, v4u{y[2].x, y[2].y, y[3].x, y[3].y}), (gather(), add()));
      SCHED();
#pragma unroll
      for (int i = 0; i < 2; i++) {
        hv[i] = v4u{pkbf(h[2 * i][0], h[2 * i][1]), pkbf(h[2 * i][2], h[2 * i][3]), pkbf(h[2 * i + 1][0], h[2 * i + 1][1]), pkbf(h[2 * i + 1][2], h[2 * i + 1][3])};
        q = mfma_bf(hv[i], hv[i], q);
        s = mfma_bf(cw[i], hv[i], s);
        SCHED();
      }
      const float qa = odd ? q[1] : q[0], qb = odd ? q[3] : q[2];
      s[2] = upper ? qb : qa;
      *(LAS f32x4*)l_sw = s;
      SCHED();
    }
    STAMP(8);
    SCHED();
    BAR();
    SCHED();
    if (wave == 1) {
      float m, s;
      const f32x4 ta = *(const LAS f32x4*)l_sr, tb = *(const LAS f32x4*)(l_sr + 1280), tc = *(const LAS f32x4*)(l_s8 + 2560);
      SCHED();
      float t[3] = {ta[0] + tb[0], ta[1] + tb[1], ta[2] + tb[2]};
#define QADD(n, how) "v_add_f32_dpp %" #n ", %" #n ", %" #n " quad_perm:" how " row_mask:0xf bank_mask:0xf\n\t"
      asm("s_nop 1\n\t" QADD(0, "[1,0,3,2]") QADD(2, "[1,0,3,2]") QADD(1, "[1,0,3,2]") QADD(0, "[2,3,0,1]") QADD(2, "[2,3,0,1]") QADD(1, "[2,3,0,1]")
          : "+v"(t[0]), "+v"(t[1]), "+v"(t[2]));
      t[1] += tc[1];
      ln_round(t[0] + tc[0], t[2] + tc[2], m, s);
      const float rinv = __builtin_amdgcn_rcpf(s);
      const unsigned b = pkbf(0.f, fmaf(-m, wsum, t[1]) * rinv);
      unsigned neg = (unsigned)((int)b >> 31);
      PIN(neg);
      const unsigned okey = __builtin_amdgcn_bitop3_b32(b, neg, kmask, 0x78) ^ krow;
      st1(okey, rs, a_key);
      SCHED();
      KSTAMP(9);
      *(LAS unsigned*)l_kw = okey;
      MEMBAR();
      const v4u ok4 = *(const LAS v4u*)l_kr;
      MEMBAR();
      *(LAS v4u*)l_x = hv[0];
      *(LAS v4u*)l_x2 = hv[1];
      unsigned c = (ok4.x > okey) + (ok4.y > okey) + (ok4.z > okey) + (ok4.w > okey);
      c += dppu<0xb1, 0xf>(c);
      c += dppu<0x4e, 0xf>(c);
      *(LAS v4u*)(L_TAB + c * 16) = v4u{__float_as_uint(m), okey, __float_as_uint(rinv), hoff};
      __builtin_amdgcn_s_setprio(0);
    }
    asm volatile("" : "+s"(wave));
    if (wave != 1) {
      addresses();
      a_key = WS_RREC + near + (tid - (wave ? 64 : 0)) * 16;
      PIN(a_key);
      SCHED();
      *(LAS v4u*)l_x = hv[0];
      *(LAS v4u*)l_x2 = hv[1];
      wait_until(last, T_KEY);
    }
    BAR();
    v4u key;
    if (wave != 1) key = ld4(rs, a_key);
    STAMP(1);
    SCHED();
    unsigned nsel = 0;
#pragma unroll 1
    for (int rd = 0; rd < 4; rd++) {
      const v4u e = *(const LAS v4u*)l_t;
      unsigned hd = *(const LAS unsigned*)(l_h ^ e.w);
      const unsigned ka = __builtin_amdgcn_readlane(e.y, 0), kb = __builtin_amdgcn_readlane(e.y, 32);
      unsigned wd = nosent(ln_apply(hd, __uint_as_float(e.x), __uint_as_float(e.z)));
      hd = nosent(hd);
      PIN(wd); PIN(hd);
      SCHED();
      if (wave != 1) {
        POLL(3, valid4(key), key = ld4(rs, a_key));
        KSTAMP(10);
        u64 b[8];
#pragma unroll
        for (int i = 0; i < 4; i++) {
          b[2 * i] = __ballot(key[i] > ka);
          b[2 * i + 1] = __ballot(key[i] > kb);
        }
        asm volatile("" : "+s"(b[0]), "+s"(b[1]), "+s"(b[2]), "+s"(b[3]), "+s"(b[4]), "+s"(b[5]), "+s"(b[6]), "+s"(b[7]));
        const unsigned ca = (__popcll(b[0]) + __popcll(b[2])) + (__popcll(b[4]) + __popcll(b[6]));
        const unsigned cb = (__popcll(b[1]) + __popcll(b[3])) + (__popcll(b[5]) + __popcll(b[7]));
        asm volatile("s_mov_b64 exec, 1\n\tds_add_u32 %1, %0\n\ts_mov_b64 exec, -1" ::"v"(ca | (cb << 16)), "v"(a_cnt) : "memory");
      }
      unsigned cs, rank, po;
      asm volatile(
          "s_waitcnt lgkmcnt(0)\n\ts_barrier\n\tds_read_b32 %0, %3\n\ts_waitcnt lgkmcnt(0)\n\t"
          "v_bfe_u32 %1, %0, %4, 16\n\tv_lshl_add_u32 %2, %1, 14, %5\n\t"
          "buffer_store_dword %6, %2, %7, %8 offen sc1\n\tbuffer_store_dword %6, %2, %7, %9 offen sc1"
          : "=&v"(cs), "=&v"(rank), "=&v"(po)
          : "v"(a_cnt), "v"(half), "v"(a_pub), "v"(wd), "s"(rx), "s"(far), "s"(near)
          : "memory");
      const int ph = rank < 8u ? WS_XSEL + po + (((rank & 4) << 11) ^ flip) : OOB - 4096;
      st1(hd, rs, ph + 1152);
      st1(e.y, rs, a_pub == 0 ? ph + 2304 : OOB);
      const unsigned cu = __builtin_amdgcn_readfirstlane(cs);
      nsel += ((cu & 0xffffu) < 8u) + ((cu >> 16) < 8u);
      if ((cu >> 16) >= 8u) break;
      l_t += 32;
      a_cnt += 4;
    }
    STAMP(3);
#pragma unroll
    for (int i = 0; i < 2; i++) {
      const unsigned k = 8 * i + (lane >> 3);
      const v4u e = *(const LAS v4u*)(L_TAB + k * 16);
      const v4u hh = *(const LAS v4u*)((L_XROW + wave * 2048 + (lane & 7) * 16) ^ e.w);
      v4u o;
#pragma unroll
      for (int c = 0; c < 4; c++) o[c] = addbf(hh[c], ln_apply(hh[c], __uint_as_float(e.x), __uint_as_float(e.z)));
      st4(o, ro, k < nsel ? OOB : (16 * rb + (e.w >> 7)) * 1152 + wave * 128 + (lane & 7) * 16);
    }
    STAMP_FLUSH();
    return;
  }

  asm volatile("; conv set-up dropped" ::"{v[34:49]}"(k8));
  const bool ffn = loc < 27;
  LAS char* xw = (LAS char*)smem + wave * 4608 + (lane >> 3) * 288 + (lane & 7) * 16;
  LAS char* xr_ = (LAS char*)smem + wave * 4608 + ((lane >> 2) & 3) * 288 + (lane & 1) * 128 + g * 16 + (lane & 2) * 4;
  unsigned rw = (lane & 3) == 0 ? L_RED + wave * 128 + lane * 2 : L_UIMG + lane * 8, r8 = L_RED + (lane & 28) * 4 + (lane >> 5) * 8, rr_ = r8 + (lane & 3) * 128;
  int uo = (wave < 8 ? WS_U : WS_U2 - 128 * GSTR) + (16 * wave + (lane >> 3)) * GSTR + tile * 128 + (lane & 7) * 16;
  unsigned sa = L_MISC + (lane & 4) * 8, sw = L_MISC + (lane & 60), keep = 0xfff8fff8u, ones = 0x3f803f80u;
  int mode = loc >= 30 ? 2 : 0, far8 = 8 * GSTR;
  PIN(xw); PIN(xr_); PIN(rw); PIN(rr_); PIN(r8); PIN(uo); PIN(sa); PIN(sw); PIN(keep);
  asm volatile("" : "+s"(ones), "+s"(mode), "+s"(far8));
  if (wave == 1) __builtin_amdgcn_s_setprio(3);
  SCHED();
  asm volatile("s_waitcnt lgkmcnt(0) ; (2) first loads" : "+{s33}"(sep), "+{v[2:5]}"(ga), "+{v[6:9]}"(gb), "+{v[10:13]}"(gc), "+{v[14:17]}"(gd) : "{v1}"(epoch) : "memory");
  const unsigned tag = sep + 1u;
  v4u tb = tag_bits(tag);
  asm volatile("" : "+s"(tb));
  SCHED();
  {
#pragma unroll 1
    do {
#define DOT_(s, q, x, cs, cq) "v_dot2_f32_bf16 " s ", " x ", %[ones], " cs "\n\tv_dot2_f32_bf16 " q ", " x ", " x ", " cq "\n\t"
#define RED_(s, q, how) "v_add_f32_dpp v18, " s ", " s " " how "\n\tv_add_f32_dpp v19, " q ", " q " " how "\n\ts_nop 0\n\t"
#define SUMS_(s, q, a, b, c, d) DOT_(s, q, a, "0", "0") DOT_(s, q, b, s, q) DOT_(s, q, c, s, q) DOT_(s, q, d, s, q)
      asm volatile("s_cmp_eq_u32 %[mode], 1\n\ts_cbranch_scc1 1f\n\ts_waitcnt vmcnt(1) ; first loads\n1:\n\t"
                   "ds_write_b128 %[xw], v[2:5]\n\t" SUMS_("v18", "v19", "v2", "v3", "v4", "v5")
                   "s_cbranch_scc1 2f\n\ts_waitcnt vmcnt(0)\n2:\n\t" T3_ASM
                   "ds_write_b128 %[xw], v[6:9] offset:128\n\t" SUMS_("v20", "v21", "v6", "v7", "v8", "v9")
                   RED_("v18", "v19", "row_half_mirror row_mask:0xf bank_mask:0x5") RED_("v20", "v21", "row_half_mirror row_mask:0xf bank_mask:0xa")
                   RED_("v18", "v19", "quad_perm:[1,0,3,2] row_mask:0xf bank_mask:0xf") RED_("v18", "v19", "quad_perm:[2,3,0,1] row_mask:0xf bank_mask:0xf")
                   "s_cmp_lg_u32 %[mode], 2\n\tds_write_b64 %[rw], v[18:19]\n\ts_cbranch_scc1 3f\n\t"
                   "global_load_dwordx4 v[10:13], v10, %[x] ; own\n\tglobal_load_dwordx4 v[14:17], v14, %[x] ; own\n3:"
                   : "+{v[10:13]}"(gc), "+{v[14:17]}"(gd) T3_OUT
                   : "{v[2:5]}"(ga), "{v[6:9]}"(gb), [mode] "s"(mode), [ones] "s"(ones), [xw] "v"(xw), [rw] "v"(rw), [x] "s"(kx)
                   : "v18", "v19", "v20", "v21", "scc", "memory");
      STAMP(10);
      if (wave >= L1_EARLY) { BAR(); STAMP(11); SCHED(); }
      asm volatile("" : "+s"(wave));
      f32x2 xt[2][4];
#pragma unroll
      for (int i = 0; i < 4; i++) {
        const v2u v = ldtr(xr_ + (i >> 1) * 64 + (i & 1) * 1152);
        xt[i >> 1][2 * (i & 1)] = f32x2{blo(v.x), bhi(v.x)};
        xt[i >> 1][2 * (i & 1) + 1] = f32x2{blo(v.y), bhi(v.y)};
      }
#pragma unroll
      for (int i = 0; i < 8; i++) PIN(xt[i >> 2][i & 3]);
      if (wave < L1_EARLY) { BAR(); STAMP(11); SCHED(); }
      asm volatile("" : "+s"(wave));
      if (wave == 1) {
        const f32x2 a = *(const LAS f32x2*)rr_, b = *(const LAS f32x2*)(rr_ + 512), e = *(const LAS f32x2*)(r8 + 1024);
        SCHED();
        const f32x2 h = a + b;
        float hs = h.x, hq = h.y;
        asm("s_nop 1\n\tv_add_f32_dpp %0, %0, %0 quad_perm:[1,0,3,2] row_mask:0xf bank_mask:0xf\n\tv_add_f32_dpp %1, %1, %1 quad_perm:[1,0,3,2] row_mask:0xf bank_mask:0xf\n\t"
            "s_nop 0\n\tv_add_f32_dpp %0, %0, %0 quad_perm:[2,3,0,1] row_mask:0xf bank_mask:0xf\n\tv_add_f32_dpp %1, %1, %1 quad_perm:[2,3,0,1] row_mask:0xf bank_mask:0xf"
            : "+v"(hs), "+v"(hq));
        float m, sd;
        ln_round(hs + e.x, hq + e.y, m, sd);
        const float rinv = __builtin_amdgcn_rcpf(sd);
        *(LAS float*)sw = rinv;
        *(LAS float*)(sw + 64) = -m * rinv;
      }
      SCHED();
      STAMP(7);
      BAR();
      STAMP(12);
      const LAS f32x4* st = (const LAS f32x4*)sa;
      const f32x4 r0 = st[0], c0 = st[4], r1 = st[1], c1 = st[5];
      const f32x2 rr[4] = {r0.xy, r0.zw, r1.xy, r1.zw}, cc[4] = {c0.xy, c0.zw, c1.xy, c1.zw};
#pragma unroll
      for (int i = 0; i < 2; i++) {
        v4u o;
#pragma unroll
        for (int k = 0; k < 4; k++) o[k] = (pkh2(__builtin_elementwise_fma(xt[i][k], rr[k], cc[k])) & keep) | tb[k];
        __builtin_amdgcn_raw_buffer_store_b128(o, rs, uo, i ? far8 : 0, SC1);
        STAMP(8 + i);
        SCHED();
      }
      if (mode != 2) break;
      mode = 1;
      asm volatile("" : "+s"(mode));
      uo += 16 * 128;
      asm volatile("s_waitcnt vmcnt(2) ; second tile\n\tv_mov_b64 v[2:3], v[10:11]\n\tv_mov_b64 v[4:5], v[12:13]\n\tv_mov_b64 v[6:7], v[14:15]\n\tv_mov_b64 v[8:9], v[16:17]"
                   : "={v[2:5]}"(ga), "={v[6:9]}"(gb) : "{v[10:13]}"(gc), "{v[14:17]}"(gd) : "memory");
      STAMP(5);
      SCHED();
    } while (true);
    __builtin_amdgcn_s_setprio(0);
    BAR();
    STAMP(1);
    if (!ffn) {
      const GAS char* dn = (const GAS char*)kb_[2] + (size_t)xcd * 5184 * 128;
      unsigned pf[2];
#pragma unroll
      for (int i = 0; i < 2; i++) pf[i] = *(const GAS unsigned*)(dn + (unsigned)min((i * 5 + loc - 27) * NT + tid, 5183) * 128u);
#pragma unroll
      for (int i = 0; i < 2; i++) asm volatile("" ::"v"(pf[i]));
      STAMP_FLUSH();
      return;
    }
  }
  v8u FW[2][4];
  v4u FD[2][4];
  v4u Dn[8];
  constexpr int F_GU = 0, F_TIME = 1280, F_ALL = 2048, F_RED = 16384, RSTR = 4608;
    v4u W[2][8];
    const GAS char* wp = (const GAS char*)((lane & 8) ? kb_[1] : kb_[0]) + (unsigned)((64 * wave + 4 * g) * (FF * 2) + 128 * fj + 16 * (lane & 7));
    const rsrc_t rd = __builtin_amdgcn_make_buffer_rsrc((void*)kb_[2], 0, DM * FF * 2, 0x00020000);
    const int wdo = (64 * fj + 8 * g + 4 * (lane & 8)) * (DM * 2) + 128 * wave + 16 * (lane & 7);
    st4(sent4, rs, tid < 288 ? part_at(fj * 4608 + tid * 16) : part_at(fj * 4608 - 4608 + tid * 16) + 8192);
    SCHED();
#pragma unroll
    for (int j = 0; j < 16; j++) W[j >> 3][j & 7] = *(const GAS v4u*)(wp + (16 * (j >> 2) + (j & 3)) * (FF * 2));
    SCHED();
    STAMP(11);
  asm volatile("s_waitcnt vmcnt(16)" ::: "memory");
  if (wave >= 4) BAR();
  asm volatile("" : "+s"(wave));
  const int tk = xcd, fcol = (loc - 18) * 64 + lane;
  int a_x = slot_at(lane >> 3) + side + wave * 128 + (lane & 7) * 16, a_f = slot_at(tk) + side + (fcol >> 1) * 4;
  int a_po = part_at(fj * 4608 + ((g & 2) + (cl >> 3)) * 1152 + wave * 128 + (cl & 7) * 16) + ((g & 1) << 13);
  int a_pi = part_at((tid >> 3) * 4608 + (xcd & 3) * 1152 + (loc - 18) * 128 + (lane & 7) * 16) + side;
  unsigned cap = 0xFFFFFFFEu;
  PIN(cap);
  v4u pick;
#pragma unroll
  for (int i = 0; i < 4; i++) pick[i] = ((cl >> 1) & 3) == i ? 0x3f80u << ((cl & 1) * 16) : 0u;
  PIN(a_po); PIN(a_pi); PIN(pick);
  unsigned l_xw = F_ALL + wave * 1152 + (lane >> 3) * 144 + (lane & 7) * 16, l_xr = F_ALL + wave * 1152 + (lane & 7) * 144 + g * 16;
  unsigned l_rw = F_RED + wave * RSTR;
  asm volatile("" : "+s"(l_rw));
  int sidx = (lane & 8) ? 0xeeee : 0x4444;
  unsigned l_rg = F_RED + (2 * wave + (g & 1)) * 288 + (lane & 32) * 4 + (lane & 8) * 8 + (lane & 7) * 4, l_ru = l_rg + 32;
  unsigned l_gw = F_GU + (((lane >> 1) & 4) + (wave & 1) * 2 + (g & 1)) * 144 + ((lane & 7) * 8 + (wave >> 1) + (lane >> 5) * 4) * 2;
  unsigned l_gr = F_GU + ((cl & 4) + ((cl + (cl >> 2 & 2)) & 3)) * 144 + g * 16;
  unsigned l_s = (lane & 40) ? F_RED + tid * 16 : F_ALL + wave * 256 + cl * 32 + g * 16, l_f = F_ALL + lane * 4;
  int a_k = slot_at(tk) + side + 2304;
  PIN(a_k); PIN(a_x); PIN(a_f); PIN(l_xw); PIN(l_xr); PIN(sidx); PIN(l_rg); PIN(l_ru); PIN(l_gw); PIN(l_gr); PIN(l_s); PIN(l_f);
#pragma unroll
    for (int ks = 0; ks < 2; ks++)
#pragma unroll
      for (int c = 0; c < 4; c++) {
#pragma unroll
        for (int i = 0; i < 8; i++)
          FW[ks][c][i] = __builtin_amdgcn_perm(W[ks][i | 1][(c >> 1) + 2 * (i & 1)], W[ks][i & 6][(c >> 1) + 2 * (i & 1)], (c & 1) ? 0x07060302u : 0x05040100u);
        PIN(FW[ks][c]);
      }
  SCHED();
  if (wave < 4) BAR();
  STAMP(12);
  st1(tag, rs, tid == 0 ? WS_INIT + (NCONV + fj) * 64 : OOB);
  v2u ts = ld2(rs, WS_TIME + xcd * 128);
  SCHED();
#pragma unroll
  for (int j = 0; j < 8; j++) Dn[j] = __builtin_amdgcn_raw_buffer_load_b128(rd, wdo, j * (DM * 2), 0);
  SCHED();
  STAMP(2);
  if (RARE(__builtin_amdgcn_readfirstlane(ts.x) != tag)) {
#pragma unroll 1
    for (int sp = 0; sp < SPIN_CAP && __builtin_amdgcn_readfirstlane(ts.x) != tag; sp++) {
      __builtin_amdgcn_s_sleep(4);
      MEMBAR();
      ts = ld2(rs, WS_TIME + xcd * 128);
    }
    PIN(ts);
  }
  const unsigned due = __builtin_amdgcn_readfirstlane(ts.y);
  {
    const int flip = (due & 1u) << 13;
    a_x ^= flip; a_f ^= flip; a_k ^= flip; a_po ^= flip; a_pi ^= flip;
    PIN(a_x); PIN(a_f); PIN(a_k); PIN(a_po); PIN(a_pi);
  }
  unsigned w2, h2, fkey, f0, f1;
  asm volatile("" : "=v"(w2), "=v"(h2), "=v"(fkey), "=v"(f0), "=v"(f1));
  auto filled = [&]() {
    f0 = ld1(rs, WS_INIT + (NCONV + lane) * 64);
    f1 = ld1(rs, WS_INIT + (NCONV + 64 + (lane & 7)) * 64);
  };
  auto flook = [&]() {
    MEMBAR();
    w2 = ld1(rs, a_f);
    h2 = ld1(rs, a_f + 1152);
    fkey = ld1(rs, a_k);
  };
  {
    STAMP(10);
    int late = (int)(now() - due) - (T_SEL - T_LATE);
    v4u xv = sent4;
    if (late >= 0) xv = ld4(rs, a_x);
    SCHED();
#pragma unroll
    for (int j = 0; j < 8; j++) {
#pragma unroll
      for (int i = 0; i < 2; i++) {
        const int lo = (int)Dn[j][i], hi = (int)Dn[j][2 + i];
        Dn[j][i] = (unsigned)__builtin_amdgcn_update_dpp(lo, hi, 0x128, 0xf, 0xc, false);
        Dn[j][2 + i] = (unsigned)__builtin_amdgcn_update_dpp(hi, lo, 0x128, 0xf, 0x3, false);
      }
      if (j & 1) {
#pragma unroll
        for (int q = 0; q < 8; q++) FD[q >> 2][q & 3][j >> 1] = __builtin_amdgcn_perm(Dn[j][q >> 1], Dn[j - 1][q >> 1], (q & 1) ? 0x07060302u : 0x05040100u);
        SCHED();
      }
    }
#pragma unroll
    for (int q = 0; q < 8; q++) PIN(FD[q >> 2][q & 3]);
    f32x4 a[4];
#pragma unroll
    for (int c = 0; c < 4; c++) { a[c] = f32x4{0.f, 0.f, 0.f, 0.f}; PIN(a[c]); }
    SCHED();
    STAMP(3);
    asm volatile("" : "+s"(late), "+s"(wave));
    if (late < 0) wait_until(due, T_SEL);
    if (wave == 1) filled();
    asm volatile("" : "+s"(late), "+s"(wave));
    if (late < 0) xv = ld4(rs, a_x);
    POLL(4, valid4(xv), xv = ld4(rs, a_x));
    STAMP(6);
    *(LAS v4u*)l_xw = xv;
    MEMBAR();
    const v4u x0 = *(const LAS v4u*)l_xr, x1 = *(const LAS v4u*)(l_xr + 64);
#pragma unroll
    for (int c = 0; c < 6; c++) {
      SCHED();
      if (c < 4) {
        a[c] = smfmac_bf(x0, FW[0][c], a[c], sidx);
        a[c] = smfmac_bf(x1, FW[1][c], a[c], sidx);
      }
      SCHED();
      if (c == 2) asm volatile("s_mov_b32 m0, %0\n\ts_nop 2" ::"s"(l_rw));
      if (c > 1)
        asm volatile("ds_write_addtid_b32 %0 offset:%4\n\tds_write_addtid_b32 %1 offset:%4 + 288\n\tds_write_addtid_b32 %2 offset:%4 + 576\n\t"
                     "ds_write_addtid_b32 %3 offset:%4 + 864" ::"v"(a[c - 2][0]), "v"(a[c - 2][1]), "v"(a[c - 2][2]), "v"(a[c - 2][3]), "n"((c - 2) * 1152) : "memory");
    }
  }
  BAR();
  STAMP(4);
  asm volatile("" : "+s"(wave));
  float fh, fw, sgm;
  int oo;
  asm volatile("" : "=v"(fh), "=v"(fw), "=v"(sgm), "=v"(oo));
  if (wave < 8) {
    f32x2 gv[4], uv[4];
#pragma unroll
    for (int w = 0; w < 4; w++) {
      gv[w] = f32x2{*(const LAS float*)(l_rg + 2 * w * RSTR), *(const LAS float*)(l_rg + (2 * w + 1) * RSTR)};
      uv[w] = f32x2{*(const LAS float*)(l_ru + 2 * w * RSTR), *(const LAS float*)(l_ru + (2 * w + 1) * RSTR)};
    }
    const float g8 = *(const LAS float*)(l_rg + 8 * RSTR), u8 = *(const LAS float*)(l_ru + 8 * RSTR);
    SCHED();
    f32x2 g2 = (gv[0] + gv[1]) + (gv[2] + gv[3]), u2 = (uv[0] + uv[1]) + (uv[2] + uv[3]);
    PIN(g2); PIN(u2);
    float gs = g2.x + g2.y;
    PIN(gs);
    gs += g8;
    PIN(gs);
    const float us = (u2.x + u2.y) + u8;
    const float gp = rbf(gs);
    const float gq = rbf(gp * __builtin_amdgcn_rcpf(1.0f + __expf(-gp)));
    *(LAS u16*)l_gw = (u16)pkbf(gq * rbf(us), 0.f);
  }
  asm volatile("" : "+s"(wave));
  if (wave == 8) flook();
  if (wave == 1) POLL(6, f0 == tag && f1 == tag, filled());
  BAR();
  STAMP(5);
  {
    const v4u a0 = *(const LAS v4u*)l_gr, a1 = *(const LAS v4u*)(l_gr + 64);
    f32x4 o[4];
#pragma unroll
    for (int c = 0; c < 4; c++) o[c] = mfma_bf(a0, FD[0][c], f32x4{0.f, 0.f, 0.f, 0.f});
    SCHED();
#pragma unroll
    for (int c = 0; c < 4; c++) {
      o[c] = mfma_bf(a1, FD[1][c], o[c]);
      SCHED();
    }
    const unsigned p0 = pkbf(o[0][0], o[1][0]), q0 = pkbf(o[0][1], o[1][1]), p1 = pkbf(o[2][0], o[3][0]), q1 = pkbf(o[2][1], o[3][1]);
    v4u v;
#define MINX(d, s, banks) "v_min_u32_dpp " d ", " s ", %5 row_ror:8 row_mask:0xf bank_mask:" banks "\n\t"
    asm("v_min_u32 v0, %1, %5\n\tv_min_u32 v2, %3, %5\n\tv_min_u32 v1, %2, %5\n\tv_min_u32 v3, %4, %5\n\t"
        MINX("v0", "%3", "0xc") MINX("v2", "%1", "0x3") MINX("v1", "%4", "0xc") MINX("v3", "%2", "0x3")
        : "=&{v[0:3]}"(v) : "v"(p0), "v"(p1), "v"(q0), "v"(q1), "v"(cap));
    st4(v, rs, a_po);
  }
  STAMP(7);
  const unsigned sent_at = now();
  asm volatile("" : "+s"(wave));
  if (wave == 8) {
    POLL(6, valid4(v4u{w2, h2, fkey, 0u}), flook());
    fw = (fcol & 1) ? bhi(w2) : blo(w2);
    fh = (fcol & 1) ? bhi(h2) : blo(h2);
    const unsigned kb = fkey & 0xffff0000u;
    const float rv = __uint_as_float((kb & 0x80000000u) ? (kb & 0x7fffffffu) : (~kb & 0xffff0000u));
    sgm = rbf(__builtin_amdgcn_rcpf(1.0f + __expf(-rv)));
    oo = (SEQ - 1 - (int)(fkey & 0x7ffu)) * 1152 + fcol * 2;
    PIN(fw); PIN(fh); PIN(sgm); PIN(oo);
  }
  wait_until(sent_at, T_PART);
  {
    v4u a;
    f32x4 t;
    auto take = [&]() {
      a = ld4(rs, a_pi);
      t = mfma_bf(pick, a, f32x4{0.f, 0.f, 0.f, 0.f});
      SCHED();
    };
    take();
    POLL(5, valid4(a), take());
    SCHED();
#define ROR8(n) "v_add_f32_dpp v" #n ", v" #n ", v" #n " row_ror:8 row_mask:0xf bank_mask:0xf\n\t"
    asm volatile("s_nop 3\n\t" ROR8(0) ROR8(1) ROR8(2) ROR8(3) "ds_write_b128 %1, v[0:3]" : "+{v[0:3]}"(t) : "v"(l_s) : "memory");
  }
  STAMP(8);
  BAR();
  if (wave == 8) {
    f32x2 t[4];
#pragma unroll
    for (int i = 0; i < 4; i++) t[i] = f32x2{*(const LAS float*)(l_f + i * 512), *(const LAS float*)(l_f + i * 512 + 256)};
    const float t8 = *(const LAS float*)(l_f + 2048);
    SCHED();
    f32x2 z = (t[0] + t[1]) + (t[2] + t[3]);
    PIN(z);
    const float y = rbf((z.x + t8) + z.y);
    __builtin_amdgcn_raw_buffer_store_b16((u16)pkbf(fh + rbf(fw + rbf(y * sgm)), 0.f), ro, oo, 0, SC1);
  }
  STAMP(9);
  STAMP_FLUSH();
  asm volatile("s_endpgm\n\t.p2align 12\n\t.skip (4096 - (Lk_conv - Lk_gen)) & 4095\nLk_gen:" ::: "memory");
}

}

extern "C" void kernel_launch(void* const* d_in, const int* in_sizes, int n_in, void* d_out, int out_size,
                              void* d_ws, size_t ws_size, hipStream_t stream) {
  (void)hipFuncSetAttribute((const void*)k_main, hipFuncAttributeMaxDynamicSharedMemorySize, LDS_BYTES);
  Params p;
  p.x = (const u16*)d_in[0];
  p.lar = (const u16*)d_in[1];
  p.aim = (const u16*)d_in[2];
  p.crl = (const u16*)d_in[3];
  p.cim = (const u16*)d_in[4];
  p.ldt = (const u16*)d_in[5];
  p.dskip = (const u16*)d_in[6];
  p.wr = (const u16*)d_in[8];
  p.wg = (const u16*)d_in[9];
  p.wu = (const u16*)d_in[10];
  p.wd = (const u16*)d_in[11];
  p.out = (u16*)d_out;
  const size_t skip = (size_t)(-(uintptr_t)d_ws) & ((2u << 20) - 1);
  p.ws = (char*)d_ws + (skip + WS_END <= ws_size ? skip : 0);
  k_main<<<256, NT, LDS_BYTES, stream>>>(p);
}
```
